```python
import torch

def _softmax(x, axis=-1):
    return torch.softmax(x, dim=axis)
def _gelu(x):
    return torch.nn.functional.gelu(x, approximate="tanh")
def _silu(x):
    return torch.nn.functional.silu(x)
def _sigmoid(x):
    return torch.sigmoid(x)
def _layernorm(x, axis=-1, eps=1e-5):
    m = x.mean(dim=axis, keepdim=True)
    v = x.var(dim=axis, keepdim=True, unbiased=False)
    return (x - m) / torch.sqrt(v + eps)
def _scatter_add(tgt, idx, src, axis=0):
    return tgt.clone().index_add_(axis, idx, src)
def _segment_sum(src, seg, n):
    out = torch.zeros((n,) + src.shape[1:], dtype=src.dtype)
    return out.index_add_(0, seg, src)
def _pad(x, pw):
    flat = tuple(v for pair in reversed(pw) for v in pair)
    return torch.nn.functional.pad(x, flat)

kernel_name = 'standalone__structured_sparse_n_m__ragged'

def setup_inputs():

    _w = (lambda a: (lambda t: t.to(torch.bfloat16) if t.is_floating_point() else t)(torch.as_tensor(a)))
    B, S, D = 16, 4096, 576
    assert D % 4 == 0, "d_model must be divisible by M=4 for 2:4 sparsity"
    return {
        "x": _w((lambda *s: torch.randn(*s, dtype=torch.bfloat16) * 0.05)(B, S, D)),
        "w": _w((lambda *s: torch.randn(*s, dtype=torch.bfloat16) * 0.05)(D, D)),
        "b": _w((lambda *s: torch.randn(*s, dtype=torch.bfloat16) * 0.05)(D)),
    }

def reference(x, w, b):
    B, S, D = x.shape
    N, M = 2, 4
    G = D // M
    wg = w.reshape(G, M, D)
    mag = torch.abs(wg)
    order = torch.argsort(mag, 1)
    thr = (lambda x, i, axis: torch.take_along_dim(x, i, dim=axis))(mag, order[:, M - N:M - N + 1, :], 1)
    mask = (lambda x, dt: x.to(dt))(mag >= thr, w.dtype)
    wp = (wg * mask).reshape(D, D)
    return torch.einsum("bsd,df->bsf", x, wp) + b
```

```cpp
#include <hip/hip_runtime.h>
#include <hip/hip_bf16.h>
#include <stdint.h>

namespace {
constexpr int DM    = 576;
constexpr int TROWS = 16;
constexpr int NBLK  = 256;
constexpr int NTILE = 16;
constexpr int LDS_END = 163776;

__global__ __launch_bounds__(256, 1) void spmm_main(const uint16_t* x, const uint16_t* w,
                                                    const uint16_t* b, uint16_t* out,
                                                    char* ws) {
  __shared__ __attribute__((aligned(16))) char smem[LDS_END];
  const unsigned lds0 = (unsigned)(size_t)(__attribute__((address_space(3))) char*)smem;
  int tid0 = threadIdx.x;
  const unsigned long long ka = (unsigned long long)__builtin_amdgcn_kernarg_segment_ptr();
  asm volatile(
    "s_load_dwordx8 s[8:15], s[34:35], 0\n\t"
    "s_load_dwordx2 s[60:61], s[34:35], 32\n\t"
    "s_memrealtime s[24:25]\n\t"
    "spmm_code_begin:\n\t"
    "s_cmp_lt_u32 s62, 224\n\t"
    "s_cbranch_scc1 .Lnocp_%=\n\t"
    "s_getpc_b64 s[40:41]\n\t"
    "s_bfe_u32 s16, s62, 0x10003\n\t"
    "s_lshl_b32 s16, s16, 15\n\t"
    "s_mov_b32 m0, 150512\n\t"
    "v_lshl_add_u32 v1, v0, 7, s16\n\t"
    "v_min_u32 v1, spmm_code_end-spmm_code_begin-24, v1\n\t"
    "global_load_lds_dword v1, s[40:41]\n\t"
    ".Lnocp_%=:\n\t"
    "v_lshrrev_b32 v2, 6, v0\n\t"
    "v_and_b32 v1, 63, v0\n\t"
    "v_readfirstlane_b32 s53, v2\n\t"
    "v_lshlrev_b32 v92, 4, v1\n\t"
    "v_and_b32 v2, 15, v1\n\t"
    "v_lshrrev_b32 v3, 4, v1\n\t"
    "s_bitcmp1_b32 s62, 2\n\t"
    "s_cbranch_scc1 .Lnone_%=\n\t"
    "s_lshr_b32 s16, s62, 3\n\t"
    "s_bfe_u32 s17, s62, 0x10001\n\t"
    "s_lshl1_add_u32 s16, s16, s17\n\t"
    "s_and_b32 s17, s62, 1\n\t"
    "s_lshl_b32 s17, s17, 6\n\t"
    "s_add_u32 s16, s16, s17\n\t"
    "s_and_b32 s17, s62, 4\n\t"
    "s_lshl_b32 s17, s17, 5\n\t"
    "s_add_u32 s63, s16, s17\n\t"
    "s_cmp_lt_u32 s63, 36\n\t"
    "s_cbranch_scc1 .Ls2_%=\n\t"
    "s_cmp_ge_u32 s63, 118\n\t"
    "s_cbranch_scc1 .Lnone_%=\n\t"
    "s_sub_u32 s20, s63, 36\n\t"
    "s_mov_b32 s21, s53\n\t"
    "s_and_b32 s22, s20, 0x7e\n\t"
    "s_and_b32 s23, s20, 1\n\t"
    "v_lshrrev_b32 v4, 5, v1\n\t"
    "v_add_u32 v4, s22, v4\n\t"
    "v_min_u32 v4, 0x50, v4\n\t"
    "v_subrev_u32 v5, 36, v4\n\t"
    "v_mul_u32_u24 v6, 0xcd, v5\n\t"
    "v_lshrrev_b32 v6, 10, v6\n\t"
    "v_mad_i32_i24 v5, v6, -5, v5\n\t"
    "v_add_u32 v5, 4, v5\n\t"
    "v_cmp_gt_u32 vcc, 36, v4\n\t"
    "v_lshrrev_b32 v7, 2, v4\n\t"
    "v_and_b32 v8, 3, v4\n\t"
    "v_cndmask_b32 v6, v6, v7, vcc\n\t"
    "v_cndmask_b32 v5, v5, v8, vcc\n\t"
    "v_and_b32 v7, 4, v5\n\t"
    "v_and_b32 v8, 3, v5\n\t"
    "v_lshlrev_b32 v7, 5, v7\n\t"
    "v_lshl_add_u32 v7, v8, 3, v7\n\t"
    "v_and_b32 v8, 12, v2\n\t"
    "v_mad_u32_u24 v7, v8, 6, v7\n\t"
    "s_lshl_b32 s16, s21, 8\n\t"
    "s_lshl_b32 s17, s21, 5\n\t"
    "s_add_u32 s17, s17, 1024\n\t"
    "v_cmp_gt_u32 vcc, 8, v5\n\t"
    "v_add_u32 v7, s16, v7\n\t"
    "v_mov_b32 v8, s17\n\t"
    "v_cndmask_b32 v7, v8, v7, vcc\n\t"
    "v_lshl_add_u32 v7, v2, 1, v7\n\t"
    "s_mul_i32 s16, s23, 9216\n\t"
    "s_add_u32 s16, s16, 4096\n\t"
    "s_mov_b32 s17, 73728\n\t"
    "v_mad_u32_u24 v7, v6, s17, v7\n\t"
    "v_and_b32 v8, 16, v1\n\t"
    "v_mul_u32_u24 v8, 2304, v8\n\t"
    "v_add3_u32 v7, v7, v8, s16\n\t"
    "v_add_u32 v8, 18432, v7\n\t"
    "s_waitcnt lgkmcnt(0)\n\t"
    "global_load_ushort v16, v7, s[10:11] offset:-4096\n\t"
    "global_load_ushort v17, v7, s[10:11] offset:-2944\n\t"
    "global_load_ushort v18, v7, s[10:11] offset:-1792\n\t"
    "global_load_ushort v19, v7, s[10:11] offset:-640\n\t"
    "global_load_ushort v20, v7, s[10:11] offset:512\n\t"
    "global_load_ushort v21, v7, s[10:11] offset:1664\n\t"
    "global_load_ushort v22, v7, s[10:11] offset:2816\n\t"
    "global_load_ushort v23, v7, s[10:11] offset:3968\n\t"
    "global_load_ushort v24, v8, s[10:11] offset:-4096\n\t"
    "global_load_ushort v25, v8, s[10:11] offset:-2944\n\t"
    "global_load_ushort v26, v8, s[10:11] offset:-1792\n\t"
    "global_load_ushort v27, v8, s[10:11] offset:-640\n\t"
    "global_load_ushort v28, v8, s[10:11] offset:512\n\t"
    "global_load_ushort v29, v8, s[10:11] offset:1664\n\t"
    "global_load_ushort v30, v8, s[10:11] offset:2816\n\t"
    "global_load_ushort v31, v8, s[10:11] offset:3968\n\t"
    "s_branch .Ls1t_%=\n\t"
    ".Ls2_%=:\n\t"
    "s_mul_i32 s21, s63, 57\n\t"
    "s_lshr_b32 s21, s21, 9\n\t"
    "s_mul_i32 s20, s21, 9\n\t"
    "s_sub_u32 s20, s63, s20\n\t"
    "s_cmp_eq_u32 s53, 3\n\t"
    "s_cbranch_scc1 .Ls2w3_%=\n\t"
    "s_lshr_b32 s16, s20, 2\n\t"
    "s_lshl_b32 s16, s16, 7\n\t"
    "s_and_b32 s17, s20, 3\n\t"
    "s_lshl3_add_u32 s16, s17, s16\n\t"
    "s_lshl_b32 s17, s21, 8\n\t"
    "s_add_u32 s16, s16, s17\n\t"
    "s_lshl_b32 s17, s21, 5\n\t"
    "s_add_u32 s17, s17, 1024\n\t"
    "s_cmp_lt_u32 s20, 8\n\t"
    "s_cselect_b32 s16, s16, s17\n\t"
    "s_cselect_b32 s17, 5, 3\n\t"
    "v_lshrrev_b32 v4, 2, v0\n\t"
    "v_and_b32 v5, 3, v0\n\t"
    "v_mul_u32_u24 v7, 4608, v4\n\t"
    "v_lshlrev_b32 v6, s17, v5\n\t"
    "v_add3_u32 v7, v7, v6, s16\n\t"
    "v_add_u32 v8, 221184, v7\n\t"
    "v_add_u32 v9, 442368, v7\n\t"
    "s_waitcnt lgkmcnt(0)\n\t"
    "global_load_dwordx2 v[16:17], v7, s[10:11] offset:0\n\t"
    "global_load_dwordx2 v[18:19], v7, s[10:11] offset:1152\n\t"
    "global_load_dwordx2 v[20:21], v7, s[10:11] offset:2304\n\t"
    "global_load_dwordx2 v[22:23], v7, s[10:11] offset:3456\n\t"
    "global_load_dwordx2 v[24:25], v8, s[10:11] offset:0\n\t"
    "global_load_dwordx2 v[26:27], v8, s[10:11] offset:1152\n\t"
    "global_load_dwordx2 v[28:29], v8, s[10:11] offset:2304\n\t"
    "global_load_dwordx2 v[30:31], v8, s[10:11] offset:3456\n\t"
    "global_load_dwordx2 v[32:33], v9, s[10:11] offset:0\n\t"
    "global_load_dwordx2 v[34:35], v9, s[10:11] offset:1152\n\t"
    "global_load_dwordx2 v[36:37], v9, s[10:11] offset:2304\n\t"
    "global_load_dwordx2 v[38:39], v9, s[10:11] offset:3456\n\t"
    "s_lshl_b32 s59, s53, 3\n\t"
    "s_lshr_b32 s59, 0x64753120, s59\n\t"
    "s_lshl_b32 s64, s59, 4\n\t"
    "s_and_b32 s64, s64, 0x70\n\t"
    "s_and_b32 s59, s59, 0x70\n\t"
    "v_cmp_gt_u32 vcc, 48, v1\n\t"
    "v_xor_b32 v237, s64, v92\n\t"
    "v_xor_b32 v239, s59, v92\n\t"
    "v_cndmask_b32 v238, v239, v237, vcc\n\t"
    "s_and_b32 s58, s62, 7\n\t"
    "s_lshl_b32 s58, s58, 5\n\t"
    "s_lshr_b32 s52, s62, 3\n\t"
    "s_add_u32 s58, s58, s52\n\t"
    "s_mul_i32 s58, s58, 18432\n\t"
    "s_mul_i32 s52, s53, 4608\n\t"
    "s_add_u32 s59, s58, s52\n\t"
    "s_mul_i32 s52, s53, 27648\n\t"
    "s_sub_u32 s42, 75497472, s59\n\t"
    "s_mov_b32 s43, 0x20000\n\t"
    "s_mov_b32 s68, 0\n\t"
    "s_mov_b32 s36, s52\n\t"
    "s_mov_b32 s18, 0x7fff7fff\n\t"
    "s_add_u32 s40, s8, s59\n\t"
    "s_addc_u32 s41, s9, 0\n\t"
    "v_min_u32 v44, 35, v1\n\t"
    "v_and_b32 v45, 3, v44\n\t"
    "v_and_b32 v46, 16, v44\n\t"
    "v_lshlrev_b32 v46, 3, v46\n\t"
    "v_and_b32 v47, 12, v44\n\t"
    "v_lshl_add_u32 v46, v47, 1, v46\n\t"
    "v_lshl_add_u32 v46, v45, 5, v46\n\t"
    "s_lshl_b32 s16, s53, 8\n\t"
    "s_lshl_b32 s17, s53, 5\n\t"
    "s_add_u32 s17, s17, 1024\n\t"
    "v_cmp_gt_u32 vcc, 32, v44\n\t"
    "v_add_u32 v46, s16, v46\n\t"
    "v_lshl_add_u32 v47, v45, 3, s17\n\t"
    "v_cndmask_b32 v198, v47, v46, vcc\n\t"
    "global_load_dwordx2 v[196:197], v198, s[12:13]\n\t"
    "s_add_u32 s16, s60, 917504\n\t"
    "s_addc_u32 s17, s61, 0\n\t"
    "s_load_dwordx2 s[30:31], s[16:17], 0\n\t"
    "s_branch .Ls2t_%=\n\t"
    ".Lnone_%=:\n\t"
    "s_lshl_b32 s59, s53, 3\n\t"
    "s_lshr_b32 s59, 0x64753120, s59\n\t"
    "s_lshl_b32 s64, s59, 4\n\t"
    "s_and_b32 s64, s64, 0x70\n\t"
    "s_and_b32 s59, s59, 0x70\n\t"
    "v_cmp_gt_u32 vcc, 48, v1\n\t"
    "v_xor_b32 v237, s64, v92\n\t"
    "v_xor_b32 v239, s59, v92\n\t"
    "v_cndmask_b32 v238, v239, v237, vcc\n\t"
    "s_and_b32 s58, s62, 7\n\t"
    "s_lshl_b32 s58, s58, 5\n\t"
    "s_lshr_b32 s52, s62, 3\n\t"
    "s_add_u32 s58, s58, s52\n\t"
    "s_mul_i32 s58, s58, 18432\n\t"
    "s_mul_i32 s52, s53, 4608\n\t"
    "s_add_u32 s59, s58, s52\n\t"
    "s_mul_i32 s52, s53, 27648\n\t"
    "s_sub_u32 s42, 75497472, s59\n\t"
    "s_mov_b32 s43, 0x20000\n\t"
    "s_mov_b32 s68, 0\n\t"
    "s_mov_b32 s36, s52\n\t"
    "s_mov_b32 m0, s52\n\t"
    "s_waitcnt lgkmcnt(0)\n\t"
    "s_add_u32 s40, s8, s59\n\t"
    "s_addc_u32 s41, s9, 0\n\t"
    "s_mov_b32 exec_hi, 0\n\t"
    "buffer_load_dwordx4 v237, s[40:43], s68 offen offset:0 nt lds\n\t"
    "s_mov_b32 exec_hi, -1\n\t"
    "buffer_load_dwordx4 v237, s[40:43], s68 offen offset:512 nt lds\n\t"
    "buffer_load_dwordx4 v238, s[40:43], s68 offen offset:1536 nt lds\n\t"
    "buffer_load_dwordx4 v239, s[40:43], s68 offen offset:2560 nt lds\n\t"
    "buffer_load_dwordx4 v239, s[40:43], s68 offen offset:3584 nt lds\n\t"
    "s_add_u32 s36, s36, 4608\n\t"
    "s_add_u32 s68, s68, 4718592\n\t"
    "v_min_u32 v44, 35, v1\n\t"
    "v_and_b32 v45, 3, v44\n\t"
    "v_and_b32 v46, 16, v44\n\t"
    "v_lshlrev_b32 v46, 3, v46\n\t"
    "v_and_b32 v47, 12, v44\n\t"
    "v_lshl_add_u32 v46, v47, 1, v46\n\t"
    "v_lshl_add_u32 v46, v45, 5, v46\n\t"
    "s_lshl_b32 s16, s53, 8\n\t"
    "s_lshl_b32 s17, s53, 5\n\t"
    "s_add_u32 s17, s17, 1024\n\t"
    "v_cmp_gt_u32 vcc, 32, v44\n\t"
    "v_add_u32 v46, s16, v46\n\t"
    "v_lshl_add_u32 v47, v45, 3, s17\n\t"
    "v_cndmask_b32 v198, v47, v46, vcc\n\t"
    "global_load_dwordx2 v[196:197], v198, s[12:13]\n\t"
    "s_add_u32 s16, s60, 917504\n\t"
    "s_addc_u32 s17, s61, 0\n\t"
    "s_load_dwordx2 s[30:31], s[16:17], 0\n\t"
    "s_bitcmp1_b32 s62, 7\n\t"
    "s_cselect_b64 s[72:73], s[14:15], s[8:9]\n\t"
    "s_bfe_u32 s70, s62, 0x40003\n\t"
    "s_bfe_u32 s71, s62, 0x10001\n\t"
    "s_lshl1_add_u32 s70, s70, s71\n\t"
    "s_mul_i32 s75, s70, 31\n\t"
    "s_lshr_b32 s75, s75, 6\n\t"
    "s_add_u32 s71, s71, 11\n\t"
    "s_cmp_lt_u32 s70, 23\n\t"
    "s_cselect_b32 s71, s75, s71\n\t"
    "s_lshl_b32 s71, s71, 5\n\t"
    "s_add_u32 s71, s71, 176\n\t"
    "s_lshl2_add_u32 s75, s62, s53\n\t"
    "s_and_b32 s75, s75, 15\n\t"
    "s_add_u32 s71, s71, s75\n\t"
    "s_lshl_b32 s71, s71, 17\n\t"
    "s_mul_i32 s75, s24, 0x9e3779b1\n\t"
    "s_lshr_b32 s75, s75, 12\n\t"
    "s_lshl_b32 s70, s70, 7\n\t"
    "s_xor_b32 s75, s75, s70\n\t"
    "s_and_b32 s75, s75, 0x1df80\n\t"
    "s_add_u32 s71, s71, s75\n\t"
    "s_lshl_b32 s75, s53, 13\n\t"
    "s_xor_b32 s75, s71, s75\n\t"
    "s_xor_b32 s70, s75, 0x1000\n\t"
    "s_branch .Lnone2_%=\n\t"
    ".Ls1t_%=:\n\t"
    "s_lshl_b32 s59, s53, 3\n\t"
    "s_lshr_b32 s59, 0x64753120, s59\n\t"
    "s_lshl_b32 s64, s59, 4\n\t"
    "s_and_b32 s64, s64, 0x70\n\t"
    "s_and_b32 s59, s59, 0x70\n\t"
    "v_cmp_gt_u32 vcc, 48, v1\n\t"
    "v_xor_b32 v237, s64, v92\n\t"
    "v_xor_b32 v239, s59, v92\n\t"
    "v_cndmask_b32 v238, v239, v237, vcc\n\t"
    "s_and_b32 s58, s62, 7\n\t"
    "s_lshl_b32 s58, s58, 5\n\t"
    "s_lshr_b32 s52, s62, 3\n\t"
    "s_add_u32 s58, s58, s52\n\t"
    "s_mul_i32 s58, s58, 18432\n\t"
    "s_mul_i32 s52, s53, 4608\n\t"
    "s_add_u32 s59, s58, s52\n\t"
    "s_mul_i32 s52, s53, 27648\n\t"
    "s_sub_u32 s42, 75497472, s59\n\t"
    "s_mov_b32 s43, 0x20000\n\t"
    "s_mov_b32 s68, 0\n\t"
    "s_mov_b32 s36, s52\n\t"
    "s_mov_b32 s18, 0x7fff7fff\n\t"
    "s_add_u32 s40, s8, s59\n\t"
    "s_addc_u32 s41, s9, 0\n\t"
    "v_min_u32 v44, 35, v1\n\t"
    "v_and_b32 v45, 3, v44\n\t"
    "v_and_b32 v46, 16, v44\n\t"
    "v_lshlrev_b32 v46, 3, v46\n\t"
    "v_and_b32 v47, 12, v44\n\t"
    "v_lshl_add_u32 v46, v47, 1, v46\n\t"
    "v_lshl_add_u32 v46, v45, 5, v46\n\t"
    "s_lshl_b32 s16, s53, 8\n\t"
    "s_lshl_b32 s17, s53, 5\n\t"
    "s_add_u32 s17, s17, 1024\n\t"
    "v_cmp_gt_u32 vcc, 32, v44\n\t"
    "v_add_u32 v46, s16, v46\n\t"
    "v_lshl_add_u32 v47, v45, 3, s17\n\t"
    "v_cndmask_b32 v198, v47, v46, vcc\n\t"
    "global_load_dwordx2 v[196:197], v198, s[12:13]\n\t"
    "s_add_u32 s16, s60, 917504\n\t"
    "s_addc_u32 s17, s61, 0\n\t"
    "s_load_dwordx2 s[30:31], s[16:17], 0\n\t"
    "s_barrier\n\t"
    "s_mov_b32 s37, 1\n\t"
    "s_call_b64 s[26:27], .Lxt_%=\n\t"
    "s_call_b64 s[28:29], .Lconst_%=\n\t"
    "s_mul_i32 s16, s21, 192512\n\t"
    "s_lshl_b32 s17, s23, 9\n\t"
    "s_add_u32 s16, s16, s17\n\t"
    "v_and_b32 v9, 0x1f0, v92\n\t"
    "v_add_u32 v9, s16, v9\n\t"
    "v_lshl_add_u32 v10, v4, 10, v9\n\t"
    "s_lshr_b32 s17, s20, 3\n\t"
    "s_lshl_b32 s17, s17, 10\n\t"
    "s_and_b32 s16, s20, 6\n\t"
    "s_lshl1_add_u32 s17, s16, s17\n\t"
    "s_add_u32 s17, s17, 82944\n\t"
    "v_add_u32 v9, s17, v9\n\t"
    "s_mov_b32 s16, 1289021588\n\t"
    "s_mov_b32 s17, 1145325716\n\t"
    "s_mov_b32 s19, 0x20002000\n\t"
    "s_movk_i32 s22, 0x202\n\t"
    "v_mov_b32 v11, 0x01000100\n\t"
    "s_mul_i32 s23, s21, 82\n\t"
    "s_add_u32 s23, s23, s20\n\t"
    "s_add_u32 s23, s23, 256\n\t"
    "s_waitcnt vmcnt(5)\n\t"
    "s_memrealtime s[4:5]\n\t"
    "v_lshl_or_b32 v16, v17, 16, v16\n\t"
    "v_lshl_or_b32 v20, v21, 16, v20\n\t"
    "v_lshl_or_b32 v24, v25, 16, v24\n\t"
    "v_lshl_or_b32 v28, v29, 16, v28\n\t"
    "v_lshl_or_b32 v18, v19, 16, v18\n\t"
    "v_lshl_or_b32 v22, v23, 16, v22\n\t"
    "v_lshl_or_b32 v26, v27, 16, v26\n\t"
    "v_lshl_or_b32 v30, v31, 16, v30\n\t"
    "v_and_b32 v17, s18, v16\n\t"
    "v_and_b32 v21, s18, v20\n\t"
    "v_and_b32 v25, s18, v24\n\t"
    "v_and_b32 v29, s18, v28\n\t"
    "v_and_b32 v19, s18, v18\n\t"
    "v_and_b32 v23, s18, v22\n\t"
    "v_and_b32 v27, s18, v26\n\t"
    "v_and_b32 v31, s18, v30\n\t"
    "v_pk_max_u16 v40, v17, v19\n\t"
    "v_pk_max_u16 v48, v21, v23\n\t"
    "v_pk_max_u16 v56, v25, v27\n\t"
    "v_pk_max_u16 v64, v29, v31\n\t"
    "v_pk_min_u16 v41, v17, v19\n\t"
    "v_pk_min_u16 v49, v21, v23\n\t"
    "v_pk_min_u16 v57, v25, v27\n\t"
    "v_pk_min_u16 v65, v29, v31\n\t"
    "v_pk_min_u16 v40, v40, v40 op_sel:[0,1] op_sel_hi:[1,0]\n\t"
    "v_pk_min_u16 v48, v48, v48 op_sel:[0,1] op_sel_hi:[1,0]\n\t"
    "v_pk_min_u16 v56, v56, v56 op_sel:[0,1] op_sel_hi:[1,0]\n\t"
    "v_pk_min_u16 v64, v64, v64 op_sel:[0,1] op_sel_hi:[1,0]\n\t"
    "v_pk_max_u16 v41, v41, v41 op_sel:[0,1] op_sel_hi:[1,0]\n\t"
    "v_pk_max_u16 v49, v49, v49 op_sel:[0,1] op_sel_hi:[1,0]\n\t"
    "v_pk_max_u16 v57, v57, v57 op_sel:[0,1] op_sel_hi:[1,0]\n\t"
    "v_pk_max_u16 v65, v65, v65 op_sel:[0,1] op_sel_hi:[1,0]\n\t"
    "v_pk_max_u16 v40, v40, v41\n\t"
    "v_pk_max_u16 v48, v48, v49\n\t"
    "v_pk_max_u16 v56, v56, v57\n\t"
    "v_pk_max_u16 v64, v64, v65\n\t"
    "v_pk_sub_i16 v17, v17, v40\n\t"
    "v_pk_sub_i16 v21, v21, v48\n\t"
    "v_pk_sub_i16 v25, v25, v56\n\t"
    "v_pk_sub_i16 v29, v29, v64\n\t"
    "v_pk_sub_i16 v19, v19, v40\n\t"
    "v_pk_sub_i16 v23, v23, v48\n\t"
    "v_pk_sub_i16 v27, v27, v56\n\t"
    "v_pk_sub_i16 v31, v31, v64\n\t"
    "v_lshrrev_b32 v17, 2, v17\n\t"
    "v_lshrrev_b32 v21, 2, v21\n\t"
    "v_lshrrev_b32 v25, 2, v25\n\t"
    "v_lshrrev_b32 v29, 2, v29\n\t"
    "v_and_b32 v19, 0x80008000, v19\n\t"
    "v_and_b32 v23, 0x80008000, v23\n\t"
    "v_and_b32 v27, 0x80008000, v27\n\t"
    "v_and_b32 v31, 0x80008000, v31\n\t"
    "v_and_or_b32 v17, v17, s19, v19\n\t"
    "v_and_or_b32 v21, v21, s19, v23\n\t"
    "v_and_or_b32 v25, v25, s19, v27\n\t"
    "v_and_or_b32 v29, v29, s19, v31\n\t"
    "v_lshl_or_b32 v17, v17, 15, v17\n\t"
    "v_lshl_or_b32 v21, v21, 15, v21\n\t"
    "v_lshl_or_b32 v25, v25, 15, v25\n\t"
    "v_lshl_or_b32 v29, v29, 15, v29\n\t"
    "v_lshrrev_b32 v17, 26, v17\n\t"
    "v_lshrrev_b32 v21, 26, v21\n\t"
    "v_lshrrev_b32 v25, 26, v25\n\t"
    "v_lshrrev_b32 v29, 26, v29\n\t"
    "v_lshrrev_b64 v[42:43], v17, s[16:17]\n\t"
    "v_lshrrev_b64 v[50:51], v21, s[16:17]\n\t"
    "v_lshrrev_b64 v[58:59], v25, s[16:17]\n\t"
    "v_lshrrev_b64 v[66:67], v29, s[16:17]\n\t"
    "v_and_b32 v42, 15, v42\n\t"
    "v_and_b32 v50, 15, v50\n\t"
    "v_and_b32 v58, 15, v58\n\t"
    "v_and_b32 v66, 15, v66\n\t"
    "v_lshl_or_b32 v17, v42, 14, v42\n\t"
    "v_lshl_or_b32 v21, v50, 14, v50\n\t"
    "v_lshl_or_b32 v25, v58, 14, v58\n\t"
    "v_lshl_or_b32 v29, v66, 14, v66\n\t"
    "v_and_b32 v17, 0x30003, v17\n\t"
    "v_and_b32 v21, 0x30003, v21\n\t"
    "v_and_b32 v25, 0x30003, v25\n\t"
    "v_and_b32 v29, 0x30003, v29\n\t"
    "v_mad_u32_u24 v17, v17, s22, v11\n\t"
    "v_mad_u32_u24 v21, v21, s22, v11\n\t"
    "v_mad_u32_u24 v25, v25, s22, v11\n\t"
    "v_mad_u32_u24 v29, v29, s22, v11\n\t"
    "v_perm_b32 v12, v18, v16, v17\n\t"
    "v_perm_b32 v13, v22, v20, v21\n\t"
    "v_perm_b32 v14, v26, v24, v25\n\t"
    "v_perm_b32 v15, v30, v28, v29\n\t"
    "global_store_dwordx4 v10, v[12:15], s[60:61] sc1\n\t"
    "v_lshl_or_b32 v42, v50, 4, v42\n\t"
    "v_lshl_or_b32 v58, v66, 4, v58\n\t"
    "v_lshl_or_b32 v4, v58, 8, v42\n\t"
    "v_mov_b32 v5, v4\n\t"
    "s_nop 1\n\t"
    "v_permlane32_swap_b32 v4, v5\n\t"
    "s_nop 0\n\t"
    "v_lshl_or_b32 v4, v5, 16, v4\n\t"
    "s_mov_b32 exec_hi, 0\n\t"
    "global_store_dword v9, v4, s[60:61] sc1\n\t"
    "s_mov_b32 exec_hi, -1\n\t"
    "s_barrier\n\t"
    "s_mov_b32 s37, 1\n\t"
    "s_call_b64 s[26:27], .Lxt_%=\n\t"
    "s_mov_b32 s22, 0\n\t"
    "s_waitcnt vmcnt(5)\n\t"
    "s_barrier\n\t"
    "s_cmp_lg_u32 s53, 0\n\t"
    "s_cbranch_scc1 .Ls1j_%=\n\t"
    "s_add_u32 s23, s20, 256\n\t"
    "s_xor_b32 s16, s30, s23\n\t"
    "s_or_b32 s17, s31, s22\n\t"
    "s_lshl_b32 s23, s23, 7\n\t"
    "s_add_u32 s23, s23, 786432\n\t"
    "v_mov_b32 v4, s16\n\t"
    "v_mov_b32 v5, s17\n\t"
    "v_mul_u32_u24 v8, 82048, v1\n\t"
    "v_add_u32 v8, s23, v8\n\t"
    "s_waitcnt lgkmcnt(0)\n\t"
    "s_sub_u32 s4, s4, 145\n\t"
    "s_sub_i32 s4, s4, s24\n\t"
    "s_max_i32 s4, s4, 0\n\t"
    "s_add_u32 s4, s4, s24\n\t"
    "v_mov_b32 v6, s4\n\t"
    "s_waitcnt vmcnt(63)\n\t"
    "s_mov_b64 exec, 0x1\n\t"
    "global_store_dwordx3 v8, v[4:6], s[60:61] sc1\n\t"
    "s_mov_b64 exec, -1\n\t"
    ".Ls1j_%=:\n\t"
    "s_mov_b32 s37, 2\n\t"
    "s_call_b64 s[26:27], .Lxu_%=\n\t"
    "s_branch .Ljoin_%=\n\t"
    ".Lxt_%=:\n\t"
    "s_mov_b32 m0, s36\n\t"
    "s_mov_b32 exec_hi, 0\n\t"
    "buffer_load_dwordx4 v237, s[40:43], s68 offen offset:0 nt lds\n\t"
    "s_mov_b32 exec_hi, -1\n\t"
    "buffer_load_dwordx4 v237, s[40:43], s68 offen offset:512 nt lds\n\t"
    "buffer_load_dwordx4 v238, s[40:43], s68 offen offset:1536 nt lds\n\t"
    "buffer_load_dwordx4 v239, s[40:43], s68 offen offset:2560 nt lds\n\t"
    "buffer_load_dwordx4 v239, s[40:43], s68 offen offset:3584 nt lds\n\t"
    "s_add_u32 s36, s36, 4608\n\t"
    "s_add_u32 s68, s68, 4718592\n\t"
    "s_sub_u32 s37, s37, 1\n\t"
    "s_cmp_lg_u32 s37, 0\n\t"
    "s_cbranch_scc1 .Lxt_%=\n\t"
    "s_setpc_b64 s[26:27]\n\t"
    ".Lxu_%=:\n\t"
    "s_mov_b32 m0, s36\n\t"
    "s_waitcnt vmcnt(4)\n\t"
    "s_mov_b32 exec_hi, 0\n\t"
    "buffer_load_dwordx4 v237, s[40:43], s68 offen offset:0 nt lds\n\t"
    "s_mov_b32 exec_hi, -1\n\t"
    "s_waitcnt vmcnt(4)\n\t"
    "buffer_load_dwordx4 v237, s[40:43], s68 offen offset:512 nt lds\n\t"
    "s_waitcnt vmcnt(4)\n\t"
    "buffer_load_dwordx4 v238, s[40:43], s68 offen offset:1536 nt lds\n\t"
    "s_waitcnt vmcnt(4)\n\t"
    "buffer_load_dwordx4 v239, s[40:43], s68 offen offset:2560 nt lds\n\t"
    "s_waitcnt vmcnt(4)\n\t"
    "buffer_load_dwordx4 v239, s[40:43], s68 offen offset:3584 nt lds\n\t"
    "s_add_u32 s36, s36, 4608\n\t"
    "s_add_u32 s68, s68, 4718592\n\t"
    "s_sub_u32 s37, s37, 1\n\t"
    "s_cmp_lg_u32 s37, 0\n\t"
    "s_cbranch_scc1 .Lxu_%=\n\t"
    "s_setpc_b64 s[26:27]\n\t"
    ".Lxs_%=:\n\t"
    "s_mov_b32 m0, s36\n\t"
    "s_waitcnt vmcnt(4)\n\t"
    "s_mov_b32 exec_hi, 0\n\t"
    "buffer_load_dwordx4 v237, s[40:43], s68 offen offset:0 nt lds\n\t"
    "s_mov_b32 exec_hi, -1\n\t"
    "s_waitcnt vmcnt(4)\n\t"
    "buffer_load_dwordx4 v237, s[40:43], s68 offen offset:512 nt lds\n\t"
    "buffer_load_dwordx4 v238, s[40:43], s68 offen offset:1536 nt lds\n\t"
    "buffer_load_dwordx4 v239, s[40:43], s68 offen offset:2560 nt lds\n\t"
    "s_memtime s[76:77]\n\t"
    "s_waitcnt lgkmcnt(0)\n\t"
    "s_load_dword s74, s[72:73], s75\n\t"
    "s_waitcnt lgkmcnt(0)\n\t"
    "s_memtime s[78:79]\n\t"
    "s_xor_b32 s75, s75, 0x2000\n\t"
    "s_waitcnt lgkmcnt(0)\n\t"
    "s_sub_u32 s81, s78, s76\n\t"
    "s_cmp_lt_u32 s81, 320\n\t"
    "s_cselect_b32 s81, -1, s81\n\t"
    "s_waitcnt vmcnt(4)\n\t"
    "buffer_load_dwordx4 v239, s[40:43], s68 offen offset:3584 nt lds\n\t"
    "s_add_u32 s36, s36, 4608\n\t"
    "s_add_u32 s68, s68, 4718592\n\t"
    "s_setpc_b64 s[26:27]\n\t"
    ".Lconst_%=:\n\t"
    "s_sub_u32 s57, s59, s58\n\t"
    "s_lshl_b32 s64, s53, 8\n\t"
    "s_add_u32 s59, s58, s64\n\t"
    "s_add_u32 s44, s14, s59\n\t"
    "s_addc_u32 s45, s15, 0\n\t"
    "s_sub_u32 s46, 75497472, s59\n\t"
    "s_mov_b32 s47, 0x20000\n\t"
    "s_sub_u32 s57, s57, s64\n\t"
    "s_add_u32 s57, s57, 1024\n\t"
    "s_mul_i32 s59, s53, 192512\n\t"
    "s_add_u32 s48, s60, s59\n\t"
    "s_addc_u32 s49, s61, 0\n\t"
    "s_mov_b32 s50, 192512\n\t"
    "s_mov_b32 s51, 0x20000\n\t"
    "v_lshrrev_b32 v96, 2, v2\n\t"
    "v_mul_u32_u24 v96, 23040, v96\n\t"
    "s_movk_i32 s59, 1152\n\t"
    "v_mad_u32_u24 v248, v2, s59, v96\n\t"
    "v_lshlrev_b32 v96, 1, v2\n\t"
    "v_and_b32 v96, 28, v96\n\t"
    "s_mov_b32 s59, 0x64753120\n\t"
    "v_bfe_u32 v96, s59, v96, 3\n\t"
    "v_lshlrev_b32 v249, 4, v96\n\t"
    "v_lshlrev_b32 v96, 5, v3\n\t"
    "v_xad_u32 v217, v96, v249, v248\n\t"
    "v_xor_b32 v236, 16, v217\n\t"
    "s_mul_i32 s83, s53, 4080\n\t"
    "s_add_u32 s83, s83, 147456\n\t"
    "v_lshl_add_u32 v244, v3, 4, s83\n\t"
    "s_mul_i32 s88, s53, 9216\n\t"
    "s_add_u32 s88, s88, 110592\n\t"
    "v_add_u32 v245, s88, v92\n\t"
    "s_mul_i32 s59, s53, 4080\n\t"
    "s_add_u32 s59, s59, 148464\n\t"
    "v_and_b32 v96, 7, v2\n\t"
    "v_xor_b32 v96, v96, v3\n\t"
    "v_lshlrev_b32 v96, 4, v96\n\t"
    "v_lshl_add_u32 v96, v2, 7, v96\n\t"
    "v_add_u32 v241, s59, v96\n\t"
    "v_xor_b32 v96, 64, v96\n\t"
    "v_add_u32 v95, s59, v96\n\t"
    "v_and_b32 v96, 14, v2\n\t"
    "v_mul_u32_u24 v241, 1152, v96\n\t"
    "v_lshl_add_u32 v241, v3, 5, v241\n\t"
    "v_and_b32 v96, 1, v2\n\t"
    "v_lshl_add_u32 v241, v96, 4, v241\n\t"
    "v_lshrrev_b32 v96, 3, v1\n\t"
    "v_and_b32 v97, 7, v1\n\t"
    "v_mul_u32_u24 v240, 1152, v96\n\t"
    "v_lshl_add_u32 v240, v97, 4, v240\n\t"
    "v_xor_b32 v97, v97, v96\n\t"
    "v_lshlrev_b32 v97, 4, v97\n\t"
    "v_lshl_add_u32 v97, v96, 7, v97\n\t"
    "v_add_u32 v243, s59, v97\n\t"
    "v_lshrrev_b32 v96, 2, v2\n\t"
    "v_mul_u32_u24 v242, 4080, v96\n\t"
    "v_and_b32 v96, 3, v2\n\t"
    "v_lshl_add_u32 v242, v96, 7, v242\n\t"
    "s_lshl_b32 s64, s53, 1\n\t"
    "v_lshrrev_b32 v97, 1, v3\n\t"
    "v_or_b32 v97, s64, v97\n\t"
    "v_xor_b32 v97, v97, v96\n\t"
    "v_lshl_add_u32 v242, v97, 4, v242\n\t"
    "v_and_b32 v96, 1, v3\n\t"
    "v_lshl_add_u32 v242, v96, 3, v242\n\t"
    "v_add_u32 v242, 150512, v242\n\t"
    "v_lshl_or_b32 v96, v249, 16, v249\n\t"
    "v_lshl_or_b32 v97, v248, 16, v248\n\t"
    "v_mov_b32 v246, v96\n\t"
    "v_mov_b32 v247, v96\n\t"
    "v_mov_b32 v94, 0xeeee\n\t"
    "v_mov_b64 v[250:251], 0\n\t"
    "v_mov_b64 v[252:253], 0\n\t"
    "v_mov_b32 v93, 0x555555\n\t"
    "v_mov_b64 v[254:255], 0\n\t"
    "s_mov_b32 s56, 18432\n\t"
    "s_lshl2_add_u32 s82, s62, s53\n\t"
    "v_mov_b64 v[100:101], 0\n\t"
    "v_mov_b64 v[102:103], 0\n\t"
    "ds_write_b128 v245, v[100:103] offset:0\n\t"
    "ds_write_b128 v245, v[100:103] offset:1024\n\t"
    "ds_write_b128 v245, v[100:103] offset:2048\n\t"
    "ds_write_b128 v245, v[100:103] offset:3072\n\t"
    "ds_write_b128 v245, v[100:103] offset:4096\n\t"
    "ds_write_b128 v245, v[100:103] offset:5120\n\t"
    "ds_write_b128 v245, v[100:103] offset:6144\n\t"
    "ds_write_b128 v245, v[100:103] offset:7168\n\t"
    "ds_write_b128 v245, v[100:103] offset:8192\n\t"
    "s_waitcnt lgkmcnt(0)\n\t"
    "s_mul_hi_u32 s16, s30, 1284865837\n\t"
    "s_mul_i32 s17, s30, 1481765933\n\t"
    "s_add_u32 s16, s16, s17\n\t"
    "s_mul_i32 s17, s31, 1284865837\n\t"
    "s_add_u32 s16, s16, s17\n\t"
    "s_mul_i32 s30, s30, 1284865837\n\t"
    "s_add_u32 s30, s30, 4150755663\n\t"
    "s_addc_u32 s31, s16, 335903614\n\t"
    "s_andn2_b32 s31, s31, 3\n\t"
    "s_setpc_b64 s[28:29]\n\t"
    ".Ls2t_%=:\n\t"
    "s_call_b64 s[28:29], .Lconst_%=\n\t"
    "v_lshlrev_b32 v10, 4, v0\n\t"
    "v_add_u32 v10, 147456, v10\n\t"
    "s_mov_b32 s16, 0x05040100\n\t"
    "s_mov_b32 s17, 0x07060302\n\t"
    "s_mov_b32 s4, 0x11111111\n\t"
    "s_mov_b32 s5, s4\n\t"
    "s_mov_b32 s6, 0xf0f0f0f0\n\t"
    "s_mov_b32 s7, s6\n\t"
    "s_lshl3_add_u32 s19, s53, 156736\n\t"
    "v_mov_b32 v11, s19\n\t"
    "v_mad_u32_u24 v4, v5, 48, v4\n\t"
    "s_waitcnt vmcnt(9)\n\t"
    "s_barrier\n\t"
    "s_mov_b32 s37, 2\n\t"
    "s_call_b64 s[26:27], .Lxt_%=\n\t"
    "v_and_b32 v48, s18, v16\n\t"
    "v_and_b32 v64, s18, v17\n\t"
    "v_and_b32 v49, s18, v18\n\t"
    "v_and_b32 v65, s18, v19\n\t"
    "v_and_b32 v50, s18, v20\n\t"
    "v_and_b32 v66, s18, v21\n\t"
    "v_and_b32 v51, s18, v22\n\t"
    "v_and_b32 v67, s18, v23\n\t"
    "v_pk_max_u16 v52, v48, v49\n\t"
    "v_pk_max_u16 v68, v64, v65\n\t"
    "v_pk_min_u16 v53, v48, v49\n\t"
    "v_pk_min_u16 v69, v64, v65\n\t"
    "v_pk_max_u16 v54, v50, v51\n\t"
    "v_pk_max_u16 v70, v66, v67\n\t"
    "v_pk_min_u16 v55, v50, v51\n\t"
    "v_pk_min_u16 v71, v66, v67\n\t"
    "v_pk_min_u16 v54, v52, v54\n\t"
    "v_pk_min_u16 v70, v68, v70\n\t"
    "v_pk_max_u16 v55, v53, v55\n\t"
    "v_pk_max_u16 v71, v69, v71\n\t"
    "v_pk_min_u16 v52, v52, v50\n\t"
    "v_pk_min_u16 v68, v68, v66\n\t"
    "v_pk_max_u16 v54, v54, v55\n\t"
    "v_pk_max_u16 v70, v70, v71\n\t"
    "v_pk_max_u16 v52, v53, v52\n\t"
    "v_pk_max_u16 v68, v69, v68\n\t"
    "v_pk_min_u16 v53, v53, v50\n\t"
    "v_pk_min_u16 v69, v69, v66\n\t"
    "v_pk_min_u16 v52, v52, v51\n\t"
    "v_pk_min_u16 v68, v68, v67\n\t"
    "v_pk_sub_i16 v53, v53, v54\n\t"
    "v_pk_sub_i16 v69, v69, v70\n\t"
    "v_pk_sub_i16 v52, v52, v54\n\t"
    "v_pk_sub_i16 v68, v68, v70\n\t"
    "v_pk_ashrrev_i16 v53, 15, v53 op_sel_hi:[0,1]\n\t"
    "v_pk_ashrrev_i16 v69, 15, v69 op_sel_hi:[0,1]\n\t"
    "v_pk_ashrrev_i16 v52, 15, v52 op_sel_hi:[0,1]\n\t"
    "v_pk_ashrrev_i16 v68, 15, v68 op_sel_hi:[0,1]\n\t"
    "v_bfi_b32 v53, v53, 0, v20\n\t"
    "v_bfi_b32 v69, v69, 0, v21\n\t"
    "v_bfi_b32 v52, v52, 0, v22\n\t"
    "v_bfi_b32 v68, v68, 0, v23\n\t"
    "v_perm_b32 v80, v52, v53, s16\n\t"
    "v_perm_b32 v82, v68, v69, s16\n\t"
    "v_perm_b32 v81, v52, v53, s17\n\t"
    "v_perm_b32 v83, v68, v69, s17\n\t"
    "ds_write_b128 v10, v[80:83] offset:0\n\t"
    "v_or3_b32 v12, v52, v53, v68\n\t"
    "v_bitop3_b32 v12, v12, v69, s18 bitop3:0xa8\n\t"
    "v_cmp_ne_u32 s[94:95], 0, v12\n\t"
    "s_waitcnt vmcnt(15)\n\t"
    "v_and_b32 v48, s18, v24\n\t"
    "v_and_b32 v64, s18, v25\n\t"
    "v_and_b32 v49, s18, v26\n\t"
    "v_and_b32 v65, s18, v27\n\t"
    "v_and_b32 v50, s18, v28\n\t"
    "v_and_b32 v66, s18, v29\n\t"
    "v_and_b32 v51, s18, v30\n\t"
    "v_and_b32 v67, s18, v31\n\t"
    "v_pk_max_u16 v52, v48, v49\n\t"
    "v_pk_max_u16 v68, v64, v65\n\t"
    "v_pk_min_u16 v53, v48, v49\n\t"
    "v_pk_min_u16 v69, v64, v65\n\t"
    "v_pk_max_u16 v54, v50, v51\n\t"
    "v_pk_max_u16 v70, v66, v67\n\t"
    "v_pk_min_u16 v55, v50, v51\n\t"
    "v_pk_min_u16 v71, v66, v67\n\t"
    "v_pk_min_u16 v54, v52, v54\n\t"
    "v_pk_min_u16 v70, v68, v70\n\t"
    "v_pk_max_u16 v55, v53, v55\n\t"
    "v_pk_max_u16 v71, v69, v71\n\t"
    "v_pk_min_u16 v52, v52, v50\n\t"
    "v_pk_min_u16 v68, v68, v66\n\t"
    "v_pk_max_u16 v54, v54, v55\n\t"
    "v_pk_max_u16 v70, v70, v71\n\t"
    "v_pk_max_u16 v52, v53, v52\n\t"
    "v_pk_max_u16 v68, v69, v68\n\t"
    "v_pk_min_u16 v53, v53, v50\n\t"
    "v_pk_min_u16 v69, v69, v66\n\t"
    "v_pk_min_u16 v52, v52, v51\n\t"
    "v_pk_min_u16 v68, v68, v67\n\t"
    "v_pk_sub_i16 v53, v53, v54\n\t"
    "v_pk_sub_i16 v69, v69, v70\n\t"
    "v_pk_sub_i16 v52, v52, v54\n\t"
    "v_pk_sub_i16 v68, v68, v70\n\t"
    "v_pk_ashrrev_i16 v53, 15, v53 op_sel_hi:[0,1]\n\t"
    "v_pk_ashrrev_i16 v69, 15, v69 op_sel_hi:[0,1]\n\t"
    "v_pk_ashrrev_i16 v52, 15, v52 op_sel_hi:[0,1]\n\t"
    "v_pk_ashrrev_i16 v68, 15, v68 op_sel_hi:[0,1]\n\t"
    "v_bfi_b32 v53, v53, 0, v28\n\t"
    "v_bfi_b32 v69, v69, 0, v29\n\t"
    "v_bfi_b32 v52, v52, 0, v30\n\t"
    "v_bfi_b32 v68, v68, 0, v31\n\t"
    "v_perm_b32 v84, v52, v53, s16\n\t"
    "v_perm_b32 v86, v68, v69, s16\n\t"
    "v_perm_b32 v85, v52, v53, s17\n\t"
    "v_perm_b32 v87, v68, v69, s17\n\t"
    "ds_write_b128 v10, v[84:87] offset:3072\n\t"
    "v_or3_b32 v12, v52, v53, v68\n\t"
    "v_bitop3_b32 v12, v12, v69, s18 bitop3:0xa8\n\t"
    "v_cmp_ne_u32 s[96:97], 0, v12\n\t"
    "s_waitcnt vmcnt(11)\n\t"
    "v_and_b32 v48, s18, v32\n\t"
    "v_and_b32 v64, s18, v33\n\t"
    "v_and_b32 v49, s18, v34\n\t"
    "v_and_b32 v65, s18, v35\n\t"
    "v_and_b32 v50, s18, v36\n\t"
    "v_and_b32 v66, s18, v37\n\t"
    "v_and_b32 v51, s18, v38\n\t"
    "v_and_b32 v67, s18, v39\n\t"
    "v_pk_max_u16 v52, v48, v49\n\t"
    "v_pk_max_u16 v68, v64, v65\n\t"
    "v_pk_min_u16 v53, v48, v49\n\t"
    "v_pk_min_u16 v69, v64, v65\n\t"
    "v_pk_max_u16 v54, v50, v51\n\t"
    "v_pk_max_u16 v70, v66, v67\n\t"
    "v_pk_min_u16 v55, v50, v51\n\t"
    "v_pk_min_u16 v71, v66, v67\n\t"
    "v_pk_min_u16 v54, v52, v54\n\t"
    "v_pk_min_u16 v70, v68, v70\n\t"
    "v_pk_max_u16 v55, v53, v55\n\t"
    "v_pk_max_u16 v71, v69, v71\n\t"
    "v_pk_min_u16 v52, v52, v50\n\t"
    "v_pk_min_u16 v68, v68, v66\n\t"
    "v_pk_max_u16 v54, v54, v55\n\t"
    "v_pk_max_u16 v70, v70, v71\n\t"
    "v_pk_max_u16 v52, v53, v52\n\t"
    "v_pk_max_u16 v68, v69, v68\n\t"
    "v_pk_min_u16 v53, v53, v50\n\t"
    "v_pk_min_u16 v69, v69, v66\n\t"
    "v_pk_min_u16 v52, v52, v51\n\t"
    "v_pk_min_u16 v68, v68, v67\n\t"
    "v_pk_sub_i16 v53, v53, v54\n\t"
    "v_pk_sub_i16 v69, v69, v70\n\t"
    "v_pk_sub_i16 v52, v52, v54\n\t"
    "v_pk_sub_i16 v68, v68, v70\n\t"
    "v_pk_ashrrev_i16 v53, 15, v53 op_sel_hi:[0,1]\n\t"
    "v_pk_ashrrev_i16 v69, 15, v69 op_sel_hi:[0,1]\n\t"
    "v_pk_ashrrev_i16 v52, 15, v52 op_sel_hi:[0,1]\n\t"
    "v_pk_ashrrev_i16 v68, 15, v68 op_sel_hi:[0,1]\n\t"
    "v_bfi_b32 v53, v53, 0, v36\n\t"
    "v_bfi_b32 v69, v69, 0, v37\n\t"
    "v_bfi_b32 v52, v52, 0, v38\n\t"
    "v_bfi_b32 v68, v68, 0, v39\n\t"
    "v_perm_b32 v88, v52, v53, s16\n\t"
    "v_perm_b32 v90, v68, v69, s16\n\t"
    "v_perm_b32 v89, v52, v53, s17\n\t"
    "v_perm_b32 v91, v68, v69, s17\n\t"
    "ds_write_b128 v10, v[88:91] offset:6144\n\t"
    "v_or3_b32 v12, v52, v53, v68\n\t"
    "v_bitop3_b32 v12, v12, v69, s18 bitop3:0xa8\n\t"
    "v_cmp_ne_u32 s[98:99], 0, v12\n\t"
    "s_nop 0\n\t"
    "s_lshr_b64 s[22:23], s[94:95], 1\n\t"
    "s_or_b64 s[94:95], s[94:95], s[22:23]\n\t"
    "s_lshr_b64 s[22:23], s[94:95], 2\n\t"
    "s_or_b64 s[94:95], s[94:95], s[22:23]\n\t"
    "s_and_b64 s[94:95], s[94:95], s[4:5]\n\t"
    "s_lshr_b64 s[22:23], s[96:97], 1\n\t"
    "s_or_b64 s[96:97], s[96:97], s[22:23]\n\t"
    "s_lshr_b64 s[22:23], s[96:97], 2\n\t"
    "s_or_b64 s[96:97], s[96:97], s[22:23]\n\t"
    "s_and_b64 s[96:97], s[96:97], s[4:5]\n\t"
    "s_lshr_b64 s[22:23], s[98:99], 1\n\t"
    "s_or_b64 s[98:99], s[98:99], s[22:23]\n\t"
    "s_lshr_b64 s[22:23], s[98:99], 2\n\t"
    "s_or_b64 s[98:99], s[98:99], s[22:23]\n\t"
    "s_and_b64 s[98:99], s[98:99], s[4:5]\n\t"
    "s_lshl_b64 s[96:97], s[96:97], 1\n\t"
    "s_lshl_b64 s[98:99], s[98:99], 2\n\t"
    "s_or_b64 s[94:95], s[94:95], s[96:97]\n\t"
    "s_or_b64 s[94:95], s[94:95], s[98:99]\n\t"
    "v_mov_b32 v12, s94\n\t"
    "v_mov_b32 v13, s95\n\t"
    "ds_write_b64 v11, v[12:13]\n\t"
    "v_mov_b32 v11, 156736\n\t"
    "s_waitcnt lgkmcnt(0)\n\t"
    "s_barrier\n\t"
    "ds_read_b128 v[12:15], v11\n\t"
    "ds_read_b64 v[16:17], v11 offset:16\n\t"
    "s_mov_b32 s37, 1\n\t"
    "s_call_b64 s[26:27], .Lxt_%=\n\t"
    "s_andn2_b64 s[22:23], s[94:95], s[6:7]\n\t"
    "s_and_b64 s[96:97], s[94:95], s[6:7]\n\t"
    "v_mbcnt_lo_u32_b32 v18, s22, 0\n\t"
    "v_mbcnt_lo_u32_b32 v19, s96, 0\n\t"
    "v_mbcnt_hi_u32_b32 v18, s23, v18\n\t"
    "v_mbcnt_hi_u32_b32 v19, s97, v19\n\t"
    "v_lshlrev_b32 v20, 6, v4\n\t"
    "v_lshlrev_b32 v21, 3, v4\n\t"
    "s_waitcnt lgkmcnt(0)\n\t"
    "v_readfirstlane_b32 s70, v12\n\t"
    "v_readfirstlane_b32 s71, v13\n\t"
    "v_readfirstlane_b32 s72, v14\n\t"
    "v_readfirstlane_b32 s73, v15\n\t"
    "v_readfirstlane_b32 s74, v16\n\t"
    "v_readfirstlane_b32 s75, v17\n\t"
    "s_andn2_b64 s[22:23], s[70:71], s[6:7]\n\t"
    "s_bcnt1_i32_b64 s76, s[22:23]\n\t"
    "s_and_b64 s[22:23], s[70:71], s[6:7]\n\t"
    "s_bcnt1_i32_b64 s79, s[22:23]\n\t"
    "s_andn2_b64 s[22:23], s[72:73], s[6:7]\n\t"
    "s_bcnt1_i32_b64 s77, s[22:23]\n\t"
    "s_and_b64 s[22:23], s[72:73], s[6:7]\n\t"
    "s_bcnt1_i32_b64 s80, s[22:23]\n\t"
    "s_andn2_b64 s[22:23], s[74:75], s[6:7]\n\t"
    "s_bcnt1_i32_b64 s78, s[22:23]\n\t"
    "s_and_b64 s[22:23], s[74:75], s[6:7]\n\t"
    "s_bcnt1_i32_b64 s81, s[22:23]\n\t"
    "s_add_u32 s77, s77, s76\n\t"
    "s_add_u32 s78, s78, s77\n\t"
    "s_add_u32 s80, s80, s79\n\t"
    "s_add_u32 s81, s81, s80\n\t"
    "s_cmp_eq_u32 s53, 0\n\t"
    "s_cselect_b32 s76, 0, s76\n\t"
    "s_cselect_b32 s79, 0, s79\n\t"
    "s_cmp_eq_u32 s53, 2\n\t"
    "s_cselect_b32 s76, s77, s76\n\t"
    "s_cselect_b32 s79, s80, s79\n\t"
    "s_sub_i32 s70, 16, s81\n\t"
    "s_max_i32 s70, s70, 8\n\t"
    "s_min_i32 s70, s70, s78\n\t"
    "s_sub_i32 s71, 16, s70\n\t"
    "s_min_i32 s71, s71, s81\n\t"
    "s_sub_u32 s72, s78, s70\n\t"
    "s_sub_u32 s73, s81, s71\n\t"
    "s_add_u32 s73, s73, s72\n\t"
    "v_add_u32 v18, s76, v18\n\t"
    "v_add_u32 v19, s79, v19\n\t"
    "v_subrev_u32 v22, s70, v18\n\t"
    "v_subrev_u32 v23, s71, v19\n\t"
    "v_sub_u32 v19, 15, v19\n\t"
    "v_cndmask_b32 v24, v22, v23, s[6:7]\n\t"
    "v_add_u32 v23, s72, v23\n\t"
    "v_cndmask_b32 v18, v18, v19, s[6:7]\n\t"
    "v_cndmask_b32 v22, v22, v23, s[6:7]\n\t"
    "v_cmp_gt_i32 vcc, 0, v24\n\t"
    "v_sub_u32 v19, 15, v18\n\t"
    "v_min_u32 v19, v18, v19\n\t"
    "v_lshrrev_b32 v18, 3, v18\n\t"
    "v_lshrrev_b32 v23, 2, v19\n\t"
    "v_lshl_add_u32 v18, v23, 1, v18\n\t"
    "v_and_b32 v19, 3, v19\n\t"
    "v_lshl_add_u32 v23, v18, 2, v19\n\t"
    "v_mov_b32 v25, v23\n\t"
    "s_mov_b32 s16, 156768\n\t"
    "v_lshl_add_u32 v23, v23, 2, s16\n\t"
    "v_lshl_add_u32 v25, v25, 2, s16\n\t"
    "v_min_u32 v22, 15, v22\n\t"
    "v_lshl_add_u32 v22, v22, 2, s16\n\t"
    "s_and_b64 exec, s[94:95], vcc\n\t"
    "ds_write_b32 v25, v20\n\t"
    "ds_write_b32 v23, v21 offset:64\n\t"
    "s_andn2_b64 exec, s[94:95], vcc\n\t"
    "ds_write_b32 v22, v20 offset:128\n\t"
    "s_mov_b64 exec, -1\n\t"
    ".Ls2b_%=:\n\t"
    "s_waitcnt lgkmcnt(0)\n\t"
    "s_barrier\n\t"
    "s_cmp_lg_u32 s53, 0\n\t"
    "s_cbranch_scc1 .Ls2j_%=\n\t"
    "v_lshl_add_u32 v10, v3, 4, s16\n\t"
    "ds_read_b128 v[12:15], v10\n\t"
    "ds_read_b128 v[16:19], v10 offset:64\n\t"
    "s_mov_b32 s17, 147456\n\t"
    "v_lshl_add_u32 v11, v2, 2, s17\n\t"
    "s_mul_i32 s19, s21, 192512\n\t"
    "v_add_u32 v26, s19, v92\n\t"
    "s_mul_i32 s17, s20, 224\n\t"
    "s_add_u32 s17, s17, 99328\n\t"
    "v_add_u32 v27, s17, v26\n\t"
    "s_lshl_b32 s17, s20, 10\n\t"
    "v_add_u32 v30, s17, v92\n\t"
    "s_lshr_b32 s17, s20, 1\n\t"
    "s_lshl_b32 s17, s17, 6\n\t"
    "s_and_b32 s22, s20, 1\n\t"
    "s_lshl3_add_u32 s17, s22, s17\n\t"
    "s_add_u32 s17, s17, 108976\n\t"
    "s_add_u32 s17, s17, s19\n\t"
    "v_lshl_add_u32 v28, v3, 4, s17\n\t"
    "s_mov_b32 s4, 0x10001\n\t"
    "s_mov_b32 s5, s4\n\t"
    "s_mov_b32 s23, s63\n\t"
    "s_mov_b32 s22, 0\n\t"
    "s_waitcnt lgkmcnt(1)\n\t"
    "v_add_u32 v12, v11, v12\n\t"
    "v_add_u32 v13, v11, v13\n\t"
    "v_add_u32 v14, v11, v14\n\t"
    "v_add_u32 v15, v11, v15\n\t"
    "ds_read_b32 v12, v12\n\t"
    "ds_read_b32 v13, v13\n\t"
    "ds_read_b32 v14, v14\n\t"
    "ds_read_b32 v15, v15\n\t"
    "s_waitcnt lgkmcnt(4)\n\t"
    "v_lshl_or_b32 v16, v17, 16, v16\n\t"
    "v_lshl_or_b32 v17, v19, 16, v18\n\t"
    "s_mov_b64 exec, s[4:5]\n\t"
    "global_store_dwordx2 v28, v[16:17], s[60:61] sc1\n\t"
    "s_mov_b64 exec, -1\n\t"
    "s_cmp_lg_u32 s73, 0\n\t"
    "s_cbranch_scc1 .Ls2x_%=\n\t"
    ".Ls2r_%=:\n\t"
    "s_waitcnt lgkmcnt(0)\n\t"
    "v_and_b32 v32, s18, v12\n\t"
    "v_and_b32 v33, s18, v13\n\t"
    "v_and_b32 v34, s18, v14\n\t"
    "v_and_b32 v35, s18, v15\n\t"
    "v_cmp_ne_u32 s[70:71], 0, v32\n\t"
    "v_cmp_ne_u32 s[72:73], 0, v33\n\t"
    "v_cmp_ne_u32 s[74:75], 0, v34\n\t"
    "v_cmp_ne_u32 s[76:77], 0, v35\n\t"
    "v_mov_b32 v40, v12\n\t"
    "v_add_u32 v41, 0, v30\n\t"
    "v_mov_b32 v42, v13\n\t"
    "v_add_u32 v43, 4, v30\n\t"
    "v_mov_b32 v44, v14\n\t"
    "v_add_u32 v45, 8, v30\n\t"
    "v_mov_b32 v46, v15\n\t"
    "v_add_u32 v47, 12, v30\n\t"
    "s_bcnt1_i32_b64 s78, s[70:71]\n\t"
    "s_bcnt1_i32_b64 s79, s[72:73]\n\t"
    "s_bcnt1_i32_b64 s80, s[74:75]\n\t"
    "s_bcnt1_i32_b64 s81, s[76:77]\n\t"
    "s_add_u32 s79, s79, s78\n\t"
    "s_add_u32 s80, s80, s79\n\t"
    "s_add_u32 s81, s81, s80\n\t"
    "s_cmp_gt_u32 s81, 28\n\t"
    "s_cbranch_scc1 .Ls2h_%=\n\t"
    "v_mbcnt_lo_u32_b32 v32, s70, 0\n\t"
    "v_mbcnt_lo_u32_b32 v33, s72, 0\n\t"
    "v_mbcnt_lo_u32_b32 v34, s74, 0\n\t"
    "v_mbcnt_lo_u32_b32 v35, s76, 0\n\t"
    "v_mbcnt_hi_u32_b32 v32, s71, v32\n\t"
    "v_mbcnt_hi_u32_b32 v33, s73, v33\n\t"
    "v_mbcnt_hi_u32_b32 v34, s75, v34\n\t"
    "v_mbcnt_hi_u32_b32 v35, s77, v35\n\t"
    "v_add_u32 v33, s78, v33\n\t"
    "v_add_u32 v34, s79, v34\n\t"
    "v_add_u32 v35, s80, v35\n\t"
    "v_lshl_add_u32 v32, v32, 3, s16\n\t"
    "v_lshl_add_u32 v33, v33, 3, s16\n\t"
    "v_lshl_add_u32 v34, v34, 3, s16\n\t"
    "v_lshl_add_u32 v35, v35, 3, s16\n\t"
    "s_mov_b64 exec, s[70:71]\n\t"
    "ds_write_b64 v32, v[40:41] offset:256\n\t"
    "s_mov_b64 exec, s[72:73]\n\t"
    "ds_write_b64 v33, v[42:43] offset:256\n\t"
    "s_mov_b64 exec, s[74:75]\n\t"
    "ds_write_b64 v34, v[44:45] offset:256\n\t"
    "s_mov_b64 exec, s[76:77]\n\t"
    "ds_write_b64 v35, v[46:47] offset:256\n\t"
    "s_mov_b64 exec, 0x3fff\n\t"
    "s_cmp_eq_u32 s20, 8\n\t"
    "s_cselect_b32 exec_lo, 0xffff, exec_lo\n\t"
    "v_add_u32 v10, s16, v92\n\t"
    "ds_read_b128 v[12:15], v10 offset:256\n\t"
    "s_waitcnt lgkmcnt(0)\n\t"
    "global_store_dwordx4 v27, v[12:15], s[60:61] sc1\n\t"
    "s_mov_b64 exec, -1\n\t"
    "s_barrier\n\t"
    "s_mov_b32 s37, 1\n\t"
    "s_call_b64 s[26:27], .Lxt_%=\n\t"
    "s_xor_b32 s16, s30, s23\n\t"
    "s_or_b32 s17, s31, s22\n\t"
    "s_lshl_b32 s23, s23, 7\n\t"
    "s_add_u32 s23, s23, 786432\n\t"
    "v_mov_b32 v4, s16\n\t"
    "v_mov_b32 v5, s17\n\t"
    "v_mul_u32_u24 v8, 82048, v1\n\t"
    "v_add_u32 v8, s23, v8\n\t"
    "v_mov_b32 v6, s24\n\t"
    "s_waitcnt vmcnt(5)\n\t"
    "s_mov_b64 exec, 0x1\n\t"
    "global_store_dwordx3 v8, v[4:6], s[60:61] sc1\n\t"
    "s_mov_b64 exec, -1\n\t"
    "s_branch .Ls2k_%=\n\t"
    ".Ls2j_%=:\n\t"
    "s_barrier\n\t"
    "s_mov_b32 s37, 1\n\t"
    "s_call_b64 s[26:27], .Lxt_%=\n\t"
    ".Ls2k_%=:\n\t"
    "s_branch .Ljoin_%=\n\t"
    ".Ls2w3_%=:\n\t"
    "s_waitcnt lgkmcnt(0)\n\t"
    "s_lshl_b32 s59, s53, 3\n\t"
    "s_lshr_b32 s59, 0x64753120, s59\n\t"
    "s_lshl_b32 s64, s59, 4\n\t"
    "s_and_b32 s64, s64, 0x70\n\t"
    "s_and_b32 s59, s59, 0x70\n\t"
    "v_cmp_gt_u32 vcc, 48, v1\n\t"
    "v_xor_b32 v237, s64, v92\n\t"
    "v_xor_b32 v239, s59, v92\n\t"
    "v_cndmask_b32 v238, v239, v237, vcc\n\t"
    "s_and_b32 s58, s62, 7\n\t"
    "s_lshl_b32 s58, s58, 5\n\t"
    "s_lshr_b32 s52, s62, 3\n\t"
    "s_add_u32 s58, s58, s52\n\t"
    "s_mul_i32 s58, s58, 18432\n\t"
    "s_mul_i32 s52, s53, 4608\n\t"
    "s_add_u32 s59, s58, s52\n\t"
    "s_mul_i32 s52, s53, 27648\n\t"
    "s_sub_u32 s42, 75497472, s59\n\t"
    "s_mov_b32 s43, 0x20000\n\t"
    "s_mov_b32 s68, 0\n\t"
    "s_mov_b32 s36, s52\n\t"
    "s_mov_b32 s18, 0x7fff7fff\n\t"
    "s_add_u32 s40, s8, s59\n\t"
    "s_addc_u32 s41, s9, 0\n\t"
    "v_min_u32 v44, 35, v1\n\t"
    "v_and_b32 v45, 3, v44\n\t"
    "v_and_b32 v46, 16, v44\n\t"
    "v_lshlrev_b32 v46, 3, v46\n\t"
    "v_and_b32 v47, 12, v44\n\t"
    "v_lshl_add_u32 v46, v47, 1, v46\n\t"
    "v_lshl_add_u32 v46, v45, 5, v46\n\t"
    "s_lshl_b32 s16, s53, 8\n\t"
    "s_lshl_b32 s17, s53, 5\n\t"
    "s_add_u32 s17, s17, 1024\n\t"
    "v_cmp_gt_u32 vcc, 32, v44\n\t"
    "v_add_u32 v46, s16, v46\n\t"
    "v_lshl_add_u32 v47, v45, 3, s17\n\t"
    "v_cndmask_b32 v198, v47, v46, vcc\n\t"
    "global_load_dwordx2 v[196:197], v198, s[12:13]\n\t"
    "s_add_u32 s16, s60, 917504\n\t"
    "s_addc_u32 s17, s61, 0\n\t"
    "s_load_dwordx2 s[30:31], s[16:17], 0\n\t"
    "s_call_b64 s[28:29], .Lconst_%=\n\t"
    "s_barrier\n\t"
    "s_mov_b32 s37, 2\n\t"
    "s_call_b64 s[26:27], .Lxt_%=\n\t"
    "s_mov_b32 s16, 156672\n\t"
    "v_lshl_add_u32 v10, v2, 2, s16\n\t"
    "v_mov_b32 v11, 0\n\t"
    "v_mov_b32 v12, 9216\n\t"
    "v_and_b32 v13, 4, v2\n\t"
    "v_lshlrev_b32 v13, 1, v13\n\t"
    "v_mov_b32 v14, 1073741824\n\t"
    "ds_write_b32 v10, v11\n\t"
    "ds_write_b32 v10, v12 offset:96\n\t"
    "ds_write_b32 v10, v13 offset:160\n\t"
    "ds_write_b32 v10, v12 offset:224\n\t"
    "ds_write_b32 v10, v14 offset:288\n\t"
    "s_mov_b32 s16, 157024\n\t"
    "v_lshl_add_u32 v10, v1, 2, s16\n\t"
    "v_mov_b32 v11, 0x40000\n\t"
    "ds_write_b32 v10, v11\n\t"
    "s_waitcnt lgkmcnt(0)\n\t"
    "s_barrier\n\t"
    "s_mov_b32 s37, 1\n\t"
    "s_call_b64 s[26:27], .Lxt_%=\n\t"
    "s_branch .Ls2b_%=\n\t"
    ".Lnone2_%=:\n\t"
    "s_call_b64 s[28:29], .Lconst_%=\n\t"
    "s_and_b32 s16, s62, 5\n\t"
    "s_cmp_eq_u32 s16, 4\n\t"
    "s_cbranch_scc0 .Lnopq_%=\n\t"
    "s_load_dword s74, s[72:73], s70\n\t"
    "s_call_b64 s[26:27], .Lxs_%=\n\t"
    "s_mov_b32 s80, s81\n\t"
    "s_mov_b32 s37, 2\n\t"
    "s_call_b64 s[26:27], .Lxu_%=\n\t"
    "s_branch .Ljoin_%=\n\t"
    ".Lnopq_%=:\n\t"
    "s_mov_b32 s37, 3\n\t"
    "s_call_b64 s[26:27], .Lxu_%=\n\t"
    ".Ljoin_%=:\n\t"
    "s_bfe_u32 s16, s62, 0x50003\n\t"
    "s_mul_i32 s16, s16, 0\n\t"
    "s_lshr_b32 s16, s16, 5\n\t"
    "s_sub_u32 s87, 0, s16\n\t"
    "s_sub_i32 s23, 20, s87\n\t"
    "s_sub_i32 s23, 15, s87\n\t"
    "s_and_b32 s59, s62, 0\n\t"
    "s_mul_i32 s59, s59, 82048\n\t"
    "s_add_u32 s59, s59, 786432\n\t"
    "v_lshl_add_u32 v44, v1, 2, s53\n\t"
    "v_min_u32 v44, 81, v44\n\t"
    "v_add_u32 v44, 256, v44\n\t"
    "v_lshl_add_u32 v43, v44, 7, s59\n\t"
    "v_xor_b32 v44, s30, v44\n\t"
    "v_mov_b32 v45, s31\n\t"
    "global_load_dwordx3 v[40:42], v43, s[60:61] sc1\n\t"
    "s_mov_b32 s69, 0\n\t"
    "s_mov_b32 s70, 0\n\t"
    "s_and_b32 s16, s62, 5\n\t"
    "s_cmp_eq_u32 s16, 4\n\t"
    "s_cbranch_scc0 .Lnopr_%=\n\t"
    "s_mov_b32 s37, 1\n\t"
    "s_call_b64 s[26:27], .Lxt_%=\n\t"
    "s_memtime s[76:77]\n\t"
    "s_waitcnt lgkmcnt(0)\n\t"
    "s_load_dword s74, s[72:73], s75\n\t"
    "s_waitcnt lgkmcnt(0)\n\t"
    "s_memtime s[78:79]\n\t"
    "s_xor_b32 s75, s75, 0x2000\n\t"
    "s_waitcnt lgkmcnt(0)\n\t"
    "s_sub_u32 s81, s78, s76\n\t"
    "s_cmp_lt_u32 s81, 320\n\t"
    "s_cselect_b32 s81, -1, s81\n\t"
    "s_bitcmp1_b32 s75, 13\n\t"
    "s_cselect_b32 s76, s81, s80\n\t"
    "s_cselect_b32 s77, s80, s81\n\t"
    "s_lshl3_add_u32 s78, s53, 149504\n\t"
    "v_mov_b32 v4, s78\n\t"
    "v_mov_b32 v6, s76\n\t"
    "v_mov_b32 v7, s77\n\t"
    "ds_write_b64 v4, v[6:7]\n\t"
    "s_waitcnt lgkmcnt(0)\n\t"
    ".Lnopr_%=:\n\t"
    "s_waitcnt vmcnt(16)\n\t"
    "v_lshlrev_b32 v4, 16, v196\n\t"
    "v_and_b32 v5, 0xffff0000, v196\n\t"
    "v_lshlrev_b32 v6, 16, v197\n\t"
    "v_and_b32 v7, 0xffff0000, v197\n\t"
    "v_add_u32 v8, s83, v92\n\t"
    "s_mov_b32 exec_hi, 15\n\t"
    "ds_write_b128 v8, v[4:7]\n\t"
    "s_mov_b32 exec_hi, -1\n\t"
    "s_and_b32 s16, s62, 3\n\t"
    "s_lshr_b32 s17, s62, 5\n\t"
    "s_lshl3_add_u32 s16, s16, s17\n\t"
    "s_mul_i32 s17, s16, 147456\n\t"
    "s_lshl_b32 s16, s62, 1\n\t"
    "s_and_b32 s16, s16, 48\n\t"
    "s_mov_b32 s38, 0xfeb87410\n\t"
    "s_mov_b32 s39, 0xdca96532\n\t"
    "s_lshr_b64 s[38:39], s[38:39], s16\n\t"
    "s_mov_b32 s39, s38\n\t"
    "s_sub_i32 s38, s17, s58\n\t"
    "s_mov_b32 s54, 0\n\t"
    "v_min_u32 v165, 10, v1\n\t"
    "s_mov_b32 s16, 4718592\n\t"
    "s_add_u32 s17, s17, 598016\n\t"
    "v_mov_b32 v164, s17\n\t"
    "v_mad_u32_u24 v165, v165, s16, v164\n\t"
    "v_lshrrev_b32 v165, 21, v165\n\t"
    "s_mov_b32 s16, 876032\n\t"
    "v_lshl_add_u32 v164, v1, 3, s16\n\t"
    "s_mul_i32 s58, s53, 9\n\t"
    "s_add_u32 s58, s58, 0\n\t"
    "v_min_u32 v89, 8, v1\n\t"
    "v_add_u32 v89, s58, v89\n\t"
    "v_lshl_add_u32 v88, v89, 7, s59\n\t"
    "v_xor_b32 v89, s30, v89\n\t"
    "s_mov_b64 exec, 0\n\t"
    "s_branch .Lpad0_%=\n\t"
    ".Lwarmr_%=:\n\t"
    "s_mov_b64 exec, -1\n\t"
    "s_and_b32 s16, s62, 5\n\t"
    "s_cmp_eq_u32 s16, 4\n\t"
    "s_cbranch_scc1 .Lprd_%=\n\t"
    "s_mov_b32 s37, 1\n\t"
    "s_call_b64 s[26:27], .Lxu_%=\n\t"
    ".Lprd_%=:\n\t"
    "s_waitcnt vmcnt(5)\n\t"
    "s_mov_b32 s58, 0x40000\n\t"
    ".Lpa_%=:\n\t"
    "v_cmp_ne_u64 vcc, v[40:41], v[44:45]\n\t"
    "s_mov_b32 s84, s24\n\t"
    "s_cbranch_vccz .Lmain_%=\n\t"
    "s_sub_u32 s58, s58, 1\n\t"
    "s_cbranch_scc1 .Lmain_%=\n\t"
    "s_sleep 1\n\t"
    "global_load_dwordx3 v[40:42], v43, s[60:61] sc1\n\t"
    "s_waitcnt vmcnt(0)\n\t"
    "s_branch .Lpa_%=\n\t"
    ".Lxinit_%=:\n\t"
    "s_cmp_eq_u32 s71, 0\n\t"
    "s_cbranch_scc1 .Lxi1_%=\n\t"
    "s_ff1_i32_b32 s59, s71\n\t"
    "s_bitset0_b32 s71, s59\n\t"
    "v_mov_b32 v250, 1073741824\n\t"
    "v_mov_b32 v251, 1073741824\n\t"
    "s_and_b32 s64, s59, 15\n\t"
    "s_mul_i32 s64, s64, 6\n\t"
    "s_and_b32 s59, s59, 16\n\t"
    "s_cselect_b32 s80, 0, -1\n\t"
    "s_lshl1_add_u32 s59, s59, s83\n\t"
    "s_lshl3_add_u32 s59, s64, s59\n\t"
    "v_bfe_u32 v1, v245, 8, 2\n\t"
    "v_lshl_add_u32 v1, v1, 3, s59\n\t"
    "s_mov_b32 exec_hi, s80\n\t"
    "ds_read_b64 v[250:251], v1 offset:576\n\t"
    "s_mov_b32 exec_hi, -1\n\t"
    "s_mov_b32 s69, s64\n\t"
    "s_cmp_eq_u32 s71, 0\n\t"
    "s_cbranch_scc1 .Lxi0_%=\n\t"
    "s_ff1_i32_b32 s59, s71\n\t"
    "s_bitset0_b32 s71, s59\n\t"
    "v_mov_b32 v254, 1073741824\n\t"
    "v_mov_b32 v255, 1073741824\n\t"
    "s_and_b32 s64, s59, 15\n\t"
    "s_mul_i32 s64, s64, 6\n\t"
    "s_and_b32 s59, s59, 16\n\t"
    "s_cselect_b32 s80, 0, -1\n\t"
    "s_lshl1_add_u32 s59, s59, s83\n\t"
    "s_lshl3_add_u32 s59, s64, s59\n\t"
    "v_bfe_u32 v1, v245, 8, 2\n\t"
    "v_lshl_add_u32 v1, v1, 3, s59\n\t"
    "s_mov_b32 exec_hi, s80\n\t"
    "ds_read_b64 v[254:255], v1 offset:576\n\t"
    "s_mov_b32 exec_hi, -1\n\t"
    "s_mov_b32 s70, s64\n\t"
    "s_waitcnt lgkmcnt(0)\n\t"
    "v_lshrrev_b32 v2, 15, v254\n\t"
    "v_and_b32 v2, 0x7fe, v2\n\t"
    "v_xor_b32 v2, v2, v249\n\t"
    "v_lshrrev_b32 v3, 15, v255\n\t"
    "v_and_b32 v3, 0x7fe, v3\n\t"
    "v_xor_b32 v3, v3, v249\n\t"
    "v_lshl_or_b32 v247, v3, 16, v2\n\t"
    "v_bfe_u32 v1, v245, 4, 4\n\t"
    "v_lshrrev_b32 v2, 26, v254\n\t"
    "v_lshrrev_b32 v3, 26, v255\n\t"
    "v_cmp_eq_u32 s[20:21], v2, v1\n\t"
    "v_cmp_eq_u32 s[80:81], v3, v1\n\t"
    "v_and_b32 v254, 0xffff, v254\n\t"
    "v_and_b32 v255, 0xffff, v255\n\t"
    "v_cndmask_b32 v254, 0, v254, s[20:21]\n\t"
    "v_cndmask_b32 v255, 0, v255, s[80:81]\n\t"
    ".Lxi0_%=:\n\t"
    "s_waitcnt lgkmcnt(0)\n\t"
    "v_lshrrev_b32 v2, 15, v250\n\t"
    "v_and_b32 v2, 0x7fe, v2\n\t"
    "v_xor_b32 v2, v2, v249\n\t"
    "v_lshrrev_b32 v3, 15, v251\n\t"
    "v_and_b32 v3, 0x7fe, v3\n\t"
    "v_xor_b32 v3, v3, v249\n\t"
    "v_lshl_or_b32 v246, v3, 16, v2\n\t"
    "v_bfe_u32 v1, v245, 4, 4\n\t"
    "v_lshrrev_b32 v2, 26, v250\n\t"
    "v_lshrrev_b32 v3, 26, v251\n\t"
    "v_cmp_eq_u32 s[20:21], v2, v1\n\t"
    "v_cmp_eq_u32 s[80:81], v3, v1\n\t"
    "v_and_b32 v250, 0xffff, v250\n\t"
    "v_and_b32 v251, 0xffff, v251\n\t"
    "v_cndmask_b32 v250, 0, v250, s[20:21]\n\t"
    "v_cndmask_b32 v251, 0, v251, s[80:81]\n\t"
    ".Lxi1_%=:\n\t"
    "s_or_b32 s58, s71, s73\n\t"
    "s_cselect_b32 s22, 1, 0\n\t"
    "s_branch .Lxinitr_%=\n\t"
    ".Lslow_%=:\n\t"
    "s_and_b32 s58, s71, s74\n\t"
    ".Lsr0_%=:\n\t"
    "s_cbranch_scc0 .Lsh0_%=\n\t"
    "s_ff1_i32_b32 s59, s58\n\t"
    "s_bitset0_b32 s58, s59\n\t"
    "v_mov_b32 v2, 1073741824\n\t"
    "v_mov_b32 v3, 1073741824\n\t"
    "s_and_b32 s64, s59, 15\n\t"
    "s_mul_i32 s64, s64, 6\n\t"
    "s_and_b32 s59, s59, 16\n\t"
    "s_cselect_b32 s80, 0, -1\n\t"
    "s_lshl1_add_u32 s59, s59, s83\n\t"
    "s_lshl3_add_u32 s59, s64, s59\n\t"
    "v_bfe_u32 v1, v245, 8, 2\n\t"
    "v_lshl_add_u32 v1, v1, 3, s59\n\t"
    "s_mov_b32 exec_hi, s80\n\t"
    "ds_read_b64 v[2:3], v1 offset:576\n\t"
    "s_mov_b32 exec_hi, -1\n\t"
    "v_add_u32 v89, s75, v248\n\t"
    "s_waitcnt lgkmcnt(0)\n\t"
    "v_lshrrev_b32 v88, 15, v2\n\t"
    "v_and_b32 v88, 0x7fe, v88\n\t"
    "v_xad_u32 v88, v88, v249, v89\n\t"
    "v_lshrrev_b32 v90, 15, v3\n\t"
    "v_and_b32 v90, 0x7fe, v90\n\t"
    "v_xad_u32 v90, v90, v249, v89\n\t"
    "v_bfe_u32 v1, v245, 4, 4\n\t"
    "v_lshrrev_b32 v0, 26, v3\n\t"
    "v_cmp_ne_u32 s[20:21], v0, v1\n\t"
    "v_lshrrev_b32 v0, 26, v2\n\t"
    "v_cmp_ne_u32 s[80:81], v0, v1\n\t"
    "v_mov_b32 v1, 0x44\n\t"
    "v_mov_b32 v0, 0x54\n\t"
    "v_cndmask_b32 v0, v1, v0, s[20:21]\n\t"
    "v_addc_co_u32 v0, s[20:21], 0, v0, s[80:81]\n\t"
    "ds_read_u16 v88, v88\n\t"
    "ds_read_u16 v90, v90\n\t"
    "s_lshr_b64 s[80:81], s[76:77], s64\n\t"
    "s_and_b32 s80, s80, 63\n\t"
    "s_lshl_b32 s80, s80, 2\n\t"
    "s_set_gpr_idx_on s80, gpr_idx(DST)\n\t"
    "s_waitcnt lgkmcnt(0)\n\t"
    "v_smfmac_f32_16x16x32_bf16 v[0:3], v[2:3], v[88:91], v0\n\t"
    "s_set_gpr_idx_off\n\t"
    "s_cmp_lg_u32 s58, 0\n\t"
    "s_branch .Lsr0_%=\n\t"
    ".Lsh0_%=:\n\t"
    "s_and_b32 s58, s73, s74\n\t"
    ".Lsh1_%=:\n\t"
    "s_cbranch_scc0 .Lsh3_%=\n\t"
    "s_ff1_i32_b32 s59, s58\n\t"
    "s_bitset0_b32 s58, s59\n\t"
    "s_mul_i32 s64, s59, 6\n\t"
    "s_lshr_b64 s[80:81], s[76:77], s64\n\t"
    "s_and_b32 s80, s80, 63\n\t"
    "s_lshl_b32 s80, s80, 2\n\t"
    "s_mul_i32 s59, s59, 9216\n\t"
    "s_add_u32 s59, s59, 109568\n\t"
    "s_mov_b32 s64, 0\n\t"
    "v_mov_b32 v0, 0xeeee\n\t"
    ".Lsh2_%=:\n\t"
    "v_bfe_u32 v1, v245, 1, 9\n\t"
    "buffer_load_dwordx2 v[2:3], v1, s[48:51], s59 offen sc1\n\t"
    "v_bfe_u32 v1, v245, 8, 2\n\t"
    "v_lshl_add_u32 v1, v1, 4, s64\n\t"
    "v_xad_u32 v1, v1, v249, v248\n\t"
    "v_add_u32 v1, s75, v1\n\t"
    "ds_read_b128 v[88:91], v1\n\t"
    "s_set_gpr_idx_on s80, gpr_idx(DST)\n\t"
    "s_waitcnt vmcnt(0) lgkmcnt(0)\n\t"
    "v_smfmac_f32_16x16x32_bf16 v[0:3], v[2:3], v[88:91], v0\n\t"
    "s_set_gpr_idx_off\n\t"
    "s_add_u32 s59, s59, 512\n\t"
    "s_add_u32 s64, s64, 64\n\t"
    "s_cmp_lt_u32 s64, 1152\n\t"
    "s_cbranch_scc1 .Lsh2_%=\n\t"
    "s_cmp_lg_u32 s58, 0\n\t"
    "s_branch .Lsh1_%=\n\t"
    ".Lsh3_%=:\n\t"
    "s_nop 3\n\t"
    "s_setpc_b64 s[78:79]\n\t"
    ".Lsl0_%=:\n\t"
    "s_or_b32 s58, s71, s73\n\t"
    "s_and_b32 s58, s58, 983055\n\t"
    "s_cbranch_scc0 .Lslr0_%=\n\t"
    "s_mov_b32 s74, 983055\n\t"
    "s_mov_b32 s75, 0\n\t"
    "s_call_b64 s[78:79], .Lslow_%=\n\t"
    "s_branch .Lslr0_%=\n\t"
    ".Lsl1_%=:\n\t"
    "s_or_b32 s58, s71, s73\n\t"
    "s_and_b32 s58, s58, 983055\n\t"
    "s_cbranch_scc0 .Lslr1_%=\n\t"
    "s_mov_b32 s74, 983055\n\t"
    "s_mov_b32 s75, 4608\n\t"
    "s_call_b64 s[78:79], .Lslow_%=\n\t"
    "s_branch .Lslr1_%=\n\t"
    ".Lsl2_%=:\n\t"
    "s_or_b32 s58, s71, s73\n\t"
    "s_and_b32 s58, s58, 983055\n\t"
    "s_cbranch_scc0 .Lslr2_%=\n\t"
    "s_mov_b32 s74, 983055\n\t"
    "s_mov_b32 s75, 9216\n\t"
    "s_call_b64 s[78:79], .Lslow_%=\n\t"
    "s_branch .Lslr2_%=\n\t"
    ".Lsl3_%=:\n\t"
    "s_or_b32 s58, s71, s73\n\t"
    "s_and_b32 s58, s58, 983055\n\t"
    "s_cbranch_scc0 .Lslr3_%=\n\t"
    "s_mov_b32 s74, 983055\n\t"
    "s_mov_b32 s75, 13824\n\t"
    "s_call_b64 s[78:79], .Lslow_%=\n\t"
    "s_branch .Lslr3_%=\n\t"
    ".Lsl4_%=:\n\t"
    "s_or_b32 s58, s71, s73\n\t"
    "s_and_b32 s58, s58, 983055\n\t"
    "s_cbranch_scc0 .Lslr4_%=\n\t"
    "s_mov_b32 s74, 983055\n\t"
    "s_mov_b32 s75, 18432\n\t"
    "s_call_b64 s[78:79], .Lslow_%=\n\t"
    "s_branch .Lslr4_%=\n\t"
    ".Lsl5_%=:\n\t"
    "s_or_b32 s58, s71, s73\n\t"
    "s_and_b32 s58, s58, 32506352\n\t"
    "s_cbranch_scc0 .Lslr5_%=\n\t"
    "s_mov_b32 s74, 32506352\n\t"
    "s_mov_b32 s75, 0\n\t"
    "s_call_b64 s[78:79], .Lslow_%=\n\t"
    "s_branch .Lslr5_%=\n\t"
    ".Lsl6_%=:\n\t"
    "s_or_b32 s58, s71, s73\n\t"
    "s_and_b32 s58, s58, 32506352\n\t"
    "s_cbranch_scc0 .Lslr6_%=\n\t"
    "s_mov_b32 s74, 32506352\n\t"
    "s_mov_b32 s75, 4608\n\t"
    "s_call_b64 s[78:79], .Lslow_%=\n\t"
    "s_branch .Lslr6_%=\n\t"
    ".Lsl7_%=:\n\t"
    "s_or_b32 s58, s71, s73\n\t"
    "s_and_b32 s58, s58, 32506352\n\t"
    "s_cbranch_scc0 .Lslr7_%=\n\t"
    "s_mov_b32 s74, 32506352\n\t"
    "s_mov_b32 s75, 9216\n\t"
    "s_call_b64 s[78:79], .Lslow_%=\n\t"
    "s_branch .Lslr7_%=\n\t"
    ".Lsl8_%=:\n\t"
    "s_or_b32 s58, s71, s73\n\t"
    "s_and_b32 s58, s58, 32506352\n\t"
    "s_cbranch_scc0 .Lslr8_%=\n\t"
    "s_mov_b32 s74, 32506352\n\t"
    "s_mov_b32 s75, 13824\n\t"
    "s_call_b64 s[78:79], .Lslow_%=\n\t"
    "s_branch .Lslr8_%=\n\t"
    ".Lsl9_%=:\n\t"
    "s_or_b32 s58, s71, s73\n\t"
    "s_and_b32 s58, s58, 32506352\n\t"
    "s_cbranch_scc0 .Lslr9_%=\n\t"
    "s_mov_b32 s74, 32506352\n\t"
    "s_mov_b32 s75, 18432\n\t"
    "s_call_b64 s[78:79], .Lslow_%=\n\t"
    "s_branch .Lslr9_%=\n\t"
    ".Lsl10_%=:\n\t"
    "s_mov_b32 s74, 33489407\n\t"
    "s_mov_b32 s75, 23040\n\t"
    "s_call_b64 s[78:79], .Lslow_%=\n\t"
    "s_branch .Lslr10_%=\n\t"
    ".Lsl11_%=:\n\t"
    "s_mov_b32 s74, 33489407\n\t"
    "s_mov_b32 s75, 0\n\t"
    "s_call_b64 s[78:79], .Lslow_%=\n\t"
    "s_branch .Lslr11_%=\n\t"
    ".Lsl12_%=:\n\t"
    "s_mov_b32 s74, 33489407\n\t"
    "s_mov_b32 s75, 4608\n\t"
    "s_call_b64 s[78:79], .Lslow_%=\n\t"
    "s_branch .Lslr12_%=\n\t"
    ".Lsl13_%=:\n\t"
    "s_mov_b32 s74, 33489407\n\t"
    "s_mov_b32 s75, 9216\n\t"
    "s_call_b64 s[78:79], .Lslow_%=\n\t"
    "s_branch .Lslr13_%=\n\t"
    ".Lsl14_%=:\n\t"
    "s_mov_b32 s74, 33489407\n\t"
    "s_mov_b32 s75, 13824\n\t"
    "s_call_b64 s[78:79], .Lslow_%=\n\t"
    "s_branch .Lslr14_%=\n\t"
    ".Lsl15_%=:\n\t"
    "s_mov_b32 s74, 33489407\n\t"
    "s_mov_b32 s75, 18432\n\t"
    "s_call_b64 s[78:79], .Lslow_%=\n\t"
    "s_branch .Lslr15_%=\n\t"
    ".Lsl16_%=:\n\t"
    "s_mov_b32 s74, 33489407\n\t"
    "s_mov_b32 s75, 23040\n\t"
    "s_call_b64 s[78:79], .Lslow_%=\n\t"
    "s_branch .Lslr16_%=\n\t"
    ".Lsl17_%=:\n\t"
    "s_mov_b32 s74, 33489407\n\t"
    "s_mov_b32 s75, 0\n\t"
    "s_call_b64 s[78:79], .Lslow_%=\n\t"
    "s_branch .Lslr17_%=\n\t"
    ".Lsl18_%=:\n\t"
    "s_mov_b32 s74, 33489407\n\t"
    "s_mov_b32 s75, 4608\n\t"
    "s_call_b64 s[78:79], .Lslow_%=\n\t"
    "s_branch .Lslr18_%=\n\t"
    ".Lsl19_%=:\n\t"
    "s_mov_b32 s74, 33489407\n\t"
    "s_mov_b32 s75, 9216\n\t"
    "s_call_b64 s[78:79], .Lslow_%=\n\t"
    "s_branch .Lslr19_%=\n\t"
    ".Lsl20_%=:\n\t"
    "s_or_b32 s58, s71, s73\n\t"
    "s_and_b32 s58, s58, 32506352\n\t"
    "s_cbranch_scc0 .Lslr20_%=\n\t"
    "s_mov_b32 s74, 32506352\n\t"
    "s_mov_b32 s75, 13824\n\t"
    "s_call_b64 s[78:79], .Lslow_%=\n\t"
    "s_branch .Lslr20_%=\n\t"
    ".Lsl21_%=:\n\t"
    "s_or_b32 s58, s71, s73\n\t"
    "s_and_b32 s58, s58, 983055\n\t"
    "s_cbranch_scc0 .Lslr21_%=\n\t"
    "s_mov_b32 s74, 983055\n\t"
    "s_mov_b32 s75, 13824\n\t"
    "s_call_b64 s[78:79], .Lslow_%=\n\t"
    "s_branch .Lslr21_%=\n\t"
    ".Ls2x_%=:\n\t"
    "s_cmp_gt_u32 s73, 12\n\t"
    "s_cbranch_scc1 .Ls2h_%=\n\t"
    "v_lshl_add_u32 v10, v3, 2, s16\n\t"
    "ds_read_b32 v50, v10 offset:128\n\t"
    "ds_read_b32 v51, v10 offset:144\n\t"
    "ds_read_b32 v52, v10 offset:160\n\t"
    "s_mov_b32 s70, 0\n\t"
    "v_lshlrev_b32 v29, 26, v2\n\t"
    "s_waitcnt lgkmcnt(0)\n\t"
    "v_add_u32 v54, v11, v50\n\t"
    "v_add_u32 v55, v11, v51\n\t"
    "v_add_u32 v56, v11, v52\n\t"
    "ds_read_b32 v54, v54\n\t"
    "ds_read_b32 v55, v55\n\t"
    "ds_read_b32 v56, v56\n\t"
    "v_lshl_or_b32 v50, v50, 12, v29\n\t"
    "v_lshl_or_b32 v51, v51, 12, v29\n\t"
    "v_lshl_or_b32 v52, v52, 12, v29\n\t"
    "s_waitcnt lgkmcnt(0)\n\t"
    "v_bfe_u32 v24, v54, 0, 16\n\t"
    "v_and_b32 v25, 0x7fff, v24\n\t"
    "v_cmp_ne_u32 vcc, 0, v25\n\t"
    "v_or_b32 v24, v50, v24\n\t"
    "v_or_b32 v24, 131072, v24\n\t"
    "v_mbcnt_lo_u32_b32 v25, vcc_lo, 0\n\t"
    "v_mbcnt_hi_u32_b32 v25, vcc_hi, v25\n\t"
    "v_add_u32 v25, s70, v25\n\t"
    "v_min_u32 v25, 15, v25\n\t"
    "v_lshl_add_u32 v25, v25, 2, s16\n\t"
    "s_bcnt1_i32_b64 s71, vcc\n\t"
    "s_add_u32 s70, s70, s71\n\t"
    "s_mov_b64 exec, vcc\n\t"
    "ds_write_b32 v25, v24 offset:192\n\t"
    "s_mov_b64 exec, -1\n\t"
    "v_bfe_u32 v24, v54, 16, 16\n\t"
    "v_and_b32 v25, 0x7fff, v24\n\t"
    "v_cmp_ne_u32 vcc, 0, v25\n\t"
    "v_or_b32 v24, v50, v24\n\t"
    "v_or_b32 v24, 196608, v24\n\t"
    "v_mbcnt_lo_u32_b32 v25, vcc_lo, 0\n\t"
    "v_mbcnt_hi_u32_b32 v25, vcc_hi, v25\n\t"
    "v_add_u32 v25, s70, v25\n\t"
    "v_min_u32 v25, 15, v25\n\t"
    "v_lshl_add_u32 v25, v25, 2, s16\n\t"
    "s_bcnt1_i32_b64 s71, vcc\n\t"
    "s_add_u32 s70, s70, s71\n\t"
    "s_mov_b64 exec, vcc\n\t"
    "ds_write_b32 v25, v24 offset:192\n\t"
    "s_mov_b64 exec, -1\n\t"
    "s_cmp_le_u32 s73, 4\n\t"
    "s_cbranch_scc1 .Ls2xe_%=\n\t"
    "v_bfe_u32 v24, v55, 0, 16\n\t"
    "v_and_b32 v25, 0x7fff, v24\n\t"
    "v_cmp_ne_u32 vcc, 0, v25\n\t"
    "v_or_b32 v24, v51, v24\n\t"
    "v_or_b32 v24, 131072, v24\n\t"
    "v_mbcnt_lo_u32_b32 v25, vcc_lo, 0\n\t"
    "v_mbcnt_hi_u32_b32 v25, vcc_hi, v25\n\t"
    "v_add_u32 v25, s70, v25\n\t"
    "v_min_u32 v25, 15, v25\n\t"
    "v_lshl_add_u32 v25, v25, 2, s16\n\t"
    "s_bcnt1_i32_b64 s71, vcc\n\t"
    "s_add_u32 s70, s70, s71\n\t"
    "s_mov_b64 exec, vcc\n\t"
    "ds_write_b32 v25, v24 offset:192\n\t"
    "s_mov_b64 exec, -1\n\t"
    "v_bfe_u32 v24, v55, 16, 16\n\t"
    "v_and_b32 v25, 0x7fff, v24\n\t"
    "v_cmp_ne_u32 vcc, 0, v25\n\t"
    "v_or_b32 v24, v51, v24\n\t"
    "v_or_b32 v24, 196608, v24\n\t"
    "v_mbcnt_lo_u32_b32 v25, vcc_lo, 0\n\t"
    "v_mbcnt_hi_u32_b32 v25, vcc_hi, v25\n\t"
    "v_add_u32 v25, s70, v25\n\t"
    "v_min_u32 v25, 15, v25\n\t"
    "v_lshl_add_u32 v25, v25, 2, s16\n\t"
    "s_bcnt1_i32_b64 s71, vcc\n\t"
    "s_add_u32 s70, s70, s71\n\t"
    "s_mov_b64 exec, vcc\n\t"
    "ds_write_b32 v25, v24 offset:192\n\t"
    "s_mov_b64 exec, -1\n\t"
    "s_cmp_le_u32 s73, 8\n\t"
    "s_cbranch_scc1 .Ls2xe_%=\n\t"
    "v_bfe_u32 v24, v56, 0, 16\n\t"
    "v_and_b32 v25, 0x7fff, v24\n\t"
    "v_cmp_ne_u32 vcc, 0, v25\n\t"
    "v_or_b32 v24, v52, v24\n\t"
    "v_or_b32 v24, 131072, v24\n\t"
    "v_mbcnt_lo_u32_b32 v25, vcc_lo, 0\n\t"
    "v_mbcnt_hi_u32_b32 v25, vcc_hi, v25\n\t"
    "v_add_u32 v25, s70, v25\n\t"
    "v_min_u32 v25, 15, v25\n\t"
    "v_lshl_add_u32 v25, v25, 2, s16\n\t"
    "s_bcnt1_i32_b64 s71, vcc\n\t"
    "s_add_u32 s70, s70, s71\n\t"
    "s_mov_b64 exec, vcc\n\t"
    "ds_write_b32 v25, v24 offset:192\n\t"
    "s_mov_b64 exec, -1\n\t"
    "v_bfe_u32 v24, v56, 16, 16\n\t"
    "v_and_b32 v25, 0x7fff, v24\n\t"
    "v_cmp_ne_u32 vcc, 0, v25\n\t"
    "v_or_b32 v24, v52, v24\n\t"
    "v_or_b32 v24, 196608, v24\n\t"
    "v_mbcnt_lo_u32_b32 v25, vcc_lo, 0\n\t"
    "v_mbcnt_hi_u32_b32 v25, vcc_hi, v25\n\t"
    "v_add_u32 v25, s70, v25\n\t"
    "v_min_u32 v25, 15, v25\n\t"
    "v_lshl_add_u32 v25, v25, 2, s16\n\t"
    "s_bcnt1_i32_b64 s71, vcc\n\t"
    "s_add_u32 s70, s70, s71\n\t"
    "s_mov_b64 exec, vcc\n\t"
    "ds_write_b32 v25, v24 offset:192\n\t"
    "s_mov_b64 exec, -1\n\t"
    ".Ls2xe_%=:\n\t"
    "s_cmp_gt_u32 s70, 12\n\t"
    "s_cbranch_scc1 .Ls2h_%=\n\t"
    "v_add_u32 v10, s16, v92\n\t"
    "ds_read_b128 v[20:23], v10 offset:192\n\t"
    "s_mul_i32 s17, s20, 48\n\t"
    "s_add_u32 s17, s17, 108544\n\t"
    "v_add_u32 v10, s17, v26\n\t"
    "s_cmp_gt_u32 s70, 8\n\t"
    "s_cselect_b32 s22, 3, 1\n\t"
    "s_waitcnt lgkmcnt(0)\n\t"
    "s_mov_b64 exec, 7\n\t"
    "global_store_dwordx4 v10, v[20:23], s[60:61] sc1\n\t"
    "s_mov_b64 exec, -1\n\t"
    "s_branch .Ls2r_%=\n\t"
    ".Ls2h_%=:\n\t"
    "s_waitcnt lgkmcnt(0)\n\t"
    "v_mov_b64 v[12:13], 0\n\t"
    "v_mov_b64 v[14:15], 0\n\t"
    "s_mov_b32 s22, 2\n\t"
    "v_lshl_add_u32 v10, v3, 7, v11\n\t"
    "s_mul_i32 s17, s20, 9216\n\t"
    "s_add_u32 s17, s17, 109568\n\t"
    "s_add_u32 s17, s17, s19\n\t"
    "v_lshl_add_u32 v24, v1, 3, s17\n\t"
    "s_mov_b32 s70, 18\n\t"
    ".Ls2hl_%=:\n\t"
    "ds_read2_b32 v[20:21], v10 offset1:16\n\t"
    "v_add_u32 v10, 512, v10\n\t"
    "s_waitcnt lgkmcnt(0)\n\t"
    "global_store_dwordx2 v24, v[20:21], s[60:61] sc1\n\t"
    "v_add_u32 v24, 512, v24\n\t"
    "s_waitcnt vmcnt(0)\n\t"
    "s_sub_u32 s70, s70, 1\n\t"
    "s_cmp_lg_u32 s70, 0\n\t"
    "s_cbranch_scc1 .Ls2hl_%=\n\t"
    "s_branch .Ls2r_%=\n\t"
    ".Lepq_%=:\n\t"
    "s_add_u32 s96, s60, 868352\n\t"
    "s_addc_u32 s97, s61, 0\n\t"
    "s_mov_b32 m0, 18432\n\t"
    "v_and_b32 v0, 0x3ff, v245\n\t"
    "global_load_lds_dwordx4 v0, s[96:97] sc1\n\t"
    "s_branch .Lepqr_%=\n\t"
    ".Lepoch_%=:\n\t"
    "s_mov_b64 s[94:95], vcc\n\t"
    "s_mov_b32 s58, 0x40000\n\t"
    "v_and_b32 v0, 0x3ff, v245\n\t"
    "v_add_u32 v0, 18432, v0\n\t"
    "s_waitcnt vmcnt(13)\n\t"
    "ds_read_b128 v[0:3], v0\n\t"
    "s_waitcnt lgkmcnt(0)\n\t"
    "s_branch .Lepc_%=\n\t"
    ".Lepl_%=:\n\t"
    "v_and_b32 v0, 0x3ff, v245\n\t"
    "global_load_dwordx4 v[0:3], v0, s[96:97] sc1\n\t"
    "s_waitcnt vmcnt(0)\n\t"
    ".Lepc_%=:\n\t"
    "v_xor_b32 v0, s30, v0\n\t"
    "v_xor_b32 v1, s30, v1\n\t"
    "v_xor_b32 v2, s30, v2\n\t"
    "v_xor_b32 v3, s30, v3\n\t"
    "v_or3_b32 v0, v0, v1, v2\n\t"
    "v_or_b32 v0, v0, v3\n\t"
    "v_cmp_ne_u32 vcc, 0, v0\n\t"
    "s_cbranch_vccz .Lepok_%=\n\t"
    "s_sub_u32 s58, s58, 1\n\t"
    "s_cbranch_scc1 .Lepok_%=\n\t"
    "s_sleep 8\n\t"
    "s_branch .Lepl_%=\n\t"
    ".Lepok_%=:\n\t"
    "v_mov_b32 v2, s30\n\t"
    "v_mov_b32 v3, s31\n\t"
    "v_mov_b32 v1, 917504\n\t"
    "s_mov_b64 exec, 1\n\t"
    "global_store_dwordx2 v1, v[2:3], s[60:61] sc1\n\t"
    "s_mov_b64 exec, -1\n\t"
    "s_mov_b64 vcc, s[94:95]\n\t"
    "s_branch .Lepochr_%=\n\t"
    ".Lprp_%=:\n\t"
    "v_mov_b32 v0, 149504\n\t"
    "ds_read_b128 v[100:103], v0\n\t"
    "ds_read_b128 v[0:3], v0 offset:16\n\t"
    "s_lshr_b32 s76, s62, 3\n\t"
    "s_bfe_u32 s77, s62, 0x10001\n\t"
    "s_lshl1_add_u32 s76, s76, s77\n\t"
    "s_lshl3_add_u32 s77, s76, 876032\n\t"
    "s_xor_b32 s76, s77, s30\n\t"
    "s_waitcnt lgkmcnt(0)\n\t"
    "v_min3_u32 v100, v100, v102, v0\n\t"
    "v_min3_u32 v101, v101, v103, v1\n\t"
    "v_min_u32 v100, v100, v2\n\t"
    "v_min_u32 v101, v101, v3\n\t"
    "v_cmp_lt_u32 vcc, v100, v101\n\t"
    "v_mov_b32 v2, s76\n\t"
    "v_mov_b32 v1, s77\n\t"
    "v_mov_b32 v3, s31\n\t"
    "v_addc_co_u32 v3, vcc, 0, v3, vcc\n\t"
    "s_mov_b64 exec, 1\n\t"
    "global_store_dwordx2 v1, v[2:3], s[60:61] sc1\n\t"
    "s_mov_b64 exec, -1\n\t"
    "s_branch .Lprpr_%=\n\t"
    ".Lprl_%=:\n\t"
    "s_sub_u32 s58, s58, 1\n\t"
    "s_cbranch_scc1 .Lprok_%=\n\t"
    "s_sleep 1\n\t"
    "global_load_dwordx2 v[166:167], v164, s[60:61] sc1\n\t"
    "s_waitcnt vmcnt(0)\n\t"
    "s_branch .Lprc_%=\n\t"
    ".Lpal_%=:\n\t"
    "s_bitcmp1_b32 s62, 3\n\t"
    "s_cselect_b32 s96, s59, s97\n\t"
    "s_xor_b32 s96, s96, 0x1555555\n\t"
    "v_bfe_u32 v166, s96, v165, 1\n\t"
    "v_cmp_eq_u32 s[96:97], 1, v166\n\t"
    "s_nop 1\n\t"
    "s_lshl_b32 s96, s96, 5\n\t"
    "s_xor_b32 s54, s54, s96\n\t"
    "s_lshr_b32 s58, s62, 2\n\t"
    "s_xor_b32 s58, s58, s62\n\t"
    "s_bitcmp1_b32 s58, 2\n\t"
    "s_cselect_b32 s58, -1, 0\n\t"
    "s_xor_b32 s54, s54, s58\n\t"
    "s_lshl_b32 s59, s53, 2\n\t"
    "s_lshr_b32 s58, s39, s59\n\t"
    "s_and_b32 s58, s58, 15\n\t"
    "s_sub_i32 s58, s58, s53\n\t"
    "s_mul_i32 s58, s58, 4608\n\t"
    "s_add_i32 s58, s58, s38\n\t"
    "s_ashr_i32 s59, s58, 31\n\t"
    "s_sub_u32 s42, s42, s58\n\t"
    "s_add_u32 s40, s40, s58\n\t"
    "s_addc_u32 s41, s41, s59\n\t"
    "s_branch .Lpalr_%=\n\t"
    ".Lpsw_%=:\n\t"
    "s_lshl_b32 s59, s53, 2\n\t"
    "s_lshr_b32 s58, s39, s59\n\t"
    "s_and_b32 s58, s58, 15\n\t"
    "s_sub_i32 s58, s58, s53\n\t"
    "s_mul_i32 s58, s58, 4608\n\t"
    "s_add_u32 s57, s57, s58\n\t"
    "s_ashr_i32 s59, s38, 31\n\t"
    "s_sub_u32 s46, s46, s38\n\t"
    "s_add_u32 s44, s44, s38\n\t"
    "s_addc_u32 s45, s45, s59\n\t"
    "v_bfe_u32 v101, v245, 6, 2\n\t"
    "v_lshlrev_b32 v102, 2, v101\n\t"
    "v_bfe_u32 v102, s39, v102, 4\n\t"
    "v_sub_u32 v102, v102, v101\n\t"
    "s_movk_i32 s58, 4608\n\t"
    "v_mad_i32_i24 v241, v102, s58, v241\n\t"
    "s_branch .Lpswr_%=\n\t"
    ".Ls2l_%=:\n\t"
    "s_sub_u32 s58, s58, 1\n\t"
    "s_cbranch_scc1 .Ls2ok_%=\n\t"
    "s_sleep 1\n\t"
    "global_load_dwordx2 v[90:91], v88, s[60:61] sc1\n\t"
    "s_waitcnt vmcnt(0)\n\t"
    "s_branch .Ls2c_%=\n\t"
    ".Lpc0_%=:\n\t"
    "s_memrealtime s[84:85]\n\t"
    "s_waitcnt lgkmcnt(0)\n\t"
    "s_sub_u32 s89, s84, s24\n\t"
    "s_sub_u32 s89, s89, 14\n\t"
    "s_cmp_lt_i32 s89, s86\n\t"
    "s_cbranch_scc1 .Lpc0_%=\n\t"
    "s_branch .Lpc0r_%=\n\t"
    ".Lpc1_%=:\n\t"
    "s_memrealtime s[84:85]\n\t"
    "s_waitcnt lgkmcnt(0)\n\t"
    "s_sub_u32 s89, s84, s24\n\t"
    "s_sub_u32 s89, s89, 14\n\t"
    "s_cmp_lt_i32 s89, s86\n\t"
    "s_cbranch_scc1 .Lpc1_%=\n\t"
    "s_branch .Lpc1r_%=\n\t"
    ".Lpc2_%=:\n\t"
    "s_memrealtime s[84:85]\n\t"
    "s_waitcnt lgkmcnt(0)\n\t"
    "s_sub_u32 s89, s84, s24\n\t"
    "s_sub_u32 s89, s89, 14\n\t"
    "s_cmp_lt_i32 s89, s86\n\t"
    "s_cbranch_scc1 .Lpc2_%=\n\t"
    "s_branch .Lpc2r_%=\n\t"
    ".Lpc3_%=:\n\t"
    "s_memrealtime s[84:85]\n\t"
    "s_waitcnt lgkmcnt(0)\n\t"
    "s_sub_u32 s89, s84, s24\n\t"
    "s_sub_u32 s89, s89, 14\n\t"
    "s_cmp_lt_i32 s89, s86\n\t"
    "s_cbranch_scc1 .Lpc3_%=\n\t"
    "s_branch .Lpc3r_%=\n\t"
    ".Lpc4_%=:\n\t"
    "s_memrealtime s[84:85]\n\t"
    "s_waitcnt lgkmcnt(0)\n\t"
    "s_sub_u32 s89, s84, s24\n\t"
    "s_sub_u32 s89, s89, 14\n\t"
    "s_cmp_lt_i32 s89, s86\n\t"
    "s_cbranch_scc1 .Lpc4_%=\n\t"
    "s_branch .Lpc4r_%=\n\t"
    ".Lpc5_%=:\n\t"
    "s_memrealtime s[84:85]\n\t"
    "s_waitcnt lgkmcnt(0)\n\t"
    "s_sub_u32 s89, s84, s24\n\t"
    "s_sub_u32 s89, s89, 14\n\t"
    "s_cmp_lt_i32 s89, s86\n\t"
    "s_cbranch_scc1 .Lpc5_%=\n\t"
    "s_branch .Lpc5r_%=\n\t"
    ".Lpc6_%=:\n\t"
    "s_memrealtime s[84:85]\n\t"
    "s_waitcnt lgkmcnt(0)\n\t"
    "s_sub_u32 s89, s84, s24\n\t"
    "s_sub_u32 s89, s89, 14\n\t"
    "s_cmp_lt_i32 s89, s86\n\t"
    "s_cbranch_scc1 .Lpc6_%=\n\t"
    "s_branch .Lpc6r_%=\n\t"
    ".Lpc7_%=:\n\t"
    "s_memrealtime s[84:85]\n\t"
    "s_waitcnt lgkmcnt(0)\n\t"
    "s_sub_u32 s89, s84, s24\n\t"
    "s_sub_u32 s89, s89, 14\n\t"
    "s_cmp_lt_i32 s89, s86\n\t"
    "s_cbranch_scc1 .Lpc7_%=\n\t"
    "s_branch .Lpc7r_%=\n\t"
    ".Lpc8_%=:\n\t"
    "s_memrealtime s[84:85]\n\t"
    "s_waitcnt lgkmcnt(0)\n\t"
    "s_sub_u32 s89, s84, s24\n\t"
    "s_sub_u32 s89, s89, 14\n\t"
    "s_cmp_lt_i32 s89, s86\n\t"
    "s_cbranch_scc1 .Lpc8_%=\n\t"
    "s_branch .Lpc8r_%=\n\t"
    ".Lpc9_%=:\n\t"
    "s_memrealtime s[84:85]\n\t"
    "s_waitcnt lgkmcnt(0)\n\t"
    "s_sub_u32 s89, s84, s24\n\t"
    "s_sub_u32 s89, s89, 14\n\t"
    "s_cmp_lt_i32 s89, s86\n\t"
    "s_cbranch_scc1 .Lpc9_%=\n\t"
    "s_branch .Lpc9r_%=\n\t"
    ".Lpc10_%=:\n\t"
    "s_memrealtime s[84:85]\n\t"
    "s_waitcnt lgkmcnt(0)\n\t"
    "s_sub_u32 s89, s84, s24\n\t"
    "s_sub_u32 s89, s89, 14\n\t"
    "s_cmp_lt_i32 s89, s86\n\t"
    "s_cbranch_scc1 .Lpc10_%=\n\t"
    "s_branch .Lpc10r_%=\n\t"
    ".Lpc11_%=:\n\t"
    "s_memrealtime s[84:85]\n\t"
    "s_waitcnt lgkmcnt(0)\n\t"
    "s_sub_u32 s89, s84, s24\n\t"
    "s_sub_u32 s89, s89, 14\n\t"
    "s_cmp_lt_i32 s89, s86\n\t"
    "s_cbranch_scc1 .Lpc11_%=\n\t"
    "s_branch .Lpc11r_%=\n\t"
    ".Lpc12_%=:\n\t"
    "s_memrealtime s[84:85]\n\t"
    "s_waitcnt lgkmcnt(0)\n\t"
    "s_sub_u32 s89, s84, s24\n\t"
    "s_sub_u32 s89, s89, 14\n\t"
    "s_cmp_lt_i32 s89, s86\n\t"
    "s_cbranch_scc1 .Lpc12_%=\n\t"
    "s_branch .Lpc12r_%=\n\t"
    ".Lpc13_%=:\n\t"
    "s_memrealtime s[84:85]\n\t"
    "s_waitcnt lgkmcnt(0)\n\t"
    "s_sub_u32 s89, s84, s24\n\t"
    "s_sub_u32 s89, s89, 14\n\t"
    "s_cmp_lt_i32 s89, s86\n\t"
    "s_cbranch_scc1 .Lpc13_%=\n\t"
    "s_branch .Lpc13r_%=\n\t"
    ".Lpc14_%=:\n\t"
    "s_memrealtime s[84:85]\n\t"
    "s_waitcnt lgkmcnt(0)\n\t"
    "s_sub_u32 s89, s84, s24\n\t"
    "s_sub_u32 s89, s89, 14\n\t"
    "s_cmp_lt_i32 s89, s86\n\t"
    "s_cbranch_scc1 .Lpc14_%=\n\t"
    "s_branch .Lpc14r_%=\n\t"
    ".p2align 12\n\t"
    ".Lmain_%=:\n\t"
    "ds_read_b128 v[36:39], v244 offset:0\n\t"
    "ds_read_b128 v[12:15], v244 offset:128\n\t"
    "ds_read_b128 v[24:27], v244 offset:64\n\t"
    "ds_read_b128 v[4:7], v244 offset:0\n\t"
    "v_readlane_b32 s16, v42, 1\n\t"
    "v_readlane_b32 s17, v42, 1\n\t"
    "s_cmp_lt_u32 s53, 2\n\t"
    "s_cselect_b32 s16, s16, s17\n\t"
    "s_sub_i32 s16, s24, s16\n\t"
    "s_max_i32 s16, s16, -150\n\t"
    "s_min_i32 s16, s16, 150\n\t"
    "s_sub_u32 s87, s87, s16\n\t"
    "s_barrier\n\t"
    "s_cmp_lg_u32 s53, 0\n\t"
    "s_cbranch_scc1 .Lnoarr_%=\n\t"
    "s_lshl2_add_u32 s58, s62, 868352\n\t"
    "v_mov_b32 v2, s30\n\t"
    "v_mov_b32 v1, s58\n\t"
    "s_mov_b64 exec, 1\n\t"
    "global_store_dword v1, v2, s[60:61] sc1\n\t"
    "s_mov_b64 exec, -1\n\t"
    ".Lnoarr_%=:\n\t"
    "ds_read_b128 v[48:51], v217 offset:4608\n\t"
    "s_mov_b32 s55, 81920\n\t"
    "buffer_load_dwordx4 v[176:179], v92, s[48:51], s55 offen offset:1024 sc1\n\t"
    "s_mov_b32 s55, 0\n\t"
    "buffer_load_dwordx4 a[0:3], v92, s[48:51], s55 offen offset:0 sc1\n\t"
    "buffer_load_dwordx4 a[4:7], v92, s[48:51], s55 offen offset:1024 sc1\n\t"
    "buffer_load_dwordx4 a[8:11], v92, s[48:51], s55 offen offset:2048 sc1\n\t"
    "buffer_load_dwordx4 a[12:15], v92, s[48:51], s55 offen offset:3072 sc1\n\t"
    "s_mov_b32 s55, 4096\n\t"
    "buffer_load_dwordx4 a[16:19], v92, s[48:51], s55 offen offset:0 sc1\n\t"
    "buffer_load_dwordx4 a[20:23], v92, s[48:51], s55 offen offset:1024 sc1\n\t"
    "ds_read_b128 v[16:19], v244 offset:192\n\t"
    "ds_read_b128 v[28:31], v244 offset:128\n\t"
    "ds_read_b128 v[40:43], v217 offset:0\n\t"
    "s_mov_b32 s65, 0\n\t"
    "ds_read_b128 v[44:47], v236 offset:0\n\t"
    "ds_read_b128 v[8:11], v244 offset:64\n\t"
    "ds_read_b128 v[20:23], v244 offset:0\n\t"
    "ds_read_b128 v[64:67], v217 offset:4736\n\t"
    "ds_read_b128 v[52:55], v236 offset:4608\n\t"
    "ds_read_b128 v[32:35], v244 offset:192\n\t"
    "ds_read_b128 v[60:63], v236 offset:128\n\t"
    "ds_read_b128 v[56:59], v217 offset:128\n\t"
    "ds_read_b128 v[68:71], v236 offset:4736\n\t"
    "s_waitcnt lgkmcnt(8)\n\t"
    "buffer_load_dwordx4 a[24:27], v92, s[48:51], s55 offen offset:2048 sc1\n\t"
    "buffer_load_dwordx4 a[28:31], v92, s[48:51], s55 offen offset:3072 sc1\n\t"
    "s_mov_b32 s55, 81920\n\t"
    "buffer_load_dwordx4 v[180:183], v92, s[48:51], s55 offen offset:2048 sc1\n\t"
    "s_mov_b32 s55, 8192\n\t"
    "buffer_load_dwordx4 a[32:35], v92, s[48:51], s55 offen offset:0 sc1\n\t"
    "buffer_load_dwordx4 a[36:39], v92, s[48:51], s55 offen offset:1024 sc1\n\t"
    "buffer_load_dwordx4 a[40:43], v92, s[48:51], s55 offen offset:2048 sc1\n\t"
    "buffer_load_dwordx4 a[44:47], v92, s[48:51], s55 offen offset:3072 sc1\n\t"
    "s_and_b32 s76, s82, 23\n\t"
    "s_cmp_eq_u32 s76, 16\n\t"
    "s_cbranch_scc1 .Lprp_%=\n\t"
    ".Lprpr_%=:\n\t"
    "s_waitcnt vmcnt(12)\n\t"
    "v_smfmac_f32_16x16x64_bf16 v[4:7], a[0:3], v[40:47], v176\n\t"
    "s_waitcnt lgkmcnt(7)\n\t"
    "s_waitcnt vmcnt(11)\n\t"
    "v_smfmac_f32_16x16x64_bf16 v[8:11], a[4:7], v[40:47], v176 abid:1\n\t"
    "s_mov_b32 s55, 12288\n\t"
    "buffer_load_dwordx4 a[48:51], v92, s[48:51], s55 offen offset:0 sc1\n\t"
    "s_waitcnt vmcnt(11)\n\t"
    "v_smfmac_f32_16x16x64_bf16 v[12:15], a[8:11], v[40:47], v177\n\t"
    "s_waitcnt vmcnt(10)\n\t"
    "v_smfmac_f32_16x16x64_bf16 v[16:19], a[12:15], v[40:47], v177 abid:1\n\t"
    "s_waitcnt lgkmcnt(4)\n\t"
    "buffer_load_dwordx4 a[52:55], v92, s[48:51], s55 offen offset:1024 sc1\n\t"
    "v_smfmac_f32_16x16x64_bf16 v[20:23], a[0:3], v[48:55], v176\n\t"
    "buffer_load_dwordx4 a[56:59], v92, s[48:51], s55 offen offset:2048 sc1\n\t"
    "v_smfmac_f32_16x16x64_bf16 v[24:27], a[4:7], v[48:55], v176 abid:1\n\t"
    "buffer_load_dwordx4 a[60:63], v92, s[48:51], s55 offen offset:3072 sc1\n\t"
    "v_smfmac_f32_16x16x64_bf16 v[28:31], a[8:11], v[48:55], v177\n\t"
    "s_waitcnt lgkmcnt(3)\n\t"
    "ds_read_b128 v[40:43], v217 offset:256\n\t"
    "v_smfmac_f32_16x16x64_bf16 v[32:35], a[12:15], v[48:55], v177 abid:1\n\t"
    "s_waitcnt lgkmcnt(2)\n\t"
    "ds_read_b128 v[52:55], v236 offset:4864\n\t"
    "s_waitcnt vmcnt(12)\n\t"
    "v_smfmac_f32_16x16x64_bf16 v[4:7], a[16:19], v[56:63], v178\n\t"
    "ds_read_b128 v[44:47], v236 offset:256\n\t"
    "s_waitcnt vmcnt(11)\n\t"
    "v_smfmac_f32_16x16x64_bf16 v[8:11], a[20:23], v[56:63], v178 abid:1\n\t"
    "s_mov_b32 s55, 81920\n\t"
    "buffer_load_dwordx4 v[184:187], v92, s[48:51], s55 offen offset:3072 sc1\n\t"
    "s_waitcnt vmcnt(11)\n\t"
    "v_smfmac_f32_16x16x64_bf16 v[12:15], a[24:27], v[56:63], v179\n\t"
    "s_mov_b32 s55, 16384\n\t"
    "buffer_load_dwordx4 a[64:67], v92, s[48:51], s55 offen offset:0 sc1\n\t"
    "s_waitcnt vmcnt(11)\n\t"
    "v_smfmac_f32_16x16x64_bf16 v[16:19], a[28:31], v[56:63], v179 abid:1\n\t"
    "s_waitcnt lgkmcnt(3)\n\t"
    "buffer_load_dwordx4 a[68:71], v92, s[48:51], s55 offen offset:1024 sc1\n\t"
    "ds_read_b128 v[48:51], v217 offset:4864\n\t"
    "v_smfmac_f32_16x16x64_bf16 v[20:23], a[16:19], v[64:71], v178\n\t"
    "buffer_load_dwordx4 a[72:75], v92, s[48:51], s55 offen offset:2048 sc1\n\t"
    "v_smfmac_f32_16x16x64_bf16 v[24:27], a[20:23], v[64:71], v178 abid:1\n\t"
    "buffer_load_dwordx4 a[76:79], v92, s[48:51], s55 offen offset:3072 sc1\n\t"
    "v_smfmac_f32_16x16x64_bf16 v[28:31], a[24:27], v[64:71], v179\n\t"
    "ds_read_b128 v[60:63], v236 offset:384\n\t"
    "v_smfmac_f32_16x16x64_bf16 v[32:35], a[28:31], v[64:71], v179 abid:1\n\t"
    "s_waitcnt lgkmcnt(2)\n\t"
    "ds_read_b128 v[56:59], v217 offset:384\n\t"
    "s_waitcnt vmcnt(12)\n\t"
    "v_smfmac_f32_16x16x64_bf16 v[4:7], a[32:35], v[40:47], v180\n\t"
    "ds_read_b128 v[64:67], v217 offset:4992\n\t"
    "s_waitcnt vmcnt(11)\n\t"
    "v_smfmac_f32_16x16x64_bf16 v[8:11], a[36:39], v[40:47], v180 abid:1\n\t"
    "s_mov_b32 s55, 20480\n\t"
    "buffer_load_dwordx4 a[80:83], v92, s[48:51], s55 offen offset:0 sc1\n\t"
    "s_waitcnt vmcnt(11)\n\t"
    "v_smfmac_f32_16x16x64_bf16 v[12:15], a[40:43], v[40:47], v181\n\t"
    "ds_read_b128 v[68:71], v236 offset:4992\n\t"
    "s_waitcnt vmcnt(10)\n\t"
    "v_smfmac_f32_16x16x64_bf16 v[16:19], a[44:47], v[40:47], v181 abid:1\n\t"
    "s_waitcnt lgkmcnt(4)\n\t"
    "buffer_load_dwordx4 a[84:87], v92, s[48:51], s55 offen offset:1024 sc1\n\t"
    "ds_read_b128 v[40:43], v217 offset:512\n\t"
    "v_smfmac_f32_16x16x64_bf16 v[20:23], a[32:35], v[48:55], v180\n\t"
    "buffer_load_dwordx4 a[88:91], v92, s[48:51], s55 offen offset:2048 sc1\n\t"
    "v_smfmac_f32_16x16x64_bf16 v[24:27], a[36:39], v[48:55], v180 abid:1\n\t"
    "global_load_dwordx2 v[90:91], v88, s[60:61] sc1\n\t"
    "buffer_load_dwordx4 a[92:95], v92, s[48:51], s55 offen offset:3072 sc1\n\t"
    "v_smfmac_f32_16x16x64_bf16 v[28:31], a[40:43], v[48:55], v181\n\t"
    "ds_read_b128 v[44:47], v236 offset:512\n\t"
    "v_smfmac_f32_16x16x64_bf16 v[32:35], a[44:47], v[48:55], v181 abid:1\n\t"
    "s_waitcnt lgkmcnt(4)\n\t"
    "ds_read_b128 v[52:55], v236 offset:5120\n\t"
    "s_waitcnt vmcnt(13)\n\t"
    "v_smfmac_f32_16x16x64_bf16 v[4:7], a[48:51], v[56:63], v182\n\t"
    "ds_read_b128 v[48:51], v217 offset:5120\n\t"
    "s_waitcnt vmcnt(12)\n\t"
    "v_smfmac_f32_16x16x64_bf16 v[8:11], a[52:55], v[56:63], v182 abid:1\n\t"
    "s_mov_b32 s77, 0\n\t"
    "s_mov_b32 s55, 86016\n\t"
    "buffer_load_dwordx4 v[188:191], v92, s[48:51], s55 offen offset:0 sc1\n\t"
    "s_waitcnt vmcnt(12)\n\t"
    "v_smfmac_f32_16x16x64_bf16 v[12:15], a[56:59], v[56:63], v183\n\t"
    "s_mov_b32 s55, 24576\n\t"
    "buffer_load_dwordx4 a[96:99], v92, s[48:51], s55 offen offset:0 sc1\n\t"
    "s_waitcnt vmcnt(12)\n\t"
    "v_smfmac_f32_16x16x64_bf16 v[16:19], a[60:63], v[56:63], v183 abid:1\n\t"
    "s_waitcnt lgkmcnt(4)\n\t"
    "buffer_load_dwordx4 a[100:103], v92, s[48:51], s55 offen offset:1024 sc1\n\t"
    "v_smfmac_f32_16x16x64_bf16 v[20:23], a[48:51], v[64:71], v182\n\t"
    "buffer_load_dwordx4 a[104:107], v92, s[48:51], s55 offen offset:2048 sc1\n\t"
    "v_smfmac_f32_16x16x64_bf16 v[24:27], a[52:55], v[64:71], v182 abid:1\n\t"
    "buffer_load_dwordx4 a[108:111], v92, s[48:51], s55 offen offset:3072 sc1\n\t"
    "v_smfmac_f32_16x16x64_bf16 v[28:31], a[56:59], v[64:71], v183\n\t"
    "ds_read_b128 v[60:63], v236 offset:640\n\t"
    "v_smfmac_f32_16x16x64_bf16 v[32:35], a[60:63], v[64:71], v183 abid:1\n\t"
    "s_waitcnt lgkmcnt(3)\n\t"
    "s_mov_b32 s76, 1060993\n\t"
    "ds_read_b128 v[56:59], v217 offset:640\n\t"
    "s_waitcnt vmcnt(13)\n\t"
    "v_smfmac_f32_16x16x64_bf16 v[4:7], a[64:67], v[40:47], v184\n\t"
    "s_mov_b32 s67, 9437184\n\t"
    "ds_read_b128 v[68:71], v236 offset:5248\n\t"
    "s_waitcnt vmcnt(12)\n\t"
    "v_smfmac_f32_16x16x64_bf16 v[8:11], a[68:71], v[40:47], v184 abid:1\n\t"
    "s_mov_b32 s55, 28672\n\t"
    "buffer_load_dwordx4 a[112:115], v92, s[48:51], s55 offen offset:0 sc1\n\t"
    "s_waitcnt vmcnt(12)\n\t"
    "v_smfmac_f32_16x16x64_bf16 v[12:15], a[72:75], v[40:47], v185\n\t"
    "ds_read_b128 v[64:67], v217 offset:5248\n\t"
    "s_waitcnt vmcnt(11)\n\t"
    "v_smfmac_f32_16x16x64_bf16 v[16:19], a[76:79], v[40:47], v185 abid:1\n\t"
    "s_waitcnt lgkmcnt(4)\n\t"
    "buffer_load_dwordx4 a[116:119], v92, s[48:51], s55 offen offset:1024 sc1\n\t"
    "v_smfmac_f32_16x16x64_bf16 v[20:23], a[64:67], v[48:55], v184\n\t"
    "buffer_load_dwordx4 a[120:123], v92, s[48:51], s55 offen offset:2048 sc1\n\t"
    "v_smfmac_f32_16x16x64_bf16 v[24:27], a[68:71], v[48:55], v184 abid:1\n\t"
    "buffer_load_dwordx4 a[124:127], v92, s[48:51], s55 offen offset:3072 sc1\n\t"
    "v_smfmac_f32_16x16x64_bf16 v[28:31], a[72:75], v[48:55], v185\n\t"
    "ds_read_b128 v[40:43], v217 offset:768\n\t"
    "v_smfmac_f32_16x16x64_bf16 v[32:35], a[76:79], v[48:55], v185 abid:1\n\t"
    "s_waitcnt lgkmcnt(3)\n\t"
    "ds_read_b128 v[48:51], v217 offset:5376\n\t"
    "s_waitcnt vmcnt(13)\n\t"
    "v_smfmac_f32_16x16x64_bf16 v[4:7], a[80:83], v[56:63], v186\n\t"
    "s_mov_b32 s55, 86016\n\t"
    "buffer_load_dwordx4 v[192:195], v92, s[48:51], s55 offen offset:1024 sc1\n\t"
    "s_waitcnt vmcnt(13)\n\t"
    "v_smfmac_f32_16x16x64_bf16 v[8:11], a[84:87], v[56:63], v186 abid:1\n\t"
    "ds_read_b128 v[44:47], v236 offset:768\n\t"
    "ds_read_b128 v[52:55], v236 offset:5376\n\t"
    "s_waitcnt vmcnt(12)\n\t"
    "v_smfmac_f32_16x16x64_bf16 v[12:15], a[88:91], v[56:63], v187\n\t"
    "s_waitcnt vmcnt(11)\n\t"
    "s_mov_b32 s58, 0x40000\n\t"
    ".Ls2c_%=:\n\t"
    "v_xor_b32 v1, v90, v89\n\t"
    "v_xor_b32 v2, s31, v91\n\t"
    "v_lshrrev_b32 v3, 2, v2\n\t"
    "v_or_b32 v1, v1, v3\n\t"
    "v_cmp_ne_u32 vcc, 0, v1\n\t"
    "s_cbranch_vccnz .Ls2l_%=\n\t"
    ".Ls2ok_%=:\n\t"
    "s_add_u32 m0, s83, 576\n\t"
    "s_mov_b32 s55, 108544\n\t"
    "s_mov_b32 exec_hi, 0x7fff\n\t"
    "buffer_load_dwordx4 v92, s[48:51], s55 offen sc1 lds\n\t"
    "s_mov_b64 exec, -1\n\t"
    "v_and_b32 v2, 3, v2\n\t"
    "v_and_b32 v3, 1, v2\n\t"
    "v_cmp_eq_u32 vcc, 2, v2\n\t"
    "v_cmp_eq_u32 s[80:81], 1, v3\n\t"
    "v_cmp_eq_u32 s[96:97], 3, v2\n\t"
    "s_nop 1\n\t"
    "s_and_b32 s73, vcc_lo, 0x1ff\n\t"
    "s_and_b32 s71, s80, 0x1ff\n\t"
    "s_and_b32 s96, s96, 0x1ff\n\t"
    "s_lshl_b32 s96, s96, 16\n\t"
    "s_or_b32 s71, s71, s96\n\t"
    "s_mov_b32 s55, 98304\n\t"
    "buffer_load_dwordx4 v[168:171], v92, s[48:51], s55 offen offset:1024 sc1\n\t"
    "buffer_load_dwordx4 v[172:175], v92, s[48:51], s55 offen offset:2048 sc1\n\t"
    "s_mov_b32 s55, 32768\n\t"
    "buffer_load_dwordx4 a[128:131], v92, s[48:51], s55 offen offset:0 sc1\n\t"
    "s_waitcnt vmcnt(14)\n\t"
    "v_smfmac_f32_16x16x64_bf16 v[16:19], a[92:95], v[56:63], v187 abid:1\n\t"
    "s_waitcnt lgkmcnt(4)\n\t"
    "buffer_load_dwordx4 a[132:135], v92, s[48:51], s55 offen offset:1024 sc1\n\t"
    "v_smfmac_f32_16x16x64_bf16 v[20:23], a[80:83], v[64:71], v186\n\t"
    "buffer_load_dwordx4 a[136:139], v92, s[48:51], s55 offen offset:2048 sc1\n\t"
    "v_smfmac_f32_16x16x64_bf16 v[24:27], a[84:87], v[64:71], v186 abid:1\n\t"
    "buffer_load_dwordx4 a[140:143], v92, s[48:51], s55 offen offset:3072 sc1\n\t"
    "v_smfmac_f32_16x16x64_bf16 v[28:31], a[88:91], v[64:71], v187\n\t"
    "ds_read_b128 v[56:59], v217 offset:896\n\t"
    "v_smfmac_f32_16x16x64_bf16 v[32:35], a[92:95], v[64:71], v187 abid:1\n\t"
    "s_waitcnt lgkmcnt(2)\n\t"
    "ds_read_b128 v[60:63], v236 offset:896\n\t"
    "ds_read_b128 v[64:67], v217 offset:5504\n\t"
    "s_waitcnt vmcnt(15)\n\t"
    "v_smfmac_f32_16x16x64_bf16 v[4:7], a[96:99], v[40:47], v188\n\t"
    "s_mov_b32 s55, 36864\n\t"
    "buffer_load_dwordx4 a[144:147], v92, s[48:51], s55 offen offset:0 sc1\n\t"
    "s_waitcnt vmcnt(15)\n\t"
    "v_smfmac_f32_16x16x64_bf16 v[8:11], a[100:103], v[40:47], v188 abid:1\n\t"
    "ds_read_b128 v[68:71], v236 offset:5504\n\t"
    "s_waitcnt vmcnt(14)\n\t"
    "v_smfmac_f32_16x16x64_bf16 v[12:15], a[104:107], v[40:47], v189\n\t"
    "s_mov_b32 s55, 86016\n\t"
    "buffer_load_dwordx4 v[196:199], v92, s[48:51], s55 offen offset:2048 sc1\n\t"
    "s_waitcnt vmcnt(14)\n\t"
    "v_smfmac_f32_16x16x64_bf16 v[16:19], a[108:111], v[40:47], v189 abid:1\n\t"
    "s_waitcnt lgkmcnt(4)\n\t"
    "s_mov_b32 s55, 40960\n\t"
    "buffer_load_dwordx4 a[164:167], v92, s[48:51], s55 offen offset:1024 sc1\n\t"
    "ds_read_b128 v[40:43], v217 offset:1024\n\t"
    "v_smfmac_f32_16x16x64_bf16 v[20:23], a[96:99], v[48:55], v188\n\t"
    "ds_read_b128 v[44:47], v236 offset:1024\n\t"
    "v_smfmac_f32_16x16x64_bf16 v[24:27], a[100:103], v[48:55], v188 abid:1\n\t"
    "s_mov_b32 s55, 36864\n\t"
    "buffer_load_dwordx4 a[148:151], v92, s[48:51], s55 offen offset:1024 sc1\n\t"
    "v_smfmac_f32_16x16x64_bf16 v[28:31], a[104:107], v[48:55], v189\n\t"
    "s_mov_b32 s55, 40960\n\t"
    "buffer_load_dwordx4 a[168:171], v92, s[48:51], s55 offen offset:2048 sc1\n\t"
    "s_waitcnt vmcnt(11)\n\t"
    "ds_read_b128 v[218:221], v244 offset:1008\n\t"
    "ds_read_b128 v[226:229], v244 offset:1136\n\t"
    "s_waitcnt vmcnt(9)\n\t"
    "v_add_u32 v173, s88, v173\n\t"
    "v_smfmac_f32_16x16x64_bf16 v[32:35], a[108:111], v[48:55], v189 abid:1\n\t"
    "s_waitcnt lgkmcnt(6)\n\t"
    "ds_read_b128 v[222:225], v244 offset:1072\n\t"
    "v_add_u32 v171, s88, v171\n\t"
    "v_smfmac_f32_16x16x64_bf16 v[4:7], a[112:115], v[56:63], v190\n\t"
    "s_mov_b32 s55, 36864\n\t"
    "buffer_load_dwordx4 a[152:155], v92, s[48:51], s55 offen offset:2048 sc1\n\t"
    "ds_read_b128 v[230:233], v244 offset:1200\n\t"
    "ds_write_b32 v173, v172\n\t"
    "s_mov_b32 s22, 0\n\t"
    "s_mov_b32 s18, 0x55555555\n\t"
    "s_mov_b32 s19, 0x55555555\n\t"
    "s_or_b32 s58, s71, s73\n\t"
    "s_cbranch_scc1 .Lxinit_%=\n\t"
    ".Lxinitr_%=:\n\t"
    "v_smfmac_f32_16x16x64_bf16 v[8:11], a[116:119], v[56:63], v190 abid:1\n\t"
    "s_mov_b32 s55, 40960\n\t"
    "buffer_load_dwordx4 a[172:175], v92, s[48:51], s55 offen offset:3072 sc1\n\t"
    "ds_read_b128 v[48:51], v217 offset:5632\n\t"
    "v_add_u32 v175, s88, v175\n\t"
    "v_smfmac_f32_16x16x64_bf16 v[12:15], a[120:123], v[56:63], v191\n\t"
    "s_lshr_b64 s[80:81], s[76:77], s69\n\t"
    "s_mov_b32 s55, 36864\n\t"
    "buffer_load_dwordx4 a[156:159], v92, s[48:51], s55 offen offset:3072 sc1\n\t"
    "ds_write_b32 v175, v174\n\t"
    "v_add_u32_sdwa v90, v248, v247 dst_sel:DWORD dst_unused:UNUSED_PAD src0_sel:DWORD src1_sel:WORD_0\n\t"
    "v_add_u32_sdwa v91, v248, v247 dst_sel:DWORD dst_unused:UNUSED_PAD src0_sel:DWORD src1_sel:WORD_1\n\t"
    "v_smfmac_f32_16x16x64_bf16 v[16:19], a[124:127], v[56:63], v191 abid:1\n\t"
    "s_waitcnt lgkmcnt(9)\n\t"
    "ds_read_b64 v[234:235], v244 offset:1264\n\t"
    "v_add_u32_sdwa v88, v248, v246 dst_sel:DWORD dst_unused:UNUSED_PAD src0_sel:DWORD src1_sel:WORD_0\n\t"
    "v_smfmac_f32_16x16x64_bf16 v[20:23], a[112:115], v[64:71], v190\n\t"
    "s_waitcnt lgkmcnt(7)\n\t"
    "s_mov_b32 s55, 45056\n\t"
    "buffer_load_dwordx4 a[176:179], v92, s[48:51], s55 offen offset:0 sc1\n\t"
    "ds_read_u16 v88, v88 offset:0\n\t"
    "ds_read_u16 v91, v91 offset:0\n\t"
    "v_xor_b32 v218, v218, v96\n\t"
    "v_xor_b32 v219, v219, v96\n\t"
    "v_xor_b32 v221, v221, v96\n\t"
    "v_add_u32_sdwa v75, v248, v221 dst_sel:DWORD dst_unused:UNUSED_PAD src0_sel:DWORD src1_sel:WORD_1\n\t"
    "v_add_u32_sdwa v89, v248, v246 dst_sel:DWORD dst_unused:UNUSED_PAD src0_sel:DWORD src1_sel:WORD_1\n\t"
    "v_smfmac_f32_16x16x64_bf16 v[24:27], a[116:119], v[64:71], v190 abid:1\n\t"
    "s_mov_b32 s77, 0\n\t"
    "s_mov_b32 s55, 40960\n\t"
    "buffer_load_dwordx4 a[160:163], v92, s[48:51], s55 offen offset:0 sc1\n\t"
    "ds_read_u16 v89, v89 offset:0\n\t"
    "ds_read_u16 v90, v90 offset:0\n\t"
    "v_smfmac_f32_16x16x64_bf16 v[28:31], a[120:123], v[64:71], v191\n\t"
    "s_waitcnt lgkmcnt(9)\n\t"
    "s_and_b32 s80, s80, 63\n\t"
    "ds_read_b128 v[52:55], v236 offset:5632\n\t"
    "v_add_u32_sdwa v72, v248, v218 dst_sel:DWORD dst_unused:UNUSED_PAD src0_sel:DWORD src1_sel:WORD_1\n\t"
    "v_add_u32_sdwa v218, v248, v218 dst_sel:DWORD dst_unused:UNUSED_PAD src0_sel:DWORD src1_sel:WORD_0\n\t"
    "v_add_u32_sdwa v73, v248, v219 dst_sel:DWORD dst_unused:UNUSED_PAD src0_sel:DWORD src1_sel:WORD_1\n\t"
    "v_xor_b32 v220, v220, v96\n\t"
    "v_xor_b32 v224, v224, v96\n\t"
    "v_add_u32 v169, s88, v169\n\t"
    "v_smfmac_f32_16x16x64_bf16 v[32:35], a[124:127], v[64:71], v191 abid:1\n\t"
    "s_mov_b32 s76, 1060993\n\t"
    "s_mov_b32 s55, 45056\n\t"
    "buffer_load_dwordx4 a[180:183], v92, s[48:51], s55 offen offset:1024 sc1\n\t"
    "ds_read_b32 v57, v72 offset:4\n\t"
    "ds_read_b32 v59, v73 offset:4\n\t"
    "ds_read_b32 v67, v75 offset:4\n\t"
    "ds_write_b32 v169, v168\n\t"
    "v_add_u32_sdwa v74, v248, v220 dst_sel:DWORD dst_unused:UNUSED_PAD src0_sel:DWORD src1_sel:WORD_1\n\t"
    "v_add_u32_sdwa v221, v248, v221 dst_sel:DWORD dst_unused:UNUSED_PAD src0_sel:DWORD src1_sel:WORD_0\n\t"
    "v_xor_b32 v222, v222, v96\n\t"
    "s_waitcnt vmcnt(14)\n\t"
    "v_smfmac_f32_16x16x64_bf16 v[4:7], a[128:131], v[40:47], v192\n\t"
    "s_waitcnt lgkmcnt(13)\n\t"
    "buffer_load_dwordx4 a[184:187], v92, s[48:51], s55 offen offset:2048 sc1\n\t"
    "ds_read_b32 v56, v218 offset:4\n\t"
    "v_add_u32_sdwa v219, v248, v219 dst_sel:DWORD dst_unused:UNUSED_PAD src0_sel:DWORD src1_sel:WORD_0\n\t"
    "v_add_u32_sdwa v76, v248, v222 dst_sel:DWORD dst_unused:UNUSED_PAD src0_sel:DWORD src1_sel:WORD_1\n\t"
    "v_add_u32_sdwa v78, v248, v224 dst_sel:DWORD dst_unused:UNUSED_PAD src0_sel:DWORD src1_sel:WORD_1\n\t"
    "v_xor_b32 v226, v226, v96\n\t"
    "v_xor_b32 v229, v229, v96\n\t"
    "v_xor_b32 v231, v231, v96\n\t"
    "s_waitcnt vmcnt(14)\n\t"
    "v_smfmac_f32_16x16x64_bf16 v[8:11], a[132:135], v[40:47], v192 abid:1\n\t"
    "s_lshl_b32 s80, s80, 2\n\t"
    "ds_read_b32 v58, v219 offset:4\n\t"
    "ds_write_b32 v171, v170\n\t"
    "v_xor_b32 v223, v223, v96\n\t"
    "v_xor_b32 v225, v225, v96\n\t"
    "s_waitcnt vmcnt(13)\n\t"
    "v_smfmac_f32_16x16x64_bf16 v[12:15], a[136:139], v[40:47], v193\n\t"
    "s_mov_b32 s66, 4718592\n\t"
    "s_mov_b32 s55, 86016\n\t"
    "buffer_load_dwordx4 v[200:203], v92, s[48:51], s55 offen offset:3072 sc1\n\t"
    "ds_read_b128 v[80:83], v245 offset:0\n\t"
    "v_add_u32_sdwa v77, v248, v223 dst_sel:DWORD dst_unused:UNUSED_PAD src0_sel:DWORD src1_sel:WORD_1\n\t"
    "v_add_u32_sdwa v224, v248, v224 dst_sel:DWORD dst_unused:UNUSED_PAD src0_sel:DWORD src1_sel:WORD_0\n\t"
    "v_add_u32_sdwa v79, v248, v225 dst_sel:DWORD dst_unused:UNUSED_PAD src0_sel:DWORD src1_sel:WORD_1\n\t"
    "v_xor_b32 v230, v230, v96\n\t"
    "s_waitcnt vmcnt(13)\n\t"
    "v_smfmac_f32_16x16x64_bf16 v[16:19], a[140:143], v[40:47], v193 abid:1\n\t"
    "s_waitcnt lgkmcnt(9)\n\t"
    "s_mov_b64 vcc, s[18:19]\n\t"
    "s_mov_b32 s55, 49152\n\t"
    "buffer_load_dwordx4 a[204:207], v92, s[48:51], s55 offen offset:3072 sc1\n\t"
    "ds_read_b32 v66, v221 offset:4\n\t"
    "v_add_u32_sdwa v220, v248, v220 dst_sel:DWORD dst_unused:UNUSED_PAD src0_sel:DWORD src1_sel:WORD_0\n\t"
    "v_add_u32_sdwa v223, v248, v223 dst_sel:DWORD dst_unused:UNUSED_PAD src0_sel:DWORD src1_sel:WORD_0\n\t"
    "s_set_gpr_idx_on s80, gpr_idx(SRC2,DST)\n\t"
    "v_mfma_f32_16x16x32_bf16 v[0:3], v[250:253], v[88:91], v[0:3]\n\t"
    "s_set_gpr_idx_off\n\t"
    "s_waitcnt lgkmcnt(9)\n\t"
    "s_lshr_b64 s[80:81], s[76:77], s70\n\t"
    "s_mov_b32 s55, 45056\n\t"
    "buffer_load_dwordx4 a[188:191], v92, s[48:51], s55 offen offset:3072 sc1\n\t"
    "ds_read_b32 v65, v74 offset:4\n\t"
    "v_add_u32_sdwa v222, v248, v222 dst_sel:DWORD dst_unused:UNUSED_PAD src0_sel:DWORD src1_sel:WORD_0\n\t"
    "v_xor_b32 v235, v235, v96\n\t"
    "v_smfmac_f32_16x16x64_bf16 v[20:23], a[128:131], v[48:55], v192\n\t"
    "s_and_b32 s80, s80, 63\n\t"
    "ds_read_b32 v64, v220 offset:4\n\t"
    "ds_read_b32 v40, v222 offset:4\n\t"
    "v_smfmac_f32_16x16x64_bf16 v[24:27], a[132:135], v[48:55], v192 abid:1\n\t"
    "s_lshl_b32 s80, s80, 2\n\t"
    "s_mov_b32 s55, 53248\n\t"
    "buffer_load_dwordx4 a[208:211], v92, s[48:51], s55 offen offset:0 sc1\n\t"
    "ds_read_b32 v41, v76 offset:4\n\t"
    "ds_read_b32 v42, v223 offset:4\n\t"
    "ds_read_b32 v43, v77 offset:4\n\t"
    "s_set_gpr_idx_on s80, gpr_idx(SRC2,DST)\n\t"
    "v_mfma_f32_16x16x32_bf16 v[0:3], v[252:255], v[88:91], v[0:3]\n\t"
    "s_set_gpr_idx_off\n\t"
    "s_mov_b32 s55, 49152\n\t"
    "buffer_load_dwordx4 a[192:195], v92, s[48:51], s55 offen offset:0 sc1\n\t"
    "v_add_u32_sdwa v225, v248, v225 dst_sel:DWORD dst_unused:UNUSED_PAD src0_sel:DWORD src1_sel:WORD_0\n\t"
    "s_cmp_lg_u32 s22, 0\n\t"
    "s_cbranch_scc1 .Lsl0_%=\n\t"
    ".Lslr0_%=:\n\t"
    "v_smfmac_f32_16x16x64_bf16 v[28:31], a[136:139], v[48:55], v193\n\t"
    "s_mov_b32 s76, 2126213\n\t"
    "s_mov_b32 s77, 0\n\t"
    "s_lshr_b64 s[80:81], s[76:77], s69\n\t"
    "s_and_b32 s80, s80, 63\n\t"
    "s_mov_b32 s77, 0\n\t"
    "v_xor_b32 v232, v232, v96\n\t"
    "v_add_u32_sdwa v88, v248, v246 dst_sel:DWORD dst_unused:UNUSED_PAD src0_sel:DWORD src1_sel:WORD_0\n\t"
    "v_add_u32_sdwa v89, v248, v246 dst_sel:DWORD dst_unused:UNUSED_PAD src0_sel:DWORD src1_sel:WORD_1\n\t"
    "v_add_u32_sdwa v90, v248, v247 dst_sel:DWORD dst_unused:UNUSED_PAD src0_sel:DWORD src1_sel:WORD_0\n\t"
    "v_add_u32_sdwa v91, v248, v247 dst_sel:DWORD dst_unused:UNUSED_PAD src0_sel:DWORD src1_sel:WORD_1\n\t"
    "v_smfmac_f32_16x16x64_bf16 v[32:35], a[140:143], v[48:55], v193 abid:1\n\t"
    "s_waitcnt lgkmcnt(7)\n\t"
    "s_lshl_b32 s80, s80, 2\n\t"
    "s_mov_b32 s55, 53248\n\t"
    "buffer_load_dwordx4 a[212:215], v92, s[48:51], s55 offen offset:1024 sc1\n\t"
    "ds_read_u16 v88, v88 offset:4608\n\t"
    "ds_read_u16 v89, v89 offset:4608\n\t"
    "ds_read_u16 v90, v90 offset:4608\n\t"
    "ds_read_u16 v91, v91 offset:4608\n\t"
    "ds_read_b32 v48, v224 offset:4\n\t"
    "ds_read_b32 v49, v78 offset:4\n\t"
    "ds_read_b32 v50, v225 offset:4\n\t"
    "ds_read_b32 v51, v79 offset:4\n\t"
    "v_mfma_f32_16x16x32_bf16 v[4:7], v[80:83], v[56:59], v[4:7]\n\t"
    "s_waitcnt lgkmcnt(4)\n\t"
    "s_mov_b32 s76, 2126213\n\t"
    "s_mov_b32 s55, 49152\n\t"
    "buffer_load_dwordx4 a[196:199], v92, s[48:51], s55 offen offset:1024 sc1\n\t"
    "ds_read_b128 v[80:83], v245 offset:1024\n\t"
    "ds_read_b32 v56, v218 offset:4612\n\t"
    "ds_read_b32 v57, v72 offset:4612\n\t"
    "ds_read_b32 v58, v219 offset:4612\n\t"
    "ds_read_b32 v59, v73 offset:4612\n\t"
    "s_set_gpr_idx_on s80, gpr_idx(SRC2,DST)\n\t"
    "v_mfma_f32_16x16x32_bf16 v[0:3], v[250:253], v[88:91], v[0:3]\n\t"
    "s_set_gpr_idx_off\n\t"
    "s_waitcnt lgkmcnt(4)\n\t"
    "s_lshr_b64 s[80:81], s[76:77], s70\n\t"
    "ds_read_b128 v[68:71], v236 offset:9344\n\t"
    "v_xor_b32 v227, v227, v96\n\t"
    "v_xor_b32 v228, v228, v96\n\t"
    "v_mfma_f32_16x16x32_bf16 v[8:11], v[80:83], v[64:67], v[8:11]\n\t"
    "s_and_b32 s80, s80, 63\n\t"
    "s_mov_b32 s55, 53248\n\t"
    "buffer_load_dwordx4 a[216:219], v92, s[48:51], s55 offen offset:2048 sc1\n\t"
    "ds_read_b128 v[80:83], v245 offset:2048\n\t"
    "ds_read_b32 v64, v220 offset:4612\n\t"
    "ds_read_b32 v65, v74 offset:4612\n\t"
    "ds_read_b32 v66, v221 offset:4612\n\t"
    "ds_read_b32 v67, v75 offset:4612\n\t"
    "s_waitcnt lgkmcnt(4)\n\t"
    "v_mfma_f32_16x16x32_bf16 v[12:15], v[80:83], v[40:43], v[12:15]\n\t"
    "s_lshl_b32 s80, s80, 2\n\t"
    "s_mov_b32 s55, 49152\n\t"
    "buffer_load_dwordx4 a[200:203], v92, s[48:51], s55 offen offset:2048 sc1\n\t"
    "ds_read_b128 v[80:83], v245 offset:3072\n\t"
    "ds_read_b128 v[60:63], v236 offset:9216\n\t"
    "ds_read_b32 v40, v222 offset:4612\n\t"
    "ds_read_b32 v41, v76 offset:4612\n\t"
    "ds_read_b32 v42, v223 offset:4612\n\t"
    "ds_read_b32 v43, v77 offset:4612\n\t"
    "v_cvt_pk_bf16_f32 v100, v4, v5\n\t"
    "v_cvt_pk_bf16_f32 v101, v6, v7\n\t"
    "s_set_gpr_idx_on s80, gpr_idx(SRC2,DST)\n\t"
    "v_mfma_f32_16x16x32_bf16 v[0:3], v[252:255], v[88:91], v[0:3]\n\t"
    "s_set_gpr_idx_off\n\t"
    "s_waitcnt lgkmcnt(5)\n\t"
    "s_mov_b32 s55, 53248\n\t"
    "buffer_load_dwordx4 a[220:223], v92, s[48:51], s55 offen offset:3072 sc1\n\t"
    "ds_read_b128 v[4:7], v244 offset:64\n\t"
    "s_cmp_lg_u32 s22, 0\n\t"
    "s_cbranch_scc1 .Lsl1_%=\n\t"
    ".Lslr1_%=:\n\t"
    "v_mfma_f32_16x16x32_bf16 v[16:19], v[80:83], v[48:51], v[16:19]\n\t"
    "s_mov_b32 s76, 794697\n\t"
    "ds_read_b128 v[80:83], v245 offset:0\n\t"
    "ds_read_b32 v48, v224 offset:4612\n\t"
    "ds_read_b32 v49, v78 offset:4612\n\t"
    "ds_read_b32 v50, v225 offset:4612\n\t"
    "ds_read_b32 v51, v79 offset:4612\n\t"
    "v_xor_b32 v234, v234, v96\n\t"
    "v_cvt_pk_bf16_f32 v102, v8, v9\n\t"
    "v_cvt_pk_bf16_f32 v103, v10, v11\n\t"
    "s_waitcnt lgkmcnt(4)\n\t"
    "v_mfma_f32_16x16x32_bf16 v[20:23], v[80:83], v[56:59], v[20:23]\n\t"
    "s_mov_b32 s55, 90112\n\t"
    "buffer_load_dwordx4 v[204:207], v92, s[48:51], s55 offen offset:0 sc1\n\t"
    "ds_read_b128 v[80:83], v245 offset:1024\n\t"
    "ds_read_b128 v[56:59], v217 offset:9216\n\t"
    "ds_read_b128 v[8:11], v244 offset:128\n\t"
    "v_xor_b32 v233, v233, v96\n\t"
    "v_cvt_pk_bf16_f32 v96, v12, v13\n\t"
    "v_cvt_pk_bf16_f32 v97, v14, v15\n\t"
    "s_waitcnt lgkmcnt(2)\n\t"
    "v_mfma_f32_16x16x32_bf16 v[24:27], v[80:83], v[64:67], v[24:27]\n\t"
    "s_mov_b32 s55, 57344\n\t"
    "buffer_load_dwordx4 a[224:227], v92, s[48:51], s55 offen offset:0 sc1\n\t"
    "ds_read_b128 v[80:83], v245 offset:2048\n\t"
    "ds_read_b128 v[12:15], v244 offset:192\n\t"
    "ds_read_b128 v[64:67], v217 offset:9344\n\t"
    "s_waitcnt lgkmcnt(2)\n\t"
    "v_mfma_f32_16x16x32_bf16 v[28:31], v[80:83], v[40:43], v[28:31]\n\t"
    "ds_read_b128 v[80:83], v245 offset:3072\n\t"
    "v_add_u32_sdwa v89, v248, v246 dst_sel:DWORD dst_unused:UNUSED_PAD src0_sel:DWORD src1_sel:WORD_1\n\t"
    "v_add_u32_sdwa v90, v248, v247 dst_sel:DWORD dst_unused:UNUSED_PAD src0_sel:DWORD src1_sel:WORD_0\n\t"
    "s_waitcnt lgkmcnt(0)\n\t"
    "v_mfma_f32_16x16x32_bf16 v[32:35], v[80:83], v[48:51], v[32:35]\n\t"
    "s_mov_b32 s77, 0\n\t"
    "s_mov_b32 s55, 61440\n\t"
    "buffer_load_dwordx4 a[244:247], v92, s[48:51], s55 offen offset:1024 sc1\n\t"
    "ds_read_b128 v[80:83], v245 offset:0\n\t"
    "ds_read_b32 v40, v218 offset:9220\n\t"
    "ds_read_b32 v41, v72 offset:9220\n\t"
    "ds_read_b32 v42, v219 offset:9220\n\t"
    "ds_read_b32 v43, v73 offset:9220\n\t"
    "v_cvt_pk_bf16_f32 v99, v18, v19\n\t"
    "v_smfmac_f32_16x16x64_bf16 v[36:39], a[0:3], v[56:63], v176\n\t"
    "s_lshr_b64 s[80:81], s[76:77], s69\n\t"
    "ds_read_b128 v[48:51], v217 offset:9472\n\t"
    "v_cvt_pk_bf16_f32 v98, v16, v17\n\t"
    "v_smfmac_f32_16x16x64_bf16 v[4:7], a[4:7], v[56:63], v176 abid:1\n\t"
    "s_mov_b32 s55, 57344\n\t"
    "buffer_load_dwordx4 a[228:231], v92, s[48:51], s55 offen offset:1024 sc1\n\t"
    "ds_read_u16 v89, v89 offset:9216\n\t"
    "v_add_u32_sdwa v88, v248, v246 dst_sel:DWORD dst_unused:UNUSED_PAD src0_sel:DWORD src1_sel:WORD_0\n\t"
    "v_smfmac_f32_16x16x64_bf16 v[8:11], a[8:11], v[56:63], v177\n\t"
    "s_and_b32 s80, s80, 63\n\t"
    "ds_read_b128 v[52:55], v236 offset:9472\n\t"
    "v_cndmask_b32_dpp v16, v96, v100, vcc quad_perm:[0,0,2,2] row_mask:0xf bank_mask:0xf\n\t"
    "v_smfmac_f32_16x16x64_bf16 v[12:15], a[12:15], v[56:63], v177 abid:1\n\t"
    "s_mov_b32 s76, 794697\n\t"
    "ds_read_b128 v[56:59], v217 offset:9600\n\t"
    "v_cndmask_b32_dpp v18, v98, v102, vcc quad_perm:[0,0,2,2] row_mask:0xf bank_mask:0xf\n\t"
    "v_cndmask_b32_dpp v19, v99, v103, vcc quad_perm:[0,0,2,2] row_mask:0xf bank_mask:0xf\n\t"
    "v_add_u32_sdwa v91, v248, v247 dst_sel:DWORD dst_unused:UNUSED_PAD src0_sel:DWORD src1_sel:WORD_1\n\t"
    "v_smfmac_f32_16x16x64_bf16 v[36:39], a[16:19], v[64:71], v178\n\t"
    "s_mov_b32 s77, 0\n\t"
    "ds_read_u16 v88, v88 offset:9216\n\t"
    "ds_read_u16 v90, v90 offset:9216\n\t"
    "ds_read_u16 v91, v91 offset:9216\n\t"
    "v_smfmac_f32_16x16x64_bf16 v[4:7], a[20:23], v[64:71], v178 abid:1\n\t"
    "s_waitcnt lgkmcnt(7)\n\t"
    "s_mov_b32 s55, 61440\n\t"
    "buffer_load_dwordx4 a[248:251], v92, s[48:51], s55 offen offset:2048 sc1\n\t"
    "v_add_u32_sdwa v105, v248, v231 dst_sel:DWORD dst_unused:UNUSED_PAD src0_sel:DWORD src1_sel:WORD_1\n\t"
    "v_mfma_f32_16x16x32_bf16 v[36:39], v[80:83], v[40:43], v[36:39]\n\t"
    "ds_read_b128 v[60:63], v236 offset:9600\n\t"
    "v_cndmask_b32_dpp v17, v97, v101, vcc quad_perm:[0,0,2,2] row_mask:0xf bank_mask:0xf\n\t"
    "v_smfmac_f32_16x16x64_bf16 v[8:11], a[24:27], v[64:71], v179\n\t"
    "s_not_b64 vcc, s[18:19]\n\t"
    "ds_read_b128 v[80:83], v245 offset:1024\n\t"
    "v_cndmask_b32_dpp v96, v100, v96, vcc quad_perm:[1,1,3,3] row_mask:0xf bank_mask:0xf\n\t"
    "v_smfmac_f32_16x16x64_bf16 v[12:15], a[28:31], v[64:71], v179 abid:1\n\t"
    "s_waitcnt lgkmcnt(6)\n\t"
    "ds_read_b32 v40, v220 offset:9220\n\t"
    "ds_read_b32 v42, v221 offset:9220\n\t"
    "v_smfmac_f32_16x16x64_bf16 v[36:39], a[32:35], v[48:55], v180\n\t"
    "buffer_store_dwordx4 v[16:19], v241, s[44:47], s65 offen offset:0 nt\n\t"
    "ds_read_b32 v43, v75 offset:9220\n\t"
    "v_cndmask_b32_dpp v98, v102, v98, vcc quad_perm:[1,1,3,3] row_mask:0xf bank_mask:0xf\n\t"
    "v_cndmask_b32_dpp v99, v103, v99, vcc quad_perm:[1,1,3,3] row_mask:0xf bank_mask:0xf\n\t"
    "v_smfmac_f32_16x16x64_bf16 v[4:7], a[36:39], v[48:55], v180 abid:1\n\t"
    "s_waitcnt lgkmcnt(5)\n\t"
    "s_lshl_b32 s80, s80, 2\n\t"
    "ds_read_b32 v41, v74 offset:9220\n\t"
    "s_set_gpr_idx_on s80, gpr_idx(SRC2,DST)\n\t"
    "v_mfma_f32_16x16x32_bf16 v[0:3], v[250:253], v[88:91], v[0:3]\n\t"
    "s_set_gpr_idx_off\n\t"
    "ds_read_b128 v[68:71], v236 offset:9728\n\t"
    "v_smfmac_f32_16x16x64_bf16 v[8:11], a[40:43], v[48:55], v181\n\t"
    "s_lshr_b64 s[80:81], s[76:77], s70\n\t"
    "ds_read_b128 v[64:67], v217 offset:9728\n\t"
    "v_cvt_pk_bf16_f32 v102, v24, v25\n\t"
    "v_smfmac_f32_16x16x64_bf16 v[12:15], a[44:47], v[48:55], v181 abid:1\n\t"
    "s_waitcnt lgkmcnt(7)\n\t"
    "global_load_dwordx2 v[166:167], v164, s[60:61] sc1\n\t"
    "v_smfmac_f32_16x16x64_bf16 v[36:39], a[48:51], v[56:63], v182\n\t"
    "ds_read_b128 v[52:55], v236 offset:9856\n\t"
    "v_cndmask_b32_dpp v97, v101, v97, vcc quad_perm:[1,1,3,3] row_mask:0xf bank_mask:0xf\n\t"
    "v_smfmac_f32_16x16x64_bf16 v[4:7], a[52:55], v[56:63], v182 abid:1\n\t"
    "s_mov_b64 vcc, s[18:19]\n\t"
    "ds_read_b128 v[48:51], v217 offset:9856\n\t"
    "v_smfmac_f32_16x16x64_bf16 v[8:11], a[56:59], v[56:63], v183\n\t"
    "s_waitcnt lgkmcnt(4)\n\t"
    "s_and_b32 s80, s80, 63\n\t"
    "ds_read_b128 v[16:19], v244 offset:192\n\t"
    "v_cvt_pk_bf16_f32 v100, v20, v21\n\t"
    "v_mfma_f32_16x16x32_bf16 v[4:7], v[80:83], v[40:43], v[4:7]\n\t"
    "s_lshl_b32 s80, s80, 2\n\t"
    "buffer_store_dwordx4 v[96:99], v241, s[44:47], s65 offen offset:1152 nt\n\t"
    "ds_read_b32 v40, v222 offset:9220\n\t"
    "ds_read_b32 v41, v76 offset:9220\n\t"
    "v_cvt_pk_bf16_f32 v101, v22, v23\n\t"
    "v_cvt_pk_bf16_f32 v103, v26, v27\n\t"
    "v_smfmac_f32_16x16x64_bf16 v[12:15], a[60:63], v[56:63], v183 abid:1\n\t"
    "s_waitcnt lgkmcnt(5)\n\t"
    "ds_read_b128 v[60:63], v236 offset:9984\n\t"
    "v_cvt_pk_bf16_f32 v96, v28, v29\n\t"
    "v_cvt_pk_bf16_f32 v98, v32, v33\n\t"
    "v_smfmac_f32_16x16x64_bf16 v[36:39], a[64:67], v[64:71], v184\n\t"
    "ds_read_b128 v[56:59], v217 offset:9984\n\t"
    "v_cvt_pk_bf16_f32 v99, v34, v35\n\t"
    "v_smfmac_f32_16x16x64_bf16 v[4:7], a[68:71], v[64:71], v184 abid:1\n\t"
    "ds_read_b128 v[80:83], v245 offset:2048\n\t"
    "v_cvt_pk_bf16_f32 v97, v30, v31\n\t"
    "v_smfmac_f32_16x16x64_bf16 v[8:11], a[72:75], v[64:71], v185\n\t"
    "s_mov_b32 s55, 57344\n\t"
    "buffer_load_dwordx4 a[232:235], v92, s[48:51], s55 offen offset:2048 sc1\n\t"
    "ds_read_b32 v42, v223 offset:9220\n\t"
    "ds_read_b32 v43, v77 offset:9220\n\t"
    "v_cndmask_b32_dpp v35, v99, v103, vcc quad_perm:[0,0,2,2] row_mask:0xf bank_mask:0xf\n\t"
    "s_set_gpr_idx_on s80, gpr_idx(SRC2,DST)\n\t"
    "v_mfma_f32_16x16x32_bf16 v[0:3], v[252:255], v[88:91], v[0:3]\n\t"
    "s_set_gpr_idx_off\n\t"
    "s_cmp_lg_u32 s22, 0\n\t"
    "s_cbranch_scc1 .Lsl2_%=\n\t"
    ".Lslr2_%=:\n\t"
    "v_smfmac_f32_16x16x64_bf16 v[12:15], a[76:79], v[64:71], v185 abid:1\n\t"
    "s_waitcnt lgkmcnt(8)\n\t"
    "s_mov_b32 s77, 0\n\t"
    "ds_read_b128 v[64:67], v217 offset:10112\n\t"
    "v_cndmask_b32_dpp v32, v96, v100, vcc quad_perm:[0,0,2,2] row_mask:0xf bank_mask:0xf\n\t"
    "v_cndmask_b32_dpp v33, v97, v101, vcc quad_perm:[0,0,2,2] row_mask:0xf bank_mask:0xf\n\t"
    "v_smfmac_f32_16x16x64_bf16 v[36:39], a[80:83], v[48:55], v186\n\t"
    "s_mov_b32 s76, 1077637\n\t"
    "ds_read_b128 v[20:23], v244 offset:0\n\t"
    "v_cndmask_b32_dpp v34, v98, v102, vcc quad_perm:[0,0,2,2] row_mask:0xf bank_mask:0xf\n\t"
    "v_smfmac_f32_16x16x64_bf16 v[4:7], a[84:87], v[48:55], v186 abid:1\n\t"
    "s_not_b64 vcc, s[18:19]\n\t"
    "buffer_store_dwordx4 v[32:35], v241, s[44:47], s66 offen offset:0 nt\n\t"
    "ds_read_b128 v[24:27], v244 offset:64\n\t"
    "v_cndmask_b32_dpp v96, v100, v96, vcc quad_perm:[1,1,3,3] row_mask:0xf bank_mask:0xf\n\t"
    "v_add_u32_sdwa v89, v248, v246 dst_sel:DWORD dst_unused:UNUSED_PAD src0_sel:DWORD src1_sel:WORD_1\n\t"
    "v_smfmac_f32_16x16x64_bf16 v[8:11], a[88:91], v[48:55], v187\n\t"
    "ds_read_b128 v[68:71], v236 offset:10112\n\t"
    "v_cndmask_b32_dpp v99, v103, v99, vcc quad_perm:[1,1,3,3] row_mask:0xf bank_mask:0xf\n\t"
    "v_smfmac_f32_16x16x64_bf16 v[12:15], a[92:95], v[48:55], v187 abid:1\n\t"
    "s_waitcnt lgkmcnt(4)\n\t"
    "ds_read_b128 v[48:51], v217 offset:10240\n\t"
    "v_cndmask_b32_dpp v98, v102, v98, vcc quad_perm:[1,1,3,3] row_mask:0xf bank_mask:0xf\n\t"
    "v_mfma_f32_16x16x32_bf16 v[8:11], v[80:83], v[40:43], v[8:11]\n\t"
    "ds_read_b128 v[52:55], v236 offset:10240\n\t"
    "ds_read_b128 v[80:83], v245 offset:3072\n\t"
    "v_smfmac_f32_16x16x64_bf16 v[36:39], a[96:99], v[56:63], v188\n\t"
    "s_mov_b32 s55, 61440\n\t"
    "buffer_load_dwordx4 a[252:255], v92, s[48:51], s55 offen offset:3072 sc1\n\t"
    "v_smfmac_f32_16x16x64_bf16 v[4:7], a[100:103], v[56:63], v188 abid:1\n\t"
    "ds_read_b32 v41, v78 offset:9220\n\t"
    "ds_read_b32 v43, v79 offset:9220\n\t"
    "v_smfmac_f32_16x16x64_bf16 v[8:11], a[104:107], v[56:63], v189\n\t"
    "ds_read_b32 v40, v224 offset:9220\n\t"
    "ds_read_b32 v42, v225 offset:9220\n\t"
    "v_add_u32_sdwa v87, v248, v229 dst_sel:DWORD dst_unused:UNUSED_PAD src0_sel:DWORD src1_sel:WORD_1\n\t"
    "v_smfmac_f32_16x16x64_bf16 v[12:15], a[108:111], v[56:63], v189 abid:1\n\t"
    "s_waitcnt lgkmcnt(7)\n\t"
    "ds_read_b128 v[56:59], v217 offset:13824\n\t"
    "v_cndmask_b32_dpp v97, v101, v97, vcc quad_perm:[1,1,3,3] row_mask:0xf bank_mask:0xf\n\t"
    "v_smfmac_f32_16x16x64_bf16 v[36:39], a[112:115], v[64:71], v190\n\t"
    "buffer_store_dwordx4 v[96:99], v241, s[44:47], s66 offen offset:1152 nt\n\t"
    "ds_read_b128 v[28:31], v244 offset:128\n\t"
    "v_add_u32_sdwa v91, v248, v247 dst_sel:DWORD dst_unused:UNUSED_PAD src0_sel:DWORD src1_sel:WORD_1\n\t"
    "v_smfmac_f32_16x16x64_bf16 v[4:7], a[116:119], v[64:71], v190 abid:1\n\t"
    "s_lshr_b64 s[80:81], s[76:77], s69\n\t"
    "ds_read_b128 v[60:63], v236 offset:13824\n\t"
    "v_add_u32_sdwa v88, v248, v246 dst_sel:DWORD dst_unused:UNUSED_PAD src0_sel:DWORD src1_sel:WORD_0\n\t"
    "v_smfmac_f32_16x16x64_bf16 v[8:11], a[120:123], v[64:71], v191\n\t"
    "s_and_b32 s80, s80, 63\n\t"
    "ds_read_u16 v91, v91 offset:13824\n\t"
    "v_add_u32_sdwa v90, v248, v247 dst_sel:DWORD dst_unused:UNUSED_PAD src0_sel:DWORD src1_sel:WORD_0\n\t"
    "v_smfmac_f32_16x16x64_bf16 v[12:15], a[124:127], v[64:71], v191 abid:1\n\t"
    "s_waitcnt lgkmcnt(9)\n\t"
    "ds_read_b128 v[64:67], v217 offset:13952\n\t"
    "ds_read_b128 v[68:71], v236 offset:13952\n\t"
    "v_smfmac_f32_16x16x64_bf16 v[36:39], a[128:131], v[48:55], v192\n\t"
    "s_waitcnt lgkmcnt(6)\n\t"
    "s_mov_b64 vcc, s[18:19]\n\t"
    "s_mov_b32 s55, 57344\n\t"
    "buffer_load_dwordx4 a[236:239], v92, s[48:51], s55 offen offset:3072 sc1\n\t"
    "v_mfma_f32_16x16x32_bf16 v[12:15], v[80:83], v[40:43], v[12:15]\n\t"
    "ds_read_b32 v41, v72 offset:13828\n\t"
    "ds_read_b32 v42, v219 offset:13828\n\t"
    "v_smfmac_f32_16x16x64_bf16 v[4:7], a[132:135], v[48:55], v192 abid:1\n\t"
    "ds_read_b128 v[80:83], v245 offset:0\n\t"
    "v_smfmac_f32_16x16x64_bf16 v[8:11], a[136:139], v[48:55], v193\n\t"
    "s_mov_b32 s65, 14155776\n\t"
    "ds_read_b32 v40, v218 offset:13828\n\t"
    "ds_read_u16 v88, v88 offset:13824\n\t"
    "v_smfmac_f32_16x16x64_bf16 v[12:15], a[140:143], v[48:55], v193 abid:1\n\t"
    "s_waitcnt lgkmcnt(8)\n\t"
    "s_mov_b32 s55, 90112\n\t"
    "buffer_load_dwordx4 v[208:211], v92, s[48:51], s55 offen offset:1024 sc1\n\t"
    "ds_read_b32 v43, v73 offset:13828\n\t"
    "ds_read_u16 v89, v89 offset:13824\n\t"
    "v_smfmac_f32_16x16x64_bf16 v[20:23], a[0:3], v[56:63], v176\n\t"
    "s_mov_b32 s76, 1077637\n\t"
    "ds_read_b128 v[52:55], v236 offset:14080\n\t"
    "v_cvt_pk_bf16_f32 v103, v6, v7\n\t"
    "v_smfmac_f32_16x16x64_bf16 v[24:27], a[4:7], v[56:63], v176 abid:1\n\t"
    "s_lshl_b32 s80, s80, 2\n\t"
    "ds_read_b128 v[48:51], v217 offset:14080\n\t"
    "v_cvt_pk_bf16_f32 v100, v36, v37\n\t"
    "v_cvt_pk_bf16_f32 v102, v4, v5\n\t"
    "v_cvt_pk_bf16_f32 v97, v10, v11\n\t"
    "v_smfmac_f32_16x16x64_bf16 v[28:31], a[8:11], v[56:63], v177\n\t"
    "ds_read_b128 v[32:35], v244 offset:0\n\t"
    "v_cvt_pk_bf16_f32 v101, v38, v39\n\t"
    "v_cvt_pk_bf16_f32 v96, v8, v9\n\t"
    "v_smfmac_f32_16x16x64_bf16 v[16:19], a[12:15], v[56:63], v177 abid:1\n\t"
    "s_waitcnt lgkmcnt(10)\n\t"
    "s_mov_b32 s55, 65536\n\t"
    "buffer_load_dwordx4 v[108:111], v92, s[48:51], s55 offen offset:0 sc1\n\t"
    "ds_read_b128 v[60:63], v236 offset:14208\n\t"
    "v_smfmac_f32_16x16x64_bf16 v[20:23], a[16:19], v[64:71], v178\n\t"
    "ds_read_u16 v90, v90 offset:13824\n\t"
    "v_cvt_pk_bf16_f32 v98, v12, v13\n\t"
    "v_smfmac_f32_16x16x64_bf16 v[24:27], a[20:23], v[64:71], v178 abid:1\n\t"
    "s_waitcnt lgkmcnt(6)\n\t"
    "ds_read_b128 v[56:59], v217 offset:14208\n\t"
    "v_cvt_pk_bf16_f32 v99, v14, v15\n\t"
    "v_mfma_f32_16x16x32_bf16 v[20:23], v[80:83], v[40:43], v[20:23]\n\t"
    "s_mov_b32 s77, 0\n\t"
    "ds_read_b32 v41, v74 offset:13828\n\t"
    "v_cndmask_b32_dpp v13, v97, v101, vcc quad_perm:[0,0,2,2] row_mask:0xf bank_mask:0xf\n\t"
    "v_smfmac_f32_16x16x64_bf16 v[28:31], a[24:27], v[64:71], v179\n\t"
    "s_mov_b32 s55, 61440\n\t"
    "buffer_load_dwordx4 a[240:243], v92, s[48:51], s55 offen offset:0 sc1\n\t"
    "ds_read_b32 v43, v75 offset:13828\n\t"
    "v_smfmac_f32_16x16x64_bf16 v[16:19], a[28:31], v[64:71], v179 abid:1\n\t"
    "s_waitcnt lgkmcnt(6)\n\t"
    "ds_read_b32 v42, v221 offset:13828\n\t"
    "v_cndmask_b32_dpp v15, v99, v103, vcc quad_perm:[0,0,2,2] row_mask:0xf bank_mask:0xf\n\t"
    "v_smfmac_f32_16x16x64_bf16 v[20:23], a[32:35], v[48:55], v180\n\t"
    "ds_read_b128 v[80:83], v245 offset:1024\n\t"
    "v_cndmask_b32_dpp v12, v96, v100, vcc quad_perm:[0,0,2,2] row_mask:0xf bank_mask:0xf\n\t"
    "v_cndmask_b32_dpp v14, v98, v102, vcc quad_perm:[0,0,2,2] row_mask:0xf bank_mask:0xf\n\t"
    "v_smfmac_f32_16x16x64_bf16 v[24:27], a[36:39], v[48:55], v180 abid:1\n\t"
    "s_waitcnt lgkmcnt(5)\n\t"
    "s_not_b64 vcc, s[18:19]\n\t"
    "ds_read_b32 v40, v220 offset:13828\n\t"
    "s_set_gpr_idx_on s80, gpr_idx(SRC2,DST)\n\t"
    "v_mfma_f32_16x16x32_bf16 v[0:3], v[250:253], v[88:91], v[0:3]\n\t"
    "s_set_gpr_idx_off\n\t"
    "buffer_store_dwordx4 v[12:15], v241, s[44:47], s67 offen offset:0 nt\n\t"
    "ds_read_b128 v[68:71], v236 offset:14336\n\t"
    "v_cndmask_b32_dpp v97, v101, v97, vcc quad_perm:[1,1,3,3] row_mask:0xf bank_mask:0xf\n\t"
    "v_cndmask_b32_dpp v98, v102, v98, vcc quad_perm:[1,1,3,3] row_mask:0xf bank_mask:0xf\n\t"
    "s_waitcnt vmcnt(10)\n\t"
    "s_mov_b32 s58, 0x40000\n\t"
    ".Lprc_%=:\n\t"
    "v_bitop3_b32 v166, v166, v164, s30 bitop3:0x96\n\t"
    "v_xor_b32 v167, s31, v167\n\t"
    "v_and_or_b32 v166, v167, -2, v166\n\t"
    "v_cmp_ne_u32 s[96:97], 0, v166\n\t"
    "s_nop 1\n\t"
    "s_cmp_lg_u64 s[96:97], 0\n\t"
    "s_cbranch_scc1 .Lprl_%=\n\t"
    ".Lprok_%=:\n\t"
    "v_cmp_eq_u32 s[96:97], 1, v167\n\t"
    "s_nop 1\n\t"
    "s_and_b32 s58, s97, 0x7fffff\n\t"
    "s_bcnt1_i32_b32 s58, s58\n\t"
    "s_cmp_ge_u32 s58, 12\n\t"
    "s_cselect_b32 s64, 0, 0x1fffff\n\t"
    "s_and_b32 s58, s97, 0xff800000\n\t"
    "s_bcnt1_i32_b32 s58, s58\n\t"
    "s_cmp_ge_u32 s58, 5\n\t"
    "s_cselect_b32 s58, 0, 0x1e00000\n\t"
    "s_or_b32 s59, s64, s58\n\t"
    "s_and_b32 s58, s96, 0x7fffff\n\t"
    "s_bcnt1_i32_b32 s58, s58\n\t"
    "s_cmp_ge_u32 s58, 12\n\t"
    "s_cselect_b32 s64, 0, 0x1fffff\n\t"
    "s_and_b32 s58, s96, 0xff800000\n\t"
    "s_bcnt1_i32_b32 s58, s58\n\t"
    "s_cmp_ge_u32 s58, 5\n\t"
    "s_cselect_b32 s58, 0, 0x1e00000\n\t"
    "s_or_b32 s97, s64, s58\n\t"
    "s_xor_b32 s58, s97, s59\n\t"
    "s_bitcmp1_b32 s58, 0\n\t"
    "s_cselect_b32 s99, 0, 6\n\t"
    "s_cbranch_scc0 .Lpal_%=\n\t"
    ".Lpalr_%=:\n\t"
    "v_smfmac_f32_16x16x64_bf16 v[28:31], a[40:43], v[48:55], v181\n\t"
    "ds_read_b128 v[64:67], v217 offset:14336\n\t"
    "v_cndmask_b32_dpp v96, v100, v96, vcc quad_perm:[1,1,3,3] row_mask:0xf bank_mask:0xf\n\t"
    "v_smfmac_f32_16x16x64_bf16 v[16:19], a[44:47], v[48:55], v181 abid:1\n\t"
    "s_waitcnt lgkmcnt(7)\n\t"
    "s_lshr_b64 s[80:81], s[76:77], s70\n\t"
    "ds_read_b128 v[52:55], v236 offset:14464\n\t"
    "v_smfmac_f32_16x16x64_bf16 v[20:23], a[48:51], v[56:63], v182\n\t"
    "ds_read_b128 v[48:51], v217 offset:14464\n\t"
    "v_cndmask_b32_dpp v99, v103, v99, vcc quad_perm:[1,1,3,3] row_mask:0xf bank_mask:0xf\n\t"
    "v_smfmac_f32_16x16x64_bf16 v[24:27], a[52:55], v[56:63], v182 abid:1\n\t"
    "s_mov_b32 s55, 65536\n\t"
    "buffer_load_dwordx4 v[112:115], v92, s[48:51], s55 offen offset:1024 sc1\n\t"
    "v_smfmac_f32_16x16x64_bf16 v[28:31], a[56:59], v[56:63], v183\n\t"
    "s_waitcnt lgkmcnt(4)\n\t"
    "ds_read_b128 v[4:7], v244 offset:128\n\t"
    "v_add_u32_sdwa v86, v248, v228 dst_sel:DWORD dst_unused:UNUSED_PAD src0_sel:DWORD src1_sel:WORD_1\n\t"
    "v_mfma_f32_16x16x32_bf16 v[24:27], v[80:83], v[40:43], v[24:27]\n\t"
    "s_and_b32 s80, s80, 63\n\t"
    "ds_read_b32 v42, v223 offset:13828\n\t"
    "ds_read_b32 v43, v77 offset:13828\n\t"
    "v_smfmac_f32_16x16x64_bf16 v[16:19], a[60:63], v[56:63], v183 abid:1\n\t"
    "s_waitcnt lgkmcnt(5)\n\t"
    "ds_read_b128 v[56:59], v217 offset:14592\n\t"
    "v_smfmac_f32_16x16x64_bf16 v[20:23], a[64:67], v[64:71], v184\n\t"
    "buffer_store_dwordx4 v[96:99], v241, s[44:47], s67 offen offset:1152 nt\n\t"
    "ds_read_b128 v[80:83], v245 offset:2048\n\t"
    "v_smfmac_f32_16x16x64_bf16 v[24:27], a[68:71], v[64:71], v184 abid:1\n\t"
    "ds_read_b32 v40, v222 offset:13828\n\t"
    "ds_read_b32 v41, v76 offset:13828\n\t"
    "v_smfmac_f32_16x16x64_bf16 v[28:31], a[72:75], v[64:71], v185\n\t"
    "s_lshl_b32 s80, s80, 2\n\t"
    "ds_read_b128 v[36:39], v244 offset:64\n\t"
    "s_set_gpr_idx_on s80, gpr_idx(SRC2,DST)\n\t"
    "v_mfma_f32_16x16x32_bf16 v[0:3], v[252:255], v[88:91], v[0:3]\n\t"
    "s_set_gpr_idx_off\n\t"
    "ds_read_b128 v[60:63], v236 offset:14592\n\t"
    "s_cmp_lg_u32 s22, 0\n\t"
    "s_cbranch_scc1 .Lsl3_%=\n\t"
    ".Lslr3_%=:\n\t"
    "v_smfmac_f32_16x16x64_bf16 v[16:19], a[76:79], v[64:71], v185 abid:1\n\t"
    "s_waitcnt lgkmcnt(9)\n\t"
    "buffer_load_dwordx4 v[116:119], v92, s[48:51], s55 offen offset:2048 sc1\n\t"
    "v_smfmac_f32_16x16x64_bf16 v[20:23], a[80:83], v[48:55], v186\n\t"
    "ds_read_b128 v[68:71], v236 offset:14720\n\t"
    "ds_read_b128 v[12:15], v244 offset:256\n\t"
    "v_smfmac_f32_16x16x64_bf16 v[24:27], a[84:87], v[48:55], v186 abid:1\n\t"
    "s_mov_b32 s77, 0\n\t"
    "ds_read_b128 v[64:67], v217 offset:14720\n\t"
    "v_add_u32_sdwa v88, v248, v246 dst_sel:DWORD dst_unused:UNUSED_PAD src0_sel:DWORD src1_sel:WORD_0\n\t"
    "v_smfmac_f32_16x16x64_bf16 v[28:31], a[88:91], v[48:55], v187\n\t"
    "s_mov_b32 s76, 528968\n\t"
    "ds_read_b128 v[8:11], v244 offset:192\n\t"
    "v_add_u32_sdwa v91, v248, v247 dst_sel:DWORD dst_unused:UNUSED_PAD src0_sel:DWORD src1_sel:WORD_1\n\t"
    "v_smfmac_f32_16x16x64_bf16 v[16:19], a[92:95], v[48:55], v187 abid:1\n\t"
    "s_waitcnt lgkmcnt(6)\n\t"
    "s_mov_b32 s55, 69632\n\t"
    "buffer_load_dwordx4 v[136:139], v92, s[48:51], s55 offen offset:3072 sc1\n\t"
    "v_mfma_f32_16x16x32_bf16 v[28:31], v[80:83], v[40:43], v[28:31]\n\t"
    "s_waitcnt lgkmcnt(4)\n\t"
    "s_barrier\n\t"
    "ds_read_b128 v[48:51], v217 offset:14848\n\t"
    "ds_read_b32 v41, v78 offset:13828\n\t"
    "ds_read_b32 v43, v79 offset:13828\n\t"
    "v_smfmac_f32_16x16x64_bf16 v[20:23], a[96:99], v[56:63], v188\n\t"
    "ds_read_b128 v[80:83], v245 offset:3072\n\t"
    "ds_read_b32 v42, v225 offset:13828\n\t"
    "v_smfmac_f32_16x16x64_bf16 v[24:27], a[100:103], v[56:63], v188 abid:1\n\t"
    "ds_read_b128 v[52:55], v236 offset:14848\n\t"
    "v_add_u32_sdwa v89, v248, v246 dst_sel:DWORD dst_unused:UNUSED_PAD src0_sel:DWORD src1_sel:WORD_1\n\t"
    "v_smfmac_f32_16x16x64_bf16 v[28:31], a[104:107], v[56:63], v189\n\t"
    "s_mov_b32 s55, 90112\n\t"
    "buffer_load_dwordx4 v[212:215], v92, s[48:51], s55 offen offset:2048 sc1\n\t"
    "ds_read_b32 v40, v224 offset:13828\n\t"
    "v_smfmac_f32_16x16x64_bf16 v[16:19], a[108:111], v[56:63], v189 abid:1\n\t"
    "s_waitcnt lgkmcnt(8)\n\t"
    "ds_read_b128 v[56:59], v217 offset:18432\n\t"
    "v_add_u32_sdwa v84, v248, v226 dst_sel:DWORD dst_unused:UNUSED_PAD src0_sel:DWORD src1_sel:WORD_1\n\t"
    "v_smfmac_f32_16x16x64_bf16 v[20:23], a[112:115], v[64:71], v190\n\t"
    "s_bitcmp1_b32 s54, 5\n\t"
    "s_mov_b32 s68, 23592960\n\t"
    "s_cmov_b32 s68, 23666688\n\t"
    "ds_read_u16 v89, v89 offset:18432\n\t"
    "v_smfmac_f32_16x16x64_bf16 v[24:27], a[116:119], v[64:71], v190 abid:1\n\t"
    "s_lshr_b64 s[80:81], s[76:77], s69\n\t"
    "ds_read_b128 v[60:63], v236 offset:18432\n\t"
    "v_smfmac_f32_16x16x64_bf16 v[28:31], a[120:123], v[64:71], v191\n\t"
    "s_mov_b32 s55, 77824\n\t"
    "buffer_load_dwordx4 v[156:159], v92, s[48:51], s55 offen offset:0 sc1\n\t"
    "ds_read_u16 v88, v88 offset:18432\n\t"
    "v_smfmac_f32_16x16x64_bf16 v[16:19], a[124:127], v[64:71], v191 abid:1\n\t"
    "s_waitcnt lgkmcnt(5)\n\t"
    "ds_read_b128 v[68:71], v236 offset:18560\n\t"
    "v_smfmac_f32_16x16x64_bf16 v[20:23], a[128:131], v[48:55], v192\n\t"
    "s_waitcnt lgkmcnt(5)\n\t"
    "ds_read_b128 v[64:67], v217 offset:18560\n\t"
    "v_mfma_f32_16x16x32_bf16 v[16:19], v[80:83], v[40:43], v[16:19]\n\t"
    "s_and_b32 s80, s80, 63\n\t"
    "ds_read_b32 v41, v72 offset:18436\n\t"
    "ds_read_b32 v43, v73 offset:18436\n\t"
    "v_add_u32_sdwa v90, v248, v247 dst_sel:DWORD dst_unused:UNUSED_PAD src0_sel:DWORD src1_sel:WORD_0\n\t"
    "v_smfmac_f32_16x16x64_bf16 v[24:27], a[132:135], v[48:55], v192 abid:1\n\t"
    "s_mov_b32 s55, 65536\n\t"
    "buffer_load_dwordx4 v[120:123], v92, s[48:51], s55 offen offset:3072 sc1\n\t"
    "ds_read_b32 v40, v218 offset:18436\n\t"
    "v_smfmac_f32_16x16x64_bf16 v[28:31], a[136:139], v[48:55], v193\n\t"
    "s_lshl_b32 s80, s80, 2\n\t"
    "ds_read_b32 v42, v219 offset:18436\n\t"
    "ds_read_u16 v91, v91 offset:18432\n\t"
    "v_smfmac_f32_16x16x64_bf16 v[16:19], a[140:143], v[48:55], v193 abid:1\n\t"
    "s_waitcnt lgkmcnt(8)\n\t"
    "s_mov_b32 s77, 0\n\t"
    "ds_read_b128 v[80:83], v245 offset:0\n\t"
    "v_cvt_pk_bf16_f32 v100, v20, v21\n\t"
    "v_smfmac_f32_16x16x64_bf16 v[32:35], a[0:3], v[56:63], v176\n\t"
    "ds_read_b128 v[48:51], v217 offset:18688\n\t"
    "v_cvt_pk_bf16_f32 v103, v26, v27\n\t"
    "v_smfmac_f32_16x16x64_bf16 v[36:39], a[4:7], v[56:63], v176 abid:1\n\t"
    "s_mov_b64 vcc, s[18:19]\n\t"
    "s_mov_b32 s55, 73728\n\t"
    "buffer_load_dwordx4 v[140:143], v92, s[48:51], s55 offen offset:0 sc1\n\t"
    "v_smfmac_f32_16x16x64_bf16 v[4:7], a[8:11], v[56:63], v177\n\t"
    "ds_read_b128 v[52:55], v236 offset:18688\n\t"
    "v_cvt_pk_bf16_f32 v102, v24, v25\n\t"
    "v_cvt_pk_bf16_f32 v96, v28, v29\n\t"
    "v_smfmac_f32_16x16x64_bf16 v[8:11], a[12:15], v[56:63], v177 abid:1\n\t"
    "s_waitcnt lgkmcnt(8)\n\t"
    "ds_read_b128 v[56:59], v217 offset:18816\n\t"
    "ds_read_u16 v90, v90 offset:18432\n\t"
    "v_smfmac_f32_16x16x64_bf16 v[32:35], a[16:19], v[64:71], v178\n\t"
    "s_mov_b32 s76, 528968\n\t"
    "ds_read_b128 v[60:63], v236 offset:18816\n\t"
    "v_cvt_pk_bf16_f32 v99, v18, v19\n\t"
    "v_smfmac_f32_16x16x64_bf16 v[36:39], a[20:23], v[64:71], v178 abid:1\n\t"
    "s_waitcnt lgkmcnt(5)\n\t"
    "s_mov_b32 s55, 77824\n\t"
    "buffer_load_dwordx4 v[160:163], v92, s[48:51], s55 offen offset:1024 sc1\n\t"
    "v_cvt_pk_bf16_f32 v98, v16, v17\n\t"
    "v_mfma_f32_16x16x32_bf16 v[32:35], v[80:83], v[40:43], v[32:35]\n\t"
    "ds_read_b32 v43, v75 offset:18436\n\t"
    "v_cvt_pk_bf16_f32 v97, v30, v31\n\t"
    "v_smfmac_f32_16x16x64_bf16 v[4:7], a[24:27], v[64:71], v179\n\t"
    "ds_read_b32 v40, v220 offset:18436\n\t"
    "v_cndmask_b32_dpp v18, v98, v102, vcc quad_perm:[0,0,2,2] row_mask:0xf bank_mask:0xf\n\t"
    "v_smfmac_f32_16x16x64_bf16 v[8:11], a[28:31], v[64:71], v179 abid:1\n\t"
    "s_waitcnt lgkmcnt(5)\n\t"
    "ds_read_b128 v[80:83], v245 offset:1024\n\t"
    "v_cvt_pk_bf16_f32 v101, v22, v23\n\t"
    "v_smfmac_f32_16x16x64_bf16 v[32:35], a[32:35], v[48:55], v180\n\t"
    "s_mov_b32 s55, 69632\n\t"
    "buffer_load_dwordx4 v[124:127], v92, s[48:51], s55 offen offset:0 sc1\n\t"
    "ds_read_b32 v42, v221 offset:18436\n\t"
    "v_smfmac_f32_16x16x64_bf16 v[36:39], a[36:39], v[48:55], v180 abid:1\n\t"
    "s_waitcnt lgkmcnt(5)\n\t"
    "ds_read_b32 v41, v74 offset:18436\n\t"
    "s_set_gpr_idx_on s80, gpr_idx(SRC2,DST)\n\t"
    "v_mfma_f32_16x16x32_bf16 v[0:3], v[250:253], v[88:91], v[0:3]\n\t"
    "s_set_gpr_idx_off\n\t"
    "ds_read_b128 v[68:71], v236 offset:18944\n\t"
    "v_smfmac_f32_16x16x64_bf16 v[4:7], a[40:43], v[48:55], v181\n\t"
    "ds_read_b128 v[64:67], v217 offset:18944\n\t"
    "v_cndmask_b32_dpp v16, v96, v100, vcc quad_perm:[0,0,2,2] row_mask:0xf bank_mask:0xf\n\t"
    "v_cndmask_b32_dpp v17, v97, v101, vcc quad_perm:[0,0,2,2] row_mask:0xf bank_mask:0xf\n\t"
    "v_cndmask_b32_dpp v19, v99, v103, vcc quad_perm:[0,0,2,2] row_mask:0xf bank_mask:0xf\n\t"
    "v_smfmac_f32_16x16x64_bf16 v[8:11], a[44:47], v[48:55], v181 abid:1\n\t"
    "s_waitcnt lgkmcnt(7)\n\t"
    "s_lshr_b64 s[80:81], s[76:77], s70\n\t"
    "buffer_store_dwordx4 v[16:19], v241, s[44:47], s65 offen offset:0 nt\n\t"
    "ds_read_b128 v[48:51], v217 offset:19072\n\t"
    "v_add_u32_sdwa v104, v248, v230 dst_sel:DWORD dst_unused:UNUSED_PAD src0_sel:DWORD src1_sel:WORD_1\n\t"
    "v_add_u32_sdwa v231, v248, v231 dst_sel:DWORD dst_unused:UNUSED_PAD src0_sel:DWORD src1_sel:WORD_0\n\t"
    "v_add_u32_sdwa v107, v248, v233 dst_sel:DWORD dst_unused:UNUSED_PAD src0_sel:DWORD src1_sel:WORD_1\n\t"
    "v_smfmac_f32_16x16x64_bf16 v[32:35], a[48:51], v[56:63], v182\n\t"
    "s_not_b64 vcc, s[18:19]\n\t"
    "ds_read_b128 v[52:55], v236 offset:19072\n\t"
    "v_cndmask_b32_dpp v99, v103, v99, vcc quad_perm:[1,1,3,3] row_mask:0xf bank_mask:0xf\n\t"
    "v_smfmac_f32_16x16x64_bf16 v[36:39], a[52:55], v[56:63], v182 abid:1\n\t"
    "s_and_b32 s80, s80, 63\n\t"
    "ds_read_b128 v[24:27], v244 offset:384\n\t"
    "v_cndmask_b32_dpp v97, v101, v97, vcc quad_perm:[1,1,3,3] row_mask:0xf bank_mask:0xf\n\t"
    "v_smfmac_f32_16x16x64_bf16 v[4:7], a[56:59], v[56:63], v183\n\t"
    "s_waitcnt lgkmcnt(5)\n\t"
    "v_mfma_f32_16x16x32_bf16 v[36:39], v[80:83], v[40:43], v[36:39]\n\t"
    "s_lshl_b32 s80, s80, 2\n\t"
    "s_mov_b32 s55, 73728\n\t"
    "buffer_load_dwordx4 v[144:147], v92, s[48:51], s55 offen offset:1024 sc1\n\t"
    "ds_read_b32 v41, v76 offset:18436\n\t"
    "ds_read_b32 v43, v77 offset:18436\n\t"
    "v_smfmac_f32_16x16x64_bf16 v[8:11], a[60:63], v[56:63], v183 abid:1\n\t"
    "s_waitcnt lgkmcnt(5)\n\t"
    "ds_read_b128 v[60:63], v236 offset:19200\n\t"
    "v_add_u32_sdwa v229, v248, v229 dst_sel:DWORD dst_unused:UNUSED_PAD src0_sel:DWORD src1_sel:WORD_0\n\t"
    "v_smfmac_f32_16x16x64_bf16 v[32:35], a[64:67], v[64:71], v184\n\t"
    "ds_read_b32 v40, v222 offset:18436\n\t"
    "v_cndmask_b32_dpp v98, v102, v98, vcc quad_perm:[1,1,3,3] row_mask:0xf bank_mask:0xf\n\t"
    "v_smfmac_f32_16x16x64_bf16 v[36:39], a[68:71], v[64:71], v184 abid:1\n\t"
    "s_mov_b32 s67, 0\n\t"
    "ds_read_b32 v42, v223 offset:18436\n\t"
    "v_cndmask_b32_dpp v96, v100, v96, vcc quad_perm:[1,1,3,3] row_mask:0xf bank_mask:0xf\n\t"
    "v_smfmac_f32_16x16x64_bf16 v[4:7], a[72:75], v[64:71], v185\n\t"
    "buffer_store_dwordx4 v[96:99], v241, s[44:47], s65 offen offset:1152 nt\n\t"
    "ds_read_b128 v[80:83], v245 offset:2048\n\t"
    "v_add_u32_sdwa v230, v248, v230 dst_sel:DWORD dst_unused:UNUSED_PAD src0_sel:DWORD src1_sel:WORD_0\n\t"
    "s_set_gpr_idx_on s80, gpr_idx(SRC2,DST)\n\t"
    "v_mfma_f32_16x16x32_bf16 v[0:3], v[252:255], v[88:91], v[0:3]\n\t"
    "s_set_gpr_idx_off\n\t"
    "ds_read_b128 v[56:59], v217 offset:19200\n\t"
    "s_cmp_lg_u32 s22, 0\n\t"
    "s_cbranch_scc1 .Lsl4_%=\n\t"
    ".Lslr4_%=:\n\t"
    "v_smfmac_f32_16x16x64_bf16 v[8:11], a[76:79], v[64:71], v185 abid:1\n\t"
    "s_waitcnt lgkmcnt(8)\n\t"
    "ds_read_b128 v[64:67], v217 offset:19328\n\t"
    "v_smfmac_f32_16x16x64_bf16 v[32:35], a[80:83], v[48:55], v186\n\t"
    "s_mov_b32 s77, 269409\n\t"
    "ds_read_b128 v[16:19], v244 offset:512\n\t"
    "v_add_u32_sdwa v90, v248, v247 dst_sel:DWORD dst_unused:UNUSED_PAD src0_sel:DWORD src1_sel:WORD_0\n\t"
    "v_smfmac_f32_16x16x64_bf16 v[36:39], a[84:87], v[48:55], v186 abid:1\n\t"
    "s_mov_b32 s55, 77824\n\t"
    "buffer_load_dwordx4 v[164:167], v92, s[48:51], s55 offen offset:2048 sc1\n\t"
    "ds_read_b128 v[28:31], v244 offset:448\n\t"
    "v_smfmac_f32_16x16x64_bf16 v[4:7], a[88:91], v[48:55], v187\n\t"
    "s_mov_b64 vcc, s[18:19]\n\t"
    "ds_read_b128 v[68:71], v236 offset:19328\n\t"
    "v_add_u32_sdwa v89, v248, v246 dst_sel:DWORD dst_unused:UNUSED_PAD src0_sel:DWORD src1_sel:WORD_1\n\t"
    "v_smfmac_f32_16x16x64_bf16 v[8:11], a[92:95], v[48:55], v187 abid:1\n\t"
    "s_waitcnt lgkmcnt(5)\n\t"
    "s_mov_b32 s76, 1124073472\n\t"
    "ds_read_b128 v[52:55], v236 offset:19456\n\t"
    "v_mfma_f32_16x16x32_bf16 v[4:7], v[80:83], v[40:43], v[4:7]\n\t"
    "s_waitcnt lgkmcnt(5)\n\t"
    "ds_read_b128 v[48:51], v217 offset:19456\n\t"
    "ds_read_b128 v[80:83], v245 offset:3072\n\t"
    "v_smfmac_f32_16x16x64_bf16 v[32:35], a[96:99], v[56:63], v188\n\t"
    "s_mov_b32 s55, 69632\n\t"
    "buffer_load_dwordx4 v[128:131], v92, s[48:51], s55 offen offset:1024 sc1\n\t"
    "v_smfmac_f32_16x16x64_bf16 v[36:39], a[100:103], v[56:63], v188 abid:1\n\t"
    "ds_read_b32 v41, v78 offset:18436\n\t"
    "ds_read_b32 v42, v225 offset:18436\n\t"
    "v_smfmac_f32_16x16x64_bf16 v[4:7], a[104:107], v[56:63], v189\n\t"
    "s_lshr_b64 s[80:81], s[76:77], s69\n\t"
    "ds_read_b32 v40, v224 offset:18436\n\t"
    "ds_read_b32 v43, v79 offset:18436\n\t"
    "v_smfmac_f32_16x16x64_bf16 v[8:11], a[108:111], v[56:63], v189 abid:1\n\t"
    "s_waitcnt lgkmcnt(7)\n\t"
    "ds_read_b128 v[60:63], v236 offset:0\n\t"
    "v_add_u32_sdwa v91, v248, v247 dst_sel:DWORD dst_unused:UNUSED_PAD src0_sel:DWORD src1_sel:WORD_1\n\t"
    "v_smfmac_f32_16x16x64_bf16 v[32:35], a[112:115], v[64:71], v190\n\t"
    "s_mov_b32 s55, 73728\n\t"
    "buffer_load_dwordx4 v[148:151], v92, s[48:51], s55 offen offset:2048 sc1\n\t"
    "v_smfmac_f32_16x16x64_bf16 v[36:39], a[116:119], v[64:71], v190 abid:1\n\t"
    "s_mov_b32 s66, 18874368\n\t"
    "ds_read_b128 v[56:59], v217 offset:0\n\t"
    "v_add_u32_sdwa v106, v248, v232 dst_sel:DWORD dst_unused:UNUSED_PAD src0_sel:DWORD src1_sel:WORD_1\n\t"
    "v_smfmac_f32_16x16x64_bf16 v[4:7], a[120:123], v[64:71], v191\n\t"
    "ds_read_b128 v[20:23], v244 offset:320\n\t"
    "v_smfmac_f32_16x16x64_bf16 v[8:11], a[124:127], v[64:71], v191 abid:1\n\t"
    "s_waitcnt lgkmcnt(8)\n\t"
    "s_and_b32 s80, s80, 63\n\t"
    "ds_read_b128 v[64:67], v217 offset:128\n\t"
    "ds_read_b128 v[68:71], v236 offset:128\n\t"
    "v_smfmac_f32_16x16x64_bf16 v[32:35], a[128:131], v[48:55], v192\n\t"
    "s_waitcnt lgkmcnt(5)\n\t"
    "s_mov_b32 s55, 77824\n\t"
    "buffer_load_dwordx4 v[168:171], v92, s[48:51], s55 offen offset:3072 sc1\n\t"
    "v_mfma_f32_16x16x32_bf16 v[8:11], v[80:83], v[40:43], v[8:11]\n\t"
    "ds_read_b32 v42, v231 offset:4\n\t"
    "v_add_u32_sdwa v228, v248, v228 dst_sel:DWORD dst_unused:UNUSED_PAD src0_sel:DWORD src1_sel:WORD_0\n\t"
    "v_smfmac_f32_16x16x64_bf16 v[36:39], a[132:135], v[48:55], v192 abid:1\n\t"
    "ds_read_b128 v[80:83], v245 offset:6144\n\t"
    "v_add_u32_sdwa v88, v248, v246 dst_sel:DWORD dst_unused:UNUSED_PAD src0_sel:DWORD src1_sel:WORD_0\n\t"
    "v_smfmac_f32_16x16x64_bf16 v[4:7], a[136:139], v[48:55], v193\n\t"
    "ds_read_b32 v40, v230 offset:4\n\t"
    "ds_read_b32 v43, v105 offset:4\n\t"
    "v_smfmac_f32_16x16x64_bf16 v[8:11], a[140:143], v[48:55], v193 abid:1\n\t"
    "s_waitcnt lgkmcnt(7)\n\t"
    "s_mov_b32 s55, 69632\n\t"
    "buffer_load_dwordx4 v[132:135], v92, s[48:51], s55 offen offset:2048 sc1\n\t"
    "ds_read_b32 v41, v104 offset:4\n\t"
    "v_cvt_pk_bf16_f32 v101, v34, v35\n\t"
    "v_smfmac_f32_16x16x64_bf16 v[12:15], a[144:147], v[56:63], v194\n\t"
    "s_waitcnt lgkmcnt(5)\n\t"
    "s_mov_b32 s76, 1124073472\n\t"
    "ds_read_b128 v[52:55], v236 offset:256\n\t"
    "v_cvt_pk_bf16_f32 v100, v32, v33\n\t"
    "v_smfmac_f32_16x16x64_bf16 v[12:15], a[164:167], v[64:71], v196 abid:1\n\t"
    "ds_read_u16 v91, v91 offset:0\n\t"
    "v_cvt_pk_bf16_f32 v103, v38, v39\n\t"
    "v_cvt_pk_bf16_f32 v96, v4, v5\n\t"
    "v_smfmac_f32_16x16x64_bf16 v[20:23], a[148:151], v[56:63], v194 abid:1\n\t"
    "ds_read_b128 v[44:47], v236 offset:384\n\t"
    "v_cvt_pk_bf16_f32 v99, v10, v11\n\t"
    "v_smfmac_f32_16x16x64_bf16 v[20:23], a[168:171], v[64:71], v197\n\t"
    "s_mov_b32 s55, 73728\n\t"
    "buffer_load_dwordx4 v[152:155], v92, s[48:51], s55 offen offset:3072 sc1\n\t"
    "v_cvt_pk_bf16_f32 v102, v36, v37\n\t"
    "v_smfmac_f32_16x16x64_bf16 v[24:27], a[152:155], v[56:63], v195\n\t"
    "ds_read_b128 v[48:51], v217 offset:256\n\t"
    "v_cvt_pk_bf16_f32 v98, v8, v9\n\t"
    "v_smfmac_f32_16x16x64_bf16 v[24:27], a[172:175], v[64:71], v197 abid:1\n\t"
    "s_waitcnt lgkmcnt(4)\n\t"
    "ds_read_u16 v89, v89 offset:0\n\t"
    "v_cndmask_b32_dpp v11, v99, v103, vcc quad_perm:[0,0,2,2] row_mask:0xf bank_mask:0xf\n\t"
    "v_mfma_f32_16x16x32_bf16 v[24:27], v[80:83], v[40:43], v[24:27]\n\t"
    "s_memrealtime s[84:85]\n\t"
    "ds_read_b128 v[40:43], v217 offset:384\n\t"
    "v_cvt_pk_bf16_f32 v97, v6, v7\n\t"
    "v_smfmac_f32_16x16x64_bf16 v[28:31], a[156:159], v[56:63], v195 abid:1\n\t"
    "s_mov_b32 s55, 90112\n\t"
    "buffer_load_dword v216, v92, s[48:51], s55 offen offset:3072 sc1\n\t"
    "v_smfmac_f32_16x16x64_bf16 v[28:31], a[176:179], v[64:71], v198\n\t"
    "s_mov_b32 s77, 269409\n\t"
    "ds_read_b128 v[80:83], v245 offset:5120\n\t"
    "v_cndmask_b32_dpp v9, v97, v101, vcc quad_perm:[0,0,2,2] row_mask:0xf bank_mask:0xf\n\t"
    "v_smfmac_f32_16x16x64_bf16 v[16:19], a[160:163], v[56:63], v196\n\t"
    "ds_read_b32 v56, v228 offset:4\n\t"
    "ds_read_b32 v57, v86 offset:4\n\t"
    "ds_read_b32 v58, v229 offset:4\n\t"
    "ds_read_b32 v59, v87 offset:4\n\t"
    "v_cndmask_b32_dpp v8, v96, v100, vcc quad_perm:[0,0,2,2] row_mask:0xf bank_mask:0xf\n\t"
    "v_smfmac_f32_16x16x64_bf16 v[16:19], a[180:183], v[64:71], v198 abid:1\n\t"
    "s_waitcnt lgkmcnt(7)\n\t"
    "s_lshl_b32 s80, s80, 2\n\t"
    "s_add_u32 s65, s57, s67\n\t"
    "v_cndmask_b32_dpp v10, v98, v102, vcc quad_perm:[0,0,2,2] row_mask:0xf bank_mask:0xf\n\t"
    "v_smfmac_f32_16x16x64_bf16 v[12:15], a[184:187], v[48:55], v199\n\t"
    "s_waitcnt lgkmcnt(5)\n\t"
    "s_not_b64 vcc, s[18:19]\n\t"
    "buffer_store_dwordx4 v[8:11], v241, s[44:47], s66 offen offset:0 nt\n\t"
    "ds_read_u16 v88, v88 offset:0\n\t"
    "ds_read_u16 v90, v90 offset:0\n\t"
    "v_cndmask_b32_dpp v98, v102, v98, vcc quad_perm:[1,1,3,3] row_mask:0xf bank_mask:0xf\n\t"
    "v_cndmask_b32_dpp v99, v103, v99, vcc quad_perm:[1,1,3,3] row_mask:0xf bank_mask:0xf\n\t"
    "v_smfmac_f32_16x16x64_bf16 v[12:15], a[204:207], v[40:47], v201 abid:1\n\t"
    "ds_read_b128 v[64:67], v217 offset:512\n\t"
    "v_cndmask_b32_dpp v97, v101, v97, vcc quad_perm:[1,1,3,3] row_mask:0xf bank_mask:0xf\n\t"
    "v_smfmac_f32_16x16x64_bf16 v[20:23], a[188:191], v[48:55], v199 abid:1\n\t"
    "ds_read_b128 v[60:63], v236 offset:640\n\t"
    "v_cndmask_b32_dpp v96, v100, v96, vcc quad_perm:[1,1,3,3] row_mask:0xf bank_mask:0xf\n\t"
    "v_smfmac_f32_16x16x64_bf16 v[20:23], a[208:211], v[40:47], v202\n\t"
    "s_waitcnt lgkmcnt(4)\n\t"
    "s_add_i32 s86, s87, 915\n\t"
    "ds_read_b128 v[68:71], v236 offset:512\n\t"
    "v_add_u32_sdwa v226, v248, v226 dst_sel:DWORD dst_unused:UNUSED_PAD src0_sel:DWORD src1_sel:WORD_0\n\t"
    "v_mfma_f32_16x16x32_bf16 v[20:23], v[80:83], v[56:59], v[20:23]\n\t"
    "s_mov_b32 s55, 81920\n\t"
    "buffer_load_dwordx4 v[172:175], v92, s[48:51], s55 offen offset:0 sc1\n\t"
    "v_smfmac_f32_16x16x64_bf16 v[24:27], a[192:195], v[48:55], v200\n\t"
    "ds_read_b128 v[56:59], v217 offset:640\n\t"
    "v_smfmac_f32_16x16x64_bf16 v[24:27], a[212:215], v[40:47], v202 abid:1\n\t"
    "s_waitcnt lgkmcnt(4)\n\t"
    "ds_read_b128 v[4:7], v244 offset:384\n\t"
    "s_set_gpr_idx_on s80, gpr_idx(SRC2,DST)\n\t"
    "v_mfma_f32_16x16x32_bf16 v[0:3], v[250:253], v[88:91], v[0:3]\n\t"
    "s_set_gpr_idx_off\n\t"
    "s_sub_u32 s89, s84, s24\n\t"
    "ds_read_b128 v[80:83], v245 offset:4096\n\t"
    "v_smfmac_f32_16x16x64_bf16 v[28:31], a[196:199], v[48:55], v200 abid:1\n\t"
    "s_lshr_b64 s[80:81], s[76:77], s70\n\t"
    "s_and_b32 s80, s80, 63\n\t"
    "s_cmp_lt_i32 s89, s86\n\t"
    "s_cbranch_scc1 .Lpc0_%=\n\t"
    ".Lpc0r_%=:\n\t"
    "buffer_store_dwordx4 v[96:99], v241, s[44:47], s66 offen offset:1152 nt\n\t"
    "ds_read_b128 v[36:39], v244 offset:320\n\t"
    "v_add_u32_sdwa v85, v248, v227 dst_sel:DWORD dst_unused:UNUSED_PAD src0_sel:DWORD src1_sel:WORD_1\n\t"
    "v_add_u32_sdwa v227, v248, v227 dst_sel:DWORD dst_unused:UNUSED_PAD src0_sel:DWORD src1_sel:WORD_0\n\t"
    "v_smfmac_f32_16x16x64_bf16 v[28:31], a[216:219], v[40:47], v203\n\t"
    "s_lshl_b32 s80, s80, 2\n\t"
    "ds_read_b128 v[8:11], v244 offset:448\n\t"
    "s_set_gpr_idx_on s80, gpr_idx(SRC2,DST)\n\t"
    "v_mfma_f32_16x16x32_bf16 v[0:3], v[252:255], v[88:91], v[0:3]\n\t"
    "s_set_gpr_idx_off\n\t"
    "s_max_i32 s98, s89, s86\n\t"
    "ds_read_b128 v[32:35], v244 offset:256\n\t"
    "s_cmp_lg_u32 s22, 0\n\t"
    "s_cbranch_scc1 .Lsl5_%=\n\t"
    ".Lslr5_%=:\n\t"
    "v_smfmac_f32_16x16x64_bf16 v[16:19], a[200:203], v[48:55], v201\n\t"
    "s_sub_i32 s16, s89, s86\n\t"
    "s_sub_i32 s16, s16, s23\n\t"
    "ds_read_b32 v48, v226 offset:4\n\t"
    "ds_read_b32 v49, v84 offset:4\n\t"
    "ds_read_b32 v50, v227 offset:4\n\t"
    "ds_read_b32 v51, v85 offset:4\n\t"
    "v_smfmac_f32_16x16x64_bf16 v[16:19], a[220:223], v[40:47], v203 abid:1\n\t"
    "s_add_u32 m0, s52, 23040\n\t"
    "s_waitcnt lgkmcnt(10)\n\t"
    "s_max_i32 s16, s16, 0\n\t"
    "s_min_i32 s16, s16, 30\n\t"
    "s_mov_b32 exec_hi, 0\n\t"
    "buffer_load_dwordx4 v237, s[40:43], s68 offen offset:0 nt lds\n\t"
    "s_mov_b32 exec_hi, -1\n\t"
    "v_add_u32_sdwa v41, v248, v234 dst_sel:DWORD dst_unused:UNUSED_PAD src0_sel:DWORD src1_sel:WORD_1\n\t"
    "v_add_u32_sdwa v43, v248, v235 dst_sel:DWORD dst_unused:UNUSED_PAD src0_sel:DWORD src1_sel:WORD_1\n\t"
    "v_smfmac_f32_16x16x64_bf16 v[12:15], a[224:227], v[64:71], v204\n\t"
    "s_waitcnt lgkmcnt(9)\n\t"
    "s_add_i32 s87, s87, s16\n\t"
    "ds_read_b32 v41, v41 offset:4\n\t"
    "v_add_u32_sdwa v40, v248, v234 dst_sel:DWORD dst_unused:UNUSED_PAD src0_sel:DWORD src1_sel:WORD_0\n\t"
    "v_smfmac_f32_16x16x64_bf16 v[12:15], a[244:247], v[56:63], v206 abid:1\n\t"
    "s_waitcnt lgkmcnt(1)\n\t"
    "s_add_i32 s23, s23, 5\n\t"
    "ds_read_b32 v43, v43 offset:4\n\t"
    "v_add_u32_sdwa v42, v248, v235 dst_sel:DWORD dst_unused:UNUSED_PAD src0_sel:DWORD src1_sel:WORD_0\n\t"
    "v_mfma_f32_16x16x32_bf16 v[12:15], v[80:83], v[48:51], v[12:15]\n\t"
    "ds_read_b128 v[52:55], v236 offset:768\n\t"
    "ds_read_b32 v40, v40 offset:4\n\t"
    "ds_read_b32 v42, v42 offset:4\n\t"
    "v_smfmac_f32_16x16x64_bf16 v[20:23], a[228:231], v[64:71], v204 abid:1\n\t"
    "buffer_load_dwordx4 v237, s[40:43], s68 offen offset:512 nt lds\n\t"
    "v_smfmac_f32_16x16x64_bf16 v[20:23], a[248:251], v[56:63], v207\n\t"
    "s_mov_b64 vcc, s[18:19]\n\t"
    "ds_read_b128 v[80:83], v245 offset:8192\n\t"
    "s_waitcnt vmcnt(33)\n\t"
    "v_smfmac_f32_16x16x64_bf16 v[24:27], a[232:235], v[64:71], v205\n\t"
    "ds_read_b128 v[48:51], v217 offset:768\n\t"
    "s_waitcnt vmcnt(31)\n\t"
    "v_smfmac_f32_16x16x64_bf16 v[24:27], a[252:255], v[56:63], v207 abid:1\n\t"
    "ds_read_b32 v88, v230 offset:4612\n\t"
    "ds_read_b32 v90, v231 offset:4612\n\t"
    "s_waitcnt vmcnt(29)\n\t"
    "v_smfmac_f32_16x16x64_bf16 v[28:31], a[236:239], v[64:71], v205 abid:1\n\t"
    "buffer_load_dwordx4 v238, s[40:43], s68 offen offset:1536 nt lds\n\t"
    "s_waitcnt vmcnt(28)\n\t"
    "v_smfmac_f32_16x16x64_bf16 v[28:31], v[108:111], v[56:63], v208\n\t"
    "v_add_u32_sdwa v233, v248, v233 dst_sel:DWORD dst_unused:UNUSED_PAD src0_sel:DWORD src1_sel:WORD_0\n\t"
    "s_waitcnt vmcnt(27)\n\t"
    "v_smfmac_f32_16x16x64_bf16 v[16:19], a[240:243], v[64:71], v206\n\t"
    "ds_read_b128 v[64:67], v217 offset:896\n\t"
    "ds_read_b128 v[68:71], v236 offset:896\n\t"
    "s_waitcnt vmcnt(25)\n\t"
    "v_smfmac_f32_16x16x64_bf16 v[16:19], v[112:115], v[56:63], v208 abid:1\n\t"
    "s_waitcnt lgkmcnt(5)\n\t"
    "ds_read_b128 v[56:59], v217 offset:1024\n\t"
    "ds_read_b128 v[60:63], v236 offset:1024\n\t"
    "v_mfma_f32_16x16x32_bf16 v[16:19], v[80:83], v[40:43], v[16:19]\n\t"
    "s_waitcnt lgkmcnt(6)\n\t"
    "buffer_load_dwordx4 v239, s[40:43], s68 offen offset:2560 nt lds\n\t"
    "s_waitcnt vmcnt(24)\n\t"
    "v_smfmac_f32_16x16x64_bf16 v[12:15], v[116:119], v[48:55], v209\n\t"
    "s_waitcnt lgkmcnt(2)\n\t"
    "ds_read_b32 v42, v233 offset:4\n\t"
    "s_waitcnt vmcnt(23)\n\t"
    "v_smfmac_f32_16x16x64_bf16 v[12:15], v[136:139], v[64:71], v211 abid:1\n\t"
    "s_waitcnt lgkmcnt(1)\n\t"
    "ds_read_b32 v43, v107 offset:4\n\t"
    "v_add_u32_sdwa v232, v248, v232 dst_sel:DWORD dst_unused:UNUSED_PAD src0_sel:DWORD src1_sel:WORD_0\n\t"
    "s_waitcnt vmcnt(21)\n\t"
    "v_smfmac_f32_16x16x64_bf16 v[12:15], v[156:159], v[56:63], v214\n\t"
    "ds_read_b128 v[44:47], v236 offset:4608\n\t"
    "s_waitcnt vmcnt(20)\n\t"
    "v_smfmac_f32_16x16x64_bf16 v[20:23], v[120:123], v[48:55], v209 abid:1\n\t"
    "buffer_load_dwordx4 v239, s[40:43], s68 offen offset:3584 nt lds\n\t"
    "s_waitcnt vmcnt(20)\n\t"
    "v_smfmac_f32_16x16x64_bf16 v[20:23], v[140:143], v[64:71], v212\n\t"
    "ds_read_b32 v40, v232 offset:4\n\t"
    "ds_read_b32 v41, v106 offset:4\n\t"
    "s_waitcnt vmcnt(19)\n\t"
    "v_smfmac_f32_16x16x64_bf16 v[20:23], v[160:163], v[56:63], v214 abid:1\n\t"
    "ds_read_b128 v[80:83], v245 offset:7168\n\t"
    "v_cvt_pk_bf16_f32 v101, v14, v15\n\t"
    "s_waitcnt vmcnt(18)\n\t"
    "v_smfmac_f32_16x16x64_bf16 v[24:27], v[124:127], v[48:55], v210\n\t"
    "ds_read_b32 v95, v87 offset:4612\n\t"
    "v_cvt_pk_bf16_f32 v100, v12, v13\n\t"
    "s_waitcnt vmcnt(16)\n\t"
    "v_smfmac_f32_16x16x64_bf16 v[24:27], v[144:147], v[64:71], v212 abid:1\n\t"
    "ds_read_b32 v94, v229 offset:4612\n\t"
    "s_waitcnt vmcnt(14)\n\t"
    "v_smfmac_f32_16x16x64_bf16 v[24:27], v[164:167], v[56:63], v215\n\t"
    "s_mov_b32 s76, 1207959552\n\t"
    "s_mov_b32 s77, 198674\n\t"
    "ds_read_b32 v89, v104 offset:4612\n\t"
    "s_waitcnt vmcnt(13)\n\t"
    "v_smfmac_f32_16x16x64_bf16 v[28:31], v[128:131], v[48:55], v210 abid:1\n\t"
    "s_lshr_b64 s[80:81], s[76:77], s69\n\t"
    "ds_read_b32 v91, v105 offset:4612\n\t"
    "v_cvt_pk_bf16_f32 v102, v20, v21\n\t"
    "s_waitcnt vmcnt(12)\n\t"
    "v_smfmac_f32_16x16x64_bf16 v[28:31], v[148:151], v[64:71], v213\n\t"
    "s_mov_b32 s66, 4718592\n\t"
    "ds_read_b32 v92, v228 offset:4612\n\t"
    "s_waitcnt vmcnt(11)\n\t"
    "v_smfmac_f32_16x16x64_bf16 v[28:31], v[168:171], v[56:63], v215 abid:1\n\t"
    "s_waitcnt lgkmcnt(5)\n\t"
    "ds_read_b32 v93, v86 offset:4612\n\t"
    "v_cvt_pk_bf16_f32 v96, v24, v25\n\t"
    "v_mfma_f32_16x16x32_bf16 v[28:31], v[80:83], v[40:43], v[28:31]\n\t"
    "s_and_b32 s80, s80, 63\n\t"
    "ds_read_b128 v[40:43], v217 offset:4608\n\t"
    "v_cvt_pk_bf16_f32 v97, v26, v27\n\t"
    "s_waitcnt vmcnt(10)\n\t"
    "v_smfmac_f32_16x16x64_bf16 v[16:19], v[132:135], v[48:55], v211\n\t"
    "ds_read_b128 v[52:55], v236 offset:4736\n\t"
    "v_cvt_pk_bf16_f32 v103, v22, v23\n\t"
    "s_waitcnt vmcnt(9)\n\t"
    "v_smfmac_f32_16x16x64_bf16 v[16:19], v[152:155], v[64:71], v213 abid:1\n\t"
    "ds_read_b128 v[48:51], v217 offset:4736\n\t"
    "s_waitcnt vmcnt(6)\n\t"
    "v_smfmac_f32_16x16x64_bf16 v[16:19], v[172:175], v[56:63], v216\n\t"
    "s_waitcnt lgkmcnt(2)\n\t"
    "s_mov_b32 s77, 198674\n\t"
    "ds_read_b128 v[80:83], v245 offset:6144\n\t"
    "v_cvt_pk_bf16_f32 v98, v28, v29\n\t"
    "v_smfmac_f32_16x16x64_bf16 v[32:35], a[144:147], v[40:47], v194\n\t"
    "s_waitcnt lgkmcnt(1)\n\t"
    "ds_read_b128 v[56:59], v217 offset:4992\n\t"
    "v_cvt_pk_bf16_f32 v99, v30, v31\n\t"
    "v_smfmac_f32_16x16x64_bf16 v[32:35], a[164:167], v[48:55], v196 abid:1\n\t"
    "ds_read_b128 v[64:67], v217 offset:4864\n\t"
    "v_cndmask_b32_dpp v29, v97, v101, vcc quad_perm:[0,0,2,2] row_mask:0xf bank_mask:0xf\n\t"
    "v_smfmac_f32_16x16x64_bf16 v[36:39], a[148:151], v[40:47], v194 abid:1\n\t"
    "ds_read_b128 v[12:15], v244 offset:512\n\t"
    "v_cvt_pk_bf16_f32 v16, v16, v17\n\t"
    "v_smfmac_f32_16x16x64_bf16 v[36:39], a[168:171], v[48:55], v197\n\t"
    "ds_read_b128 v[60:63], v236 offset:4992\n\t"
    "v_cndmask_b32_dpp v31, v99, v103, vcc quad_perm:[0,0,2,2] row_mask:0xf bank_mask:0xf\n\t"
    "v_smfmac_f32_16x16x64_bf16 v[4:7], a[152:155], v[40:47], v195\n\t"
    "ds_read_b128 v[68:71], v236 offset:4864\n\t"
    "v_cndmask_b32_dpp v28, v96, v100, vcc quad_perm:[0,0,2,2] row_mask:0xf bank_mask:0xf\n\t"
    "v_smfmac_f32_16x16x64_bf16 v[4:7], a[172:175], v[48:55], v197 abid:1\n\t"
    "s_waitcnt lgkmcnt(5)\n\t"
    "ds_read_b128 v[20:23], v244 offset:256\n\t"
    "v_cndmask_b32_dpp v30, v98, v102, vcc quad_perm:[0,0,2,2] row_mask:0xf bank_mask:0xf\n\t"
    "v_mfma_f32_16x16x32_bf16 v[4:7], v[80:83], v[88:91], v[4:7]\n\t"
    "s_memrealtime s[84:85]\n\t"
    "ds_read_b128 v[80:83], v245 offset:5120\n\t"
    "v_add_u32_sdwa v90, v248, v247 dst_sel:DWORD dst_unused:UNUSED_PAD src0_sel:DWORD src1_sel:WORD_0\n\t"
    "v_smfmac_f32_16x16x64_bf16 v[8:11], a[156:159], v[40:47], v195 abid:1\n\t"
    "ds_read_u16 v90, v90 offset:4608\n\t"
    "v_add_u32_sdwa v91, v248, v247 dst_sel:DWORD dst_unused:UNUSED_PAD src0_sel:DWORD src1_sel:WORD_1\n\t"
    "v_smfmac_f32_16x16x64_bf16 v[8:11], a[176:179], v[48:55], v198\n\t"
    "s_waitcnt lgkmcnt(5)\n\t"
    "s_not_b64 vcc, s[18:19]\n\t"
    "buffer_store_dwordx4 v[28:31], v241, s[44:47], s67 offen offset:128 sc1\n\t"
    "ds_read_u16 v91, v91 offset:4608\n\t"
    "v_cndmask_b32_dpp v97, v101, v97, vcc quad_perm:[1,1,3,3] row_mask:0xf bank_mask:0xf\n\t"
    "v_cvt_pk_bf16_f32 v17, v18, v19\n\t"
    "v_add_u32_sdwa v89, v248, v246 dst_sel:DWORD dst_unused:UNUSED_PAD src0_sel:DWORD src1_sel:WORD_1\n\t"
    "v_smfmac_f32_16x16x64_bf16 v[12:15], a[160:163], v[40:47], v196\n\t"
    "ds_read_b128 v[40:43], v217 offset:5120\n\t"
    "v_add_u32_sdwa v88, v248, v246 dst_sel:DWORD dst_unused:UNUSED_PAD src0_sel:DWORD src1_sel:WORD_0\n\t"
    "v_smfmac_f32_16x16x64_bf16 v[12:15], a[180:183], v[48:55], v198 abid:1\n\t"
    "s_waitcnt lgkmcnt(5)\n\t"
    "ds_read_u16 v88, v88 offset:4608\n\t"
    "v_cndmask_b32_dpp v96, v100, v96, vcc quad_perm:[1,1,3,3] row_mask:0xf bank_mask:0xf\n\t"
    "v_smfmac_f32_16x16x64_bf16 v[32:35], a[184:187], v[64:71], v199\n\t"
    "ds_read_u16 v89, v89 offset:4608\n\t"
    "v_smfmac_f32_16x16x64_bf16 v[32:35], a[204:207], v[56:63], v201 abid:1\n\t"
    "s_lshl_b32 s80, s80, 2\n\t"
    "ds_read_b128 v[44:47], v236 offset:5120\n\t"
    "v_cndmask_b32_dpp v99, v103, v99, vcc quad_perm:[1,1,3,3] row_mask:0xf bank_mask:0xf\n\t"
    "v_smfmac_f32_16x16x64_bf16 v[36:39], a[188:191], v[64:71], v199 abid:1\n\t"
    "ds_read_b128 v[52:55], v236 offset:5248\n\t"
    "v_cndmask_b32_dpp v98, v102, v98, vcc quad_perm:[1,1,3,3] row_mask:0xf bank_mask:0xf\n\t"
    "v_smfmac_f32_16x16x64_bf16 v[36:39], a[208:211], v[56:63], v202\n\t"
    "s_waitcnt lgkmcnt(7)\n\t"
    "s_add_i32 s86, s87, 1011\n\t"
    "buffer_store_dwordx4 v[96:99], v241, s[44:47], s67 offen offset:1280 sc1\n\t"
    "ds_read_b128 v[48:51], v217 offset:5248\n\t"
    "ds_write_b64 v242, v[16:17] offset:0\n\t"
    "v_mfma_f32_16x16x32_bf16 v[36:39], v[80:83], v[92:95], v[36:39]\n\t"
    "s_bitcmp1_b32 s54, 6\n\t"
    "s_mov_b32 s68, 28311552\n\t"
    "s_cmov_b32 s68, 28385280\n\t"
    "s_add_i32 s98, s98, 93\n\t"
    "v_smfmac_f32_16x16x64_bf16 v[4:7], a[192:195], v[64:71], v200\n\t"
    "s_mov_b32 s76, 1207959552\n\t"
    "s_add_i32 s89, s86, s23\n\t"
    "s_min_i32 s98, s98, s89\n\t"
    "ds_read_b128 v[80:83], v245 offset:4096\n\t"
    "v_smfmac_f32_16x16x64_bf16 v[4:7], a[212:215], v[56:63], v202 abid:1\n\t"
    "s_waitcnt lgkmcnt(5)\n\t"
    "s_max_i32 s86, s86, s98\n\t"
    "ds_read_b32 v94, v227 offset:4612\n\t"
    "s_set_gpr_idx_on s80, gpr_idx(SRC2,DST)\n\t"
    "v_mfma_f32_16x16x32_bf16 v[0:3], v[250:253], v[88:91], v[0:3]\n\t"
    "s_set_gpr_idx_off\n\t"
    "s_waitcnt lgkmcnt(2)\n\t"
    "s_sub_u32 s89, s84, s24\n\t"
    "s_barrier\n\t"
    "ds_read_b32 v92, v226 offset:4612\n\t"
    "ds_read_b32 v93, v84 offset:4612\n\t"
    "ds_read_b32 v95, v85 offset:4612\n\t"
    "v_smfmac_f32_16x16x64_bf16 v[8:11], a[196:199], v[64:71], v200 abid:1\n\t"
    "s_lshr_b64 s[80:81], s[76:77], s70\n\t"
    "s_cmp_lt_i32 s89, s86\n\t"
    "s_cbranch_scc1 .Lpc1_%=\n\t"
    ".Lpc1r_%=:\n\t"
    "ds_read_b128 v[100:103], v243 offset:2048\n\t"
    "v_smfmac_f32_16x16x64_bf16 v[8:11], a[216:219], v[56:63], v203\n\t"
    "s_add_u32 m0, s52, 0\n\t"
    "s_and_b32 s80, s80, 63\n\t"
    "s_lshl_b32 s80, s80, 2\n\t"
    "s_mov_b32 exec_hi, 0\n\t"
    "buffer_load_dwordx4 v237, s[40:43], s68 offen offset:0 nt lds\n\t"
    "s_mov_b32 exec_hi, -1\n\t"
    "s_set_gpr_idx_on s80, gpr_idx(SRC2,DST)\n\t"
    "v_mfma_f32_16x16x32_bf16 v[0:3], v[252:255], v[88:91], v[0:3]\n\t"
    "s_set_gpr_idx_off\n\t"
    "s_max_i32 s98, s89, s86\n\t"
    "ds_read_b128 v[16:19], v244 offset:384\n\t"
    "s_cmp_lg_u32 s22, 0\n\t"
    "s_cbranch_scc1 .Lsl6_%=\n\t"
    ".Lslr6_%=:\n\t"
    "v_smfmac_f32_16x16x64_bf16 v[12:15], a[200:203], v[64:71], v201\n\t"
    "s_sub_i32 s23, s23, 3\n\t"
    "ds_read_b128 v[68:71], v236 offset:5376\n\t"
    "v_add_u32_sdwa v89, v248, v234 dst_sel:DWORD dst_unused:UNUSED_PAD src0_sel:DWORD src1_sel:WORD_1\n\t"
    "v_smfmac_f32_16x16x64_bf16 v[12:15], a[220:223], v[56:63], v203 abid:1\n\t"
    "ds_read_b32 v89, v89 offset:4612\n\t"
    "v_smfmac_f32_16x16x64_bf16 v[32:35], a[224:227], v[40:47], v204\n\t"
    "ds_read_b128 v[64:67], v217 offset:5376\n\t"
    "v_add_u32_sdwa v91, v248, v235 dst_sel:DWORD dst_unused:UNUSED_PAD src0_sel:DWORD src1_sel:WORD_1\n\t"
    "v_smfmac_f32_16x16x64_bf16 v[32:35], a[244:247], v[48:55], v206 abid:1\n\t"
    "s_waitcnt lgkmcnt(4)\n\t"
    "s_mov_b32 exec_hi, 0\n\t"
    "buffer_store_dwordx4 v[100:103], v240, s[44:47], s65 offen offset:0 sc1\n\t"
    "s_mov_b32 exec_hi, -1\n\t"
    "ds_read_b32 v91, v91 offset:4612\n\t"
    "v_add_u32_sdwa v88, v248, v234 dst_sel:DWORD dst_unused:UNUSED_PAD src0_sel:DWORD src1_sel:WORD_0\n\t"
    "v_add_u32_sdwa v90, v248, v235 dst_sel:DWORD dst_unused:UNUSED_PAD src0_sel:DWORD src1_sel:WORD_0\n\t"
    "v_mfma_f32_16x16x32_bf16 v[32:35], v[80:83], v[92:95], v[32:35]\n\t"
    "s_mov_b32 s76, 2231369728\n\t"
    "ds_read_b32 v88, v88 offset:4612\n\t"
    "v_smfmac_f32_16x16x64_bf16 v[36:39], a[228:231], v[40:47], v204 abid:1\n\t"
    "ds_read_b32 v90, v90 offset:4612\n\t"
    "v_smfmac_f32_16x16x64_bf16 v[36:39], a[248:251], v[48:55], v207\n\t"
    "s_mov_b32 s77, 531521\n\t"
    "ds_read_b128 v[80:83], v245 offset:8192\n\t"
    "v_smfmac_f32_16x16x64_bf16 v[4:7], a[232:235], v[40:47], v205\n\t"
    "ds_read_b128 v[60:63], v236 offset:5504\n\t"
    "v_smfmac_f32_16x16x64_bf16 v[4:7], a[252:255], v[48:55], v207 abid:1\n\t"
    "ds_read_b128 v[56:59], v217 offset:5504\n\t"
    "v_smfmac_f32_16x16x64_bf16 v[8:11], a[236:239], v[40:47], v205 abid:1\n\t"
    "ds_read_b128 v[28:31], v244 offset:448\n\t"
    "v_smfmac_f32_16x16x64_bf16 v[8:11], v[108:111], v[48:55], v208\n\t"
    "s_add_u32 m0, s52, 0\n\t"
    "s_nop 0\n\t"
    "buffer_load_dwordx4 v237, s[40:43], s68 offen offset:512 nt lds\n\t"
    "v_smfmac_f32_16x16x64_bf16 v[12:15], a[240:243], v[40:47], v206\n\t"
    "ds_read_b128 v[40:43], v217 offset:5632\n\t"
    "ds_read_b128 v[44:47], v236 offset:5632\n\t"
    "v_smfmac_f32_16x16x64_bf16 v[12:15], v[112:115], v[48:55], v208 abid:1\n\t"
    "s_waitcnt lgkmcnt(5)\n\t"
    "s_lshr_b64 s[80:81], s[76:77], s69\n\t"
    "s_and_b32 s80, s80, 63\n\t"
    "ds_read_b128 v[52:55], v236 offset:9216\n\t"
    "v_mfma_f32_16x16x32_bf16 v[12:15], v[80:83], v[88:91], v[12:15]\n\t"
    "ds_read_b32 v92, v232 offset:4612\n\t"
    "v_smfmac_f32_16x16x64_bf16 v[32:35], v[116:119], v[64:71], v209\n\t"
    "s_waitcnt lgkmcnt(5)\n\t"
    "ds_read_b32 v94, v233 offset:4612\n\t"
    "v_smfmac_f32_16x16x64_bf16 v[32:35], v[136:139], v[56:63], v211 abid:1\n\t"
    "s_waitcnt lgkmcnt(3)\n\t"
    "ds_read_b128 v[48:51], v217 offset:9216\n\t"
    "v_smfmac_f32_16x16x64_bf16 v[32:35], v[156:159], v[40:47], v214\n\t"
    "ds_read_b32 v95, v107 offset:4612\n\t"
    "v_smfmac_f32_16x16x64_bf16 v[36:39], v[120:123], v[64:71], v209 abid:1\n\t"
    "ds_read_b128 v[80:83], v245 offset:7168\n\t"
    "v_smfmac_f32_16x16x64_bf16 v[36:39], v[140:143], v[56:63], v212\n\t"
    "ds_read_b128 v[24:27], v244 offset:320\n\t"
    "v_smfmac_f32_16x16x64_bf16 v[36:39], v[160:163], v[40:47], v214 abid:1\n\t"
    "ds_read_b32 v93, v106 offset:4612\n\t"
    "v_cvt_pk_bf16_f32 v96, v32, v33\n\t"
    "v_smfmac_f32_16x16x64_bf16 v[4:7], v[124:127], v[64:71], v210\n\t"
    "ds_read_b32 v88, v230 offset:9220\n\t"
    "v_smfmac_f32_16x16x64_bf16 v[4:7], v[144:147], v[56:63], v212 abid:1\n\t"
    "s_add_u32 s67, s57, s66\n\t"
    "ds_read_b32 v91, v105 offset:9220\n\t"
    "v_cvt_pk_bf16_f32 v97, v34, v35\n\t"
    "v_smfmac_f32_16x16x64_bf16 v[4:7], v[164:167], v[40:47], v215\n\t"
    "buffer_load_dwordx4 v238, s[40:43], s68 offen offset:1536 nt lds\n\t"
    "ds_read_b32 v89, v104 offset:9220\n\t"
    "v_smfmac_f32_16x16x64_bf16 v[8:11], v[128:131], v[64:71], v210 abid:1\n\t"
    "s_lshl_b32 s80, s80, 2\n\t"
    "ds_read_b32 v90, v231 offset:9220\n\t"
    "v_smfmac_f32_16x16x64_bf16 v[8:11], v[148:151], v[56:63], v213\n\t"
    "ds_read_b128 v[32:35], v244 offset:512\n\t"
    "v_cvt_pk_bf16_f32 v99, v38, v39\n\t"
    "v_smfmac_f32_16x16x64_bf16 v[8:11], v[168:171], v[40:47], v215 abid:1\n\t"
    "s_waitcnt lgkmcnt(5)\n\t"
    "v_cvt_pk_bf16_f32 v100, v4, v5\n\t"
    "v_mfma_f32_16x16x32_bf16 v[8:11], v[80:83], v[92:95], v[8:11]\n\t"
    "ds_read_b32 v94, v229 offset:9220\n\t"
    "ds_read_b32 v95, v87 offset:9220\n\t"
    "v_smfmac_f32_16x16x64_bf16 v[12:15], v[132:135], v[64:71], v211\n\t"
    "ds_read_b128 v[64:67], v217 offset:9344\n\t"
    "ds_read_b128 v[68:71], v236 offset:9344\n\t"
    "v_smfmac_f32_16x16x64_bf16 v[12:15], v[152:155], v[56:63], v213 abid:1\n\t"
    "s_mov_b64 vcc, s[18:19]\n\t"
    "ds_read_b128 v[80:83], v245 offset:6144\n\t"
    "v_cvt_pk_bf16_f32 v98, v36, v37\n\t"
    "v_smfmac_f32_16x16x64_bf16 v[12:15], v[172:175], v[40:47], v216\n\t"
    "s_mov_b32 s65, 9437184\n\t"
    "ds_read_b32 v92, v228 offset:9220\n\t"
    "v_cvt_pk_bf16_f32 v101, v6, v7\n\t"
    "v_smfmac_f32_16x16x64_bf16 v[20:23], a[144:147], v[48:55], v194\n\t"
    "s_waitcnt lgkmcnt(2)\n\t"
    "s_mov_b32 s76, 2231369728\n\t"
    "ds_read_b128 v[60:63], v236 offset:9472\n\t"
    "v_cvt_pk_bf16_f32 v103, v10, v11\n\t"
    "v_smfmac_f32_16x16x64_bf16 v[20:23], a[164:167], v[64:71], v196 abid:1\n\t"
    "ds_read_b128 v[44:47], v236 offset:9600\n\t"
    "v_cvt_pk_bf16_f32 v102, v8, v9\n\t"
    "v_smfmac_f32_16x16x64_bf16 v[24:27], a[148:151], v[48:55], v194 abid:1\n\t"
    "s_mov_b32 s77, 531521\n\t"
    "ds_read_b128 v[56:59], v217 offset:9472\n\t"
    "v_cndmask_b32_dpp v8, v100, v96, vcc quad_perm:[0,0,2,2] row_mask:0xf bank_mask:0xf\n\t"
    "v_smfmac_f32_16x16x64_bf16 v[24:27], a[168:171], v[64:71], v197\n\t"
    "ds_read_b128 v[40:43], v217 offset:9600\n\t"
    "v_cndmask_b32_dpp v10, v102, v98, vcc quad_perm:[0,0,2,2] row_mask:0xf bank_mask:0xf\n\t"
    "v_cndmask_b32_dpp v11, v103, v99, vcc quad_perm:[0,0,2,2] row_mask:0xf bank_mask:0xf\n\t"
    "v_cvt_pk_bf16_f32 v12, v12, v13\n\t"
    "v_smfmac_f32_16x16x64_bf16 v[16:19], a[152:155], v[48:55], v195\n\t"
    "buffer_load_dwordx4 v239, s[40:43], s68 offen offset:2560 nt lds\n\t"
    "v_smfmac_f32_16x16x64_bf16 v[16:19], a[172:175], v[64:71], v197 abid:1\n\t"
    "s_waitcnt lgkmcnt(5)\n\t"
    "ds_read_b32 v93, v86 offset:9220\n\t"
    "v_cndmask_b32_dpp v9, v101, v97, vcc quad_perm:[0,0,2,2] row_mask:0xf bank_mask:0xf\n\t"
    "v_mfma_f32_16x16x32_bf16 v[16:19], v[80:83], v[88:91], v[16:19]\n\t"
    "s_memrealtime s[84:85]\n\t"
    "ds_read_b128 v[80:83], v245 offset:5120\n\t"
    "v_add_u32_sdwa v88, v248, v246 dst_sel:DWORD dst_unused:UNUSED_PAD src0_sel:DWORD src1_sel:WORD_0\n\t"
    "v_smfmac_f32_16x16x64_bf16 v[28:31], a[156:159], v[48:55], v195 abid:1\n\t"
    "ds_read_u16 v88, v88 offset:9216\n\t"
    "v_add_u32_sdwa v91, v248, v247 dst_sel:DWORD dst_unused:UNUSED_PAD src0_sel:DWORD src1_sel:WORD_1\n\t"
    "v_smfmac_f32_16x16x64_bf16 v[28:31], a[176:179], v[64:71], v198\n\t"
    "s_not_b64 vcc, s[18:19]\n\t"
    "buffer_store_dwordx4 v[8:11], v241, s[44:47], s66 offen offset:128 sc1\n\t"
    "ds_read_u16 v91, v91 offset:9216\n\t"
    "v_cndmask_b32_dpp v101, v97, v101, vcc quad_perm:[1,1,3,3] row_mask:0xf bank_mask:0xf\n\t"
    "v_add_u32_sdwa v89, v248, v246 dst_sel:DWORD dst_unused:UNUSED_PAD src0_sel:DWORD src1_sel:WORD_1\n\t"
    "v_add_u32_sdwa v90, v248, v247 dst_sel:DWORD dst_unused:UNUSED_PAD src0_sel:DWORD src1_sel:WORD_0\n\t"
    "v_smfmac_f32_16x16x64_bf16 v[32:35], a[160:163], v[48:55], v196\n\t"
    "ds_read_u16 v90, v90 offset:9216\n\t"
    "v_cndmask_b32_dpp v103, v99, v103, vcc quad_perm:[1,1,3,3] row_mask:0xf bank_mask:0xf\n\t"
    "v_smfmac_f32_16x16x64_bf16 v[32:35], a[180:183], v[64:71], v198 abid:1\n\t"
    "s_waitcnt lgkmcnt(6)\n\t"
    "ds_read_b128 v[52:55], v236 offset:9728\n\t"
    "v_cvt_pk_bf16_f32 v13, v14, v15\n\t"
    "v_smfmac_f32_16x16x64_bf16 v[20:23], a[184:187], v[56:63], v199\n\t"
    "s_waitcnt lgkmcnt(6)\n\t"
    "s_sub_i32 s87, s87, s99\n\t"
    "ds_read_u16 v89, v89 offset:9216\n\t"
    "v_cndmask_b32_dpp v102, v98, v102, vcc quad_perm:[1,1,3,3] row_mask:0xf bank_mask:0xf\n\t"
    "v_smfmac_f32_16x16x64_bf16 v[20:23], a[204:207], v[40:47], v201 abid:1\n\t"
    "ds_read_b128 v[64:67], v217 offset:9856\n\t"
    "v_smfmac_f32_16x16x64_bf16 v[24:27], a[188:191], v[56:63], v199 abid:1\n\t"
    "s_sub_i32 s98, s98, s99\n\t"
    "ds_read_b128 v[48:51], v217 offset:9728\n\t"
    "v_cndmask_b32_dpp v100, v96, v100, vcc quad_perm:[1,1,3,3] row_mask:0xf bank_mask:0xf\n\t"
    "v_smfmac_f32_16x16x64_bf16 v[24:27], a[208:211], v[40:47], v202\n\t"
    "s_waitcnt lgkmcnt(7)\n\t"
    "s_add_i32 s86, s87, 1103\n\t"
    "buffer_store_dwordx4 v[100:103], v241, s[44:47], s66 offen offset:1280 sc1\n\t"
    "ds_read_b128 v[68:71], v236 offset:9856\n\t"
    "ds_write_b64 v242, v[12:13] offset:512\n\t"
    "v_mfma_f32_16x16x32_bf16 v[24:27], v[80:83], v[92:95], v[24:27]\n\t"
    "s_add_i32 s98, s98, 89\n\t"
    "ds_read_b32 v92, v226 offset:9220\n\t"
    "ds_read_b32 v94, v227 offset:9220\n\t"
    "v_smfmac_f32_16x16x64_bf16 v[16:19], a[192:195], v[56:63], v200\n\t"
    "s_add_i32 s89, s86, s23\n\t"
    "s_min_i32 s98, s98, s89\n\t"
    "ds_read_b32 v95, v85 offset:9220\n\t"
    "v_smfmac_f32_16x16x64_bf16 v[16:19], a[212:215], v[40:47], v202 abid:1\n\t"
    "s_waitcnt lgkmcnt(7)\n\t"
    "s_max_i32 s86, s86, s98\n\t"
    "ds_read_b128 v[80:83], v245 offset:4096\n\t"
    "s_set_gpr_idx_on s80, gpr_idx(SRC2,DST)\n\t"
    "v_mfma_f32_16x16x32_bf16 v[0:3], v[250:253], v[88:91], v[0:3]\n\t"
    "s_set_gpr_idx_off\n\t"
    "s_add_u32 m0, s52, 0\n\t"
    "s_waitcnt lgkmcnt(4)\n\t"
    "s_sub_u32 s89, s84, s24\n\t"
    "s_barrier\n\t"
    "buffer_load_dwordx4 v239, s[40:43], s68 offen offset:3584 nt lds\n\t"
    "ds_read_b128 v[96:99], v243 offset:2560\n\t"
    "v_smfmac_f32_16x16x64_bf16 v[28:31], a[196:199], v[56:63], v200 abid:1\n\t"
    "s_lshr_b64 s[80:81], s[76:77], s70\n\t"
    "s_cmp_lt_i32 s89, s86\n\t"
    "s_cbranch_scc1 .Lpc2_%=\n\t"
    ".Lpc2r_%=:\n\t"
    "ds_read_b32 v93, v84 offset:9220\n\t"
    "v_smfmac_f32_16x16x64_bf16 v[28:31], a[216:219], v[40:47], v203\n\t"
    "s_and_b32 s80, s80, 63\n\t"
    "s_lshl_b32 s80, s80, 2\n\t"
    "s_set_gpr_idx_on s80, gpr_idx(SRC2,DST)\n\t"
    "v_mfma_f32_16x16x32_bf16 v[0:3], v[252:255], v[88:91], v[0:3]\n\t"
    "s_set_gpr_idx_off\n\t"
    "s_max_i32 s98, s89, s86\n\t"
    "ds_read_b128 v[4:7], v244 offset:320\n\t"
    "s_cmp_lg_u32 s22, 0\n\t"
    "s_cbranch_scc1 .Lsl7_%=\n\t"
    ".Lslr7_%=:\n\t"
    "v_smfmac_f32_16x16x64_bf16 v[32:35], a[200:203], v[56:63], v201\n\t"
    "s_sub_i32 s23, s23, 3\n\t"
    "ds_read_b128 v[60:63], v236 offset:9984\n\t"
    "v_add_u32_sdwa v89, v248, v234 dst_sel:DWORD dst_unused:UNUSED_PAD src0_sel:DWORD src1_sel:WORD_1\n\t"
    "v_smfmac_f32_16x16x64_bf16 v[32:35], a[220:223], v[40:47], v203 abid:1\n\t"
    "ds_read_b128 v[56:59], v217 offset:9984\n\t"
    "v_smfmac_f32_16x16x64_bf16 v[20:23], a[224:227], v[48:55], v204\n\t"
    "ds_read_b32 v89, v89 offset:9220\n\t"
    "v_smfmac_f32_16x16x64_bf16 v[20:23], a[244:247], v[64:71], v206 abid:1\n\t"
    "s_waitcnt lgkmcnt(4)\n\t"
    "s_mov_b32 exec_hi, 0\n\t"
    "buffer_store_dwordx4 v[96:99], v240, s[44:47], s67 offen offset:0 sc1\n\t"
    "s_mov_b32 exec_hi, -1\n\t"
    "ds_read_b128 v[44:47], v236 offset:10112\n\t"
    "v_add_u32_sdwa v90, v248, v235 dst_sel:DWORD dst_unused:UNUSED_PAD src0_sel:DWORD src1_sel:WORD_0\n\t"
    "v_add_u32_sdwa v91, v248, v235 dst_sel:DWORD dst_unused:UNUSED_PAD src0_sel:DWORD src1_sel:WORD_1\n\t"
    "v_mfma_f32_16x16x32_bf16 v[20:23], v[80:83], v[92:95], v[20:23]\n\t"
    "ds_read_b128 v[80:83], v245 offset:8192\n\t"
    "v_add_u32_sdwa v88, v248, v234 dst_sel:DWORD dst_unused:UNUSED_PAD src0_sel:DWORD src1_sel:WORD_0\n\t"
    "v_smfmac_f32_16x16x64_bf16 v[24:27], a[228:231], v[48:55], v204 abid:1\n\t"
    "ds_read_b32 v88, v88 offset:9220\n\t"
    "ds_read_b32 v91, v91 offset:9220\n\t"
    "v_smfmac_f32_16x16x64_bf16 v[24:27], a[248:251], v[64:71], v207\n\t"
    "s_mov_b32 s77, 329776\n\t"
    "s_bitcmp1_b32 s54, 7\n\t"
    "s_mov_b32 s68, 33030144\n\t"
    "s_cmov_b32 s68, 33103872\n\t"
    "ds_read_b32 v90, v90 offset:9220\n\t"
    "v_smfmac_f32_16x16x64_bf16 v[16:19], a[232:235], v[48:55], v205\n\t"
    "s_add_u32 m0, s52, 4608\n\t"
    "s_mov_b32 exec_hi, 0\n\t"
    "buffer_load_dwordx4 v237, s[40:43], s68 offen offset:0 nt lds\n\t"
    "s_mov_b32 exec_hi, -1\n\t"
    "v_smfmac_f32_16x16x64_bf16 v[16:19], a[252:255], v[64:71], v207 abid:1\n\t"
    "ds_read_b128 v[40:43], v217 offset:10112\n\t"
    "v_smfmac_f32_16x16x64_bf16 v[28:31], a[236:239], v[48:55], v205 abid:1\n\t"
    "s_mov_b32 s76, 1224736768\n\t"
    "ds_read_b32 v94, v233 offset:9220\n\t"
    "v_smfmac_f32_16x16x64_bf16 v[28:31], v[108:111], v[64:71], v208\n\t"
    "ds_read_b32 v93, v106 offset:9220\n\t"
    "v_smfmac_f32_16x16x64_bf16 v[32:35], a[240:243], v[48:55], v206\n\t"
    "s_lshr_b64 s[80:81], s[76:77], s69\n\t"
    "ds_read_b128 v[48:51], v217 offset:10240\n\t"
    "ds_read_b128 v[52:55], v236 offset:10240\n\t"
    "v_smfmac_f32_16x16x64_bf16 v[32:35], v[112:115], v[64:71], v208 abid:1\n\t"
    "s_waitcnt lgkmcnt(5)\n\t"
    "ds_read_b32 v95, v107 offset:9220\n\t"
    "v_mfma_f32_16x16x32_bf16 v[32:35], v[80:83], v[88:91], v[32:35]\n\t"
    "ds_read_b128 v[68:71], v236 offset:13824\n\t"
    "v_smfmac_f32_16x16x64_bf16 v[20:23], v[116:119], v[56:63], v209\n\t"
    "s_waitcnt lgkmcnt(6)\n\t"
    "v_smfmac_f32_16x16x64_bf16 v[20:23], v[136:139], v[40:47], v211 abid:1\n\t"
    "s_waitcnt lgkmcnt(2)\n\t"
    "s_add_u32 s66, s57, s65\n\t"
    "ds_read_b128 v[12:15], v244 offset:384\n\t"
    "v_smfmac_f32_16x16x64_bf16 v[20:23], v[156:159], v[48:55], v214\n\t"
    "ds_read_b128 v[80:83], v245 offset:7168\n\t"
    "v_smfmac_f32_16x16x64_bf16 v[24:27], v[120:123], v[56:63], v209 abid:1\n\t"
    "s_and_b32 s80, s80, 63\n\t"
    "ds_read_b128 v[36:39], v244 offset:256\n\t"
    "v_smfmac_f32_16x16x64_bf16 v[24:27], v[140:143], v[40:47], v212\n\t"
    "ds_read_b32 v90, v231 offset:13828\n\t"
    "v_smfmac_f32_16x16x64_bf16 v[24:27], v[160:163], v[48:55], v214 abid:1\n\t"
    "buffer_load_dwordx4 v237, s[40:43], s68 offen offset:512 nt lds\n\t"
    "ds_read_b32 v92, v232 offset:9220\n\t"
    "v_smfmac_f32_16x16x64_bf16 v[16:19], v[124:127], v[56:63], v210\n\t"
    "s_mov_b32 s76, 1224736768\n\t"
    "ds_read_b32 v91, v105 offset:13828\n\t"
    "v_smfmac_f32_16x16x64_bf16 v[16:19], v[144:147], v[40:47], v212 abid:1\n\t"
    "ds_read_b32 v88, v230 offset:13828\n\t"
    "v_smfmac_f32_16x16x64_bf16 v[16:19], v[164:167], v[48:55], v215\n\t"
    "ds_read_b128 v[8:11], v244 offset:448\n\t"
    "v_cvt_pk_bf16_f32 v102, v24, v25\n\t"
    "v_smfmac_f32_16x16x64_bf16 v[28:31], v[128:131], v[56:63], v210 abid:1\n\t"
    "s_mov_b64 vcc, s[18:19]\n\t"
    "ds_read_b128 v[64:67], v217 offset:13824\n\t"
    "v_cvt_pk_bf16_f32 v100, v20, v21\n\t"
    "v_smfmac_f32_16x16x64_bf16 v[28:31], v[148:151], v[40:47], v213\n\t"
    "ds_read_b32 v89, v104 offset:13828\n\t"
    "v_cvt_pk_bf16_f32 v101, v22, v23\n\t"
    "v_smfmac_f32_16x16x64_bf16 v[28:31], v[168:171], v[48:55], v215 abid:1\n\t"
    "s_waitcnt lgkmcnt(5)\n\t"
    "ds_read_b128 v[20:23], v244 offset:512\n\t"
    "v_mfma_f32_16x16x32_bf16 v[28:31], v[80:83], v[92:95], v[28:31]\n\t"
    "ds_read_b32 v94, v229 offset:13828\n\t"
    "v_cvt_pk_bf16_f32 v103, v26, v27\n\t"
    "v_smfmac_f32_16x16x64_bf16 v[32:35], v[132:135], v[56:63], v211\n\t"
    "ds_read_b128 v[56:59], v217 offset:13952\n\t"
    "ds_read_b128 v[60:63], v236 offset:13952\n\t"
    "v_smfmac_f32_16x16x64_bf16 v[32:35], v[152:155], v[40:47], v213 abid:1\n\t"
    "ds_read_b128 v[80:83], v245 offset:6144\n\t"
    "v_cvt_pk_bf16_f32 v96, v16, v17\n\t"
    "v_smfmac_f32_16x16x64_bf16 v[32:35], v[172:175], v[48:55], v216\n\t"
    "s_waitcnt lgkmcnt(6)\n\t"
    "ds_read_b128 v[44:47], v236 offset:14080\n\t"
    "v_cvt_pk_bf16_f32 v97, v18, v19\n\t"
    "v_smfmac_f32_16x16x64_bf16 v[36:39], a[144:147], v[64:71], v194\n\t"
    "s_waitcnt lgkmcnt(2)\n\t"
    "ds_read_b128 v[52:55], v236 offset:14208\n\t"
    "v_cvt_pk_bf16_f32 v98, v28, v29\n\t"
    "v_smfmac_f32_16x16x64_bf16 v[36:39], a[164:167], v[56:63], v196 abid:1\n\t"
    "ds_read_b128 v[48:51], v217 offset:14208\n\t"
    "v_cvt_pk_bf16_f32 v99, v30, v31\n\t"
    "v_smfmac_f32_16x16x64_bf16 v[4:7], a[148:151], v[64:71], v194 abid:1\n\t"
    "buffer_load_dwordx4 v238, s[40:43], s68 offen offset:1536 nt lds\n\t"
    "v_smfmac_f32_16x16x64_bf16 v[4:7], a[168:171], v[56:63], v197\n\t"
    "s_mov_b32 s77, 329776\n\t"
    "ds_read_b128 v[40:43], v217 offset:14080\n\t"
    "v_cndmask_b32_dpp v29, v97, v101, vcc quad_perm:[0,0,2,2] row_mask:0xf bank_mask:0xf\n\t"
    "v_smfmac_f32_16x16x64_bf16 v[12:15], a[152:155], v[64:71], v195\n\t"
    "s_lshl_b32 s80, s80, 2\n\t"
    "ds_read_b32 v93, v86 offset:13828\n\t"
    "v_cndmask_b32_dpp v28, v96, v100, vcc quad_perm:[0,0,2,2] row_mask:0xf bank_mask:0xf\n\t"
    "v_smfmac_f32_16x16x64_bf16 v[12:15], a[172:175], v[56:63], v197 abid:1\n\t"
    "s_waitcnt lgkmcnt(5)\n\t"
    "ds_read_b32 v95, v87 offset:13828\n\t"
    "v_cndmask_b32_dpp v30, v98, v102, vcc quad_perm:[0,0,2,2] row_mask:0xf bank_mask:0xf\n\t"
    "v_mfma_f32_16x16x32_bf16 v[12:15], v[80:83], v[88:91], v[12:15]\n\t"
    "s_memrealtime s[84:85]\n\t"
    "ds_read_b32 v92, v228 offset:13828\n\t"
    "v_cndmask_b32_dpp v31, v99, v103, vcc quad_perm:[0,0,2,2] row_mask:0xf bank_mask:0xf\n\t"
    "v_cvt_pk_bf16_f32 v32, v32, v33\n\t"
    "v_add_u32_sdwa v89, v248, v246 dst_sel:DWORD dst_unused:UNUSED_PAD src0_sel:DWORD src1_sel:WORD_1\n\t"
    "v_smfmac_f32_16x16x64_bf16 v[8:11], a[156:159], v[64:71], v195 abid:1\n\t"
    "s_not_b64 vcc, s[18:19]\n\t"
    "ds_read_u16 v89, v89 offset:13824\n\t"
    "v_add_u32_sdwa v90, v248, v247 dst_sel:DWORD dst_unused:UNUSED_PAD src0_sel:DWORD src1_sel:WORD_0\n\t"
    "v_smfmac_f32_16x16x64_bf16 v[8:11], a[176:179], v[56:63], v198\n\t"
    "buffer_store_dwordx4 v[28:31], v241, s[44:47], s65 offen offset:128 sc1\n\t"
    "ds_read_b128 v[80:83], v245 offset:5120\n\t"
    "v_cndmask_b32_dpp v96, v100, v96, vcc quad_perm:[1,1,3,3] row_mask:0xf bank_mask:0xf\n\t"
    "v_add_u32_sdwa v88, v248, v246 dst_sel:DWORD dst_unused:UNUSED_PAD src0_sel:DWORD src1_sel:WORD_0\n\t"
    "v_add_u32_sdwa v91, v248, v247 dst_sel:DWORD dst_unused:UNUSED_PAD src0_sel:DWORD src1_sel:WORD_1\n\t"
    "v_smfmac_f32_16x16x64_bf16 v[20:23], a[160:163], v[64:71], v196\n\t"
    "ds_read_b128 v[64:67], v217 offset:14336\n\t"
    "v_cndmask_b32_dpp v98, v102, v98, vcc quad_perm:[1,1,3,3] row_mask:0xf bank_mask:0xf\n\t"
    "v_smfmac_f32_16x16x64_bf16 v[20:23], a[180:183], v[56:63], v198 abid:1\n\t"
    "s_waitcnt lgkmcnt(6)\n\t"
    "ds_read_u16 v90, v90 offset:13824\n\t"
    "ds_read_u16 v91, v91 offset:13824\n\t"
    "v_smfmac_f32_16x16x64_bf16 v[36:39], a[184:187], v[40:47], v199\n\t"
    "s_sub_i32 s87, s87, s99\n\t"
    "ds_read_u16 v88, v88 offset:13824\n\t"
    "v_cndmask_b32_dpp v99, v103, v99, vcc quad_perm:[1,1,3,3] row_mask:0xf bank_mask:0xf\n\t"
    "v_smfmac_f32_16x16x64_bf16 v[36:39], a[204:207], v[48:55], v201 abid:1\n\t"
    "v_cndmask_b32_dpp v97, v101, v97, vcc quad_perm:[1,1,3,3] row_mask:0xf bank_mask:0xf\n\t"
    "v_smfmac_f32_16x16x64_bf16 v[4:7], a[188:191], v[40:47], v199 abid:1\n\t"
    "s_sub_i32 s98, s98, s99\n\t"
    "ds_read_b128 v[60:63], v236 offset:14464\n\t"
    "v_cvt_pk_bf16_f32 v33, v34, v35\n\t"
    "v_smfmac_f32_16x16x64_bf16 v[4:7], a[208:211], v[48:55], v202\n\t"
    "s_waitcnt lgkmcnt(5)\n\t"
    "s_add_i32 s86, s87, 1191\n\t"
    "buffer_store_dwordx4 v[96:99], v241, s[44:47], s65 offen offset:1280 sc1\n\t"
    "ds_read_b128 v[56:59], v217 offset:14464\n\t"
    "ds_write_b64 v242, v[32:33] offset:0\n\t"
    "v_mfma_f32_16x16x32_bf16 v[4:7], v[80:83], v[92:95], v[4:7]\n\t"
    "s_add_i32 s98, s98, 85\n\t"
    "ds_read_b128 v[68:71], v236 offset:14336\n\t"
    "v_smfmac_f32_16x16x64_bf16 v[12:15], a[192:195], v[40:47], v200\n\t"
    "s_add_i32 s89, s86, s23\n\t"
    "s_min_i32 s98, s98, s89\n\t"
    "ds_read_b32 v92, v226 offset:13828\n\t"
    "ds_read_b32 v95, v85 offset:13828\n\t"
    "v_smfmac_f32_16x16x64_bf16 v[12:15], a[212:215], v[48:55], v202 abid:1\n\t"
    "s_waitcnt lgkmcnt(6)\n\t"
    "s_max_i32 s86, s86, s98\n\t"
    "ds_read_b128 v[80:83], v245 offset:4096\n\t"
    "s_set_gpr_idx_on s80, gpr_idx(SRC2,DST)\n\t"
    "v_mfma_f32_16x16x32_bf16 v[0:3], v[250:253], v[88:91], v[0:3]\n\t"
    "s_set_gpr_idx_off\n\t"
    "s_add_u32 m0, s52, 4608\n\t"
    "s_waitcnt lgkmcnt(4)\n\t"
    "s_lshr_b64 s[80:81], s[76:77], s70\n\t"
    "s_sub_u32 s89, s84, s24\n\t"
    "s_barrier\n\t"
    "buffer_load_dwordx4 v239, s[40:43], s68 offen offset:2560 nt lds\n\t"
    "ds_read_b128 v[100:103], v243 offset:2048\n\t"
    "v_smfmac_f32_16x16x64_bf16 v[8:11], a[196:199], v[40:47], v200 abid:1\n\t"
    "s_cmp_lt_i32 s89, s86\n\t"
    "s_cbranch_scc1 .Lpc3_%=\n\t"
    ".Lpc3r_%=:\n\t"
    "ds_read_b32 v93, v84 offset:13828\n\t"
    "ds_read_b32 v94, v227 offset:13828\n\t"
    "v_smfmac_f32_16x16x64_bf16 v[8:11], a[216:219], v[48:55], v203\n\t"
    "s_and_b32 s80, s80, 63\n\t"
    "s_lshl_b32 s80, s80, 2\n\t"
    "s_set_gpr_idx_on s80, gpr_idx(SRC2,DST)\n\t"
    "v_mfma_f32_16x16x32_bf16 v[0:3], v[252:255], v[88:91], v[0:3]\n\t"
    "s_set_gpr_idx_off\n\t"
    "s_max_i32 s98, s89, s86\n\t"
    "s_cmp_lg_u32 s22, 0\n\t"
    "s_cbranch_scc1 .Lsl8_%=\n\t"
    ".Lslr8_%=:\n\t"
    "v_smfmac_f32_16x16x64_bf16 v[20:23], a[200:203], v[40:47], v201\n\t"
    "s_sub_i32 s23, s23, 3\n\t"
    "ds_read_b128 v[44:47], v236 offset:14592\n\t"
    "v_add_u32_sdwa v90, v248, v235 dst_sel:DWORD dst_unused:UNUSED_PAD src0_sel:DWORD src1_sel:WORD_0\n\t"
    "v_smfmac_f32_16x16x64_bf16 v[20:23], a[220:223], v[48:55], v203 abid:1\n\t"
    "s_waitcnt lgkmcnt(7)\n\t"
    "ds_read_b128 v[48:51], v217 offset:14720\n\t"
    "ds_read_b32 v90, v90 offset:13828\n\t"
    "v_smfmac_f32_16x16x64_bf16 v[36:39], a[224:227], v[64:71], v204\n\t"
    "ds_read_b128 v[40:43], v217 offset:14592\n\t"
    "v_smfmac_f32_16x16x64_bf16 v[36:39], a[244:247], v[56:63], v206 abid:1\n\t"
    "s_waitcnt lgkmcnt(4)\n\t"
    "s_mov_b32 s76, 100663296\n\t"
    "s_mov_b32 exec_hi, 0\n\t"
    "buffer_store_dwordx4 v[100:103], v240, s[44:47], s66 offen offset:0 sc1\n\t"
    "s_mov_b32 exec_hi, -1\n\t"
    "v_add_u32_sdwa v88, v248, v234 dst_sel:DWORD dst_unused:UNUSED_PAD src0_sel:DWORD src1_sel:WORD_0\n\t"
    "v_add_u32_sdwa v91, v248, v235 dst_sel:DWORD dst_unused:UNUSED_PAD src0_sel:DWORD src1_sel:WORD_1\n\t"
    "v_mfma_f32_16x16x32_bf16 v[36:39], v[80:83], v[92:95], v[36:39]\n\t"
    "ds_read_b128 v[52:55], v236 offset:14720\n\t"
    "v_add_u32_sdwa v89, v248, v234 dst_sel:DWORD dst_unused:UNUSED_PAD src0_sel:DWORD src1_sel:WORD_1\n\t"
    "v_smfmac_f32_16x16x64_bf16 v[4:7], a[228:231], v[64:71], v204 abid:1\n\t"
    "ds_read_b32 v88, v88 offset:13828\n\t"
    "ds_read_b32 v91, v91 offset:13828\n\t"
    "v_smfmac_f32_16x16x64_bf16 v[4:7], a[248:251], v[56:63], v207\n\t"
    "ds_read_b128 v[80:83], v245 offset:8192\n\t"
    "ds_read_b32 v89, v89 offset:13828\n\t"
    "v_smfmac_f32_16x16x64_bf16 v[12:15], a[232:235], v[64:71], v205\n\t"
    "s_add_u32 m0, s52, 4608\n\t"
    "s_nop 0\n\t"
    "buffer_load_dwordx4 v239, s[40:43], s68 offen offset:3584 nt lds\n\t"
    "v_smfmac_f32_16x16x64_bf16 v[12:15], a[252:255], v[56:63], v207 abid:1\n\t"
    "s_mov_b32 s67, 14155776\n\t"
    "s_bitcmp1_b32 s54, 8\n\t"
    "s_mov_b32 s68, 37748736\n\t"
    "s_cmov_b32 s68, 37822464\n\t"
    "v_smfmac_f32_16x16x64_bf16 v[8:11], a[236:239], v[64:71], v205 abid:1\n\t"
    "ds_read_b128 v[16:19], v244 offset:320\n\t"
    "v_smfmac_f32_16x16x64_bf16 v[8:11], v[108:111], v[56:63], v208\n\t"
    "ds_read_b128 v[24:27], v244 offset:256\n\t"
    "v_smfmac_f32_16x16x64_bf16 v[20:23], a[240:243], v[64:71], v206\n\t"
    "ds_read_b128 v[64:67], v217 offset:14848\n\t"
    "ds_read_b128 v[68:71], v236 offset:14848\n\t"
    "v_smfmac_f32_16x16x64_bf16 v[20:23], v[112:115], v[56:63], v208 abid:1\n\t"
    "s_waitcnt lgkmcnt(4)\n\t"
    "ds_read_b32 v92, v232 offset:13828\n\t"
    "v_mfma_f32_16x16x32_bf16 v[20:23], v[80:83], v[88:91], v[20:23]\n\t"
    "ds_read_b32 v93, v106 offset:13828\n\t"
    "v_smfmac_f32_16x16x64_bf16 v[36:39], v[116:119], v[40:47], v209\n\t"
    "s_mov_b64 vcc, s[18:19]\n\t"
    "s_mov_b32 s77, 597121\n\t"
    "s_lshr_b64 s[80:81], s[76:77], s69\n\t"
    "v_smfmac_f32_16x16x64_bf16 v[36:39], v[136:139], v[48:55], v211 abid:1\n\t"
    "s_waitcnt lgkmcnt(2)\n\t"
    "ds_read_b32 v94, v233 offset:13828\n\t"
    "ds_read_b32 v95, v107 offset:13828\n\t"
    "v_smfmac_f32_16x16x64_bf16 v[36:39], v[156:159], v[64:71], v214\n\t"
    "ds_read_b32 v90, v231 offset:18436\n\t"
    "v_smfmac_f32_16x16x64_bf16 v[4:7], v[120:123], v[40:47], v209 abid:1\n\t"
    "ds_read_b128 v[80:83], v245 offset:7168\n\t"
    "v_smfmac_f32_16x16x64_bf16 v[4:7], v[140:143], v[48:55], v212\n\t"
    "s_add_u32 m0, s52, 9216\n\t"
    "s_mov_b32 exec_hi, 0\n\t"
    "buffer_load_dwordx4 v237, s[40:43], s68 offen offset:0 nt lds\n\t"
    "s_mov_b32 exec_hi, -1\n\t"
    "v_smfmac_f32_16x16x64_bf16 v[4:7], v[160:163], v[64:71], v214 abid:1\n\t"
    "ds_read_b128 v[60:63], v236 offset:18432\n\t"
    "v_cvt_pk_bf16_f32 v96, v36, v37\n\t"
    "v_smfmac_f32_16x16x64_bf16 v[12:15], v[124:127], v[40:47], v210\n\t"
    "ds_read_b128 v[56:59], v217 offset:18432\n\t"
    "v_smfmac_f32_16x16x64_bf16 v[12:15], v[144:147], v[48:55], v212 abid:1\n\t"
    "ds_read_b128 v[28:31], v244 offset:448\n\t"
    "v_cvt_pk_bf16_f32 v97, v38, v39\n\t"
    "v_smfmac_f32_16x16x64_bf16 v[12:15], v[164:167], v[64:71], v215\n\t"
    "s_and_b32 s80, s80, 63\n\t"
    "ds_read_b128 v[36:39], v244 offset:512\n\t"
    "v_cvt_pk_bf16_f32 v98, v4, v5\n\t"
    "v_smfmac_f32_16x16x64_bf16 v[8:11], v[128:131], v[40:47], v210 abid:1\n\t"
    "ds_read_b128 v[32:35], v244 offset:384\n\t"
    "v_smfmac_f32_16x16x64_bf16 v[8:11], v[148:151], v[48:55], v213\n\t"
    "s_mov_b32 s77, 597121\n\t"
    "ds_read_b32 v91, v105 offset:18436\n\t"
    "v_cvt_pk_bf16_f32 v99, v6, v7\n\t"
    "v_smfmac_f32_16x16x64_bf16 v[8:11], v[168:171], v[64:71], v215 abid:1\n\t"
    "s_waitcnt lgkmcnt(6)\n\t"
    "ds_read_b32 v89, v104 offset:18436\n\t"
    "v_mfma_f32_16x16x32_bf16 v[8:11], v[80:83], v[92:95], v[8:11]\n\t"
    "ds_read_b32 v88, v230 offset:18436\n\t"
    "v_smfmac_f32_16x16x64_bf16 v[20:23], v[132:135], v[40:47], v211\n\t"
    "ds_read_b128 v[40:43], v217 offset:18560\n\t"
    "ds_read_b128 v[44:47], v236 offset:18560\n\t"
    "v_smfmac_f32_16x16x64_bf16 v[20:23], v[152:155], v[48:55], v213 abid:1\n\t"
    "ds_read_b128 v[80:83], v245 offset:6144\n\t"
    "v_cvt_pk_bf16_f32 v101, v14, v15\n\t"
    "v_smfmac_f32_16x16x64_bf16 v[20:23], v[172:175], v[64:71], v216\n\t"
    "s_waitcnt lgkmcnt(9)\n\t"
    "ds_read_b32 v95, v87 offset:18436\n\t"
    "v_cvt_pk_bf16_f32 v103, v10, v11\n\t"
    "v_smfmac_f32_16x16x64_bf16 v[24:27], a[144:147], v[56:63], v194\n\t"
    "s_waitcnt lgkmcnt(2)\n\t"
    "ds_read_b128 v[52:55], v236 offset:18688\n\t"
    "v_cvt_pk_bf16_f32 v102, v8, v9\n\t"
    "v_smfmac_f32_16x16x64_bf16 v[24:27], a[164:167], v[40:47], v196 abid:1\n\t"
    "ds_read_b128 v[48:51], v217 offset:18688\n\t"
    "v_smfmac_f32_16x16x64_bf16 v[16:19], a[148:151], v[56:63], v194 abid:1\n\t"
    "s_add_u32 s65, s57, s67\n\t"
    "ds_read_b128 v[64:67], v217 offset:18816\n\t"
    "v_cndmask_b32_dpp v9, v101, v97, vcc quad_perm:[0,0,2,2] row_mask:0xf bank_mask:0xf\n\t"
    "v_smfmac_f32_16x16x64_bf16 v[16:19], a[168:171], v[40:47], v197\n\t"
    "s_lshl_b32 s80, s80, 2\n\t"
    "ds_read_b32 v93, v86 offset:18436\n\t"
    "v_cvt_pk_bf16_f32 v100, v12, v13\n\t"
    "v_smfmac_f32_16x16x64_bf16 v[32:35], a[152:155], v[56:63], v195\n\t"
    "ds_read_b128 v[68:71], v236 offset:18816\n\t"
    "v_cndmask_b32_dpp v10, v102, v98, vcc quad_perm:[0,0,2,2] row_mask:0xf bank_mask:0xf\n\t"
    "v_smfmac_f32_16x16x64_bf16 v[32:35], a[172:175], v[40:47], v197 abid:1\n\t"
    "s_waitcnt lgkmcnt(6)\n\t"
    "s_mov_b32 s76, 100663296\n\t"
    "ds_read_b32 v92, v228 offset:18436\n\t"
    "v_cndmask_b32_dpp v11, v103, v99, vcc quad_perm:[0,0,2,2] row_mask:0xf bank_mask:0xf\n\t"
    "v_mfma_f32_16x16x32_bf16 v[32:35], v[80:83], v[88:91], v[32:35]\n\t"
    "s_memrealtime s[84:85]\n\t"
    "ds_read_b32 v94, v229 offset:18436\n\t"
    "v_add_u32_sdwa v88, v248, v246 dst_sel:DWORD dst_unused:UNUSED_PAD src0_sel:DWORD src1_sel:WORD_0\n\t"
    "v_add_u32_sdwa v90, v248, v247 dst_sel:DWORD dst_unused:UNUSED_PAD src0_sel:DWORD src1_sel:WORD_0\n\t"
    "v_add_u32_sdwa v91, v248, v247 dst_sel:DWORD dst_unused:UNUSED_PAD src0_sel:DWORD src1_sel:WORD_1\n\t"
    "v_smfmac_f32_16x16x64_bf16 v[28:31], a[156:159], v[56:63], v195 abid:1\n\t"
    "ds_read_u16 v90, v90 offset:18432\n\t"
    "v_cndmask_b32_dpp v8, v100, v96, vcc quad_perm:[0,0,2,2] row_mask:0xf bank_mask:0xf\n\t"
    "v_smfmac_f32_16x16x64_bf16 v[28:31], a[176:179], v[40:47], v198\n\t"
    "s_not_b64 vcc, s[18:19]\n\t"
    "buffer_store_dwordx4 v[8:11], v241, s[44:47], s67 offen offset:128 sc1\n\t"
    "ds_read_b128 v[80:83], v245 offset:5120\n\t"
    "v_cndmask_b32_dpp v100, v96, v100, vcc quad_perm:[1,1,3,3] row_mask:0xf bank_mask:0xf\n\t"
    "v_cvt_pk_bf16_f32 v20, v20, v21\n\t"
    "v_add_u32_sdwa v89, v248, v246 dst_sel:DWORD dst_unused:UNUSED_PAD src0_sel:DWORD src1_sel:WORD_1\n\t"
    "v_smfmac_f32_16x16x64_bf16 v[36:39], a[160:163], v[56:63], v196\n\t"
    "ds_read_u16 v89, v89 offset:18432\n\t"
    "ds_read_u16 v91, v91 offset:18432\n\t"
    "v_smfmac_f32_16x16x64_bf16 v[36:39], a[180:183], v[40:47], v198 abid:1\n\t"
    "s_waitcnt lgkmcnt(9)\n\t"
    "s_mov_b32 s66, 18874368\n\t"
    "ds_read_b128 v[60:63], v236 offset:18944\n\t"
    "v_cndmask_b32_dpp v101, v97, v101, vcc quad_perm:[1,1,3,3] row_mask:0xf bank_mask:0xf\n\t"
    "v_smfmac_f32_16x16x64_bf16 v[24:27], a[184:187], v[48:55], v199\n\t"
    "s_waitcnt lgkmcnt(7)\n\t"
    "s_sub_i32 s87, s87, s99\n\t"
    "ds_read_u16 v88, v88 offset:18432\n\t"
    "v_cndmask_b32_dpp v103, v99, v103, vcc quad_perm:[1,1,3,3] row_mask:0xf bank_mask:0xf\n\t"
    "v_smfmac_f32_16x16x64_bf16 v[24:27], a[204:207], v[64:71], v201 abid:1\n\t"
    "buffer_load_dwordx4 v237, s[40:43], s68 offen offset:512 nt lds\n\t"
    "v_smfmac_f32_16x16x64_bf16 v[16:19], a[188:191], v[48:55], v199 abid:1\n\t"
    "s_sub_i32 s98, s98, s99\n\t"
    "ds_read_b128 v[56:59], v217 offset:18944\n\t"
    "v_cndmask_b32_dpp v102, v98, v102, vcc quad_perm:[1,1,3,3] row_mask:0xf bank_mask:0xf\n\t"
    "v_smfmac_f32_16x16x64_bf16 v[16:19], a[208:211], v[64:71], v202\n\t"
    "s_waitcnt lgkmcnt(5)\n\t"
    "s_add_i32 s86, s87, 1275\n\t"
    "ds_read_b128 v[40:43], v217 offset:19072\n\t"
    "v_cvt_pk_bf16_f32 v21, v22, v23\n\t"
    "v_mfma_f32_16x16x32_bf16 v[16:19], v[80:83], v[92:95], v[16:19]\n\t"
    "s_add_i32 s98, s98, 81\n\t"
    "ds_write_b64 v242, v[20:21] offset:512\n\t"
    "v_smfmac_f32_16x16x64_bf16 v[32:35], a[192:195], v[48:55], v200\n\t"
    "s_add_i32 s89, s86, s23\n\t"
    "s_min_i32 s98, s98, s89\n\t"
    "buffer_store_dwordx4 v[100:103], v241, s[44:47], s67 offen offset:1280 sc1\n\t"
    "ds_read_b128 v[44:47], v236 offset:19072\n\t"
    "ds_read_b32 v94, v227 offset:18436\n\t"
    "ds_read_b32 v95, v85 offset:18436\n\t"
    "v_smfmac_f32_16x16x64_bf16 v[32:35], a[212:215], v[64:71], v202 abid:1\n\t"
    "s_waitcnt lgkmcnt(6)\n\t"
    "s_max_i32 s86, s86, s98\n\t"
    "ds_read_b128 v[80:83], v245 offset:4096\n\t"
    "s_set_gpr_idx_on s80, gpr_idx(SRC2,DST)\n\t"
    "v_mfma_f32_16x16x32_bf16 v[0:3], v[250:253], v[88:91], v[0:3]\n\t"
    "s_set_gpr_idx_off\n\t"
    "s_waitcnt lgkmcnt(4)\n\t"
    "s_sub_u32 s89, s84, s24\n\t"
    "s_waitcnt vmcnt(23)\n\t"
    "s_barrier\n\t"
    "ds_read_b128 v[96:99], v243 offset:2560\n\t"
    "v_smfmac_f32_16x16x64_bf16 v[28:31], a[196:199], v[48:55], v200 abid:1\n\t"
    "s_cmp_lt_i32 s89, s86\n\t"
    "s_cbranch_scc1 .Lpc4_%=\n\t"
    ".Lpc4r_%=:\n\t"
    "ds_read_b32 v92, v226 offset:18436\n\t"
    "ds_read_b32 v93, v84 offset:18436\n\t"
    "v_smfmac_f32_16x16x64_bf16 v[28:31], a[216:219], v[64:71], v203\n\t"
    "s_lshr_b64 s[80:81], s[76:77], s70\n\t"
    "s_and_b32 s80, s80, 63\n\t"
    "s_lshl_b32 s80, s80, 2\n\t"
    "s_set_gpr_idx_on s80, gpr_idx(SRC2,DST)\n\t"
    "v_mfma_f32_16x16x32_bf16 v[0:3], v[252:255], v[88:91], v[0:3]\n\t"
    "s_set_gpr_idx_off\n\t"
    "s_max_i32 s98, s89, s86\n\t"
    "s_cmp_lg_u32 s22, 0\n\t"
    "s_cbranch_scc1 .Lsl9_%=\n\t"
    ".Lslr9_%=:\n\t"
    "v_smfmac_f32_16x16x64_bf16 v[36:39], a[200:203], v[48:55], v201\n\t"
    "s_sub_i32 s23, s23, 3\n\t"
    "ds_read_b128 v[52:55], v236 offset:19200\n\t"
    "v_add_u32_sdwa v90, v248, v235 dst_sel:DWORD dst_unused:UNUSED_PAD src0_sel:DWORD src1_sel:WORD_0\n\t"
    "v_smfmac_f32_16x16x64_bf16 v[36:39], a[220:223], v[64:71], v203 abid:1\n\t"
    "ds_read_b128 v[48:51], v217 offset:19200\n\t"
    "v_add_u32_sdwa v91, v248, v235 dst_sel:DWORD dst_unused:UNUSED_PAD src0_sel:DWORD src1_sel:WORD_1\n\t"
    "v_smfmac_f32_16x16x64_bf16 v[24:27], a[224:227], v[56:63], v204\n\t"
    "s_waitcnt lgkmcnt(8)\n\t"
    "s_mov_b64 vcc, s[18:19]\n\t"
    "ds_read_b128 v[68:71], v236 offset:19328\n\t"
    "v_add_u32_sdwa v89, v248, v234 dst_sel:DWORD dst_unused:UNUSED_PAD src0_sel:DWORD src1_sel:WORD_1\n\t"
    "v_smfmac_f32_16x16x64_bf16 v[24:27], a[244:247], v[40:47], v206 abid:1\n\t"
    "s_waitcnt lgkmcnt(3)\n\t"
    "s_mov_b32 exec_hi, 0\n\t"
    "buffer_store_dwordx4 v[96:99], v240, s[44:47], s65 offen offset:0 sc1\n\t"
    "s_mov_b32 exec_hi, -1\n\t"
    "ds_read_b128 v[64:67], v217 offset:19328\n\t"
    "ds_read_b32 v89, v89 offset:18436\n\t"
    "v_mfma_f32_16x16x32_bf16 v[24:27], v[80:83], v[92:95], v[24:27]\n\t"
    "v_add_u32_sdwa v88, v248, v234 dst_sel:DWORD dst_unused:UNUSED_PAD src0_sel:DWORD src1_sel:WORD_0\n\t"
    "v_smfmac_f32_16x16x64_bf16 v[16:19], a[228:231], v[56:63], v204 abid:1\n\t"
    "ds_read_b32 v88, v88 offset:18436\n\t"
    "ds_read_b32 v91, v91 offset:18436\n\t"
    "v_smfmac_f32_16x16x64_bf16 v[16:19], a[248:251], v[40:47], v207\n\t"
    "s_mov_b32 s76, 101208257\n\t"
    "ds_read_b128 v[80:83], v245 offset:8192\n\t"
    "ds_read_b32 v90, v90 offset:18436\n\t"
    "v_smfmac_f32_16x16x64_bf16 v[32:35], a[232:235], v[56:63], v205\n\t"
    "s_add_u32 m0, s52, 9216\n\t"
    "s_nop 0\n\t"
    "buffer_load_dwordx4 v238, s[40:43], s68 offen offset:1536 nt lds\n\t"
    "v_smfmac_f32_16x16x64_bf16 v[32:35], a[252:255], v[40:47], v207 abid:1\n\t"
    "ds_read_b32 v94, v233 offset:18436\n\t"
    "v_smfmac_f32_16x16x64_bf16 v[28:31], a[236:239], v[56:63], v205 abid:1\n\t"
    "ds_read_b128 v[4:7], v244 offset:0\n\t"
    "v_smfmac_f32_16x16x64_bf16 v[28:31], v[108:111], v[40:47], v208\n\t"
    "v_smfmac_f32_16x16x64_bf16 v[36:39], a[240:243], v[56:63], v206\n\t"
    "ds_read_b128 v[56:59], v217 offset:19456\n\t"
    "ds_read_b128 v[60:63], v236 offset:19456\n\t"
    "v_smfmac_f32_16x16x64_bf16 v[36:39], v[112:115], v[40:47], v208 abid:1\n\t"
    "s_waitcnt lgkmcnt(4)\n\t"
    "ds_read_b32 v95, v107 offset:18436\n\t"
    "v_mfma_f32_16x16x32_bf16 v[36:39], v[80:83], v[88:91], v[36:39]\n\t"
    "ds_read_b32 v93, v106 offset:18436\n\t"
    "v_smfmac_f32_16x16x64_bf16 v[24:27], v[116:119], v[48:55], v209\n\t"
    "s_mov_b32 s77, 468097\n\t"
    "ds_read_b128 v[20:23], v244 offset:128\n\t"
    "v_smfmac_f32_16x16x64_bf16 v[24:27], v[136:139], v[64:71], v211 abid:1\n\t"
    "s_waitcnt lgkmcnt(3)\n\t"
    "ds_read_b128 v[12:15], v244 offset:64\n\t"
    "v_smfmac_f32_16x16x64_bf16 v[24:27], v[156:159], v[56:63], v214\n\t"
    "ds_read_b32 v92, v232 offset:18436\n\t"
    "v_smfmac_f32_16x16x64_bf16 v[16:19], v[120:123], v[48:55], v209 abid:1\n\t"
    "s_lshr_b64 s[80:81], s[76:77], s69\n\t"
    "ds_read_b128 v[80:83], v245 offset:7168\n\t"
    "v_smfmac_f32_16x16x64_bf16 v[16:19], v[140:143], v[64:71], v212\n\t"
    "ds_read_b32 v89, v76 offset:23044\n\t"
    "v_smfmac_f32_16x16x64_bf16 v[16:19], v[160:163], v[56:63], v214 abid:1\n\t"
    "ds_read_b32 v91, v77 offset:23044\n\t"
    "v_smfmac_f32_16x16x64_bf16 v[32:35], v[124:127], v[48:55], v210\n\t"
    "ds_read_b32 v88, v222 offset:23044\n\t"
    "v_cvt_pk_bf16_f32 v100, v24, v25\n\t"
    "v_smfmac_f32_16x16x64_bf16 v[32:35], v[144:147], v[64:71], v212 abid:1\n\t"
    "ds_read_b128 v[40:43], v217 offset:23040\n\t"
    "v_smfmac_f32_16x16x64_bf16 v[32:35], v[164:167], v[56:63], v215\n\t"
    "ds_read_b128 v[44:47], v236 offset:23040\n\t"
    "v_cvt_pk_bf16_f32 v102, v16, v17\n\t"
    "v_smfmac_f32_16x16x64_bf16 v[28:31], v[128:131], v[48:55], v210 abid:1\n\t"
    "ds_read_b128 v[8:11], v244 offset:192\n\t"
    "v_cvt_pk_bf16_f32 v101, v26, v27\n\t"
    "v_smfmac_f32_16x16x64_bf16 v[28:31], v[148:151], v[64:71], v213\n\t"
    "ds_read_b32 v90, v223 offset:23044\n\t"
    "v_cvt_pk_bf16_f32 v103, v18, v19\n\t"
    "v_smfmac_f32_16x16x64_bf16 v[28:31], v[168:171], v[56:63], v215 abid:1\n\t"
    "s_waitcnt lgkmcnt(7)\n\t"
    "buffer_load_dwordx4 v239, s[40:43], s68 offen offset:2560 nt lds\n\t"
    "v_mfma_f32_16x16x32_bf16 v[28:31], v[80:83], v[92:95], v[28:31]\n\t"
    "ds_read_b128 v[80:83], v245 offset:2048\n\t"
    "v_smfmac_f32_16x16x64_bf16 v[36:39], v[132:135], v[48:55], v211\n\t"
    "ds_read_b128 v[48:51], v217 offset:23168\n\t"
    "ds_read_b128 v[52:55], v236 offset:23168\n\t"
    "v_smfmac_f32_16x16x64_bf16 v[36:39], v[152:155], v[64:71], v213 abid:1\n\t"
    "ds_read_b32 v94, v229 offset:23044\n\t"
    "v_smfmac_f32_16x16x64_bf16 v[36:39], v[172:175], v[56:63], v216\n\t"
    "s_waitcnt lgkmcnt(6)\n\t"
    "ds_read_b128 v[16:19], v244 offset:320\n\t"
    "v_cvt_pk_bf16_f32 v98, v28, v29\n\t"
    "v_smfmac_f32_16x16x64_bf16 v[4:7], a[0:3], v[40:47], v176\n\t"
    "s_waitcnt lgkmcnt(2)\n\t"
    "ds_read_b128 v[60:63], v236 offset:23424\n\t"
    "v_cvt_pk_bf16_f32 v99, v30, v31\n\t"
    "v_smfmac_f32_16x16x64_bf16 v[4:7], a[16:19], v[48:55], v178\n\t"
    "s_and_b32 s80, s80, 63\n\t"
    "ds_read_b128 v[64:67], v217 offset:23296\n\t"
    "v_cvt_pk_bf16_f32 v96, v32, v33\n\t"
    "v_smfmac_f32_16x16x64_bf16 v[12:15], a[4:7], v[40:47], v176 abid:1\n\t"
    "ds_read_b128 v[68:71], v236 offset:23296\n\t"
    "v_cvt_pk_bf16_f32 v97, v34, v35\n\t"
    "v_smfmac_f32_16x16x64_bf16 v[12:15], a[20:23], v[48:55], v178 abid:1\n\t"
    "ds_read_b128 v[24:27], v244 offset:256\n\t"
    "v_cndmask_b32_dpp v30, v98, v102, vcc quad_perm:[0,0,2,2] row_mask:0xf bank_mask:0xf\n\t"
    "v_smfmac_f32_16x16x64_bf16 v[20:23], a[8:11], v[40:47], v177\n\t"
    "v_cndmask_b32_dpp v28, v96, v100, vcc quad_perm:[0,0,2,2] row_mask:0xf bank_mask:0xf\n\t"
    "v_smfmac_f32_16x16x64_bf16 v[20:23], a[24:27], v[48:55], v179\n\t"
    "ds_read_b128 v[32:35], v244 offset:384\n\t"
    "v_cndmask_b32_dpp v31, v99, v103, vcc quad_perm:[0,0,2,2] row_mask:0xf bank_mask:0xf\n\t"
    "v_mfma_f32_16x16x32_bf16 v[20:23], v[80:83], v[88:91], v[20:23]\n\t"
    "ds_read_b32 v95, v87 offset:23044\n\t"
    "v_add_u32_sdwa v90, v248, v235 dst_sel:DWORD dst_unused:UNUSED_PAD src0_sel:DWORD src1_sel:WORD_0\n\t"
    "v_smfmac_f32_16x16x64_bf16 v[8:11], a[12:15], v[40:47], v177 abid:1\n\t"
    "ds_read_b32 v92, v228 offset:23044\n\t"
    "v_cndmask_b32_dpp v29, v97, v101, vcc quad_perm:[0,0,2,2] row_mask:0xf bank_mask:0xf\n\t"
    "v_smfmac_f32_16x16x64_bf16 v[8:11], a[28:31], v[48:55], v179 abid:1\n\t"
    "s_waitcnt lgkmcnt(4)\n\t"
    "s_not_b64 vcc, s[18:19]\n\t"
    "buffer_store_dwordx4 v[28:31], v241, s[44:47], s66 offen offset:128 sc1\n\t"
    "ds_read_b128 v[80:83], v245 offset:5120\n\t"
    "v_cndmask_b32_dpp v96, v100, v96, vcc quad_perm:[1,1,3,3] row_mask:0xf bank_mask:0xf\n\t"
    "v_cndmask_b32_dpp v98, v102, v98, vcc quad_perm:[1,1,3,3] row_mask:0xf bank_mask:0xf\n\t"
    "v_smfmac_f32_16x16x64_bf16 v[8:11], a[44:47], v[64:71], v181 abid:1\n\t"
    "s_waitcnt lgkmcnt(4)\n\t"
    "ds_read_b32 v93, v86 offset:23044\n\t"
    "v_cvt_pk_bf16_f32 v36, v36, v37\n\t"
    "v_smfmac_f32_16x16x64_bf16 v[24:27], a[144:147], v[40:47], v194\n\t"
    "s_lshl_b32 s4, s80, 2\n\t"
    "ds_read_b32 v90, v90 offset:23044\n\t"
    "v_cndmask_b32_dpp v99, v103, v99, vcc quad_perm:[1,1,3,3] row_mask:0xf bank_mask:0xf\n\t"
    "v_smfmac_f32_16x16x64_bf16 v[24:27], a[164:167], v[48:55], v196 abid:1\n\t"
    "v_cvt_pk_bf16_f32 v37, v38, v39\n\t"
    "v_smfmac_f32_16x16x64_bf16 v[24:27], a[184:187], v[64:71], v199\n\t"
    "ds_write_b64 v242, v[36:37] offset:0\n\t"
    "v_smfmac_f32_16x16x64_bf16 v[16:19], a[148:151], v[40:47], v194 abid:1\n\t"
    "ds_read_b128 v[56:59], v217 offset:23424\n\t"
    "v_cndmask_b32_dpp v97, v101, v97, vcc quad_perm:[1,1,3,3] row_mask:0xf bank_mask:0xf\n\t"
    "v_smfmac_f32_16x16x64_bf16 v[16:19], a[168:171], v[48:55], v197\n\t"
    "buffer_store_dwordx4 v[96:99], v241, s[44:47], s66 offen offset:1280 sc1\n\t"
    "ds_read_b128 v[36:39], v244 offset:448\n\t"
    "v_add_u32_sdwa v89, v248, v234 dst_sel:DWORD dst_unused:UNUSED_PAD src0_sel:DWORD src1_sel:WORD_1\n\t"
    "v_smfmac_f32_16x16x64_bf16 v[16:19], a[188:191], v[64:71], v199 abid:1\n\t"
    "s_waitcnt lgkmcnt(4)\n\t"
    "v_mfma_f32_16x16x32_bf16 v[16:19], v[80:83], v[92:95], v[16:19]\n\t"
    "ds_read_b128 v[28:31], v244 offset:512\n\t"
    "v_add_u32_sdwa v91, v248, v235 dst_sel:DWORD dst_unused:UNUSED_PAD src0_sel:DWORD src1_sel:WORD_1\n\t"
    "v_smfmac_f32_16x16x64_bf16 v[32:35], a[152:155], v[40:47], v195\n\t"
    "ds_read_b32 v91, v91 offset:23044\n\t"
    "ds_read_b32 v94, v221 offset:23044\n\t"
    "v_smfmac_f32_16x16x64_bf16 v[32:35], a[172:175], v[48:55], v197 abid:1\n\t"
    "buffer_load_dwordx4 v239, s[40:43], s68 offen offset:3584 nt lds\n\t"
    "v_smfmac_f32_16x16x64_bf16 v[32:35], a[192:195], v[64:71], v200\n\t"
    "s_waitcnt lgkmcnt(4)\n\t"
    "ds_read_b128 v[80:83], v245 offset:8192\n\t"
    "v_add_u32_sdwa v88, v248, v234 dst_sel:DWORD dst_unused:UNUSED_PAD src0_sel:DWORD src1_sel:WORD_0\n\t"
    "v_smfmac_f32_16x16x64_bf16 v[32:35], a[212:215], v[56:63], v202 abid:1\n\t"
    "s_waitcnt lgkmcnt(4)\n\t"
    "s_bitcmp1_b32 s54, 9\n\t"
    "s_mov_b32 s68, 42467328\n\t"
    "s_cmov_b32 s68, 42541056\n\t"
    "v_smfmac_f32_16x16x64_bf16 v[36:39], a[156:159], v[40:47], v195 abid:1\n\t"
    "ds_read_b32 v88, v88 offset:23044\n\t"
    "ds_read_b32 v89, v89 offset:23044\n\t"
    "v_smfmac_f32_16x16x64_bf16 v[36:39], a[176:179], v[48:55], v198\n\t"
    "s_add_u32 m0, s52, 13824\n\t"
    "s_mov_b32 exec_hi, 0\n\t"
    "buffer_load_dwordx4 v237, s[40:43], s68 offen offset:0 nt lds\n\t"
    "s_mov_b32 exec_hi, -1\n\t"
    "v_smfmac_f32_16x16x64_bf16 v[36:39], a[196:199], v[64:71], v200 abid:1\n\t"
    "ds_read_b32 v93, v74 offset:23044\n\t"
    "ds_read_b32 v95, v75 offset:23044\n\t"
    "v_smfmac_f32_16x16x64_bf16 v[36:39], a[216:219], v[56:63], v203\n\t"
    "s_waitcnt lgkmcnt(7)\n\t"
    "s_lshr_b64 s[80:81], s[76:77], s70\n\t"
    "v_smfmac_f32_16x16x64_bf16 v[28:31], a[160:163], v[40:47], v196\n\t"
    "ds_read_b128 v[40:43], v217 offset:23552\n\t"
    "ds_read_b128 v[44:47], v236 offset:23552\n\t"
    "v_smfmac_f32_16x16x64_bf16 v[28:31], a[180:183], v[48:55], v198 abid:1\n\t"
    "buffer_load_dwordx4 v237, s[40:43], s68 offen offset:512 nt lds\n\t"
    "v_smfmac_f32_16x16x64_bf16 v[28:31], a[200:203], v[64:71], v201\n\t"
    "ds_read_b128 v[48:51], v217 offset:23680\n\t"
    "v_smfmac_f32_16x16x64_bf16 v[28:31], a[220:223], v[56:63], v203 abid:1\n\t"
    "s_waitcnt lgkmcnt(5)\n\t"
    "ds_read_b32 v92, v220 offset:23044\n\t"
    "v_mfma_f32_16x16x32_bf16 v[28:31], v[80:83], v[88:91], v[28:31]\n\t"
    "ds_read_b128 v[80:83], v245 offset:1024\n\t"
    "v_add_u32_sdwa v90, v248, v247 dst_sel:DWORD dst_unused:UNUSED_PAD src0_sel:DWORD src1_sel:WORD_0\n\t"
    "v_smfmac_f32_16x16x64_bf16 v[4:7], a[32:35], v[64:71], v180\n\t"
    "s_and_b32 s80, s80, 63\n\t"
    "ds_read_u16 v90, v90 offset:23040\n\t"
    "v_add_u32_sdwa v88, v248, v246 dst_sel:DWORD dst_unused:UNUSED_PAD src0_sel:DWORD src1_sel:WORD_0\n\t"
    "v_smfmac_f32_16x16x64_bf16 v[4:7], a[48:51], v[56:63], v182\n\t"
    "s_waitcnt lgkmcnt(4)\n\t"
    "ds_read_u16 v88, v88 offset:23040\n\t"
    "v_add_u32_sdwa v91, v248, v247 dst_sel:DWORD dst_unused:UNUSED_PAD src0_sel:DWORD src1_sel:WORD_1\n\t"
    "v_smfmac_f32_16x16x64_bf16 v[4:7], a[64:67], v[40:47], v184\n\t"
    "v_add_u32_sdwa v89, v248, v246 dst_sel:DWORD dst_unused:UNUSED_PAD src0_sel:DWORD src1_sel:WORD_1\n\t"
    "v_smfmac_f32_16x16x64_bf16 v[12:15], a[36:39], v[64:71], v180 abid:1\n\t"
    "s_waitcnt vmcnt(21)\n\t"
    "s_barrier\n\t"
    "ds_read_u16 v89, v89 offset:23040\n\t"
    "ds_read_u16 v91, v91 offset:23040\n\t"
    "v_smfmac_f32_16x16x64_bf16 v[12:15], a[52:55], v[56:63], v182 abid:1\n\t"
    "ds_read_b128 v[100:103], v243 offset:2048\n\t"
    "v_smfmac_f32_16x16x64_bf16 v[12:15], a[68:71], v[40:47], v184 abid:1\n\t"
    "s_waitcnt lgkmcnt(5)\n\t"
    "ds_read_b128 v[52:55], v236 offset:23680\n\t"
    "v_mfma_f32_16x16x32_bf16 v[12:15], v[80:83], v[92:95], v[12:15]\n\t"
    "s_mov_b64 vcc, s[18:19]\n\t"
    "v_smfmac_f32_16x16x64_bf16 v[20:23], a[40:43], v[64:71], v181\n\t"
    "ds_read_b32 v93, v84 offset:23044\n\t"
    "ds_read_b32 v95, v85 offset:23044\n\t"
    "v_smfmac_f32_16x16x64_bf16 v[20:23], a[56:59], v[56:63], v183\n\t"
    "s_add_u32 s67, s57, s66\n\t"
    "ds_read_b128 v[80:83], v245 offset:4096\n\t"
    "v_smfmac_f32_16x16x64_bf16 v[20:23], a[72:75], v[40:47], v185\n\t"
    "s_waitcnt lgkmcnt(5)\n\t"
    "ds_read_b128 v[64:67], v217 offset:23808\n\t"
    "s_set_gpr_idx_on s4, gpr_idx(SRC2,DST)\n\t"
    "v_mfma_f32_16x16x32_bf16 v[0:3], v[250:253], v[88:91], v[0:3]\n\t"
    "s_set_gpr_idx_off\n\t"
    "s_waitcnt lgkmcnt(5)\n\t"
    "s_mov_b32 exec_hi, 0\n\t"
    "buffer_store_dwordx4 v[100:103], v240, s[44:47], s67 offen offset:0 sc1\n\t"
    "s_mov_b32 exec_hi, -1\n\t"
    "v_smfmac_f32_16x16x64_bf16 v[8:11], a[60:63], v[56:63], v183 abid:1\n\t"
    "s_lshl_b32 s5, s80, 2\n\t"
    "ds_read_b32 v92, v226 offset:23044\n\t"
    "ds_read_b32 v94, v227 offset:23044\n\t"
    "v_smfmac_f32_16x16x64_bf16 v[8:11], a[76:79], v[40:47], v185 abid:1\n\t"
    "s_waitcnt lgkmcnt(6)\n\t"
    "ds_read_b128 v[68:71], v236 offset:23808\n\t"
    "v_smfmac_f32_16x16x64_bf16 v[8:11], a[92:95], v[48:55], v187 abid:1\n\t"
    "s_set_gpr_idx_on s5, gpr_idx(SRC2,DST)\n\t"
    "v_mfma_f32_16x16x32_bf16 v[0:3], v[252:255], v[88:91], v[0:3]\n\t"
    "s_set_gpr_idx_off\n\t"
    "s_add_u32 m0, s52, 13824\n\t"
    "s_nop 0\n\t"
    "buffer_load_dwordx4 v238, s[40:43], s68 offen offset:1536 nt lds\n\t"
    "s_cmp_lg_u32 s22, 0\n\t"
    "s_cbranch_scc1 .Lsl10_%=\n\t"
    ".Lslr10_%=:\n\t"
    "v_smfmac_f32_16x16x64_bf16 v[24:27], a[204:207], v[56:63], v201 abid:1\n\t"
    "ds_read_b32 v88, v232 offset:23044\n\t"
    "ds_read_b32 v89, v106 offset:23044\n\t"
    "v_smfmac_f32_16x16x64_bf16 v[24:27], a[224:227], v[40:47], v204\n\t"
    "s_memrealtime s[84:85]\n\t"
    "ds_read_b32 v90, v233 offset:23044\n\t"
    "v_smfmac_f32_16x16x64_bf16 v[24:27], a[244:247], v[48:55], v206 abid:1\n\t"
    "s_waitcnt lgkmcnt(4)\n\t"
    "v_mfma_f32_16x16x32_bf16 v[24:27], v[80:83], v[92:95], v[24:27]\n\t"
    "ds_read_b32 v91, v107 offset:23044\n\t"
    "v_smfmac_f32_16x16x64_bf16 v[16:19], a[208:211], v[56:63], v202\n\t"
    "ds_read_b128 v[80:83], v245 offset:7168\n\t"
    "v_smfmac_f32_16x16x64_bf16 v[16:19], a[228:231], v[40:47], v204 abid:1\n\t"
    "ds_read_b32 v94, v219 offset:23044\n\t"
    "v_smfmac_f32_16x16x64_bf16 v[16:19], a[248:251], v[48:55], v207\n\t"
    "s_sub_i32 s87, s87, s99\n\t"
    "s_cmp_lg_u32 s99, 0\n\t"
    "s_cbranch_scc1 .Lpsw_%=\n\t"
    ".Lpswr_%=:\n\t"
    "v_smfmac_f32_16x16x64_bf16 v[32:35], a[232:235], v[40:47], v205\n\t"
    "ds_read_b32 v92, v218 offset:23044\n\t"
    "v_smfmac_f32_16x16x64_bf16 v[32:35], a[252:255], v[48:55], v207 abid:1\n\t"
    "s_waitcnt lgkmcnt(7)\n\t"
    "s_sub_i32 s98, s98, s99\n\t"
    "ds_read_b32 v93, v72 offset:23044\n\t"
    "v_smfmac_f32_16x16x64_bf16 v[32:35], v[124:127], v[64:71], v210\n\t"
    "s_add_i32 s86, s87, 1409\n\t"
    "ds_read_b32 v95, v73 offset:23044\n\t"
    "v_smfmac_f32_16x16x64_bf16 v[36:39], a[236:239], v[40:47], v205 abid:1\n\t"
    "s_add_i32 s98, s98, 130\n\t"
    "ds_read_b128 v[56:59], v217 offset:23936\n\t"
    "v_smfmac_f32_16x16x64_bf16 v[36:39], v[108:111], v[48:55], v208\n\t"
    "s_add_i32 s89, s86, s23\n\t"
    "s_min_i32 s98, s98, s89\n\t"
    "v_smfmac_f32_16x16x64_bf16 v[36:39], v[128:131], v[64:71], v210 abid:1\n\t"
    "s_waitcnt lgkmcnt(5)\n\t"
    "s_max_i32 s86, s86, s98\n\t"
    "ds_read_b128 v[60:63], v236 offset:23936\n\t"
    "v_mfma_f32_16x16x32_bf16 v[36:39], v[80:83], v[88:91], v[36:39]\n\t"
    "s_sub_u32 s89, s84, s24\n\t"
    "ds_read_b128 v[80:83], v245 offset:0\n\t"
    "v_smfmac_f32_16x16x64_bf16 v[28:31], a[240:243], v[40:47], v206\n\t"
    "s_cmp_lt_i32 s89, s86\n\t"
    "s_cbranch_scc1 .Lpc5_%=\n\t"
    ".Lpc5r_%=:\n\t"
    "ds_read_b128 v[40:43], v217 offset:24064\n\t"
    "ds_read_b128 v[44:47], v236 offset:24064\n\t"
    "v_smfmac_f32_16x16x64_bf16 v[28:31], v[112:115], v[48:55], v208 abid:1\n\t"
    "v_smfmac_f32_16x16x64_bf16 v[28:31], v[132:135], v[64:71], v211\n\t"
    "s_bitcmp1_b32 s54, 5\n\t"
    "s_mov_b32 s65, 23592960\n\t"
    "s_cmov_b32 s65, 23666688\n\t"
    "s_max_i32 s98, s89, s86\n\t"
    "v_smfmac_f32_16x16x64_bf16 v[4:7], a[80:83], v[48:55], v186\n\t"
    "s_sub_i32 s23, s23, 3\n\t"
    "v_smfmac_f32_16x16x64_bf16 v[4:7], a[96:99], v[64:71], v188\n\t"
    "s_waitcnt lgkmcnt(3)\n\t"
    "ds_read_b32 v90, v225 offset:23044\n\t"
    "ds_read_b32 v91, v79 offset:23044\n\t"
    "v_smfmac_f32_16x16x64_bf16 v[4:7], a[112:115], v[56:63], v190\n\t"
    "s_add_u32 m0, s52, 13824\n\t"
    "s_waitcnt lgkmcnt(2)\n\t"
    "buffer_load_dwordx4 v239, s[40:43], s68 offen offset:2560 nt lds\n\t"
    "v_smfmac_f32_16x16x64_bf16 v[4:7], a[128:131], v[40:47], v192\n\t"
    "ds_read_b32 v89, v78 offset:23044\n\t"
    "v_mfma_f32_16x16x32_bf16 v[4:7], v[80:83], v[92:95], v[4:7]\n\t"
    "ds_read_b32 v93, v104 offset:23044\n\t"
    "v_smfmac_f32_16x16x64_bf16 v[12:15], a[84:87], v[48:55], v186 abid:1\n\t"
    "ds_read_b32 v88, v224 offset:23044\n\t"
    "v_smfmac_f32_16x16x64_bf16 v[12:15], a[100:103], v[64:71], v188 abid:1\n\t"
    "v_smfmac_f32_16x16x64_bf16 v[12:15], a[116:119], v[56:63], v190 abid:1\n\t"
    "ds_read_b128 v[80:83], v245 offset:3072\n\t"
    "v_cvt_pk_bf16_f32 v96, v4, v5\n\t"
    "v_smfmac_f32_16x16x64_bf16 v[12:15], a[132:135], v[40:47], v192 abid:1\n\t"
    "ds_read_b32 v95, v105 offset:23044\n\t"
    "v_smfmac_f32_16x16x64_bf16 v[20:23], a[88:91], v[48:55], v187\n\t"
    "ds_read_b128 v[52:55], v236 offset:0\n\t"
    "v_smfmac_f32_16x16x64_bf16 v[20:23], a[104:107], v[64:71], v189\n\t"
    "v_smfmac_f32_16x16x64_bf16 v[20:23], a[120:123], v[56:63], v191\n\t"
    "ds_read_b32 v94, v231 offset:23044\n\t"
    "v_cvt_pk_bf16_f32 v99, v14, v15\n\t"
    "v_smfmac_f32_16x16x64_bf16 v[20:23], a[136:139], v[40:47], v193\n\t"
    "ds_read_b128 v[48:51], v217 offset:0\n\t"
    "v_cvt_pk_bf16_f32 v98, v12, v13\n\t"
    "v_smfmac_f32_16x16x64_bf16 v[8:11], a[108:111], v[64:71], v189 abid:1\n\t"
    "ds_read_b32 v92, v230 offset:23044\n\t"
    "v_smfmac_f32_16x16x64_bf16 v[8:11], a[124:127], v[56:63], v191 abid:1\n\t"
    "v_smfmac_f32_16x16x64_bf16 v[8:11], a[140:143], v[40:47], v193 abid:1\n\t"
    "s_waitcnt lgkmcnt(5)\n\t"
    "ds_read_b128 v[12:15], v244 offset:64\n\t"
    "v_mfma_f32_16x16x32_bf16 v[8:11], v[80:83], v[88:91], v[8:11]\n\t"
    "ds_read_b32 v90, v223 offset:4\n\t"
    "v_smfmac_f32_16x16x64_bf16 v[24:27], v[116:119], v[64:71], v209\n\t"
    "ds_read_b128 v[80:83], v245 offset:6144\n\t"
    "v_cvt_pk_bf16_f32 v97, v6, v7\n\t"
    "v_smfmac_f32_16x16x64_bf16 v[24:27], v[136:139], v[56:63], v211 abid:1\n\t"
    "v_cvt_pk_bf16_f32 v101, v22, v23\n\t"
    "v_smfmac_f32_16x16x64_bf16 v[24:27], v[156:159], v[40:47], v214\n\t"
    "ds_read_b32 v89, v76 offset:4\n\t"
    "v_cvt_pk_bf16_f32 v103, v10, v11\n\t"
    "v_smfmac_f32_16x16x64_bf16 v[16:19], v[120:123], v[64:71], v209 abid:1\n\t"
    "ds_read_b128 v[4:7], v244 offset:0\n\t"
    "v_cvt_pk_bf16_f32 v100, v20, v21\n\t"
    "v_smfmac_f32_16x16x64_bf16 v[16:19], v[140:143], v[56:63], v212\n\t"
    "ds_read_b128 v[64:67], v217 offset:128\n\t"
    "v_cvt_pk_bf16_f32 v102, v8, v9\n\t"
    "v_smfmac_f32_16x16x64_bf16 v[16:19], v[160:163], v[40:47], v214 abid:1\n\t"
    "ds_read_b128 v[68:71], v236 offset:128\n\t"
    "v_cndmask_b32_dpp v9, v101, v97, vcc quad_perm:[0,0,2,2] row_mask:0xf bank_mask:0xf\n\t"
    "v_smfmac_f32_16x16x64_bf16 v[32:35], v[144:147], v[56:63], v212 abid:1\n\t"
    "ds_read_b32 v88, v222 offset:4\n\t"
    "v_cndmask_b32_dpp v10, v102, v98, vcc quad_perm:[0,0,2,2] row_mask:0xf bank_mask:0xf\n\t"
    "v_smfmac_f32_16x16x64_bf16 v[32:35], v[164:167], v[40:47], v215\n\t"
    "s_waitcnt lgkmcnt(5)\n\t"
    "ds_read_b128 v[20:23], v244 offset:128\n\t"
    "v_mfma_f32_16x16x32_bf16 v[32:35], v[80:83], v[92:95], v[32:35]\n\t"
    "v_cndmask_b32_dpp v11, v103, v99, vcc quad_perm:[0,0,2,2] row_mask:0xf bank_mask:0xf\n\t"
    "v_smfmac_f32_16x16x64_bf16 v[36:39], v[148:151], v[56:63], v213\n\t"
    "ds_read_b32 v93, v86 offset:4\n\t"
    "v_cndmask_b32_dpp v8, v100, v96, vcc quad_perm:[0,0,2,2] row_mask:0xf bank_mask:0xf\n\t"
    "v_smfmac_f32_16x16x64_bf16 v[36:39], v[168:171], v[40:47], v215 abid:1\n\t"
    "s_not_b64 vcc, s[18:19]\n\t"
    "ds_read_b32 v91, v77 offset:4\n\t"
    "v_cndmask_b32_dpp v103, v99, v103, vcc quad_perm:[1,1,3,3] row_mask:0xf bank_mask:0xf\n\t"
    "v_smfmac_f32_16x16x64_bf16 v[28:31], v[152:155], v[56:63], v213 abid:1\n\t"
    "buffer_store_dwordx4 v[8:11], v241, s[44:47], s65 offen offset:0 sc1\n\t"
    "ds_read_b128 v[80:83], v245 offset:2048\n\t"
    "v_cndmask_b32_dpp v101, v97, v101, vcc quad_perm:[1,1,3,3] row_mask:0xf bank_mask:0xf\n\t"
    "v_cndmask_b32_dpp v102, v98, v102, vcc quad_perm:[1,1,3,3] row_mask:0xf bank_mask:0xf\n\t"
    "v_cvt_pk_bf16_f32 v99, v18, v19\n\t"
    "v_smfmac_f32_16x16x64_bf16 v[28:31], v[172:175], v[40:47], v216\n\t"
    "s_waitcnt lgkmcnt(7)\n\t"
    "ds_read_b32 v92, v228 offset:4\n\t"
    "v_cndmask_b32_dpp v100, v96, v100, vcc quad_perm:[1,1,3,3] row_mask:0xf bank_mask:0xf\n\t"
    "v_smfmac_f32_16x16x64_bf16 v[4:7], a[0:3], v[48:55], v176\n\t"
    "s_waitcnt lgkmcnt(6)\n\t"
    "ds_read_b128 v[8:11], v244 offset:192\n\t"
    "v_cvt_pk_bf16_f32 v96, v24, v25\n\t"
    "v_smfmac_f32_16x16x64_bf16 v[4:7], a[16:19], v[64:71], v178\n\t"
    "s_mov_b64 vcc, s[18:19]\n\t"
    "ds_read_b128 v[60:63], v236 offset:256\n\t"
    "v_cvt_pk_bf16_f32 v97, v26, v27\n\t"
    "v_smfmac_f32_16x16x64_bf16 v[12:15], a[4:7], v[48:55], v176 abid:1\n\t"
    "ds_read_b128 v[56:59], v217 offset:256\n\t"
    "v_cvt_pk_bf16_f32 v98, v16, v17\n\t"
    "v_smfmac_f32_16x16x64_bf16 v[12:15], a[20:23], v[64:71], v178 abid:1\n\t"
    "s_waitcnt lgkmcnt(7)\n\t"
    "ds_read_b128 v[24:27], v244 offset:256\n\t"
    "v_smfmac_f32_16x16x64_bf16 v[20:23], a[8:11], v[48:55], v177\n\t"
    "buffer_store_dwordx4 v[100:103], v241, s[44:47], s65 offen offset:1152 sc1\n\t"
    "ds_read_b128 v[44:47], v236 offset:384\n\t"
    "v_cvt_pk_bf16_f32 v28, v28, v29\n\t"
    "v_smfmac_f32_16x16x64_bf16 v[20:23], a[24:27], v[64:71], v179\n\t"
    "s_waitcnt lgkmcnt(6)\n\t"
    "ds_read_b32 v95, v87 offset:4\n\t"
    "v_cvt_pk_bf16_f32 v101, v34, v35\n\t"
    "v_mfma_f32_16x16x32_bf16 v[20:23], v[80:83], v[88:91], v[20:23]\n\t"
    "s_waitcnt lgkmcnt(5)\n\t"
    "ds_read_b128 v[16:19], v244 offset:320\n\t"
    "v_cvt_pk_bf16_f32 v102, v36, v37\n\t"
    "v_add_u32_sdwa v89, v248, v234 dst_sel:DWORD dst_unused:UNUSED_PAD src0_sel:DWORD src1_sel:WORD_1\n\t"
    "v_smfmac_f32_16x16x64_bf16 v[8:11], a[12:15], v[48:55], v177 abid:1\n\t"
    "ds_read_b128 v[40:43], v217 offset:384\n\t"
    "v_cvt_pk_bf16_f32 v103, v38, v39\n\t"
    "v_smfmac_f32_16x16x64_bf16 v[8:11], a[28:31], v[64:71], v179 abid:1\n\t"
    "s_waitcnt lgkmcnt(5)\n\t"
    "buffer_load_dwordx4 v239, s[40:43], s68 offen offset:3584 nt lds\n\t"
    "ds_read_b32 v94, v229 offset:4\n\t"
    "v_cndmask_b32_dpp v37, v101, v97, vcc quad_perm:[0,0,2,2] row_mask:0xf bank_mask:0xf\n\t"
    "v_cndmask_b32_dpp v38, v102, v98, vcc quad_perm:[0,0,2,2] row_mask:0xf bank_mask:0xf\n\t"
    "v_cvt_pk_bf16_f32 v29, v30, v31\n\t"
    "v_add_u32_sdwa v91, v248, v235 dst_sel:DWORD dst_unused:UNUSED_PAD src0_sel:DWORD src1_sel:WORD_1\n\t"
    "v_smfmac_f32_16x16x64_bf16 v[8:11], a[44:47], v[56:63], v181 abid:1\n\t"
    "s_waitcnt lgkmcnt(5)\n\t"
    "ds_read_b128 v[80:83], v245 offset:5120\n\t"
    "v_cvt_pk_bf16_f32 v100, v32, v33\n\t"
    "v_smfmac_f32_16x16x64_bf16 v[24:27], a[144:147], v[48:55], v194\n\t"
    "ds_read_b128 v[32:35], v244 offset:384\n\t"
    "v_cndmask_b32_dpp v39, v103, v99, vcc quad_perm:[0,0,2,2] row_mask:0xf bank_mask:0xf\n\t"
    "v_smfmac_f32_16x16x64_bf16 v[24:27], a[164:167], v[64:71], v196 abid:1\n\t"
    "v_cndmask_b32_dpp v36, v100, v96, vcc quad_perm:[0,0,2,2] row_mask:0xf bank_mask:0xf\n\t"
    "v_smfmac_f32_16x16x64_bf16 v[24:27], a[184:187], v[56:63], v199\n\t"
    "s_waitcnt lgkmcnt(4)\n\t"
    "buffer_store_dwordx4 v[36:39], v241, s[44:47], s65 offen offset:128 sc1\n\t"
    "ds_write_b64 v242, v[28:29] offset:512\n\t"
    "v_add_u32_sdwa v90, v248, v235 dst_sel:DWORD dst_unused:UNUSED_PAD src0_sel:DWORD src1_sel:WORD_0\n\t"
    "v_smfmac_f32_16x16x64_bf16 v[16:19], a[148:151], v[48:55], v194 abid:1\n\t"
    "s_not_b64 vcc, s[18:19]\n\t"
    "ds_read_b32 v91, v91 offset:4\n\t"
    "v_cndmask_b32_dpp v102, v98, v102, vcc quad_perm:[1,1,3,3] row_mask:0xf bank_mask:0xf\n\t"
    "v_smfmac_f32_16x16x64_bf16 v[16:19], a[168:171], v[64:71], v197\n\t"
    "ds_read_b128 v[36:39], v244 offset:448\n\t"
    "v_cndmask_b32_dpp v100, v96, v100, vcc quad_perm:[1,1,3,3] row_mask:0xf bank_mask:0xf\n\t"
    "v_smfmac_f32_16x16x64_bf16 v[16:19], a[188:191], v[56:63], v199 abid:1\n\t"
    "s_waitcnt lgkmcnt(4)\n\t"
    "s_bitcmp1_b32 s54, 10\n\t"
    "s_mov_b32 s68, 47185920\n\t"
    "s_cmov_b32 s68, 47259648\n\t"
    "v_mfma_f32_16x16x32_bf16 v[16:19], v[80:83], v[92:95], v[16:19]\n\t"
    "s_waitcnt lgkmcnt(3)\n\t"
    "ds_read_b32 v89, v89 offset:4\n\t"
    "v_cndmask_b32_dpp v103, v99, v103, vcc quad_perm:[1,1,3,3] row_mask:0xf bank_mask:0xf\n\t"
    "v_smfmac_f32_16x16x64_bf16 v[32:35], a[152:155], v[48:55], v195\n\t"
    "ds_read_b128 v[28:31], v244 offset:512\n\t"
    "v_cndmask_b32_dpp v101, v97, v101, vcc quad_perm:[1,1,3,3] row_mask:0xf bank_mask:0xf\n\t"
    "v_smfmac_f32_16x16x64_bf16 v[32:35], a[172:175], v[64:71], v197 abid:1\n\t"
    "buffer_store_dwordx4 v[100:103], v241, s[44:47], s65 offen offset:1280 sc1\n\t"
    "ds_read_b128 v[80:83], v245 offset:8192\n\t"
    "v_smfmac_f32_16x16x64_bf16 v[32:35], a[192:195], v[56:63], v200\n\t"
    "v_add_u32_sdwa v88, v248, v234 dst_sel:DWORD dst_unused:UNUSED_PAD src0_sel:DWORD src1_sel:WORD_0\n\t"
    "v_smfmac_f32_16x16x64_bf16 v[32:35], a[212:215], v[40:47], v202 abid:1\n\t"
    "s_waitcnt lgkmcnt(3)\n\t"
    "s_add_u32 s66, s57, s65\n\t"
    "ds_read_b32 v88, v88 offset:4\n\t"
    "ds_read_b32 v90, v90 offset:4\n\t"
    "v_smfmac_f32_16x16x64_bf16 v[36:39], a[156:159], v[48:55], v195 abid:1\n\t"
    "ds_read_b32 v93, v74 offset:4\n\t"
    "v_smfmac_f32_16x16x64_bf16 v[36:39], a[176:179], v[64:71], v198\n\t"
    "s_add_u32 m0, s52, 18432\n\t"
    "s_mov_b32 exec_hi, 0\n\t"
    "buffer_load_dwordx4 v237, s[40:43], s68 offen offset:0 nt lds\n\t"
    "s_mov_b32 exec_hi, -1\n\t"
    "v_smfmac_f32_16x16x64_bf16 v[36:39], a[196:199], v[56:63], v200 abid:1\n\t"
    "ds_read_b32 v94, v221 offset:4\n\t"
    "v_smfmac_f32_16x16x64_bf16 v[36:39], a[216:219], v[40:47], v203\n\t"
    "s_waitcnt lgkmcnt(5)\n\t"
    "ds_read_b32 v92, v220 offset:4\n\t"
    "v_smfmac_f32_16x16x64_bf16 v[28:31], a[160:163], v[48:55], v196\n\t"
    "ds_read_b128 v[48:51], v217 offset:512\n\t"
    "ds_read_b128 v[52:55], v236 offset:512\n\t"
    "v_smfmac_f32_16x16x64_bf16 v[28:31], a[180:183], v[64:71], v198 abid:1\n\t"
    "v_smfmac_f32_16x16x64_bf16 v[28:31], a[200:203], v[56:63], v201\n\t"
    "s_mov_b64 vcc, s[18:19]\n\t"
    "ds_read_b32 v95, v75 offset:4\n\t"
    "v_smfmac_f32_16x16x64_bf16 v[28:31], a[220:223], v[40:47], v203 abid:1\n\t"
    "s_waitcnt lgkmcnt(6)\n\t"
    "ds_read_b128 v[68:71], v236 offset:640\n\t"
    "v_mfma_f32_16x16x32_bf16 v[28:31], v[80:83], v[88:91], v[28:31]\n\t"
    "ds_read_b128 v[80:83], v245 offset:1024\n\t"
    "v_add_u32_sdwa v90, v248, v247 dst_sel:DWORD dst_unused:UNUSED_PAD src0_sel:DWORD src1_sel:WORD_0\n\t"
    "v_smfmac_f32_16x16x64_bf16 v[4:7], a[32:35], v[56:63], v180\n\t"
    "v_add_u32_sdwa v91, v248, v247 dst_sel:DWORD dst_unused:UNUSED_PAD src0_sel:DWORD src1_sel:WORD_1\n\t"
    "v_smfmac_f32_16x16x64_bf16 v[4:7], a[48:51], v[40:47], v182\n\t"
    "s_waitcnt lgkmcnt(3)\n\t"
    "ds_read_u16 v90, v90 offset:0\n\t"
    "v_add_u32_sdwa v88, v248, v246 dst_sel:DWORD dst_unused:UNUSED_PAD src0_sel:DWORD src1_sel:WORD_0\n\t"
    "v_smfmac_f32_16x16x64_bf16 v[4:7], a[64:67], v[48:55], v184\n\t"
    "ds_read_u16 v88, v88 offset:0\n\t"
    "v_add_u32_sdwa v89, v248, v246 dst_sel:DWORD dst_unused:UNUSED_PAD src0_sel:DWORD src1_sel:WORD_1\n\t"
    "v_smfmac_f32_16x16x64_bf16 v[12:15], a[36:39], v[56:63], v180 abid:1\n\t"
    "s_waitcnt vmcnt(21)\n\t"
    "s_barrier\n\t"
    "ds_read_u16 v89, v89 offset:0\n\t"
    "ds_read_u16 v91, v91 offset:0\n\t"
    "v_smfmac_f32_16x16x64_bf16 v[12:15], a[52:55], v[40:47], v182 abid:1\n\t"
    "buffer_load_dwordx4 v237, s[40:43], s68 offen offset:512 nt lds\n\t"
    "v_smfmac_f32_16x16x64_bf16 v[12:15], a[68:71], v[48:55], v184 abid:1\n\t"
    "s_waitcnt lgkmcnt(4)\n\t"
    "ds_read_b128 v[96:99], v243 offset:2560\n\t"
    "v_mfma_f32_16x16x32_bf16 v[12:15], v[80:83], v[92:95], v[12:15]\n\t"
    "ds_read_b128 v[64:67], v217 offset:640\n\t"
    "v_smfmac_f32_16x16x64_bf16 v[20:23], a[40:43], v[56:63], v181\n\t"
    "ds_read_b128 v[80:83], v245 offset:4096\n\t"
    "v_smfmac_f32_16x16x64_bf16 v[20:23], a[56:59], v[40:47], v183\n\t"
    "ds_read_b32 v95, v85 offset:4\n\t"
    "v_smfmac_f32_16x16x64_bf16 v[20:23], a[72:75], v[48:55], v185\n\t"
    "s_waitcnt lgkmcnt(4)\n\t"
    "ds_read_b32 v94, v227 offset:4\n\t"
    "s_set_gpr_idx_on s4, gpr_idx(SRC2,DST)\n\t"
    "v_mfma_f32_16x16x32_bf16 v[0:3], v[250:253], v[88:91], v[0:3]\n\t"
    "s_set_gpr_idx_off\n\t"
    "s_waitcnt lgkmcnt(4)\n\t"
    "s_mov_b32 exec_hi, 0\n\t"
    "buffer_store_dwordx4 v[96:99], v240, s[44:47], s66 offen offset:0 sc1\n\t"
    "s_mov_b32 exec_hi, -1\n\t"
    "ds_read_b32 v93, v84 offset:4\n\t"
    "v_smfmac_f32_16x16x64_bf16 v[8:11], a[60:63], v[40:47], v183 abid:1\n\t"
    "ds_read_b32 v92, v226 offset:4\n\t"
    "v_smfmac_f32_16x16x64_bf16 v[8:11], a[76:79], v[48:55], v185 abid:1\n\t"
    "s_waitcnt lgkmcnt(5)\n\t"
    "ds_read_b128 v[56:59], v217 offset:768\n\t"
    "v_smfmac_f32_16x16x64_bf16 v[8:11], a[92:95], v[64:71], v187 abid:1\n\t"
    "ds_read_b128 v[60:63], v236 offset:768\n\t"
    "s_set_gpr_idx_on s5, gpr_idx(SRC2,DST)\n\t"
    "v_mfma_f32_16x16x32_bf16 v[0:3], v[252:255], v[88:91], v[0:3]\n\t"
    "s_set_gpr_idx_off\n\t"
    "s_cmp_lg_u32 s22, 0\n\t"
    "s_cbranch_scc1 .Lsl11_%=\n\t"
    ".Lslr11_%=:\n\t"
    "v_smfmac_f32_16x16x64_bf16 v[24:27], a[204:207], v[40:47], v201 abid:1\n\t"
    "ds_read_b32 v90, v233 offset:4\n\t"
    "v_smfmac_f32_16x16x64_bf16 v[24:27], a[224:227], v[48:55], v204\n\t"
    "s_memrealtime s[84:85]\n\t"
    "ds_read_b32 v88, v232 offset:4\n\t"
    "v_smfmac_f32_16x16x64_bf16 v[24:27], a[244:247], v[64:71], v206 abid:1\n\t"
    "s_waitcnt lgkmcnt(4)\n\t"
    "ds_read_b32 v91, v107 offset:4\n\t"
    "v_mfma_f32_16x16x32_bf16 v[24:27], v[80:83], v[92:95], v[24:27]\n\t"
    "ds_read_b32 v89, v106 offset:4\n\t"
    "v_smfmac_f32_16x16x64_bf16 v[16:19], a[208:211], v[40:47], v202\n\t"
    "s_add_u32 m0, s52, 18432\n\t"
    "s_nop 0\n\t"
    "buffer_load_dwordx4 v238, s[40:43], s68 offen offset:1536 nt lds\n\t"
    "v_smfmac_f32_16x16x64_bf16 v[16:19], a[228:231], v[48:55], v204 abid:1\n\t"
    "ds_read_b32 v94, v219 offset:4\n\t"
    "v_smfmac_f32_16x16x64_bf16 v[16:19], a[248:251], v[64:71], v207\n\t"
    "s_sub_i32 s87, s87, s99\n\t"
    "ds_read_b128 v[80:83], v245 offset:7168\n\t"
    "v_smfmac_f32_16x16x64_bf16 v[32:35], a[232:235], v[48:55], v205\n\t"
    "ds_read_b32 v92, v218 offset:4\n\t"
    "v_smfmac_f32_16x16x64_bf16 v[32:35], a[252:255], v[64:71], v207 abid:1\n\t"
    "s_waitcnt lgkmcnt(7)\n\t"
    "s_sub_i32 s98, s98, s99\n\t"
    "ds_read_b128 v[40:43], v217 offset:896\n\t"
    "v_smfmac_f32_16x16x64_bf16 v[32:35], v[124:127], v[56:63], v210\n\t"
    "s_add_i32 s86, s87, 1531\n\t"
    "ds_read_b32 v95, v73 offset:4\n\t"
    "v_smfmac_f32_16x16x64_bf16 v[36:39], a[236:239], v[48:55], v205 abid:1\n\t"
    "s_add_i32 s98, s98, 114\n\t"
    "ds_read_b32 v93, v72 offset:4\n\t"
    "v_smfmac_f32_16x16x64_bf16 v[36:39], v[108:111], v[64:71], v208\n\t"
    "s_bitcmp1_b32 s54, 6\n\t"
    "s_mov_b32 s67, 28311552\n\t"
    "s_cmov_b32 s67, 28385280\n\t"
    "s_add_i32 s89, s86, s23\n\t"
    "s_min_i32 s98, s98, s89\n\t"
    "v_smfmac_f32_16x16x64_bf16 v[36:39], v[128:131], v[56:63], v210 abid:1\n\t"
    "s_waitcnt lgkmcnt(4)\n\t"
    "s_max_i32 s86, s86, s98\n\t"
    "ds_read_b128 v[44:47], v236 offset:896\n\t"
    "v_mfma_f32_16x16x32_bf16 v[36:39], v[80:83], v[88:91], v[36:39]\n\t"
    "s_sub_u32 s89, s84, s24\n\t"
    "ds_read_b128 v[80:83], v245 offset:0\n\t"
    "v_smfmac_f32_16x16x64_bf16 v[28:31], a[240:243], v[48:55], v206\n\t"
    "s_cmp_lt_i32 s89, s86\n\t"
    "s_cbranch_scc1 .Lpc6_%=\n\t"
    ".Lpc6r_%=:\n\t"
    "ds_read_b128 v[48:51], v217 offset:1024\n\t"
    "ds_read_b128 v[52:55], v236 offset:1024\n\t"
    "v_smfmac_f32_16x16x64_bf16 v[28:31], v[112:115], v[64:71], v208 abid:1\n\t"
    "v_smfmac_f32_16x16x64_bf16 v[28:31], v[132:135], v[56:63], v211\n\t"
    "s_max_i32 s98, s89, s86\n\t"
    "v_smfmac_f32_16x16x64_bf16 v[4:7], a[80:83], v[64:71], v186\n\t"
    "s_sub_i32 s23, s23, 3\n\t"
    "ds_read_b32 v90, v225 offset:4\n\t"
    "v_smfmac_f32_16x16x64_bf16 v[4:7], a[96:99], v[56:63], v188\n\t"
    "s_waitcnt lgkmcnt(4)\n\t"
    "ds_read_b32 v88, v224 offset:4\n\t"
    "ds_read_b32 v91, v79 offset:4\n\t"
    "v_smfmac_f32_16x16x64_bf16 v[4:7], a[112:115], v[40:47], v190\n\t"
    "s_waitcnt lgkmcnt(3)\n\t"
    "buffer_load_dwordx4 v239, s[40:43], s68 offen offset:2560 nt lds\n\t"
    "v_smfmac_f32_16x16x64_bf16 v[4:7], a[128:131], v[48:55], v192\n\t"
    "v_mfma_f32_16x16x32_bf16 v[4:7], v[80:83], v[92:95], v[4:7]\n\t"
    "ds_read_b128 v[80:83], v245 offset:3072\n\t"
    "v_smfmac_f32_16x16x64_bf16 v[12:15], a[84:87], v[64:71], v186 abid:1\n\t"
    "s_add_u32 s65, s57, s67\n\t"
    "ds_read_b32 v92, v230 offset:4\n\t"
    "v_smfmac_f32_16x16x64_bf16 v[12:15], a[100:103], v[56:63], v188 abid:1\n\t"
    "ds_read_b32 v89, v78 offset:4\n\t"
    "v_smfmac_f32_16x16x64_bf16 v[12:15], a[116:119], v[40:47], v190 abid:1\n\t"
    "ds_read_b32 v93, v104 offset:4\n\t"
    "v_smfmac_f32_16x16x64_bf16 v[12:15], a[132:135], v[48:55], v192 abid:1\n\t"
    "v_smfmac_f32_16x16x64_bf16 v[20:23], a[88:91], v[64:71], v187\n\t"
    "ds_read_b32 v95, v105 offset:4\n\t"
    "v_cvt_pk_bf16_f32 v100, v4, v5\n\t"
    "v_smfmac_f32_16x16x64_bf16 v[20:23], a[104:107], v[56:63], v189\n\t"
    "ds_read_b32 v94, v231 offset:4\n\t"
    "v_cvt_pk_bf16_f32 v101, v6, v7\n\t"
    "v_smfmac_f32_16x16x64_bf16 v[20:23], a[120:123], v[40:47], v191\n\t"
    "v_smfmac_f32_16x16x64_bf16 v[20:23], a[136:139], v[48:55], v193\n\t"
    "ds_read_b128 v[4:7], v244 offset:0\n\t"
    "v_cvt_pk_bf16_f32 v103, v14, v15\n\t"
    "v_smfmac_f32_16x16x64_bf16 v[8:11], a[108:111], v[56:63], v189 abid:1\n\t"
    "ds_read_b128 v[68:71], v236 offset:4608\n\t"
    "v_smfmac_f32_16x16x64_bf16 v[8:11], a[124:127], v[40:47], v191 abid:1\n\t"
    "ds_read_b128 v[64:67], v217 offset:4608\n\t"
    "v_smfmac_f32_16x16x64_bf16 v[8:11], a[140:143], v[48:55], v193 abid:1\n\t"
    "s_waitcnt lgkmcnt(6)\n\t"
    "v_cvt_pk_bf16_f32 v102, v12, v13\n\t"
    "v_mfma_f32_16x16x32_bf16 v[8:11], v[80:83], v[88:91], v[8:11]\n\t"
    "ds_read_b32 v89, v76 offset:4612\n\t"
    "ds_read_b32 v90, v223 offset:4612\n\t"
    "v_smfmac_f32_16x16x64_bf16 v[24:27], v[116:119], v[56:63], v209\n\t"
    "v_cvt_pk_bf16_f32 v97, v22, v23\n\t"
    "v_smfmac_f32_16x16x64_bf16 v[24:27], v[136:139], v[40:47], v211 abid:1\n\t"
    "ds_read_b128 v[80:83], v245 offset:6144\n\t"
    "v_cvt_pk_bf16_f32 v96, v20, v21\n\t"
    "v_smfmac_f32_16x16x64_bf16 v[24:27], v[156:159], v[48:55], v214\n\t"
    "ds_read_b128 v[20:23], v244 offset:128\n\t"
    "v_cvt_pk_bf16_f32 v98, v8, v9\n\t"
    "v_cndmask_b32_dpp v9, v97, v101, vcc quad_perm:[0,0,2,2] row_mask:0xf bank_mask:0xf\n\t"
    "v_smfmac_f32_16x16x64_bf16 v[16:19], v[120:123], v[56:63], v209 abid:1\n\t"
    "ds_read_b128 v[56:59], v217 offset:4736\n\t"
    "v_cvt_pk_bf16_f32 v99, v10, v11\n\t"
    "v_smfmac_f32_16x16x64_bf16 v[16:19], v[140:143], v[40:47], v212\n\t"
    "ds_read_b128 v[60:63], v236 offset:4736\n\t"
    "v_cndmask_b32_dpp v8, v96, v100, vcc quad_perm:[0,0,2,2] row_mask:0xf bank_mask:0xf\n\t"
    "v_smfmac_f32_16x16x64_bf16 v[16:19], v[160:163], v[48:55], v214 abid:1\n\t"
    "ds_read_b32 v91, v77 offset:4612\n\t"
    "v_cndmask_b32_dpp v10, v98, v102, vcc quad_perm:[0,0,2,2] row_mask:0xf bank_mask:0xf\n\t"
    "v_smfmac_f32_16x16x64_bf16 v[32:35], v[144:147], v[40:47], v212 abid:1\n\t"
    "ds_read_b128 v[12:15], v244 offset:64\n\t"
    "v_cndmask_b32_dpp v11, v99, v103, vcc quad_perm:[0,0,2,2] row_mask:0xf bank_mask:0xf\n\t"
    "v_smfmac_f32_16x16x64_bf16 v[32:35], v[164:167], v[48:55], v215\n\t"
    "s_waitcnt lgkmcnt(5)\n\t"
    "s_not_b64 vcc, s[18:19]\n\t"
    "ds_read_b32 v88, v222 offset:4612\n\t"
    "v_cndmask_b32_dpp v99, v103, v99, vcc quad_perm:[1,1,3,3] row_mask:0xf bank_mask:0xf\n\t"
    "v_mfma_f32_16x16x32_bf16 v[32:35], v[80:83], v[92:95], v[32:35]\n\t"
    "ds_read_b32 v94, v229 offset:4612\n\t"
    "v_cndmask_b32_dpp v96, v100, v96, vcc quad_perm:[1,1,3,3] row_mask:0xf bank_mask:0xf\n\t"
    "v_smfmac_f32_16x16x64_bf16 v[36:39], v[148:151], v[40:47], v213\n\t"
    "ds_read_b128 v[80:83], v245 offset:2048\n\t"
    "v_smfmac_f32_16x16x64_bf16 v[36:39], v[168:171], v[48:55], v215 abid:1\n\t"
    "v_cndmask_b32_dpp v97, v101, v97, vcc quad_perm:[1,1,3,3] row_mask:0xf bank_mask:0xf\n\t"
    "v_smfmac_f32_16x16x64_bf16 v[28:31], v[152:155], v[40:47], v213 abid:1\n\t"
    "buffer_store_dwordx4 v[8:11], v241, s[44:47], s67 offen offset:0 sc1\n\t"
    "ds_read_b128 v[40:43], v217 offset:4864\n\t"
    "v_cndmask_b32_dpp v98, v102, v98, vcc quad_perm:[1,1,3,3] row_mask:0xf bank_mask:0xf\n\t"
    "v_cvt_pk_bf16_f32 v100, v24, v25\n\t"
    "v_cvt_pk_bf16_f32 v103, v18, v19\n\t"
    "v_smfmac_f32_16x16x64_bf16 v[28:31], v[172:175], v[48:55], v216\n\t"
    "ds_read_b128 v[8:11], v244 offset:192\n\t"
    "ds_read_b128 v[44:47], v236 offset:4864\n\t"
    "v_smfmac_f32_16x16x64_bf16 v[4:7], a[0:3], v[64:71], v176\n\t"
    "s_waitcnt lgkmcnt(8)\n\t"
    "ds_read_b128 v[52:55], v236 offset:4992\n\t"
    "v_smfmac_f32_16x16x64_bf16 v[4:7], a[16:19], v[56:63], v178\n\t"
    "s_waitcnt lgkmcnt(7)\n\t"
    "v_cvt_pk_bf16_f32 v101, v26, v27\n\t"
    "v_smfmac_f32_16x16x64_bf16 v[12:15], a[4:7], v[64:71], v176 abid:1\n\t"
    "ds_read_b128 v[24:27], v244 offset:256\n\t"
    "v_cvt_pk_bf16_f32 v28, v28, v29\n\t"
    "v_smfmac_f32_16x16x64_bf16 v[12:15], a[20:23], v[56:63], v178 abid:1\n\t"
    "ds_read_b32 v93, v86 offset:4612\n\t"
    "v_cvt_pk_bf16_f32 v29, v30, v31\n\t"
    "v_smfmac_f32_16x16x64_bf16 v[20:23], a[8:11], v[64:71], v177\n\t"
    "buffer_store_dwordx4 v[96:99], v241, s[44:47], s67 offen offset:1152 sc1\n\t"
    "ds_read_b32 v95, v87 offset:4612\n\t"
    "v_cvt_pk_bf16_f32 v102, v16, v17\n\t"
    "v_smfmac_f32_16x16x64_bf16 v[20:23], a[24:27], v[56:63], v179\n\t"
    "s_waitcnt lgkmcnt(7)\n\t"
    "ds_read_b128 v[48:51], v217 offset:4992\n\t"
    "v_cvt_pk_bf16_f32 v96, v32, v33\n\t"
    "v_mfma_f32_16x16x32_bf16 v[20:23], v[80:83], v[88:91], v[20:23]\n\t"
    "s_waitcnt lgkmcnt(6)\n\t"
    "ds_read_b128 v[16:19], v244 offset:320\n\t"
    "v_cvt_pk_bf16_f32 v97, v34, v35\n\t"
    "v_smfmac_f32_16x16x64_bf16 v[8:11], a[12:15], v[64:71], v177 abid:1\n\t"
    "s_mov_b64 vcc, s[18:19]\n\t"
    "ds_read_b32 v92, v228 offset:4612\n\t"
    "v_cvt_pk_bf16_f32 v98, v36, v37\n\t"
    "v_smfmac_f32_16x16x64_bf16 v[8:11], a[28:31], v[56:63], v179 abid:1\n\t"
    "s_waitcnt lgkmcnt(7)\n\t"
    "buffer_load_dwordx4 v239, s[40:43], s68 offen offset:3584 nt lds\n\t"
    "v_smfmac_f32_16x16x64_bf16 v[8:11], a[44:47], v[40:47], v181 abid:1\n\t"
    "s_waitcnt lgkmcnt(5)\n\t"
    "ds_read_b128 v[80:83], v245 offset:5120\n\t"
    "v_cvt_pk_bf16_f32 v99, v38, v39\n\t"
    "v_cndmask_b32_dpp v36, v96, v100, vcc quad_perm:[0,0,2,2] row_mask:0xf bank_mask:0xf\n\t"
    "v_cndmask_b32_dpp v37, v97, v101, vcc quad_perm:[0,0,2,2] row_mask:0xf bank_mask:0xf\n\t"
    "v_smfmac_f32_16x16x64_bf16 v[24:27], a[144:147], v[64:71], v194\n\t"
    "ds_read_b128 v[32:35], v244 offset:384\n\t"
    "v_cndmask_b32_dpp v38, v98, v102, vcc quad_perm:[0,0,2,2] row_mask:0xf bank_mask:0xf\n\t"
    "v_smfmac_f32_16x16x64_bf16 v[24:27], a[164:167], v[56:63], v196 abid:1\n\t"
    "v_cndmask_b32_dpp v39, v99, v103, vcc quad_perm:[0,0,2,2] row_mask:0xf bank_mask:0xf\n\t"
    "v_smfmac_f32_16x16x64_bf16 v[24:27], a[184:187], v[40:47], v199\n\t"
    "s_waitcnt lgkmcnt(3)\n\t"
    "s_not_b64 vcc, s[18:19]\n\t"
    "buffer_store_dwordx4 v[36:39], v241, s[44:47], s67 offen offset:128 sc1\n\t"
    "ds_write_b64 v242, v[28:29] offset:0\n\t"
    "v_add_u32_sdwa v88, v248, v234 dst_sel:DWORD dst_unused:UNUSED_PAD src0_sel:DWORD src1_sel:WORD_0\n\t"
    "v_add_u32_sdwa v89, v248, v234 dst_sel:DWORD dst_unused:UNUSED_PAD src0_sel:DWORD src1_sel:WORD_1\n\t"
    "v_smfmac_f32_16x16x64_bf16 v[16:19], a[148:151], v[64:71], v194 abid:1\n\t"
    "v_add_u32_sdwa v90, v248, v235 dst_sel:DWORD dst_unused:UNUSED_PAD src0_sel:DWORD src1_sel:WORD_0\n\t"
    "v_smfmac_f32_16x16x64_bf16 v[16:19], a[168:171], v[56:63], v197\n\t"
    "ds_read_b128 v[36:39], v244 offset:448\n\t"
    "v_cndmask_b32_dpp v96, v100, v96, vcc quad_perm:[1,1,3,3] row_mask:0xf bank_mask:0xf\n\t"
    "v_smfmac_f32_16x16x64_bf16 v[16:19], a[188:191], v[40:47], v199 abid:1\n\t"
    "s_waitcnt lgkmcnt(3)\n\t"
    "ds_read_b32 v90, v90 offset:4612\n\t"
    "v_cndmask_b32_dpp v98, v102, v98, vcc quad_perm:[1,1,3,3] row_mask:0xf bank_mask:0xf\n\t"
    "v_mfma_f32_16x16x32_bf16 v[16:19], v[80:83], v[92:95], v[16:19]\n\t"
    "s_waitcnt lgkmcnt(3)\n\t"
    "ds_read_b128 v[28:31], v244 offset:512\n\t"
    "v_cndmask_b32_dpp v99, v103, v99, vcc quad_perm:[1,1,3,3] row_mask:0xf bank_mask:0xf\n\t"
    "v_smfmac_f32_16x16x64_bf16 v[32:35], a[152:155], v[64:71], v195\n\t"
    "ds_read_b128 v[80:83], v245 offset:8192\n\t"
    "v_cndmask_b32_dpp v97, v101, v97, vcc quad_perm:[1,1,3,3] row_mask:0xf bank_mask:0xf\n\t"
    "v_smfmac_f32_16x16x64_bf16 v[32:35], a[172:175], v[56:63], v197 abid:1\n\t"
    "buffer_store_dwordx4 v[96:99], v241, s[44:47], s67 offen offset:1280 sc1\n\t"
    "ds_read_b32 v94, v221 offset:4612\n\t"
    "v_add_u32_sdwa v91, v248, v235 dst_sel:DWORD dst_unused:UNUSED_PAD src0_sel:DWORD src1_sel:WORD_1\n\t"
    "v_smfmac_f32_16x16x64_bf16 v[32:35], a[192:195], v[40:47], v200\n\t"
    "ds_read_b32 v88, v88 offset:4612\n\t"
    "v_smfmac_f32_16x16x64_bf16 v[32:35], a[212:215], v[48:55], v202 abid:1\n\t"
    "s_waitcnt lgkmcnt(5)\n\t"
    "s_bitcmp1_b32 s54, 11\n\t"
    "s_mov_b32 s68, 51904512\n\t"
    "s_cmov_b32 s68, 51978240\n\t"
    "v_smfmac_f32_16x16x64_bf16 v[36:39], a[156:159], v[64:71], v195 abid:1\n\t"
    "ds_read_b32 v89, v89 offset:4612\n\t"
    "ds_read_b32 v91, v91 offset:4612\n\t"
    "v_smfmac_f32_16x16x64_bf16 v[36:39], a[176:179], v[56:63], v198\n\t"
    "s_add_u32 m0, s52, 23040\n\t"
    "s_mov_b32 exec_hi, 0\n\t"
    "buffer_load_dwordx4 v237, s[40:43], s68 offen offset:0 nt lds\n\t"
    "s_mov_b32 exec_hi, -1\n\t"
    "v_smfmac_f32_16x16x64_bf16 v[36:39], a[196:199], v[40:47], v200 abid:1\n\t"
    "ds_read_b32 v92, v220 offset:4612\n\t"
    "v_smfmac_f32_16x16x64_bf16 v[36:39], a[216:219], v[48:55], v203\n\t"
    "s_waitcnt lgkmcnt(6)\n\t"
    "ds_read_b32 v93, v74 offset:4612\n\t"
    "v_smfmac_f32_16x16x64_bf16 v[28:31], a[160:163], v[64:71], v196\n\t"
    "ds_read_b128 v[64:67], v217 offset:5120\n\t"
    "ds_read_b128 v[68:71], v236 offset:5120\n\t"
    "v_smfmac_f32_16x16x64_bf16 v[28:31], a[180:183], v[56:63], v198 abid:1\n\t"
    "v_smfmac_f32_16x16x64_bf16 v[28:31], a[200:203], v[40:47], v201\n\t"
    "ds_read_b32 v95, v75 offset:4612\n\t"
    "v_smfmac_f32_16x16x64_bf16 v[28:31], a[220:223], v[48:55], v203 abid:1\n\t"
    "s_waitcnt lgkmcnt(5)\n\t"
    "ds_read_b128 v[60:63], v236 offset:5248\n\t"
    "v_mfma_f32_16x16x32_bf16 v[28:31], v[80:83], v[88:91], v[28:31]\n\t"
    "ds_read_b128 v[80:83], v245 offset:1024\n\t"
    "v_add_u32_sdwa v90, v248, v247 dst_sel:DWORD dst_unused:UNUSED_PAD src0_sel:DWORD src1_sel:WORD_0\n\t"
    "v_add_u32_sdwa v91, v248, v247 dst_sel:DWORD dst_unused:UNUSED_PAD src0_sel:DWORD src1_sel:WORD_1\n\t"
    "v_smfmac_f32_16x16x64_bf16 v[4:7], a[32:35], v[40:47], v180\n\t"
    "ds_read_u16 v90, v90 offset:4608\n\t"
    "v_add_u32_sdwa v89, v248, v246 dst_sel:DWORD dst_unused:UNUSED_PAD src0_sel:DWORD src1_sel:WORD_1\n\t"
    "v_smfmac_f32_16x16x64_bf16 v[4:7], a[48:51], v[48:55], v182\n\t"
    "s_waitcnt lgkmcnt(4)\n\t"
    "ds_read_u16 v89, v89 offset:4608\n\t"
    "v_add_u32_sdwa v88, v248, v246 dst_sel:DWORD dst_unused:UNUSED_PAD src0_sel:DWORD src1_sel:WORD_0\n\t"
    "v_smfmac_f32_16x16x64_bf16 v[4:7], a[64:67], v[64:71], v184\n\t"
    "ds_read_u16 v88, v88 offset:4608\n\t"
    "ds_read_u16 v91, v91 offset:4608\n\t"
    "v_smfmac_f32_16x16x64_bf16 v[12:15], a[36:39], v[40:47], v180 abid:1\n\t"
    "s_waitcnt vmcnt(21)\n\t"
    "s_barrier\n\t"
    "ds_read_b128 v[100:103], v243 offset:2048\n\t"
    "v_smfmac_f32_16x16x64_bf16 v[12:15], a[52:55], v[48:55], v182 abid:1\n\t"
    "buffer_load_dwordx4 v237, s[40:43], s68 offen offset:512 nt lds\n\t"
    "v_smfmac_f32_16x16x64_bf16 v[12:15], a[68:71], v[64:71], v184 abid:1\n\t"
    "s_waitcnt lgkmcnt(5)\n\t"
    "v_mfma_f32_16x16x32_bf16 v[12:15], v[80:83], v[92:95], v[12:15]\n\t"
    "ds_read_b128 v[56:59], v217 offset:5248\n\t"
    "v_smfmac_f32_16x16x64_bf16 v[20:23], a[40:43], v[40:47], v181\n\t"
    "ds_read_b32 v93, v84 offset:4612\n\t"
    "ds_read_b32 v95, v85 offset:4612\n\t"
    "v_smfmac_f32_16x16x64_bf16 v[20:23], a[56:59], v[48:55], v183\n\t"
    "ds_read_b32 v94, v227 offset:4612\n\t"
    "v_smfmac_f32_16x16x64_bf16 v[20:23], a[72:75], v[64:71], v185\n\t"
    "s_waitcnt lgkmcnt(5)\n\t"
    "s_set_gpr_idx_on s4, gpr_idx(SRC2,DST)\n\t"
    "v_mfma_f32_16x16x32_bf16 v[0:3], v[250:253], v[88:91], v[0:3]\n\t"
    "s_set_gpr_idx_off\n\t"
    "s_waitcnt lgkmcnt(4)\n\t"
    "s_mov_b32 exec_hi, 0\n\t"
    "buffer_store_dwordx4 v[100:103], v240, s[44:47], s65 offen offset:0 sc1\n\t"
    "s_mov_b32 exec_hi, -1\n\t"
    "ds_read_b32 v92, v226 offset:4612\n\t"
    "v_smfmac_f32_16x16x64_bf16 v[8:11], a[60:63], v[48:55], v183 abid:1\n\t"
    "ds_read_b128 v[80:83], v245 offset:4096\n\t"
    "v_smfmac_f32_16x16x64_bf16 v[8:11], a[76:79], v[64:71], v185 abid:1\n\t"
    "s_waitcnt lgkmcnt(5)\n\t"
    "ds_read_b128 v[44:47], v236 offset:5376\n\t"
    "v_smfmac_f32_16x16x64_bf16 v[8:11], a[92:95], v[56:63], v187 abid:1\n\t"
    "ds_read_b128 v[40:43], v217 offset:5376\n\t"
    "s_set_gpr_idx_on s5, gpr_idx(SRC2,DST)\n\t"
    "v_mfma_f32_16x16x32_bf16 v[0:3], v[252:255], v[88:91], v[0:3]\n\t"
    "s_set_gpr_idx_off\n\t"
    "s_cmp_lg_u32 s22, 0\n\t"
    "s_cbranch_scc1 .Lsl12_%=\n\t"
    ".Lslr12_%=:\n\t"
    "v_smfmac_f32_16x16x64_bf16 v[24:27], a[204:207], v[48:55], v201 abid:1\n\t"
    "ds_read_b32 v89, v106 offset:4612\n\t"
    "v_smfmac_f32_16x16x64_bf16 v[24:27], a[224:227], v[64:71], v204\n\t"
    "s_memrealtime s[84:85]\n\t"
    "ds_read_b32 v90, v233 offset:4612\n\t"
    "ds_read_b32 v91, v107 offset:4612\n\t"
    "v_smfmac_f32_16x16x64_bf16 v[24:27], a[244:247], v[56:63], v206 abid:1\n\t"
    "s_waitcnt lgkmcnt(5)\n\t"
    "v_mfma_f32_16x16x32_bf16 v[24:27], v[80:83], v[92:95], v[24:27]\n\t"
    "ds_read_b128 v[80:83], v245 offset:7168\n\t"
    "v_smfmac_f32_16x16x64_bf16 v[16:19], a[208:211], v[48:55], v202\n\t"
    "s_add_u32 m0, s52, 23040\n\t"
    "s_nop 0\n\t"
    "buffer_load_dwordx4 v238, s[40:43], s68 offen offset:1536 nt lds\n\t"
    "v_smfmac_f32_16x16x64_bf16 v[16:19], a[228:231], v[64:71], v204 abid:1\n\t"
    "ds_read_b32 v88, v232 offset:4612\n\t"
    "ds_read_b32 v95, v73 offset:4612\n\t"
    "v_smfmac_f32_16x16x64_bf16 v[16:19], a[248:251], v[56:63], v207\n\t"
    "s_sub_i32 s87, s87, s99\n\t"
    "v_smfmac_f32_16x16x64_bf16 v[32:35], a[232:235], v[64:71], v205\n\t"
    "ds_read_b32 v93, v72 offset:4612\n\t"
    "v_smfmac_f32_16x16x64_bf16 v[32:35], a[252:255], v[56:63], v207 abid:1\n\t"
    "s_waitcnt lgkmcnt(7)\n\t"
    "s_sub_i32 s98, s98, s99\n\t"
    "ds_read_b32 v94, v219 offset:4612\n\t"
    "v_smfmac_f32_16x16x64_bf16 v[32:35], v[124:127], v[40:47], v210\n\t"
    "s_add_i32 s86, s87, 1653\n\t"
    "ds_read_b128 v[52:55], v236 offset:5504\n\t"
    "v_smfmac_f32_16x16x64_bf16 v[36:39], a[236:239], v[64:71], v205 abid:1\n\t"
    "s_add_i32 s98, s98, 72\n\t"
    "ds_read_b32 v92, v218 offset:4612\n\t"
    "v_smfmac_f32_16x16x64_bf16 v[36:39], v[108:111], v[56:63], v208\n\t"
    "s_max_i32 s86, s86, s98\n\t"
    "ds_read_b128 v[48:51], v217 offset:5504\n\t"
    "v_smfmac_f32_16x16x64_bf16 v[36:39], v[128:131], v[40:47], v210 abid:1\n\t"
    "s_waitcnt lgkmcnt(6)\n\t"
    "v_mfma_f32_16x16x32_bf16 v[36:39], v[80:83], v[88:91], v[36:39]\n\t"
    "s_sub_u32 s89, s84, s24\n\t"
    "ds_read_b128 v[80:83], v245 offset:0\n\t"
    "v_smfmac_f32_16x16x64_bf16 v[28:31], a[240:243], v[64:71], v206\n\t"
    "s_cmp_lt_i32 s89, s86\n\t"
    "s_cbranch_scc1 .Lpc7_%=\n\t"
    ".Lpc7r_%=:\n\t"
    "ds_read_b128 v[64:67], v217 offset:5632\n\t"
    "ds_read_b128 v[68:71], v236 offset:5632\n\t"
    "v_smfmac_f32_16x16x64_bf16 v[28:31], v[112:115], v[56:63], v208 abid:1\n\t"
    "v_smfmac_f32_16x16x64_bf16 v[28:31], v[132:135], v[40:47], v211\n\t"
    "s_bitcmp1_b32 s54, 7\n\t"
    "s_mov_b32 s66, 33030144\n\t"
    "s_cmov_b32 s66, 33103872\n\t"
    "s_max_i32 s98, s89, s86\n\t"
    "v_smfmac_f32_16x16x64_bf16 v[4:7], a[80:83], v[56:63], v186\n\t"
    "ds_read_b32 v88, v224 offset:4612\n\t"
    "v_smfmac_f32_16x16x64_bf16 v[4:7], a[96:99], v[40:47], v188\n\t"
    "s_waitcnt lgkmcnt(4)\n\t"
    "ds_read_b32 v90, v225 offset:4612\n\t"
    "v_smfmac_f32_16x16x64_bf16 v[4:7], a[112:115], v[48:55], v190\n\t"
    "s_waitcnt lgkmcnt(2)\n\t"
    "buffer_load_dwordx4 v239, s[40:43], s68 offen offset:2560 nt lds\n\t"
    "v_smfmac_f32_16x16x64_bf16 v[4:7], a[128:131], v[64:71], v192\n\t"
    "ds_read_b32 v91, v79 offset:4612\n\t"
    "v_mfma_f32_16x16x32_bf16 v[4:7], v[80:83], v[92:95], v[4:7]\n\t"
    "ds_read_b128 v[80:83], v245 offset:3072\n\t"
    "v_smfmac_f32_16x16x64_bf16 v[12:15], a[84:87], v[56:63], v186 abid:1\n\t"
    "ds_read_b32 v92, v230 offset:4612\n\t"
    "v_smfmac_f32_16x16x64_bf16 v[12:15], a[100:103], v[40:47], v188 abid:1\n\t"
    "v_smfmac_f32_16x16x64_bf16 v[12:15], a[116:119], v[48:55], v190 abid:1\n\t"
    "ds_read_b32 v93, v104 offset:4612\n\t"
    "v_smfmac_f32_16x16x64_bf16 v[12:15], a[132:135], v[64:71], v192 abid:1\n\t"
    "s_mov_b64 vcc, s[18:19]\n\t"
    "ds_read_b32 v89, v78 offset:4612\n\t"
    "v_smfmac_f32_16x16x64_bf16 v[20:23], a[88:91], v[56:63], v187\n\t"
    "ds_read_b128 v[60:63], v236 offset:9216\n\t"
    "v_cvt_pk_bf16_f32 v96, v4, v5\n\t"
    "v_smfmac_f32_16x16x64_bf16 v[20:23], a[104:107], v[40:47], v189\n\t"
    "v_cvt_pk_bf16_f32 v97, v6, v7\n\t"
    "v_smfmac_f32_16x16x64_bf16 v[20:23], a[120:123], v[48:55], v191\n\t"
    "ds_read_b128 v[56:59], v217 offset:9216\n\t"
    "v_smfmac_f32_16x16x64_bf16 v[20:23], a[136:139], v[64:71], v193\n\t"
    "ds_read_b32 v95, v105 offset:4612\n\t"
    "v_smfmac_f32_16x16x64_bf16 v[8:11], a[108:111], v[40:47], v189 abid:1\n\t"
    "ds_read_b32 v94, v231 offset:4612\n\t"
    "v_cvt_pk_bf16_f32 v98, v12, v13\n\t"
    "v_smfmac_f32_16x16x64_bf16 v[8:11], a[124:127], v[48:55], v191 abid:1\n\t"
    "ds_read_b128 v[4:7], v244 offset:0\n\t"
    "v_cvt_pk_bf16_f32 v99, v14, v15\n\t"
    "v_smfmac_f32_16x16x64_bf16 v[8:11], a[140:143], v[64:71], v193 abid:1\n\t"
    "s_waitcnt lgkmcnt(5)\n\t"
    "ds_read_b128 v[12:15], v244 offset:64\n\t"
    "v_cvt_pk_bf16_f32 v100, v20, v21\n\t"
    "v_mfma_f32_16x16x32_bf16 v[8:11], v[80:83], v[88:91], v[8:11]\n\t"
    "ds_read_b128 v[80:83], v245 offset:6144\n\t"
    "v_smfmac_f32_16x16x64_bf16 v[24:27], v[116:119], v[40:47], v209\n\t"
    "v_smfmac_f32_16x16x64_bf16 v[24:27], v[136:139], v[48:55], v211 abid:1\n\t"
    "ds_read_b32 v90, v223 offset:9220\n\t"
    "v_cvt_pk_bf16_f32 v101, v22, v23\n\t"
    "v_smfmac_f32_16x16x64_bf16 v[24:27], v[156:159], v[64:71], v214\n\t"
    "s_add_u32 s67, s57, s66\n\t"
    "ds_read_b128 v[20:23], v244 offset:128\n\t"
    "v_smfmac_f32_16x16x64_bf16 v[16:19], v[120:123], v[40:47], v209 abid:1\n\t"
    "ds_read_b128 v[40:43], v217 offset:9344\n\t"
    "v_cvt_pk_bf16_f32 v102, v8, v9\n\t"
    "v_smfmac_f32_16x16x64_bf16 v[16:19], v[140:143], v[48:55], v212\n\t"
    "ds_read_b32 v89, v76 offset:9220\n\t"
    "v_cvt_pk_bf16_f32 v103, v10, v11\n\t"
    "v_smfmac_f32_16x16x64_bf16 v[16:19], v[160:163], v[64:71], v214 abid:1\n\t"
    "v_cndmask_b32_dpp v8, v100, v96, vcc quad_perm:[0,0,2,2] row_mask:0xf bank_mask:0xf\n\t"
    "v_smfmac_f32_16x16x64_bf16 v[32:35], v[144:147], v[48:55], v212 abid:1\n\t"
    "ds_read_b32 v91, v77 offset:9220\n\t"
    "v_cndmask_b32_dpp v9, v101, v97, vcc quad_perm:[0,0,2,2] row_mask:0xf bank_mask:0xf\n\t"
    "v_smfmac_f32_16x16x64_bf16 v[32:35], v[164:167], v[64:71], v215\n\t"
    "s_waitcnt lgkmcnt(5)\n\t"
    "ds_read_b128 v[44:47], v236 offset:9344\n\t"
    "v_cndmask_b32_dpp v11, v103, v99, vcc quad_perm:[0,0,2,2] row_mask:0xf bank_mask:0xf\n\t"
    "v_mfma_f32_16x16x32_bf16 v[32:35], v[80:83], v[92:95], v[32:35]\n\t"
    "ds_read_b32 v88, v222 offset:9220\n\t"
    "v_cndmask_b32_dpp v10, v102, v98, vcc quad_perm:[0,0,2,2] row_mask:0xf bank_mask:0xf\n\t"
    "v_smfmac_f32_16x16x64_bf16 v[36:39], v[148:151], v[48:55], v213\n\t"
    "ds_read_b128 v[80:83], v245 offset:2048\n\t"
    "v_smfmac_f32_16x16x64_bf16 v[36:39], v[168:171], v[64:71], v215 abid:1\n\t"
    "s_not_b64 vcc, s[18:19]\n\t"
    "ds_read_b32 v92, v228 offset:9220\n\t"
    "v_cndmask_b32_dpp v101, v97, v101, vcc quad_perm:[1,1,3,3] row_mask:0xf bank_mask:0xf\n\t"
    "v_smfmac_f32_16x16x64_bf16 v[28:31], v[152:155], v[48:55], v213 abid:1\n\t"
    "buffer_store_dwordx4 v[8:11], v241, s[44:47], s66 offen offset:0 sc1\n\t"
    "ds_read_b128 v[52:55], v236 offset:9472\n\t"
    "v_cndmask_b32_dpp v100, v96, v100, vcc quad_perm:[1,1,3,3] row_mask:0xf bank_mask:0xf\n\t"
    "v_cndmask_b32_dpp v102, v98, v102, vcc quad_perm:[1,1,3,3] row_mask:0xf bank_mask:0xf\n\t"
    "v_cvt_pk_bf16_f32 v98, v16, v17\n\t"
    "v_smfmac_f32_16x16x64_bf16 v[28:31], v[172:175], v[64:71], v216\n\t"
    "ds_read_b32 v94, v229 offset:9220\n\t"
    "v_cndmask_b32_dpp v103, v99, v103, vcc quad_perm:[1,1,3,3] row_mask:0xf bank_mask:0xf\n\t"
    "v_smfmac_f32_16x16x64_bf16 v[4:7], a[0:3], v[56:63], v176\n\t"
    "s_waitcnt lgkmcnt(5)\n\t"
    "s_mov_b64 vcc, s[18:19]\n\t"
    "ds_read_b128 v[8:11], v244 offset:192\n\t"
    "v_cvt_pk_bf16_f32 v97, v26, v27\n\t"
    "v_smfmac_f32_16x16x64_bf16 v[4:7], a[16:19], v[40:47], v178\n\t"
    "ds_read_b128 v[48:51], v217 offset:9472\n\t"
    "v_smfmac_f32_16x16x64_bf16 v[12:15], a[4:7], v[56:63], v176 abid:1\n\t"
    "ds_read_b128 v[68:71], v236 offset:9600\n\t"
    "v_cvt_pk_bf16_f32 v96, v24, v25\n\t"
    "v_smfmac_f32_16x16x64_bf16 v[12:15], a[20:23], v[40:47], v178 abid:1\n\t"
    "ds_read_b128 v[24:27], v244 offset:256\n\t"
    "v_cvt_pk_bf16_f32 v28, v28, v29\n\t"
    "v_smfmac_f32_16x16x64_bf16 v[20:23], a[8:11], v[56:63], v177\n\t"
    "buffer_store_dwordx4 v[100:103], v241, s[44:47], s66 offen offset:1152 sc1\n\t"
    "ds_read_b128 v[64:67], v217 offset:9600\n\t"
    "v_cvt_pk_bf16_f32 v99, v18, v19\n\t"
    "v_cvt_pk_bf16_f32 v29, v30, v31\n\t"
    "v_smfmac_f32_16x16x64_bf16 v[20:23], a[24:27], v[40:47], v179\n\t"
    "s_waitcnt lgkmcnt(8)\n\t"
    "ds_read_b128 v[16:19], v244 offset:320\n\t"
    "v_cvt_pk_bf16_f32 v101, v34, v35\n\t"
    "v_mfma_f32_16x16x32_bf16 v[20:23], v[80:83], v[88:91], v[20:23]\n\t"
    "s_waitcnt lgkmcnt(5)\n\t"
    "ds_read_b32 v93, v86 offset:9220\n\t"
    "v_cvt_pk_bf16_f32 v100, v32, v33\n\t"
    "v_cvt_pk_bf16_f32 v102, v36, v37\n\t"
    "v_smfmac_f32_16x16x64_bf16 v[8:11], a[12:15], v[56:63], v177 abid:1\n\t"
    "ds_read_b128 v[80:83], v245 offset:5120\n\t"
    "v_cvt_pk_bf16_f32 v103, v38, v39\n\t"
    "v_smfmac_f32_16x16x64_bf16 v[8:11], a[28:31], v[40:47], v179 abid:1\n\t"
    "s_waitcnt lgkmcnt(6)\n\t"
    "buffer_load_dwordx4 v239, s[40:43], s68 offen offset:3584 nt lds\n\t"
    "v_smfmac_f32_16x16x64_bf16 v[8:11], a[44:47], v[48:55], v181 abid:1\n\t"
    "s_waitcnt lgkmcnt(4)\n\t"
    "ds_read_b32 v95, v87 offset:9220\n\t"
    "v_cndmask_b32_dpp v36, v100, v96, vcc quad_perm:[0,0,2,2] row_mask:0xf bank_mask:0xf\n\t"
    "v_cndmask_b32_dpp v37, v101, v97, vcc quad_perm:[0,0,2,2] row_mask:0xf bank_mask:0xf\n\t"
    "v_cndmask_b32_dpp v38, v102, v98, vcc quad_perm:[0,0,2,2] row_mask:0xf bank_mask:0xf\n\t"
    "v_smfmac_f32_16x16x64_bf16 v[24:27], a[144:147], v[56:63], v194\n\t"
    "ds_read_b128 v[32:35], v244 offset:384\n\t"
    "v_add_u32_sdwa v89, v248, v234 dst_sel:DWORD dst_unused:UNUSED_PAD src0_sel:DWORD src1_sel:WORD_1\n\t"
    "v_smfmac_f32_16x16x64_bf16 v[24:27], a[164:167], v[40:47], v196 abid:1\n\t"
    "v_cndmask_b32_dpp v39, v103, v99, vcc quad_perm:[0,0,2,2] row_mask:0xf bank_mask:0xf\n\t"
    "v_smfmac_f32_16x16x64_bf16 v[24:27], a[184:187], v[48:55], v199\n\t"
    "s_waitcnt lgkmcnt(4)\n\t"
    "s_not_b64 vcc, s[18:19]\n\t"
    "buffer_store_dwordx4 v[36:39], v241, s[44:47], s66 offen offset:128 sc1\n\t"
    "ds_write_b64 v242, v[28:29] offset:512\n\t"
    "v_add_u32_sdwa v88, v248, v234 dst_sel:DWORD dst_unused:UNUSED_PAD src0_sel:DWORD src1_sel:WORD_0\n\t"
    "v_smfmac_f32_16x16x64_bf16 v[16:19], a[148:151], v[56:63], v194 abid:1\n\t"
    "ds_read_b32 v89, v89 offset:9220\n\t"
    "v_cndmask_b32_dpp v102, v98, v102, vcc quad_perm:[1,1,3,3] row_mask:0xf bank_mask:0xf\n\t"
    "v_smfmac_f32_16x16x64_bf16 v[16:19], a[168:171], v[40:47], v197\n\t"
    "ds_read_b128 v[36:39], v244 offset:448\n\t"
    "v_cndmask_b32_dpp v101, v97, v101, vcc quad_perm:[1,1,3,3] row_mask:0xf bank_mask:0xf\n\t"
    "v_smfmac_f32_16x16x64_bf16 v[16:19], a[188:191], v[48:55], v199 abid:1\n\t"
    "s_waitcnt lgkmcnt(4)\n\t"
    "v_cndmask_b32_dpp v103, v99, v103, vcc quad_perm:[1,1,3,3] row_mask:0xf bank_mask:0xf\n\t"
    "v_mfma_f32_16x16x32_bf16 v[16:19], v[80:83], v[92:95], v[16:19]\n\t"
    "s_waitcnt lgkmcnt(3)\n\t"
    "s_bitcmp1_b32 s54, 12\n\t"
    "s_mov_b32 s68, 56623104\n\t"
    "s_cmov_b32 s68, 56696832\n\t"
    "ds_read_b32 v95, v75 offset:9220\n\t"
    "v_smfmac_f32_16x16x64_bf16 v[32:35], a[152:155], v[56:63], v195\n\t"
    "ds_read_b32 v92, v220 offset:9220\n\t"
    "v_cndmask_b32_dpp v100, v96, v100, vcc quad_perm:[1,1,3,3] row_mask:0xf bank_mask:0xf\n\t"
    "v_smfmac_f32_16x16x64_bf16 v[32:35], a[172:175], v[40:47], v197 abid:1\n\t"
    "buffer_store_dwordx4 v[100:103], v241, s[44:47], s66 offen offset:1280 sc1\n\t"
    "ds_read_b128 v[28:31], v244 offset:512\n\t"
    "v_smfmac_f32_16x16x64_bf16 v[32:35], a[192:195], v[48:55], v200\n\t"
    "ds_read_b128 v[80:83], v245 offset:8192\n\t"
    "v_add_u32_sdwa v91, v248, v235 dst_sel:DWORD dst_unused:UNUSED_PAD src0_sel:DWORD src1_sel:WORD_1\n\t"
    "v_smfmac_f32_16x16x64_bf16 v[32:35], a[212:215], v[64:71], v202 abid:1\n\t"
    "s_waitcnt lgkmcnt(4)\n\t"
    "v_add_u32_sdwa v90, v248, v235 dst_sel:DWORD dst_unused:UNUSED_PAD src0_sel:DWORD src1_sel:WORD_0\n\t"
    "v_smfmac_f32_16x16x64_bf16 v[36:39], a[156:159], v[56:63], v195 abid:1\n\t"
    "ds_read_b32 v88, v88 offset:9220\n\t"
    "ds_read_b32 v91, v91 offset:9220\n\t"
    "v_smfmac_f32_16x16x64_bf16 v[36:39], a[176:179], v[40:47], v198\n\t"
    "ds_read_b32 v90, v90 offset:9220\n\t"
    "v_smfmac_f32_16x16x64_bf16 v[36:39], a[196:199], v[48:55], v200 abid:1\n\t"
    "ds_read_b32 v93, v74 offset:9220\n\t"
    "v_smfmac_f32_16x16x64_bf16 v[36:39], a[216:219], v[64:71], v203\n\t"
    "s_add_u32 m0, s52, 0\n\t"
    "s_waitcnt lgkmcnt(5)\n\t"
    "s_mov_b32 exec_hi, 0\n\t"
    "buffer_load_dwordx4 v237, s[40:43], s68 offen offset:0 nt lds\n\t"
    "s_mov_b32 exec_hi, -1\n\t"
    "v_smfmac_f32_16x16x64_bf16 v[28:31], a[160:163], v[56:63], v196\n\t"
    "ds_read_b128 v[56:59], v217 offset:9728\n\t"
    "ds_read_b128 v[60:63], v236 offset:9728\n\t"
    "v_smfmac_f32_16x16x64_bf16 v[28:31], a[180:183], v[40:47], v198 abid:1\n\t"
    "v_smfmac_f32_16x16x64_bf16 v[28:31], a[200:203], v[48:55], v201\n\t"
    "ds_read_b32 v94, v221 offset:9220\n\t"
    "v_smfmac_f32_16x16x64_bf16 v[28:31], a[220:223], v[64:71], v203 abid:1\n\t"
    "s_waitcnt lgkmcnt(4)\n\t"
    "ds_read_b128 v[44:47], v236 offset:9856\n\t"
    "v_mfma_f32_16x16x32_bf16 v[28:31], v[80:83], v[88:91], v[28:31]\n\t"
    "ds_read_b128 v[80:83], v245 offset:1024\n\t"
    "v_add_u32_sdwa v88, v248, v246 dst_sel:DWORD dst_unused:UNUSED_PAD src0_sel:DWORD src1_sel:WORD_0\n\t"
    "v_smfmac_f32_16x16x64_bf16 v[4:7], a[32:35], v[48:55], v180\n\t"
    "ds_read_u16 v88, v88 offset:9216\n\t"
    "v_add_u32_sdwa v90, v248, v247 dst_sel:DWORD dst_unused:UNUSED_PAD src0_sel:DWORD src1_sel:WORD_0\n\t"
    "v_smfmac_f32_16x16x64_bf16 v[4:7], a[48:51], v[64:71], v182\n\t"
    "s_waitcnt lgkmcnt(4)\n\t"
    "v_add_u32_sdwa v91, v248, v247 dst_sel:DWORD dst_unused:UNUSED_PAD src0_sel:DWORD src1_sel:WORD_1\n\t"
    "v_smfmac_f32_16x16x64_bf16 v[4:7], a[64:67], v[56:63], v184\n\t"
    "ds_read_u16 v91, v91 offset:9216\n\t"
    "v_add_u32_sdwa v89, v248, v246 dst_sel:DWORD dst_unused:UNUSED_PAD src0_sel:DWORD src1_sel:WORD_1\n\t"
    "v_smfmac_f32_16x16x64_bf16 v[12:15], a[36:39], v[48:55], v180 abid:1\n\t"
    "s_waitcnt vmcnt(23)\n\t"
    "s_barrier\n\t"
    "ds_read_u16 v89, v89 offset:9216\n\t"
    "ds_read_u16 v90, v90 offset:9216\n\t"
    "v_smfmac_f32_16x16x64_bf16 v[12:15], a[52:55], v[64:71], v182 abid:1\n\t"
    "ds_read_b128 v[96:99], v243 offset:2560\n\t"
    "v_smfmac_f32_16x16x64_bf16 v[12:15], a[68:71], v[56:63], v184 abid:1\n\t"
    "s_waitcnt lgkmcnt(5)\n\t"
    "v_mfma_f32_16x16x32_bf16 v[12:15], v[80:83], v[92:95], v[12:15]\n\t"
    "s_mov_b64 vcc, s[18:19]\n\t"
    "ds_read_b128 v[40:43], v217 offset:9856\n\t"
    "v_smfmac_f32_16x16x64_bf16 v[20:23], a[40:43], v[48:55], v181\n\t"
    "ds_read_b32 v93, v84 offset:9220\n\t"
    "v_smfmac_f32_16x16x64_bf16 v[20:23], a[56:59], v[64:71], v183\n\t"
    "ds_read_b32 v92, v226 offset:9220\n\t"
    "v_smfmac_f32_16x16x64_bf16 v[20:23], a[72:75], v[56:63], v185\n\t"
    "s_waitcnt lgkmcnt(4)\n\t"
    "ds_read_b32 v95, v85 offset:9220\n\t"
    "s_set_gpr_idx_on s4, gpr_idx(SRC2,DST)\n\t"
    "v_mfma_f32_16x16x32_bf16 v[0:3], v[250:253], v[88:91], v[0:3]\n\t"
    "s_set_gpr_idx_off\n\t"
    "s_waitcnt lgkmcnt(4)\n\t"
    "s_mov_b32 exec_hi, 0\n\t"
    "buffer_store_dwordx4 v[96:99], v240, s[44:47], s67 offen offset:0 sc1\n\t"
    "s_mov_b32 exec_hi, -1\n\t"
    "ds_read_b32 v94, v227 offset:9220\n\t"
    "v_smfmac_f32_16x16x64_bf16 v[8:11], a[60:63], v[64:71], v183 abid:1\n\t"
    "ds_read_b128 v[80:83], v245 offset:4096\n\t"
    "v_smfmac_f32_16x16x64_bf16 v[8:11], a[76:79], v[56:63], v185 abid:1\n\t"
    "s_waitcnt lgkmcnt(5)\n\t"
    "v_smfmac_f32_16x16x64_bf16 v[8:11], a[92:95], v[40:47], v187 abid:1\n\t"
    "ds_read_b128 v[52:55], v236 offset:9984\n\t"
    "s_set_gpr_idx_on s5, gpr_idx(SRC2,DST)\n\t"
    "v_mfma_f32_16x16x32_bf16 v[0:3], v[252:255], v[88:91], v[0:3]\n\t"
    "s_set_gpr_idx_off\n\t"
    "s_add_u32 m0, s52, 0\n\t"
    "s_nop 0\n\t"
    "buffer_load_dwordx4 v237, s[40:43], s68 offen offset:512 nt lds\n\t"
    "s_cmp_lg_u32 s22, 0\n\t"
    "s_cbranch_scc1 .Lsl13_%=\n\t"
    ".Lslr13_%=:\n\t"
    "v_smfmac_f32_16x16x64_bf16 v[24:27], a[204:207], v[64:71], v201 abid:1\n\t"
    "ds_read_b128 v[48:51], v217 offset:9984\n\t"
    "v_smfmac_f32_16x16x64_bf16 v[24:27], a[224:227], v[56:63], v204\n\t"
    "s_memrealtime s[84:85]\n\t"
    "ds_read_b32 v91, v107 offset:9220\n\t"
    "v_smfmac_f32_16x16x64_bf16 v[24:27], a[244:247], v[40:47], v206 abid:1\n\t"
    "s_waitcnt lgkmcnt(3)\n\t"
    "ds_read_b32 v88, v232 offset:9220\n\t"
    "v_mfma_f32_16x16x32_bf16 v[24:27], v[80:83], v[92:95], v[24:27]\n\t"
    "ds_read_b32 v90, v233 offset:9220\n\t"
    "v_smfmac_f32_16x16x64_bf16 v[16:19], a[208:211], v[64:71], v202\n\t"
    "ds_read_b32 v89, v106 offset:9220\n\t"
    "v_smfmac_f32_16x16x64_bf16 v[16:19], a[228:231], v[56:63], v204 abid:1\n\t"
    "ds_read_b128 v[80:83], v245 offset:7168\n\t"
    "v_smfmac_f32_16x16x64_bf16 v[16:19], a[248:251], v[40:47], v207\n\t"
    "s_sub_i32 s87, s87, s99\n\t"
    "v_smfmac_f32_16x16x64_bf16 v[32:35], a[232:235], v[56:63], v205\n\t"
    "ds_read_b32 v93, v72 offset:9220\n\t"
    "ds_read_b32 v94, v219 offset:9220\n\t"
    "v_smfmac_f32_16x16x64_bf16 v[32:35], a[252:255], v[40:47], v207 abid:1\n\t"
    "s_waitcnt lgkmcnt(7)\n\t"
    "s_sub_i32 s98, s98, s99\n\t"
    "ds_read_b32 v92, v218 offset:9220\n\t"
    "v_smfmac_f32_16x16x64_bf16 v[32:35], v[124:127], v[48:55], v210\n\t"
    "s_add_i32 s86, s87, 1775\n\t"
    "v_smfmac_f32_16x16x64_bf16 v[36:39], a[236:239], v[56:63], v205 abid:1\n\t"
    "s_add_i32 s98, s98, 72\n\t"
    "ds_read_b128 v[68:71], v236 offset:10112\n\t"
    "v_smfmac_f32_16x16x64_bf16 v[36:39], v[108:111], v[40:47], v208\n\t"
    "s_max_i32 s86, s86, s98\n\t"
    "ds_read_b32 v95, v73 offset:9220\n\t"
    "v_smfmac_f32_16x16x64_bf16 v[36:39], v[128:131], v[48:55], v210 abid:1\n\t"
    "s_waitcnt lgkmcnt(5)\n\t"
    "ds_read_b128 v[64:67], v217 offset:10112\n\t"
    "v_mfma_f32_16x16x32_bf16 v[36:39], v[80:83], v[88:91], v[36:39]\n\t"
    "s_sub_u32 s89, s84, s24\n\t"
    "ds_read_b128 v[80:83], v245 offset:0\n\t"
    "v_smfmac_f32_16x16x64_bf16 v[28:31], a[240:243], v[56:63], v206\n\t"
    "s_cmp_lt_i32 s89, s86\n\t"
    "s_cbranch_scc1 .Lpc8_%=\n\t"
    ".Lpc8r_%=:\n\t"
    "ds_read_b128 v[60:63], v236 offset:10240\n\t"
    "v_smfmac_f32_16x16x64_bf16 v[28:31], v[112:115], v[40:47], v208 abid:1\n\t"
    "ds_read_b128 v[56:59], v217 offset:10240\n\t"
    "v_smfmac_f32_16x16x64_bf16 v[28:31], v[132:135], v[48:55], v211\n\t"
    "s_max_i32 s98, s89, s86\n\t"
    "v_smfmac_f32_16x16x64_bf16 v[4:7], a[80:83], v[40:47], v186\n\t"
    "ds_read_b32 v90, v225 offset:9220\n\t"
    "v_smfmac_f32_16x16x64_bf16 v[4:7], a[96:99], v[48:55], v188\n\t"
    "s_waitcnt lgkmcnt(4)\n\t"
    "ds_read_b32 v88, v224 offset:9220\n\t"
    "v_smfmac_f32_16x16x64_bf16 v[4:7], a[112:115], v[64:71], v190\n\t"
    "s_add_u32 m0, s52, 0\n\t"
    "s_waitcnt lgkmcnt(2)\n\t"
    "buffer_load_dwordx4 v238, s[40:43], s68 offen offset:1536 nt lds\n\t"
    "v_smfmac_f32_16x16x64_bf16 v[4:7], a[128:131], v[56:63], v192\n\t"
    "ds_read_b32 v89, v78 offset:9220\n\t"
    "v_mfma_f32_16x16x32_bf16 v[4:7], v[80:83], v[92:95], v[4:7]\n\t"
    "ds_read_b32 v92, v230 offset:9220\n\t"
    "v_smfmac_f32_16x16x64_bf16 v[12:15], a[84:87], v[40:47], v186 abid:1\n\t"
    "ds_read_b32 v91, v79 offset:9220\n\t"
    "v_smfmac_f32_16x16x64_bf16 v[12:15], a[100:103], v[48:55], v188 abid:1\n\t"
    "v_smfmac_f32_16x16x64_bf16 v[12:15], a[116:119], v[64:71], v190 abid:1\n\t"
    "ds_read_b32 v93, v104 offset:9220\n\t"
    "v_cvt_pk_bf16_f32 v101, v6, v7\n\t"
    "v_smfmac_f32_16x16x64_bf16 v[12:15], a[132:135], v[56:63], v192 abid:1\n\t"
    "ds_read_b128 v[80:83], v245 offset:3072\n\t"
    "v_cvt_pk_bf16_f32 v100, v4, v5\n\t"
    "v_smfmac_f32_16x16x64_bf16 v[20:23], a[88:91], v[40:47], v187\n\t"
    "ds_read_b128 v[40:43], v217 offset:13824\n\t"
    "v_smfmac_f32_16x16x64_bf16 v[20:23], a[104:107], v[48:55], v189\n\t"
    "ds_read_b32 v95, v105 offset:9220\n\t"
    "v_smfmac_f32_16x16x64_bf16 v[20:23], a[120:123], v[64:71], v191\n\t"
    "ds_read_b32 v94, v231 offset:9220\n\t"
    "v_cvt_pk_bf16_f32 v103, v14, v15\n\t"
    "v_smfmac_f32_16x16x64_bf16 v[20:23], a[136:139], v[56:63], v193\n\t"
    "s_bitcmp1_b32 s54, 8\n\t"
    "s_mov_b32 s65, 37748736\n\t"
    "s_cmov_b32 s65, 37822464\n\t"
    "v_smfmac_f32_16x16x64_bf16 v[8:11], a[108:111], v[48:55], v189 abid:1\n\t"
    "ds_read_b128 v[44:47], v236 offset:13824\n\t"
    "v_cvt_pk_bf16_f32 v102, v12, v13\n\t"
    "v_smfmac_f32_16x16x64_bf16 v[8:11], a[124:127], v[64:71], v191 abid:1\n\t"
    "ds_read_b128 v[12:15], v244 offset:64\n\t"
    "v_smfmac_f32_16x16x64_bf16 v[8:11], a[140:143], v[56:63], v193 abid:1\n\t"
    "s_waitcnt lgkmcnt(5)\n\t"
    "ds_read_b128 v[4:7], v244 offset:0\n\t"
    "v_mfma_f32_16x16x32_bf16 v[8:11], v[80:83], v[88:91], v[8:11]\n\t"
    "ds_read_b32 v90, v223 offset:13828\n\t"
    "v_smfmac_f32_16x16x64_bf16 v[24:27], v[116:119], v[48:55], v209\n\t"
    "ds_read_b32 v91, v77 offset:13828\n\t"
    "v_cvt_pk_bf16_f32 v97, v22, v23\n\t"
    "v_smfmac_f32_16x16x64_bf16 v[24:27], v[136:139], v[64:71], v211 abid:1\n\t"
    "ds_read_b128 v[80:83], v245 offset:6144\n\t"
    "v_cvt_pk_bf16_f32 v96, v20, v21\n\t"
    "v_smfmac_f32_16x16x64_bf16 v[24:27], v[156:159], v[56:63], v214\n\t"
    "v_smfmac_f32_16x16x64_bf16 v[16:19], v[120:123], v[48:55], v209 abid:1\n\t"
    "ds_read_b128 v[48:51], v217 offset:13952\n\t"
    "v_cvt_pk_bf16_f32 v98, v8, v9\n\t"
    "v_smfmac_f32_16x16x64_bf16 v[16:19], v[140:143], v[64:71], v212\n\t"
    "ds_read_b128 v[52:55], v236 offset:13952\n\t"
    "v_cndmask_b32_dpp v8, v96, v100, vcc quad_perm:[0,0,2,2] row_mask:0xf bank_mask:0xf\n\t"
    "v_smfmac_f32_16x16x64_bf16 v[16:19], v[160:163], v[56:63], v214 abid:1\n\t"
    "ds_read_b32 v88, v222 offset:13828\n\t"
    "v_cvt_pk_bf16_f32 v99, v10, v11\n\t"
    "v_smfmac_f32_16x16x64_bf16 v[32:35], v[144:147], v[64:71], v212 abid:1\n\t"
    "ds_read_b128 v[20:23], v244 offset:128\n\t"
    "v_smfmac_f32_16x16x64_bf16 v[32:35], v[164:167], v[56:63], v215\n\t"
    "s_waitcnt lgkmcnt(4)\n\t"
    "v_cndmask_b32_dpp v11, v99, v103, vcc quad_perm:[0,0,2,2] row_mask:0xf bank_mask:0xf\n\t"
    "v_mfma_f32_16x16x32_bf16 v[32:35], v[80:83], v[92:95], v[32:35]\n\t"
    "ds_read_b128 v[80:83], v245 offset:2048\n\t"
    "v_cndmask_b32_dpp v9, v97, v101, vcc quad_perm:[0,0,2,2] row_mask:0xf bank_mask:0xf\n\t"
    "v_smfmac_f32_16x16x64_bf16 v[36:39], v[148:151], v[64:71], v213\n\t"
    "ds_read_b32 v94, v229 offset:13828\n\t"
    "v_cndmask_b32_dpp v10, v98, v102, vcc quad_perm:[0,0,2,2] row_mask:0xf bank_mask:0xf\n\t"
    "v_smfmac_f32_16x16x64_bf16 v[36:39], v[168:171], v[56:63], v215 abid:1\n\t"
    "s_not_b64 vcc, s[18:19]\n\t"
    "ds_read_b32 v92, v228 offset:13828\n\t"
    "v_cndmask_b32_dpp v99, v103, v99, vcc quad_perm:[1,1,3,3] row_mask:0xf bank_mask:0xf\n\t"
    "v_smfmac_f32_16x16x64_bf16 v[28:31], v[152:155], v[64:71], v213 abid:1\n\t"
    "buffer_store_dwordx4 v[8:11], v241, s[44:47], s65 offen offset:0 sc1\n\t"
    "v_cndmask_b32_dpp v96, v100, v96, vcc quad_perm:[1,1,3,3] row_mask:0xf bank_mask:0xf\n\t"
    "v_cndmask_b32_dpp v97, v101, v97, vcc quad_perm:[1,1,3,3] row_mask:0xf bank_mask:0xf\n\t"
    "v_cndmask_b32_dpp v98, v102, v98, vcc quad_perm:[1,1,3,3] row_mask:0xf bank_mask:0xf\n\t"
    "v_cvt_pk_bf16_f32 v102, v16, v17\n\t"
    "v_smfmac_f32_16x16x64_bf16 v[28:31], v[172:175], v[56:63], v216\n\t"
    "s_add_u32 s66, s57, s65\n\t"
    "ds_read_b32 v89, v76 offset:13828\n\t"
    "v_cvt_pk_bf16_f32 v103, v18, v19\n\t"
    "v_smfmac_f32_16x16x64_bf16 v[4:7], a[0:3], v[40:47], v176\n\t"
    "s_waitcnt lgkmcnt(6)\n\t"
    "s_mov_b64 vcc, s[18:19]\n\t"
    "ds_read_b128 v[8:11], v244 offset:192\n\t"
    "v_cvt_pk_bf16_f32 v100, v24, v25\n\t"
    "v_smfmac_f32_16x16x64_bf16 v[4:7], a[16:19], v[48:55], v178\n\t"
    "ds_read_b128 v[68:71], v236 offset:14080\n\t"
    "v_cvt_pk_bf16_f32 v101, v26, v27\n\t"
    "v_smfmac_f32_16x16x64_bf16 v[12:15], a[4:7], v[40:47], v176 abid:1\n\t"
    "ds_read_b128 v[64:67], v217 offset:14080\n\t"
    "v_smfmac_f32_16x16x64_bf16 v[12:15], a[20:23], v[48:55], v178 abid:1\n\t"
    "s_waitcnt lgkmcnt(7)\n\t"
    "ds_read_b128 v[24:27], v244 offset:256\n\t"
    "v_smfmac_f32_16x16x64_bf16 v[20:23], a[8:11], v[40:47], v177\n\t"
    "buffer_store_dwordx4 v[96:99], v241, s[44:47], s65 offen offset:1152 sc1\n\t"
    "ds_read_b32 v95, v87 offset:13828\n\t"
    "v_cvt_pk_bf16_f32 v28, v28, v29\n\t"
    "v_cvt_pk_bf16_f32 v29, v30, v31\n\t"
    "v_smfmac_f32_16x16x64_bf16 v[20:23], a[24:27], v[48:55], v179\n\t"
    "s_waitcnt lgkmcnt(5)\n\t"
    "ds_read_b128 v[56:59], v217 offset:14208\n\t"
    "v_cvt_pk_bf16_f32 v98, v36, v37\n\t"
    "v_mfma_f32_16x16x32_bf16 v[20:23], v[80:83], v[88:91], v[20:23]\n\t"
    "s_waitcnt lgkmcnt(5)\n\t"
    "ds_read_b128 v[16:19], v244 offset:320\n\t"
    "v_cvt_pk_bf16_f32 v96, v32, v33\n\t"
    "v_cvt_pk_bf16_f32 v99, v38, v39\n\t"
    "v_smfmac_f32_16x16x64_bf16 v[8:11], a[12:15], v[40:47], v177 abid:1\n\t"
    "ds_read_b32 v93, v86 offset:13828\n\t"
    "v_cvt_pk_bf16_f32 v97, v34, v35\n\t"
    "v_smfmac_f32_16x16x64_bf16 v[8:11], a[28:31], v[48:55], v179 abid:1\n\t"
    "s_waitcnt lgkmcnt(5)\n\t"
    "buffer_load_dwordx4 v239, s[40:43], s68 offen offset:2560 nt lds\n\t"
    "ds_read_b128 v[32:35], v244 offset:384\n\t"
    "v_cndmask_b32_dpp v38, v98, v102, vcc quad_perm:[0,0,2,2] row_mask:0xf bank_mask:0xf\n\t"
    "v_smfmac_f32_16x16x64_bf16 v[8:11], a[44:47], v[64:71], v181 abid:1\n\t"
    "s_waitcnt lgkmcnt(5)\n\t"
    "ds_read_b128 v[80:83], v245 offset:5120\n\t"
    "v_cndmask_b32_dpp v37, v97, v101, vcc quad_perm:[0,0,2,2] row_mask:0xf bank_mask:0xf\n\t"
    "v_smfmac_f32_16x16x64_bf16 v[24:27], a[144:147], v[40:47], v194\n\t"
    "ds_read_b128 v[60:63], v236 offset:14208\n\t"
    "v_cndmask_b32_dpp v36, v96, v100, vcc quad_perm:[0,0,2,2] row_mask:0xf bank_mask:0xf\n\t"
    "v_smfmac_f32_16x16x64_bf16 v[24:27], a[164:167], v[48:55], v196 abid:1\n\t"
    "v_cndmask_b32_dpp v39, v99, v103, vcc quad_perm:[0,0,2,2] row_mask:0xf bank_mask:0xf\n\t"
    "v_smfmac_f32_16x16x64_bf16 v[24:27], a[184:187], v[64:71], v199\n\t"
    "s_waitcnt lgkmcnt(4)\n\t"
    "s_not_b64 vcc, s[18:19]\n\t"
    "buffer_store_dwordx4 v[36:39], v241, s[44:47], s65 offen offset:128 sc1\n\t"
    "ds_write_b64 v242, v[28:29] offset:0\n\t"
    "v_cndmask_b32_dpp v98, v102, v98, vcc quad_perm:[1,1,3,3] row_mask:0xf bank_mask:0xf\n\t"
    "v_add_u32_sdwa v90, v248, v235 dst_sel:DWORD dst_unused:UNUSED_PAD src0_sel:DWORD src1_sel:WORD_0\n\t"
    "v_smfmac_f32_16x16x64_bf16 v[16:19], a[148:151], v[40:47], v194 abid:1\n\t"
    "ds_read_b128 v[28:31], v244 offset:512\n\t"
    "v_add_u32_sdwa v91, v248, v235 dst_sel:DWORD dst_unused:UNUSED_PAD src0_sel:DWORD src1_sel:WORD_1\n\t"
    "v_smfmac_f32_16x16x64_bf16 v[16:19], a[168:171], v[48:55], v197\n\t"
    "ds_read_b128 v[36:39], v244 offset:448\n\t"
    "v_cndmask_b32_dpp v96, v100, v96, vcc quad_perm:[1,1,3,3] row_mask:0xf bank_mask:0xf\n\t"
    "v_smfmac_f32_16x16x64_bf16 v[16:19], a[188:191], v[64:71], v199 abid:1\n\t"
    "s_waitcnt lgkmcnt(4)\n\t"
    "ds_read_b32 v90, v90 offset:13828\n\t"
    "v_add_u32_sdwa v88, v248, v234 dst_sel:DWORD dst_unused:UNUSED_PAD src0_sel:DWORD src1_sel:WORD_0\n\t"
    "v_mfma_f32_16x16x32_bf16 v[16:19], v[80:83], v[92:95], v[16:19]\n\t"
    "ds_read_b32 v88, v88 offset:13828\n\t"
    "v_cndmask_b32_dpp v97, v101, v97, vcc quad_perm:[1,1,3,3] row_mask:0xf bank_mask:0xf\n\t"
    "v_smfmac_f32_16x16x64_bf16 v[32:35], a[152:155], v[40:47], v195\n\t"
    "ds_read_b32 v94, v221 offset:13828\n\t"
    "v_cndmask_b32_dpp v99, v103, v99, vcc quad_perm:[1,1,3,3] row_mask:0xf bank_mask:0xf\n\t"
    "v_smfmac_f32_16x16x64_bf16 v[32:35], a[172:175], v[48:55], v197 abid:1\n\t"
    "buffer_store_dwordx4 v[96:99], v241, s[44:47], s65 offen offset:1280 sc1\n\t"
    "ds_read_b32 v91, v91 offset:13828\n\t"
    "v_smfmac_f32_16x16x64_bf16 v[32:35], a[192:195], v[64:71], v200\n\t"
    "s_waitcnt lgkmcnt(7)\n\t"
    "v_add_u32_sdwa v89, v248, v234 dst_sel:DWORD dst_unused:UNUSED_PAD src0_sel:DWORD src1_sel:WORD_1\n\t"
    "v_smfmac_f32_16x16x64_bf16 v[32:35], a[212:215], v[56:63], v202 abid:1\n\t"
    "s_waitcnt lgkmcnt(4)\n\t"
    "ds_read_b32 v89, v89 offset:13828\n\t"
    "ds_read_b32 v93, v74 offset:13828\n\t"
    "v_smfmac_f32_16x16x64_bf16 v[36:39], a[156:159], v[40:47], v195 abid:1\n\t"
    "ds_read_b128 v[80:83], v245 offset:8192\n\t"
    "v_smfmac_f32_16x16x64_bf16 v[36:39], a[176:179], v[48:55], v198\n\t"
    "buffer_load_dwordx4 v239, s[40:43], s68 offen offset:3584 nt lds\n\t"
    "v_smfmac_f32_16x16x64_bf16 v[36:39], a[196:199], v[64:71], v200 abid:1\n\t"
    "s_bitcmp1_b32 s54, 13\n\t"
    "s_mov_b32 s68, 61341696\n\t"
    "s_cmov_b32 s68, 61415424\n\t"
    "v_smfmac_f32_16x16x64_bf16 v[36:39], a[216:219], v[56:63], v203\n\t"
    "ds_read_b32 v92, v220 offset:13828\n\t"
    "v_smfmac_f32_16x16x64_bf16 v[28:31], a[160:163], v[40:47], v196\n\t"
    "ds_read_b128 v[40:43], v217 offset:14336\n\t"
    "ds_read_b128 v[44:47], v236 offset:14336\n\t"
    "v_smfmac_f32_16x16x64_bf16 v[28:31], a[180:183], v[48:55], v198 abid:1\n\t"
    "v_smfmac_f32_16x16x64_bf16 v[28:31], a[200:203], v[64:71], v201\n\t"
    "ds_read_b32 v95, v75 offset:13828\n\t"
    "v_smfmac_f32_16x16x64_bf16 v[28:31], a[220:223], v[56:63], v203 abid:1\n\t"
    "s_waitcnt lgkmcnt(4)\n\t"
    "ds_read_b128 v[48:51], v217 offset:14464\n\t"
    "v_mfma_f32_16x16x32_bf16 v[28:31], v[80:83], v[88:91], v[28:31]\n\t"
    "ds_read_b128 v[80:83], v245 offset:1024\n\t"
    "v_add_u32_sdwa v90, v248, v247 dst_sel:DWORD dst_unused:UNUSED_PAD src0_sel:DWORD src1_sel:WORD_0\n\t"
    "v_smfmac_f32_16x16x64_bf16 v[4:7], a[32:35], v[64:71], v180\n\t"
    "ds_read_u16 v90, v90 offset:13824\n\t"
    "v_add_u32_sdwa v91, v248, v247 dst_sel:DWORD dst_unused:UNUSED_PAD src0_sel:DWORD src1_sel:WORD_1\n\t"
    "v_smfmac_f32_16x16x64_bf16 v[4:7], a[48:51], v[56:63], v182\n\t"
    "s_waitcnt lgkmcnt(4)\n\t"
    "ds_read_u16 v91, v91 offset:13824\n\t"
    "v_add_u32_sdwa v88, v248, v246 dst_sel:DWORD dst_unused:UNUSED_PAD src0_sel:DWORD src1_sel:WORD_0\n\t"
    "v_smfmac_f32_16x16x64_bf16 v[4:7], a[64:67], v[40:47], v184\n\t"
    "v_add_u32_sdwa v89, v248, v246 dst_sel:DWORD dst_unused:UNUSED_PAD src0_sel:DWORD src1_sel:WORD_1\n\t"
    "v_smfmac_f32_16x16x64_bf16 v[12:15], a[36:39], v[64:71], v180 abid:1\n\t"
    "s_waitcnt vmcnt(22)\n\t"
    "s_barrier\n\t"
    "ds_read_u16 v88, v88 offset:13824\n\t"
    "ds_read_u16 v89, v89 offset:13824\n\t"
    "v_smfmac_f32_16x16x64_bf16 v[12:15], a[52:55], v[56:63], v182 abid:1\n\t"
    "s_add_u32 m0, s52, 4608\n\t"
    "s_mov_b32 exec_hi, 0\n\t"
    "buffer_load_dwordx4 v237, s[40:43], s68 offen offset:0 nt lds\n\t"
    "s_mov_b32 exec_hi, -1\n\t"
    "ds_read_b128 v[100:103], v243 offset:2048\n\t"
    "v_smfmac_f32_16x16x64_bf16 v[12:15], a[68:71], v[40:47], v184 abid:1\n\t"
    "s_waitcnt lgkmcnt(5)\n\t"
    "ds_read_b128 v[52:55], v236 offset:14464\n\t"
    "v_mfma_f32_16x16x32_bf16 v[12:15], v[80:83], v[92:95], v[12:15]\n\t"
    "v_smfmac_f32_16x16x64_bf16 v[20:23], a[40:43], v[64:71], v181\n\t"
    "ds_read_b32 v92, v226 offset:13828\n\t"
    "v_smfmac_f32_16x16x64_bf16 v[20:23], a[56:59], v[56:63], v183\n\t"
    "ds_read_b128 v[80:83], v245 offset:4096\n\t"
    "v_smfmac_f32_16x16x64_bf16 v[20:23], a[72:75], v[40:47], v185\n\t"
    "s_waitcnt lgkmcnt(4)\n\t"
    "ds_read_b32 v93, v84 offset:13828\n\t"
    "s_set_gpr_idx_on s4, gpr_idx(SRC2,DST)\n\t"
    "v_mfma_f32_16x16x32_bf16 v[0:3], v[250:253], v[88:91], v[0:3]\n\t"
    "s_set_gpr_idx_off\n\t"
    "s_waitcnt lgkmcnt(4)\n\t"
    "s_mov_b32 exec_hi, 0\n\t"
    "buffer_store_dwordx4 v[100:103], v240, s[44:47], s66 offen offset:0 sc1\n\t"
    "s_mov_b32 exec_hi, -1\n\t"
    "ds_read_b32 v95, v85 offset:13828\n\t"
    "v_smfmac_f32_16x16x64_bf16 v[8:11], a[60:63], v[56:63], v183 abid:1\n\t"
    "ds_read_b32 v94, v227 offset:13828\n\t"
    "v_smfmac_f32_16x16x64_bf16 v[8:11], a[76:79], v[40:47], v185 abid:1\n\t"
    "s_waitcnt lgkmcnt(5)\n\t"
    "ds_read_b128 v[68:71], v236 offset:14592\n\t"
    "v_smfmac_f32_16x16x64_bf16 v[8:11], a[92:95], v[48:55], v187 abid:1\n\t"
    "ds_read_b128 v[64:67], v217 offset:14592\n\t"
    "s_set_gpr_idx_on s5, gpr_idx(SRC2,DST)\n\t"
    "v_mfma_f32_16x16x32_bf16 v[0:3], v[252:255], v[88:91], v[0:3]\n\t"
    "s_set_gpr_idx_off\n\t"
    "s_cmp_lg_u32 s22, 0\n\t"
    "s_cbranch_scc1 .Lsl14_%=\n\t"
    ".Lslr14_%=:\n\t"
    "v_smfmac_f32_16x16x64_bf16 v[24:27], a[204:207], v[56:63], v201 abid:1\n\t"
    "ds_read_b32 v90, v233 offset:13828\n\t"
    "v_smfmac_f32_16x16x64_bf16 v[24:27], a[224:227], v[40:47], v204\n\t"
    "s_memrealtime s[84:85]\n\t"
    "ds_read_b32 v89, v106 offset:13828\n\t"
    "v_smfmac_f32_16x16x64_bf16 v[24:27], a[244:247], v[48:55], v206 abid:1\n\t"
    "s_waitcnt lgkmcnt(4)\n\t"
    "ds_read_b32 v88, v232 offset:13828\n\t"
    "v_mfma_f32_16x16x32_bf16 v[24:27], v[80:83], v[92:95], v[24:27]\n\t"
    "ds_read_b128 v[80:83], v245 offset:7168\n\t"
    "v_smfmac_f32_16x16x64_bf16 v[16:19], a[208:211], v[56:63], v202\n\t"
    "s_add_u32 m0, s52, 4608\n\t"
    "s_mov_b64 vcc, s[18:19]\n\t"
    "buffer_load_dwordx4 v237, s[40:43], s68 offen offset:512 nt lds\n\t"
    "v_smfmac_f32_16x16x64_bf16 v[16:19], a[228:231], v[40:47], v204 abid:1\n\t"
    "ds_read_b128 v[60:63], v236 offset:14720\n\t"
    "v_smfmac_f32_16x16x64_bf16 v[16:19], a[248:251], v[48:55], v207\n\t"
    "s_sub_i32 s87, s87, s99\n\t"
    "ds_read_b32 v91, v107 offset:13828\n\t"
    "v_smfmac_f32_16x16x64_bf16 v[32:35], a[232:235], v[40:47], v205\n\t"
    "ds_read_b128 v[56:59], v217 offset:14720\n\t"
    "v_smfmac_f32_16x16x64_bf16 v[32:35], a[252:255], v[48:55], v207 abid:1\n\t"
    "s_waitcnt lgkmcnt(7)\n\t"
    "s_sub_i32 s98, s98, s99\n\t"
    "ds_read_b32 v92, v218 offset:13828\n\t"
    "v_smfmac_f32_16x16x64_bf16 v[32:35], v[124:127], v[64:71], v210\n\t"
    "s_add_i32 s86, s87, 1897\n\t"
    "ds_read_b32 v93, v72 offset:13828\n\t"
    "v_smfmac_f32_16x16x64_bf16 v[36:39], a[236:239], v[40:47], v205 abid:1\n\t"
    "s_add_i32 s98, s98, 72\n\t"
    "ds_read_b32 v95, v73 offset:13828\n\t"
    "v_smfmac_f32_16x16x64_bf16 v[36:39], v[108:111], v[48:55], v208\n\t"
    "s_max_i32 s86, s86, s98\n\t"
    "ds_read_b32 v94, v219 offset:13828\n\t"
    "v_smfmac_f32_16x16x64_bf16 v[36:39], v[128:131], v[64:71], v210 abid:1\n\t"
    "s_waitcnt lgkmcnt(5)\n\t"
    "v_mfma_f32_16x16x32_bf16 v[36:39], v[80:83], v[88:91], v[36:39]\n\t"
    "s_sub_u32 s89, s84, s24\n\t"
    "ds_read_b128 v[80:83], v245 offset:0\n\t"
    "v_smfmac_f32_16x16x64_bf16 v[28:31], a[240:243], v[40:47], v206\n\t"
    "s_cmp_lt_i32 s89, s86\n\t"
    "s_cbranch_scc1 .Lpc9_%=\n\t"
    ".Lpc9r_%=:\n\t"
    "ds_read_b128 v[40:43], v217 offset:14848\n\t"
    "ds_read_b128 v[44:47], v236 offset:14848\n\t"
    "v_smfmac_f32_16x16x64_bf16 v[28:31], v[112:115], v[48:55], v208 abid:1\n\t"
    "v_smfmac_f32_16x16x64_bf16 v[28:31], v[132:135], v[64:71], v211\n\t"
    "s_max_i32 s98, s89, s86\n\t"
    "ds_read_b32 v90, v225 offset:13828\n\t"
    "v_smfmac_f32_16x16x64_bf16 v[4:7], a[80:83], v[48:55], v186\n\t"
    "v_smfmac_f32_16x16x64_bf16 v[4:7], a[96:99], v[64:71], v188\n\t"
    "s_waitcnt lgkmcnt(8)\n\t"
    "ds_read_b32 v89, v78 offset:13828\n\t"
    "ds_read_b32 v91, v79 offset:13828\n\t"
    "v_smfmac_f32_16x16x64_bf16 v[4:7], a[112:115], v[56:63], v190\n\t"
    "s_waitcnt lgkmcnt(3)\n\t"
    "buffer_load_dwordx4 v238, s[40:43], s68 offen offset:1536 nt lds\n\t"
    "v_smfmac_f32_16x16x64_bf16 v[4:7], a[128:131], v[40:47], v192\n\t"
    "ds_read_b32 v88, v224 offset:13828\n\t"
    "v_mfma_f32_16x16x32_bf16 v[4:7], v[80:83], v[92:95], v[4:7]\n\t"
    "v_smfmac_f32_16x16x64_bf16 v[12:15], a[84:87], v[48:55], v186 abid:1\n\t"
    "ds_read_b32 v92, v230 offset:13828\n\t"
    "v_smfmac_f32_16x16x64_bf16 v[12:15], a[100:103], v[64:71], v188 abid:1\n\t"
    "ds_read_b128 v[80:83], v245 offset:3072\n\t"
    "v_smfmac_f32_16x16x64_bf16 v[12:15], a[116:119], v[56:63], v190 abid:1\n\t"
    "ds_read_b32 v93, v104 offset:13828\n\t"
    "v_smfmac_f32_16x16x64_bf16 v[12:15], a[132:135], v[40:47], v192 abid:1\n\t"
    "s_bitcmp1_b32 s54, 9\n\t"
    "s_mov_b32 s67, 42467328\n\t"
    "s_cmov_b32 s67, 42541056\n\t"
    "v_smfmac_f32_16x16x64_bf16 v[20:23], a[88:91], v[48:55], v187\n\t"
    "ds_read_b32 v95, v105 offset:13828\n\t"
    "v_smfmac_f32_16x16x64_bf16 v[20:23], a[104:107], v[64:71], v189\n\t"
    "ds_read_b32 v94, v231 offset:13828\n\t"
    "v_cvt_pk_bf16_f32 v96, v4, v5\n\t"
    "v_smfmac_f32_16x16x64_bf16 v[20:23], a[120:123], v[56:63], v191\n\t"
    "v_cvt_pk_bf16_f32 v98, v12, v13\n\t"
    "v_smfmac_f32_16x16x64_bf16 v[20:23], a[136:139], v[40:47], v193\n\t"
    "ds_read_b128 v[48:51], v217 offset:18432\n\t"
    "v_cvt_pk_bf16_f32 v97, v6, v7\n\t"
    "v_smfmac_f32_16x16x64_bf16 v[8:11], a[108:111], v[64:71], v189 abid:1\n\t"
    "ds_read_b128 v[52:55], v236 offset:18432\n\t"
    "v_smfmac_f32_16x16x64_bf16 v[8:11], a[124:127], v[56:63], v191 abid:1\n\t"
    "ds_read_b128 v[4:7], v244 offset:0\n\t"
    "v_smfmac_f32_16x16x64_bf16 v[8:11], a[140:143], v[40:47], v193 abid:1\n\t"
    "s_waitcnt lgkmcnt(6)\n\t"
    "v_mfma_f32_16x16x32_bf16 v[8:11], v[80:83], v[88:91], v[8:11]\n\t"
    "ds_read_b128 v[80:83], v245 offset:6144\n\t"
    "v_cvt_pk_bf16_f32 v99, v14, v15\n\t"
    "v_smfmac_f32_16x16x64_bf16 v[24:27], v[116:119], v[64:71], v209\n\t"
    "ds_read_b32 v88, v222 offset:18436\n\t"
    "v_cvt_pk_bf16_f32 v101, v22, v23\n\t"
    "v_smfmac_f32_16x16x64_bf16 v[24:27], v[136:139], v[56:63], v211 abid:1\n\t"
    "ds_read_b32 v91, v77 offset:18436\n\t"
    "v_smfmac_f32_16x16x64_bf16 v[24:27], v[156:159], v[40:47], v214\n\t"
    "ds_read_b32 v90, v223 offset:18436\n\t"
    "v_cvt_pk_bf16_f32 v103, v10, v11\n\t"
    "v_smfmac_f32_16x16x64_bf16 v[16:19], v[120:123], v[64:71], v209 abid:1\n\t"
    "ds_read_b32 v89, v76 offset:18436\n\t"
    "v_cvt_pk_bf16_f32 v100, v20, v21\n\t"
    "v_smfmac_f32_16x16x64_bf16 v[16:19], v[140:143], v[56:63], v212\n\t"
    "ds_read_b128 v[20:23], v244 offset:128\n\t"
    "v_cvt_pk_bf16_f32 v102, v8, v9\n\t"
    "v_smfmac_f32_16x16x64_bf16 v[16:19], v[160:163], v[40:47], v214 abid:1\n\t"
    "ds_read_b128 v[68:71], v236 offset:18560\n\t"
    "v_cndmask_b32_dpp v8, v100, v96, vcc quad_perm:[0,0,2,2] row_mask:0xf bank_mask:0xf\n\t"
    "v_smfmac_f32_16x16x64_bf16 v[32:35], v[144:147], v[56:63], v212 abid:1\n\t"
    "s_add_u32 s65, s57, s67\n\t"
    "ds_read_b128 v[12:15], v244 offset:64\n\t"
    "v_cndmask_b32_dpp v10, v102, v98, vcc quad_perm:[0,0,2,2] row_mask:0xf bank_mask:0xf\n\t"
    "v_smfmac_f32_16x16x64_bf16 v[32:35], v[164:167], v[40:47], v215\n\t"
    "s_waitcnt lgkmcnt(7)\n\t"
    "ds_read_b128 v[64:67], v217 offset:18560\n\t"
    "v_cndmask_b32_dpp v11, v103, v99, vcc quad_perm:[0,0,2,2] row_mask:0xf bank_mask:0xf\n\t"
    "v_mfma_f32_16x16x32_bf16 v[32:35], v[80:83], v[92:95], v[32:35]\n\t"
    "ds_read_b32 v95, v87 offset:18436\n\t"
    "v_cndmask_b32_dpp v9, v101, v97, vcc quad_perm:[0,0,2,2] row_mask:0xf bank_mask:0xf\n\t"
    "v_smfmac_f32_16x16x64_bf16 v[36:39], v[148:151], v[56:63], v213\n\t"
    "s_not_b64 vcc, s[18:19]\n\t"
    "ds_read_b128 v[80:83], v245 offset:2048\n\t"
    "v_cndmask_b32_dpp v102, v98, v102, vcc quad_perm:[1,1,3,3] row_mask:0xf bank_mask:0xf\n\t"
    "v_smfmac_f32_16x16x64_bf16 v[36:39], v[168:171], v[40:47], v215 abid:1\n\t"
    "v_cndmask_b32_dpp v103, v99, v103, vcc quad_perm:[1,1,3,3] row_mask:0xf bank_mask:0xf\n\t"
    "v_smfmac_f32_16x16x64_bf16 v[28:31], v[152:155], v[56:63], v213 abid:1\n\t"
    "buffer_store_dwordx4 v[8:11], v241, s[44:47], s67 offen offset:0 sc1\n\t"
    "ds_read_b128 v[60:63], v236 offset:18688\n\t"
    "v_cndmask_b32_dpp v100, v96, v100, vcc quad_perm:[1,1,3,3] row_mask:0xf bank_mask:0xf\n\t"
    "v_cndmask_b32_dpp v101, v97, v101, vcc quad_perm:[1,1,3,3] row_mask:0xf bank_mask:0xf\n\t"
    "v_cvt_pk_bf16_f32 v98, v16, v17\n\t"
    "v_smfmac_f32_16x16x64_bf16 v[28:31], v[172:175], v[40:47], v216\n\t"
    "ds_read_b32 v94, v229 offset:18436\n\t"
    "v_smfmac_f32_16x16x64_bf16 v[4:7], a[0:3], v[48:55], v176\n\t"
    ".Lpad0_%=:\n\t"
    "s_cbranch_execz .Lpad1_%=\n\t"
    "s_waitcnt lgkmcnt(4)\n\t"
    "ds_read_b128 v[8:11], v244 offset:192\n\t"
    "v_cvt_pk_bf16_f32 v96, v24, v25\n\t"
    "v_smfmac_f32_16x16x64_bf16 v[4:7], a[16:19], v[64:71], v178\n\t"
    "ds_read_b32 v93, v86 offset:18436\n\t"
    "v_cvt_pk_bf16_f32 v97, v26, v27\n\t"
    "v_smfmac_f32_16x16x64_bf16 v[12:15], a[4:7], v[48:55], v176 abid:1\n\t"
    "s_mov_b64 vcc, s[18:19]\n\t"
    "ds_read_b128 v[56:59], v217 offset:18688\n\t"
    "v_cvt_pk_bf16_f32 v99, v18, v19\n\t"
    "v_smfmac_f32_16x16x64_bf16 v[12:15], a[20:23], v[64:71], v178 abid:1\n\t"
    "ds_read_b128 v[24:27], v244 offset:256\n\t"
    "v_cvt_pk_bf16_f32 v28, v28, v29\n\t"
    "v_smfmac_f32_16x16x64_bf16 v[20:23], a[8:11], v[48:55], v177\n\t"
    "buffer_store_dwordx4 v[100:103], v241, s[44:47], s67 offen offset:1152 sc1\n\t"
    "ds_read_b128 v[16:19], v244 offset:320\n\t"
    "v_cvt_pk_bf16_f32 v29, v30, v31\n\t"
    "v_smfmac_f32_16x16x64_bf16 v[20:23], a[24:27], v[64:71], v179\n\t"
    "s_waitcnt lgkmcnt(7)\n\t"
    "ds_read_b32 v92, v228 offset:18436\n\t"
    "v_cvt_pk_bf16_f32 v101, v34, v35\n\t"
    "v_mfma_f32_16x16x32_bf16 v[20:23], v[80:83], v[88:91], v[20:23]\n\t"
    "s_waitcnt lgkmcnt(5)\n\t"
    "ds_read_b128 v[44:47], v236 offset:18816\n\t"
    "v_cvt_pk_bf16_f32 v100, v32, v33\n\t"
    "v_cvt_pk_bf16_f32 v102, v36, v37\n\t"
    "v_add_u32_sdwa v89, v248, v234 dst_sel:DWORD dst_unused:UNUSED_PAD src0_sel:DWORD src1_sel:WORD_1\n\t"
    "v_smfmac_f32_16x16x64_bf16 v[8:11], a[12:15], v[48:55], v177 abid:1\n\t"
    "ds_read_b128 v[32:35], v244 offset:384\n\t"
    "v_cvt_pk_bf16_f32 v103, v38, v39\n\t"
    "v_smfmac_f32_16x16x64_bf16 v[8:11], a[28:31], v[64:71], v179 abid:1\n\t"
    "s_waitcnt lgkmcnt(5)\n\t"
    "buffer_load_dwordx4 v239, s[40:43], s68 offen offset:2560 nt lds\n\t"
    "v_smfmac_f32_16x16x64_bf16 v[8:11], a[44:47], v[56:63], v181 abid:1\n\t"
    "s_waitcnt lgkmcnt(4)\n\t"
    "ds_read_b128 v[80:83], v245 offset:5120\n\t"
    "v_cndmask_b32_dpp v36, v100, v96, vcc quad_perm:[0,0,2,2] row_mask:0xf bank_mask:0xf\n\t"
    "v_cndmask_b32_dpp v37, v101, v97, vcc quad_perm:[0,0,2,2] row_mask:0xf bank_mask:0xf\n\t"
    "v_smfmac_f32_16x16x64_bf16 v[24:27], a[144:147], v[48:55], v194\n\t"
    "ds_read_b128 v[40:43], v217 offset:18816\n\t"
    "v_cndmask_b32_dpp v38, v102, v98, vcc quad_perm:[0,0,2,2] row_mask:0xf bank_mask:0xf\n\t"
    "v_smfmac_f32_16x16x64_bf16 v[24:27], a[164:167], v[64:71], v196 abid:1\n\t"
    "v_cndmask_b32_dpp v39, v103, v99, vcc quad_perm:[0,0,2,2] row_mask:0xf bank_mask:0xf\n\t"
    "v_smfmac_f32_16x16x64_bf16 v[24:27], a[184:187], v[56:63], v199\n\t"
    "s_waitcnt lgkmcnt(5)\n\t"
    "s_not_b64 vcc, s[18:19]\n\t"
    "buffer_store_dwordx4 v[36:39], v241, s[44:47], s67 offen offset:128 sc1\n\t"
    "ds_write_b64 v242, v[28:29] offset:512\n\t"
    "v_cndmask_b32_dpp v100, v96, v100, vcc quad_perm:[1,1,3,3] row_mask:0xf bank_mask:0xf\n\t"
    "v_smfmac_f32_16x16x64_bf16 v[16:19], a[148:151], v[48:55], v194 abid:1\n\t"
    "ds_read_b128 v[36:39], v244 offset:448\n\t"
    "v_add_u32_sdwa v90, v248, v235 dst_sel:DWORD dst_unused:UNUSED_PAD src0_sel:DWORD src1_sel:WORD_0\n\t"
    "v_smfmac_f32_16x16x64_bf16 v[16:19], a[168:171], v[64:71], v197\n\t"
    "v_cndmask_b32_dpp v101, v97, v101, vcc quad_perm:[1,1,3,3] row_mask:0xf bank_mask:0xf\n\t"
    "v_smfmac_f32_16x16x64_bf16 v[16:19], a[188:191], v[56:63], v199 abid:1\n\t"
    "s_waitcnt lgkmcnt(3)\n\t"
    "ds_read_b128 v[28:31], v244 offset:512\n\t"
    "v_cndmask_b32_dpp v103, v99, v103, vcc quad_perm:[1,1,3,3] row_mask:0xf bank_mask:0xf\n\t"
    "v_mfma_f32_16x16x32_bf16 v[16:19], v[80:83], v[92:95], v[16:19]\n\t"
    "ds_read_b32 v89, v89 offset:18436\n\t"
    "v_add_u32_sdwa v91, v248, v235 dst_sel:DWORD dst_unused:UNUSED_PAD src0_sel:DWORD src1_sel:WORD_1\n\t"
    "v_smfmac_f32_16x16x64_bf16 v[32:35], a[152:155], v[48:55], v195\n\t"
    "ds_read_b32 v94, v221 offset:18436\n\t"
    "v_cndmask_b32_dpp v102, v98, v102, vcc quad_perm:[1,1,3,3] row_mask:0xf bank_mask:0xf\n\t"
    "v_smfmac_f32_16x16x64_bf16 v[32:35], a[172:175], v[64:71], v197 abid:1\n\t"
    "buffer_store_dwordx4 v[100:103], v241, s[44:47], s67 offen offset:1280 sc1\n\t"
    "ds_read_b32 v90, v90 offset:18436\n\t"
    "ds_read_b32 v91, v91 offset:18436\n\t"
    "v_smfmac_f32_16x16x64_bf16 v[32:35], a[192:195], v[56:63], v200\n\t"
    "s_waitcnt lgkmcnt(7)\n\t"
    "v_add_u32_sdwa v88, v248, v234 dst_sel:DWORD dst_unused:UNUSED_PAD src0_sel:DWORD src1_sel:WORD_0\n\t"
    "v_smfmac_f32_16x16x64_bf16 v[32:35], a[212:215], v[40:47], v202 abid:1\n\t"
    "s_waitcnt lgkmcnt(5)\n\t"
    "v_smfmac_f32_16x16x64_bf16 v[36:39], a[156:159], v[48:55], v195 abid:1\n\t"
    "ds_read_b32 v88, v88 offset:18436\n\t"
    "ds_read_b32 v93, v74 offset:18436\n\t"
    "v_smfmac_f32_16x16x64_bf16 v[36:39], a[176:179], v[64:71], v198\n\t"
    "ds_read_b128 v[80:83], v245 offset:8192\n\t"
    "v_smfmac_f32_16x16x64_bf16 v[36:39], a[196:199], v[56:63], v200 abid:1\n\t"
    "ds_read_b32 v92, v220 offset:18436\n\t"
    "v_smfmac_f32_16x16x64_bf16 v[36:39], a[216:219], v[40:47], v203\n\t"
    "s_waitcnt lgkmcnt(8)\n\t"
    "buffer_load_dwordx4 v239, s[40:43], s68 offen offset:3584 nt lds\n\t"
    "v_smfmac_f32_16x16x64_bf16 v[28:31], a[160:163], v[48:55], v196\n\t"
    "ds_read_b128 v[48:51], v217 offset:18944\n\t"
    "ds_read_b128 v[52:55], v236 offset:18944\n\t"
    "v_smfmac_f32_16x16x64_bf16 v[28:31], a[180:183], v[64:71], v198 abid:1\n\t"
    "v_smfmac_f32_16x16x64_bf16 v[28:31], a[200:203], v[56:63], v201\n\t"
    "ds_read_b128 v[64:67], v217 offset:19072\n\t"
    "v_smfmac_f32_16x16x64_bf16 v[28:31], a[220:223], v[40:47], v203 abid:1\n\t"
    "s_waitcnt lgkmcnt(4)\n\t"
    "ds_read_b32 v95, v75 offset:18436\n\t"
    "v_mfma_f32_16x16x32_bf16 v[28:31], v[80:83], v[88:91], v[28:31]\n\t"
    "ds_read_b128 v[80:83], v245 offset:1024\n\t"
    "v_add_u32_sdwa v89, v248, v246 dst_sel:DWORD dst_unused:UNUSED_PAD src0_sel:DWORD src1_sel:WORD_1\n\t"
    "v_add_u32_sdwa v90, v248, v247 dst_sel:DWORD dst_unused:UNUSED_PAD src0_sel:DWORD src1_sel:WORD_0\n\t"
    "v_smfmac_f32_16x16x64_bf16 v[4:7], a[32:35], v[56:63], v180\n\t"
    "ds_read_u16 v89, v89 offset:18432\n\t"
    "v_add_u32_sdwa v88, v248, v246 dst_sel:DWORD dst_unused:UNUSED_PAD src0_sel:DWORD src1_sel:WORD_0\n\t"
    "v_smfmac_f32_16x16x64_bf16 v[4:7], a[48:51], v[40:47], v182\n\t"
    "s_waitcnt lgkmcnt(4)\n\t"
    "ds_read_u16 v88, v88 offset:18432\n\t"
    "v_add_u32_sdwa v91, v248, v247 dst_sel:DWORD dst_unused:UNUSED_PAD src0_sel:DWORD src1_sel:WORD_1\n\t"
    "v_smfmac_f32_16x16x64_bf16 v[4:7], a[64:67], v[48:55], v184\n\t"
    "s_bitcmp1_b32 s54, 14\n\t"
    "s_mov_b32 s68, 66060288\n\t"
    "s_cmov_b32 s68, 66134016\n\t"
    "v_smfmac_f32_16x16x64_bf16 v[12:15], a[36:39], v[56:63], v180 abid:1\n\t"
    "s_waitcnt vmcnt(22)\n\t"
    "s_barrier\n\t"
    "ds_read_u16 v90, v90 offset:18432\n\t"
    "ds_read_u16 v91, v91 offset:18432\n\t"
    "v_smfmac_f32_16x16x64_bf16 v[12:15], a[52:55], v[40:47], v182 abid:1\n\t"
    "ds_read_b128 v[96:99], v243 offset:2560\n\t"
    "v_smfmac_f32_16x16x64_bf16 v[12:15], a[68:71], v[48:55], v184 abid:1\n\t"
    "s_waitcnt lgkmcnt(5)\n\t"
    "ds_read_b128 v[68:71], v236 offset:19072\n\t"
    "v_mfma_f32_16x16x32_bf16 v[12:15], v[80:83], v[92:95], v[12:15]\n\t"
    "v_smfmac_f32_16x16x64_bf16 v[20:23], a[40:43], v[56:63], v181\n\t"
    "ds_read_b32 v93, v84 offset:18436\n\t"
    "ds_read_b32 v95, v85 offset:18436\n\t"
    "v_smfmac_f32_16x16x64_bf16 v[20:23], a[56:59], v[40:47], v183\n\t"
    "ds_read_b128 v[80:83], v245 offset:4096\n\t"
    "v_smfmac_f32_16x16x64_bf16 v[20:23], a[72:75], v[48:55], v185\n\t"
    "s_waitcnt lgkmcnt(5)\n\t"
    "s_set_gpr_idx_on s4, gpr_idx(SRC2,DST)\n\t"
    "v_mfma_f32_16x16x32_bf16 v[0:3], v[250:253], v[88:91], v[0:3]\n\t"
    "s_set_gpr_idx_off\n\t"
    "s_waitcnt lgkmcnt(4)\n\t"
    "s_mov_b32 exec_hi, 0\n\t"
    "buffer_store_dwordx4 v[96:99], v240, s[44:47], s65 offen offset:0 sc1\n\t"
    "s_mov_b32 exec_hi, -1\n\t"
    "ds_read_b32 v94, v227 offset:18436\n\t"
    "v_smfmac_f32_16x16x64_bf16 v[8:11], a[60:63], v[40:47], v183 abid:1\n\t"
    "ds_read_b32 v92, v226 offset:18436\n\t"
    "v_smfmac_f32_16x16x64_bf16 v[8:11], a[76:79], v[48:55], v185 abid:1\n\t"
    "s_waitcnt lgkmcnt(5)\n\t"
    "ds_read_b128 v[60:63], v236 offset:19200\n\t"
    "v_smfmac_f32_16x16x64_bf16 v[8:11], a[92:95], v[64:71], v187 abid:1\n\t"
    "ds_read_b128 v[56:59], v217 offset:19200\n\t"
    "s_set_gpr_idx_on s5, gpr_idx(SRC2,DST)\n\t"
    "v_mfma_f32_16x16x32_bf16 v[0:3], v[252:255], v[88:91], v[0:3]\n\t"
    "s_set_gpr_idx_off\n\t"
    "s_add_u32 m0, s52, 9216\n\t"
    "s_mov_b32 exec_hi, 0\n\t"
    "buffer_load_dwordx4 v237, s[40:43], s68 offen offset:0 nt lds\n\t"
    "s_mov_b32 exec_hi, -1\n\t"
    "s_cmp_lg_u32 s22, 0\n\t"
    "s_cbranch_scc1 .Lsl15_%=\n\t"
    ".Lslr15_%=:\n\t"
    "v_smfmac_f32_16x16x64_bf16 v[24:27], a[204:207], v[40:47], v201 abid:1\n\t"
    "ds_read_b32 v90, v233 offset:18436\n\t"
    "v_smfmac_f32_16x16x64_bf16 v[24:27], a[224:227], v[48:55], v204\n\t"
    "s_memrealtime s[84:85]\n\t"
    "ds_read_b32 v89, v106 offset:18436\n\t"
    "v_smfmac_f32_16x16x64_bf16 v[24:27], a[244:247], v[64:71], v206 abid:1\n\t"
    "s_waitcnt lgkmcnt(4)\n\t"
    "ds_read_b32 v88, v232 offset:18436\n\t"
    "v_mfma_f32_16x16x32_bf16 v[24:27], v[80:83], v[92:95], v[24:27]\n\t"
    "ds_read_b128 v[80:83], v245 offset:7168\n\t"
    "v_smfmac_f32_16x16x64_bf16 v[16:19], a[208:211], v[40:47], v202\n\t"
    "ds_read_b32 v93, v72 offset:18436\n\t"
    "v_smfmac_f32_16x16x64_bf16 v[16:19], a[228:231], v[48:55], v204 abid:1\n\t"
    "ds_read_b32 v91, v107 offset:18436\n\t"
    "v_smfmac_f32_16x16x64_bf16 v[16:19], a[248:251], v[64:71], v207\n\t"
    "s_sub_i32 s87, s87, s99\n\t"
    "ds_read_b128 v[44:47], v236 offset:19328\n\t"
    "v_smfmac_f32_16x16x64_bf16 v[32:35], a[232:235], v[48:55], v205\n\t"
    "v_smfmac_f32_16x16x64_bf16 v[32:35], a[252:255], v[64:71], v207 abid:1\n\t"
    "s_waitcnt lgkmcnt(7)\n\t"
    "s_sub_i32 s98, s98, s99\n\t"
    "ds_read_b128 v[40:43], v217 offset:19328\n\t"
    "v_smfmac_f32_16x16x64_bf16 v[32:35], v[124:127], v[56:63], v210\n\t"
    "s_add_i32 s86, s87, 2019\n\t"
    "ds_read_b32 v92, v218 offset:18436\n\t"
    "v_smfmac_f32_16x16x64_bf16 v[36:39], a[236:239], v[48:55], v205 abid:1\n\t"
    "s_add_i32 s98, s98, 72\n\t"
    "ds_read_b32 v94, v219 offset:18436\n\t"
    "v_smfmac_f32_16x16x64_bf16 v[36:39], v[108:111], v[64:71], v208\n\t"
    "s_max_i32 s86, s86, s98\n\t"
    "ds_read_b32 v95, v73 offset:18436\n\t"
    "v_smfmac_f32_16x16x64_bf16 v[36:39], v[128:131], v[56:63], v210 abid:1\n\t"
    "s_waitcnt lgkmcnt(5)\n\t"
    "v_mfma_f32_16x16x32_bf16 v[36:39], v[80:83], v[88:91], v[36:39]\n\t"
    "s_sub_u32 s89, s84, s24\n\t"
    "ds_read_b128 v[80:83], v245 offset:0\n\t"
    "v_smfmac_f32_16x16x64_bf16 v[28:31], a[240:243], v[48:55], v206\n\t"
    "s_cmp_lt_i32 s89, s86\n\t"
    "s_cbranch_scc1 .Lpc10_%=\n\t"
    ".Lpc10r_%=:\n\t"
    "ds_read_b128 v[52:55], v236 offset:19456\n\t"
    "v_smfmac_f32_16x16x64_bf16 v[28:31], v[112:115], v[64:71], v208 abid:1\n\t"
    "ds_read_b128 v[48:51], v217 offset:19456\n\t"
    "v_smfmac_f32_16x16x64_bf16 v[28:31], v[132:135], v[56:63], v211\n\t"
    "s_max_i32 s98, s89, s86\n\t"
    "v_smfmac_f32_16x16x64_bf16 v[4:7], a[80:83], v[64:71], v186\n\t"
    "ds_read_b32 v89, v78 offset:18436\n\t"
    "v_smfmac_f32_16x16x64_bf16 v[4:7], a[96:99], v[56:63], v188\n\t"
    "s_waitcnt lgkmcnt(7)\n\t"
    "s_mov_b64 vcc, s[18:19]\n\t"
    "ds_read_b32 v88, v224 offset:18436\n\t"
    "ds_read_b32 v90, v225 offset:18436\n\t"
    "v_smfmac_f32_16x16x64_bf16 v[4:7], a[112:115], v[40:47], v190\n\t"
    "s_add_u32 m0, s52, 9216\n\t"
    "s_waitcnt lgkmcnt(3)\n\t"
    "buffer_load_dwordx4 v237, s[40:43], s68 offen offset:512 nt lds\n\t"
    "v_smfmac_f32_16x16x64_bf16 v[4:7], a[128:131], v[48:55], v192\n\t"
    "v_mfma_f32_16x16x32_bf16 v[4:7], v[80:83], v[92:95], v[4:7]\n\t"
    "ds_read_b32 v92, v230 offset:18436\n\t"
    "v_smfmac_f32_16x16x64_bf16 v[12:15], a[84:87], v[64:71], v186 abid:1\n\t"
    "ds_read_b32 v95, v105 offset:18436\n\t"
    "v_smfmac_f32_16x16x64_bf16 v[12:15], a[100:103], v[56:63], v188 abid:1\n\t"
    "ds_read_b32 v94, v231 offset:18436\n\t"
    "v_smfmac_f32_16x16x64_bf16 v[12:15], a[116:119], v[40:47], v190 abid:1\n\t"
    "ds_read_b128 v[80:83], v245 offset:3072\n\t"
    "v_smfmac_f32_16x16x64_bf16 v[12:15], a[132:135], v[48:55], v192 abid:1\n\t"
    "s_bitcmp1_b32 s54, 10\n\t"
    "s_mov_b32 s66, 47185920\n\t"
    "s_cmov_b32 s66, 47259648\n\t"
    "v_smfmac_f32_16x16x64_bf16 v[20:23], a[88:91], v[64:71], v187\n\t"
    "ds_read_b32 v91, v79 offset:18436\n\t"
    "v_smfmac_f32_16x16x64_bf16 v[20:23], a[104:107], v[56:63], v189\n\t"
    "ds_read_b32 v93, v104 offset:18436\n\t"
    "v_cvt_pk_bf16_f32 v101, v6, v7\n\t"
    "v_smfmac_f32_16x16x64_bf16 v[20:23], a[120:123], v[40:47], v191\n\t"
    "ds_read_b128 v[64:67], v217 offset:23040\n\t"
    "v_cvt_pk_bf16_f32 v102, v12, v13\n\t"
    "v_smfmac_f32_16x16x64_bf16 v[20:23], a[136:139], v[48:55], v193\n\t"
    "v_cvt_pk_bf16_f32 v103, v14, v15\n\t"
    "v_smfmac_f32_16x16x64_bf16 v[8:11], a[108:111], v[56:63], v189 abid:1\n\t"
    "ds_read_b128 v[68:71], v236 offset:23040\n\t"
    "v_cvt_pk_bf16_f32 v100, v4, v5\n\t"
    "v_smfmac_f32_16x16x64_bf16 v[8:11], a[124:127], v[40:47], v191 abid:1\n\t"
    "ds_read_b128 v[4:7], v244 offset:0\n\t"
    "v_smfmac_f32_16x16x64_bf16 v[8:11], a[140:143], v[48:55], v193 abid:1\n\t"
    "s_waitcnt lgkmcnt(4)\n\t"
    "v_cvt_pk_bf16_f32 v96, v20, v21\n\t"
    "v_mfma_f32_16x16x32_bf16 v[8:11], v[80:83], v[88:91], v[8:11]\n\t"
    "s_add_u32 s67, s57, s66\n\t"
    "ds_read_b128 v[12:15], v244 offset:64\n\t"
    "v_cvt_pk_bf16_f32 v97, v22, v23\n\t"
    "v_smfmac_f32_16x16x64_bf16 v[24:27], v[116:119], v[56:63], v209\n\t"
    "ds_read_b128 v[20:23], v244 offset:128\n\t"
    "v_smfmac_f32_16x16x64_bf16 v[24:27], v[136:139], v[40:47], v211 abid:1\n\t"
    "ds_read_b128 v[80:83], v245 offset:6144\n\t"
    "v_smfmac_f32_16x16x64_bf16 v[24:27], v[156:159], v[48:55], v214\n\t"
    "ds_read_b32 v90, v223 offset:23044\n\t"
    "v_cvt_pk_bf16_f32 v98, v8, v9\n\t"
    "v_smfmac_f32_16x16x64_bf16 v[16:19], v[120:123], v[56:63], v209 abid:1\n\t"
    "v_cvt_pk_bf16_f32 v99, v10, v11\n\t"
    "v_smfmac_f32_16x16x64_bf16 v[16:19], v[140:143], v[40:47], v212\n\t"
    "ds_read_b128 v[60:63], v236 offset:23168\n\t"
    "v_cndmask_b32_dpp v9, v97, v101, vcc quad_perm:[0,0,2,2] row_mask:0xf bank_mask:0xf\n\t"
    "v_smfmac_f32_16x16x64_bf16 v[16:19], v[160:163], v[48:55], v214 abid:1\n\t"
    "ds_read_b128 v[56:59], v217 offset:23168\n\t"
    "v_cndmask_b32_dpp v11, v99, v103, vcc quad_perm:[0,0,2,2] row_mask:0xf bank_mask:0xf\n\t"
    "v_smfmac_f32_16x16x64_bf16 v[32:35], v[144:147], v[40:47], v212 abid:1\n\t"
    "ds_read_b32 v91, v77 offset:23044\n\t"
    "v_cndmask_b32_dpp v10, v98, v102, vcc quad_perm:[0,0,2,2] row_mask:0xf bank_mask:0xf\n\t"
    "v_smfmac_f32_16x16x64_bf16 v[32:35], v[164:167], v[48:55], v215\n\t"
    "s_waitcnt lgkmcnt(4)\n\t"
    "v_mfma_f32_16x16x32_bf16 v[32:35], v[80:83], v[92:95], v[32:35]\n\t"
    "ds_read_b32 v89, v76 offset:23044\n\t"
    "v_cndmask_b32_dpp v8, v96, v100, vcc quad_perm:[0,0,2,2] row_mask:0xf bank_mask:0xf\n\t"
    "v_smfmac_f32_16x16x64_bf16 v[36:39], v[148:151], v[40:47], v213\n\t"
    "s_not_b64 vcc, s[18:19]\n\t"
    "ds_read_b32 v88, v222 offset:23044\n\t"
    "ds_read_b32 v92, v228 offset:23044\n\t"
    "v_smfmac_f32_16x16x64_bf16 v[36:39], v[168:171], v[48:55], v215 abid:1\n\t"
    "v_cndmask_b32_dpp v98, v102, v98, vcc quad_perm:[1,1,3,3] row_mask:0xf bank_mask:0xf\n\t"
    "v_smfmac_f32_16x16x64_bf16 v[28:31], v[152:155], v[40:47], v213 abid:1\n\t"
    "buffer_store_dwordx4 v[8:11], v241, s[44:47], s66 offen offset:0 sc1\n\t"
    "ds_read_b128 v[80:83], v245 offset:2048\n\t"
    "v_cndmask_b32_dpp v96, v100, v96, vcc quad_perm:[1,1,3,3] row_mask:0xf bank_mask:0xf\n\t"
    "v_cndmask_b32_dpp v99, v103, v99, vcc quad_perm:[1,1,3,3] row_mask:0xf bank_mask:0xf\n\t"
    "v_smfmac_f32_16x16x64_bf16 v[28:31], v[172:175], v[48:55], v216\n\t"
    "ds_read_b128 v[8:11], v244 offset:192\n\t"
    "v_cndmask_b32_dpp v97, v101, v97, vcc quad_perm:[1,1,3,3] row_mask:0xf bank_mask:0xf\n\t"
    "v_smfmac_f32_16x16x64_bf16 v[4:7], a[0:3], v[64:71], v176\n\t"
    "s_waitcnt lgkmcnt(6)\n\t"
    "ds_read_b128 v[48:51], v217 offset:23424\n\t"
    "v_cvt_pk_bf16_f32 v100, v24, v25\n\t"
    "v_smfmac_f32_16x16x64_bf16 v[4:7], a[16:19], v[56:63], v178\n\t"
    "s_mov_b64 vcc, s[18:19]\n\t"
    "ds_read_b128 v[40:43], v217 offset:23296\n\t"
    "v_cvt_pk_bf16_f32 v102, v16, v17\n\t"
    "v_smfmac_f32_16x16x64_bf16 v[12:15], a[4:7], v[64:71], v176 abid:1\n\t"
    "ds_read_b128 v[44:47], v236 offset:23296\n\t"
    "v_cvt_pk_bf16_f32 v101, v26, v27\n\t"
    "v_smfmac_f32_16x16x64_bf16 v[12:15], a[20:23], v[56:63], v178 abid:1\n\t"
    "ds_read_b128 v[24:27], v244 offset:256\n\t"
    "v_cvt_pk_bf16_f32 v28, v28, v29\n\t"
    "v_cvt_pk_bf16_f32 v29, v30, v31\n\t"
    "v_smfmac_f32_16x16x64_bf16 v[20:23], a[8:11], v[64:71], v177\n\t"
    "buffer_store_dwordx4 v[96:99], v241, s[44:47], s66 offen offset:1152 sc1\n\t"
    "ds_write_b64 v242, v[28:29] offset:0\n\t"
    "v_cvt_pk_bf16_f32 v103, v18, v19\n\t"
    "v_smfmac_f32_16x16x64_bf16 v[20:23], a[24:27], v[56:63], v179\n\t"
    "s_waitcnt lgkmcnt(6)\n\t"
    "ds_read_b32 v94, v229 offset:23044\n\t"
    "v_cvt_pk_bf16_f32 v99, v38, v39\n\t"
    "v_mfma_f32_16x16x32_bf16 v[20:23], v[80:83], v[88:91], v[20:23]\n\t"
    "s_waitcnt lgkmcnt(6)\n\t"
    "ds_read_b128 v[16:19], v244 offset:320\n\t"
    "v_cvt_pk_bf16_f32 v96, v32, v33\n\t"
    "v_cvt_pk_bf16_f32 v97, v34, v35\n\t"
    "v_smfmac_f32_16x16x64_bf16 v[8:11], a[12:15], v[64:71], v177 abid:1\n\t"
    "ds_read_b128 v[80:83], v245 offset:5120\n\t"
    "v_cvt_pk_bf16_f32 v98, v36, v37\n\t"
    "v_smfmac_f32_16x16x64_bf16 v[8:11], a[28:31], v[56:63], v179 abid:1\n\t"
    "s_waitcnt lgkmcnt(5)\n\t"
    "buffer_load_dwordx4 v238, s[40:43], s68 offen offset:1536 nt lds\n\t"
    "ds_read_b32 v95, v87 offset:23044\n\t"
    "v_cndmask_b32_dpp v39, v99, v103, vcc quad_perm:[0,0,2,2] row_mask:0xf bank_mask:0xf\n\t"
    "v_smfmac_f32_16x16x64_bf16 v[8:11], a[44:47], v[40:47], v181 abid:1\n\t"
    "s_waitcnt lgkmcnt(5)\n\t"
    "ds_read_b32 v93, v86 offset:23044\n\t"
    "v_cndmask_b32_dpp v38, v98, v102, vcc quad_perm:[0,0,2,2] row_mask:0xf bank_mask:0xf\n\t"
    "v_smfmac_f32_16x16x64_bf16 v[24:27], a[144:147], v[64:71], v194\n\t"
    "ds_read_b128 v[32:35], v244 offset:384\n\t"
    "v_cndmask_b32_dpp v36, v96, v100, vcc quad_perm:[0,0,2,2] row_mask:0xf bank_mask:0xf\n\t"
    "v_smfmac_f32_16x16x64_bf16 v[24:27], a[164:167], v[56:63], v196 abid:1\n\t"
    "v_cndmask_b32_dpp v37, v97, v101, vcc quad_perm:[0,0,2,2] row_mask:0xf bank_mask:0xf\n\t"
    "v_smfmac_f32_16x16x64_bf16 v[24:27], a[184:187], v[40:47], v199\n\t"
    "s_waitcnt lgkmcnt(4)\n\t"
    "s_not_b64 vcc, s[18:19]\n\t"
    "buffer_store_dwordx4 v[36:39], v241, s[44:47], s66 offen offset:128 sc1\n\t"
    "ds_read_b128 v[52:55], v236 offset:23424\n\t"
    "v_cndmask_b32_dpp v97, v101, v97, vcc quad_perm:[1,1,3,3] row_mask:0xf bank_mask:0xf\n\t"
    "v_add_u32_sdwa v88, v248, v234 dst_sel:DWORD dst_unused:UNUSED_PAD src0_sel:DWORD src1_sel:WORD_0\n\t"
    "v_add_u32_sdwa v91, v248, v235 dst_sel:DWORD dst_unused:UNUSED_PAD src0_sel:DWORD src1_sel:WORD_1\n\t"
    "v_smfmac_f32_16x16x64_bf16 v[16:19], a[148:151], v[64:71], v194 abid:1\n\t"
    "ds_read_b128 v[28:31], v244 offset:512\n\t"
    "v_add_u32_sdwa v90, v248, v235 dst_sel:DWORD dst_unused:UNUSED_PAD src0_sel:DWORD src1_sel:WORD_0\n\t"
    "v_smfmac_f32_16x16x64_bf16 v[16:19], a[168:171], v[56:63], v197\n\t"
    "ds_read_b128 v[36:39], v244 offset:448\n\t"
    "v_cndmask_b32_dpp v96, v100, v96, vcc quad_perm:[1,1,3,3] row_mask:0xf bank_mask:0xf\n\t"
    "v_smfmac_f32_16x16x64_bf16 v[16:19], a[188:191], v[40:47], v199 abid:1\n\t"
    "s_waitcnt lgkmcnt(4)\n\t"
    "ds_read_b32 v91, v91 offset:23044\n\t"
    "v_cndmask_b32_dpp v98, v102, v98, vcc quad_perm:[1,1,3,3] row_mask:0xf bank_mask:0xf\n\t"
    "v_mfma_f32_16x16x32_bf16 v[16:19], v[80:83], v[92:95], v[16:19]\n\t"
    "s_waitcnt lgkmcnt(4)\n\t"
    "s_bitcmp1_b32 s54, 11\n\t"
    "s_mov_b32 s65, 51904512\n\t"
    "s_cmov_b32 s65, 51978240\n\t"
    "v_smfmac_f32_16x16x64_bf16 v[32:35], a[152:155], v[64:71], v195\n\t"
    "ds_read_b32 v90, v90 offset:23044\n\t"
    "v_cndmask_b32_dpp v99, v103, v99, vcc quad_perm:[1,1,3,3] row_mask:0xf bank_mask:0xf\n\t"
    "v_smfmac_f32_16x16x64_bf16 v[32:35], a[172:175], v[56:63], v197 abid:1\n\t"
    "buffer_store_dwordx4 v[96:99], v241, s[44:47], s66 offen offset:1280 sc1\n\t"
    "ds_read_b32 v88, v88 offset:23044\n\t"
    "v_smfmac_f32_16x16x64_bf16 v[32:35], a[192:195], v[40:47], v200\n\t"
    "s_waitcnt lgkmcnt(5)\n\t"
    "ds_read_b32 v92, v220 offset:23044\n\t"
    "ds_read_b32 v95, v75 offset:23044\n\t"
    "v_smfmac_f32_16x16x64_bf16 v[32:35], a[212:215], v[48:55], v202 abid:1\n\t"
    "s_waitcnt lgkmcnt(5)\n\t"
    "v_smfmac_f32_16x16x64_bf16 v[36:39], a[156:159], v[64:71], v195 abid:1\n\t"
    "ds_read_b128 v[80:83], v245 offset:8192\n\t"
    "v_add_u32_sdwa v89, v248, v234 dst_sel:DWORD dst_unused:UNUSED_PAD src0_sel:DWORD src1_sel:WORD_1\n\t"
    "v_smfmac_f32_16x16x64_bf16 v[36:39], a[176:179], v[56:63], v198\n\t"
    "buffer_load_dwordx4 v239, s[40:43], s68 offen offset:2560 nt lds\n\t"
    "ds_read_b32 v89, v89 offset:23044\n\t"
    "v_smfmac_f32_16x16x64_bf16 v[36:39], a[196:199], v[40:47], v200 abid:1\n\t"
    "ds_read_b32 v93, v74 offset:23044\n\t"
    "v_smfmac_f32_16x16x64_bf16 v[36:39], a[216:219], v[48:55], v203\n\t"
    "v_smfmac_f32_16x16x64_bf16 v[28:31], a[160:163], v[64:71], v196\n\t"
    "ds_read_b128 v[64:67], v217 offset:23552\n\t"
    "ds_read_b128 v[68:71], v236 offset:23552\n\t"
    "v_smfmac_f32_16x16x64_bf16 v[28:31], a[180:183], v[56:63], v198 abid:1\n\t"
    "v_smfmac_f32_16x16x64_bf16 v[28:31], a[200:203], v[40:47], v201\n\t"
    "ds_read_b32 v94, v221 offset:23044\n\t"
    "v_smfmac_f32_16x16x64_bf16 v[28:31], a[220:223], v[48:55], v203 abid:1\n\t"
    "s_waitcnt lgkmcnt(4)\n\t"
    "ds_read_b128 v[60:63], v236 offset:23680\n\t"
    "v_mfma_f32_16x16x32_bf16 v[28:31], v[80:83], v[88:91], v[28:31]\n\t"
    "ds_read_b128 v[80:83], v245 offset:1024\n\t"
    "v_add_u32_sdwa v91, v248, v247 dst_sel:DWORD dst_unused:UNUSED_PAD src0_sel:DWORD src1_sel:WORD_1\n\t"
    "v_smfmac_f32_16x16x64_bf16 v[4:7], a[32:35], v[40:47], v180\n\t"
    "ds_read_u16 v91, v91 offset:23040\n\t"
    "v_add_u32_sdwa v89, v248, v246 dst_sel:DWORD dst_unused:UNUSED_PAD src0_sel:DWORD src1_sel:WORD_1\n\t"
    "v_smfmac_f32_16x16x64_bf16 v[4:7], a[48:51], v[48:55], v182\n\t"
    "s_waitcnt lgkmcnt(4)\n\t"
    "ds_read_u16 v89, v89 offset:23040\n\t"
    "v_add_u32_sdwa v88, v248, v246 dst_sel:DWORD dst_unused:UNUSED_PAD src0_sel:DWORD src1_sel:WORD_0\n\t"
    "v_smfmac_f32_16x16x64_bf16 v[4:7], a[64:67], v[64:71], v184\n\t"
    "ds_read_u16 v88, v88 offset:23040\n\t"
    "v_add_u32_sdwa v90, v248, v247 dst_sel:DWORD dst_unused:UNUSED_PAD src0_sel:DWORD src1_sel:WORD_0\n\t"
    "v_smfmac_f32_16x16x64_bf16 v[12:15], a[36:39], v[40:47], v180 abid:1\n\t"
    "s_waitcnt vmcnt(19)\n\t"
    "s_barrier\n\t"
    "ds_read_b128 v[100:103], v243 offset:2048\n\t"
    "ds_read_u16 v90, v90 offset:23040\n\t"
    "v_smfmac_f32_16x16x64_bf16 v[12:15], a[52:55], v[48:55], v182 abid:1\n\t"
    "buffer_load_dwordx4 v239, s[40:43], s68 offen offset:3584 nt lds\n\t"
    "v_smfmac_f32_16x16x64_bf16 v[12:15], a[68:71], v[64:71], v184 abid:1\n\t"
    "s_waitcnt lgkmcnt(5)\n\t"
    "ds_read_b128 v[56:59], v217 offset:23680\n\t"
    "v_mfma_f32_16x16x32_bf16 v[12:15], v[80:83], v[92:95], v[12:15]\n\t"
    "ds_read_b32 v93, v84 offset:23044\n\t"
    "v_smfmac_f32_16x16x64_bf16 v[20:23], a[40:43], v[40:47], v181\n\t"
    "ds_read_b128 v[80:83], v245 offset:4096\n\t"
    "v_smfmac_f32_16x16x64_bf16 v[20:23], a[56:59], v[48:55], v183\n\t"
    "ds_read_b128 v[40:43], v217 offset:23808\n\t"
    "v_smfmac_f32_16x16x64_bf16 v[20:23], a[72:75], v[64:71], v185\n\t"
    "s_waitcnt lgkmcnt(4)\n\t"
    "s_set_gpr_idx_on s4, gpr_idx(SRC2,DST)\n\t"
    "v_mfma_f32_16x16x32_bf16 v[0:3], v[250:253], v[88:91], v[0:3]\n\t"
    "s_set_gpr_idx_off\n\t"
    "s_mov_b32 exec_hi, 0\n\t"
    "buffer_store_dwordx4 v[100:103], v240, s[44:47], s67 offen offset:0 sc1\n\t"
    "s_mov_b32 exec_hi, -1\n\t"
    "ds_read_b32 v92, v226 offset:23044\n\t"
    "v_smfmac_f32_16x16x64_bf16 v[8:11], a[60:63], v[48:55], v183 abid:1\n\t"
    "ds_read_b32 v94, v227 offset:23044\n\t"
    "ds_read_b32 v95, v85 offset:23044\n\t"
    "v_smfmac_f32_16x16x64_bf16 v[8:11], a[76:79], v[64:71], v185 abid:1\n\t"
    "s_waitcnt lgkmcnt(6)\n\t"
    "s_bitcmp1_b32 s54, 15\n\t"
    "s_mov_b32 s68, 70778880\n\t"
    "s_cmov_b32 s68, 70852608\n\t"
    "v_smfmac_f32_16x16x64_bf16 v[8:11], a[92:95], v[56:63], v187 abid:1\n\t"
    "ds_read_b128 v[44:47], v236 offset:23808\n\t"
    "s_set_gpr_idx_on s5, gpr_idx(SRC2,DST)\n\t"
    "v_mfma_f32_16x16x32_bf16 v[0:3], v[252:255], v[88:91], v[0:3]\n\t"
    "s_set_gpr_idx_off\n\t"
    "s_cmp_lg_u32 s22, 0\n\t"
    "s_cbranch_scc1 .Lsl16_%=\n\t"
    ".Lslr16_%=:\n\t"
    "v_smfmac_f32_16x16x64_bf16 v[24:27], a[204:207], v[48:55], v201 abid:1\n\t"
    "ds_read_b32 v88, v232 offset:23044\n\t"
    "ds_read_b32 v89, v106 offset:23044\n\t"
    "v_smfmac_f32_16x16x64_bf16 v[24:27], a[224:227], v[64:71], v204\n\t"
    "s_memrealtime s[84:85]\n\t"
    "v_smfmac_f32_16x16x64_bf16 v[24:27], a[244:247], v[56:63], v206 abid:1\n\t"
    "s_waitcnt lgkmcnt(3)\n\t"
    "ds_read_b32 v90, v233 offset:23044\n\t"
    "v_mfma_f32_16x16x32_bf16 v[24:27], v[80:83], v[92:95], v[24:27]\n\t"
    "ds_read_b32 v94, v219 offset:23044\n\t"
    "v_smfmac_f32_16x16x64_bf16 v[16:19], a[208:211], v[48:55], v202\n\t"
    "s_add_u32 m0, s52, 13824\n\t"
    "s_mov_b32 exec_hi, 0\n\t"
    "buffer_load_dwordx4 v237, s[40:43], s68 offen offset:0 nt lds\n\t"
    "s_mov_b32 exec_hi, -1\n\t"
    "v_smfmac_f32_16x16x64_bf16 v[16:19], a[228:231], v[64:71], v204 abid:1\n\t"
    "ds_read_b128 v[80:83], v245 offset:7168\n\t"
    "v_smfmac_f32_16x16x64_bf16 v[16:19], a[248:251], v[56:63], v207\n\t"
    "s_sub_i32 s87, s87, s99\n\t"
    "ds_read_b32 v91, v107 offset:23044\n\t"
    "v_smfmac_f32_16x16x64_bf16 v[32:35], a[232:235], v[64:71], v205\n\t"
    "ds_read_b32 v93, v72 offset:23044\n\t"
    "v_smfmac_f32_16x16x64_bf16 v[32:35], a[252:255], v[56:63], v207 abid:1\n\t"
    "s_waitcnt lgkmcnt(7)\n\t"
    "s_sub_i32 s98, s98, s99\n\t"
    "ds_read_b128 v[52:55], v236 offset:23936\n\t"
    "v_smfmac_f32_16x16x64_bf16 v[32:35], v[124:127], v[40:47], v210\n\t"
    "s_add_i32 s86, s87, 2140\n\t"
    "ds_read_b32 v95, v73 offset:23044\n\t"
    "v_smfmac_f32_16x16x64_bf16 v[36:39], a[236:239], v[64:71], v205 abid:1\n\t"
    "s_add_i32 s98, s98, 71\n\t"
    "ds_read_b128 v[48:51], v217 offset:23936\n\t"
    "v_smfmac_f32_16x16x64_bf16 v[36:39], v[108:111], v[56:63], v208\n\t"
    "s_max_i32 s86, s86, s98\n\t"
    "ds_read_b32 v92, v218 offset:23044\n\t"
    "v_smfmac_f32_16x16x64_bf16 v[36:39], v[128:131], v[40:47], v210 abid:1\n\t"
    "s_waitcnt lgkmcnt(5)\n\t"
    "v_mfma_f32_16x16x32_bf16 v[36:39], v[80:83], v[88:91], v[36:39]\n\t"
    "s_mov_b64 vcc, s[18:19]\n\t"
    "s_sub_u32 s89, s84, s24\n\t"
    "ds_read_b128 v[80:83], v245 offset:0\n\t"
    "v_smfmac_f32_16x16x64_bf16 v[28:31], a[240:243], v[64:71], v206\n\t"
    "s_cmp_lt_i32 s89, s86\n\t"
    "s_cbranch_scc1 .Lpc11_%=\n\t"
    ".Lpc11r_%=:\n\t"
    "ds_read_b128 v[64:67], v217 offset:24064\n\t"
    "ds_read_b128 v[68:71], v236 offset:24064\n\t"
    "v_smfmac_f32_16x16x64_bf16 v[28:31], v[112:115], v[56:63], v208 abid:1\n\t"
    "v_smfmac_f32_16x16x64_bf16 v[28:31], v[132:135], v[40:47], v211\n\t"
    "s_max_i32 s98, s89, s86\n\t"
    "v_smfmac_f32_16x16x64_bf16 v[4:7], a[80:83], v[56:63], v186\n\t"
    "ds_read_b32 v90, v225 offset:23044\n\t"
    "v_smfmac_f32_16x16x64_bf16 v[4:7], a[96:99], v[40:47], v188\n\t"
    "s_waitcnt lgkmcnt(5)\n\t"
    "ds_read_b32 v89, v78 offset:23044\n\t"
    "ds_read_b32 v91, v79 offset:23044\n\t"
    "v_smfmac_f32_16x16x64_bf16 v[4:7], a[112:115], v[48:55], v190\n\t"
    "s_waitcnt lgkmcnt(3)\n\t"
    "buffer_load_dwordx4 v237, s[40:43], s68 offen offset:512 nt lds\n\t"
    "v_smfmac_f32_16x16x64_bf16 v[4:7], a[128:131], v[64:71], v192\n\t"
    "v_mfma_f32_16x16x32_bf16 v[4:7], v[80:83], v[92:95], v[4:7]\n\t"
    "ds_read_b32 v93, v104 offset:23044\n\t"
    "v_smfmac_f32_16x16x64_bf16 v[12:15], a[84:87], v[56:63], v186 abid:1\n\t"
    "ds_read_b32 v88, v224 offset:23044\n\t"
    "v_smfmac_f32_16x16x64_bf16 v[12:15], a[100:103], v[40:47], v188 abid:1\n\t"
    "ds_read_b32 v92, v230 offset:23044\n\t"
    "v_smfmac_f32_16x16x64_bf16 v[12:15], a[116:119], v[48:55], v190 abid:1\n\t"
    "v_cvt_pk_bf16_f32 v97, v6, v7\n\t"
    "v_smfmac_f32_16x16x64_bf16 v[12:15], a[132:135], v[64:71], v192 abid:1\n\t"
    "ds_read_b32 v95, v105 offset:23044\n\t"
    "v_smfmac_f32_16x16x64_bf16 v[20:23], a[88:91], v[56:63], v187\n\t"
    "ds_read_b128 v[80:83], v245 offset:3072\n\t"
    "v_smfmac_f32_16x16x64_bf16 v[20:23], a[104:107], v[40:47], v189\n\t"
    "v_smfmac_f32_16x16x64_bf16 v[20:23], a[120:123], v[48:55], v191\n\t"
    "ds_read_b128 v[56:59], v217 offset:0\n\t"
    "v_cvt_pk_bf16_f32 v96, v4, v5\n\t"
    "s_cmp_eq_u32 s82, 1008\n\t"
    "s_cbranch_scc1 .Lepq_%=\n\t"
    ".Lepqr_%=:\n\t"
    "v_smfmac_f32_16x16x64_bf16 v[20:23], a[136:139], v[64:71], v193\n\t"
    "ds_read_b128 v[60:63], v236 offset:0\n\t"
    "v_smfmac_f32_16x16x64_bf16 v[8:11], a[108:111], v[40:47], v189 abid:1\n\t"
    "ds_read_b32 v94, v231 offset:23044\n\t"
    "v_cvt_pk_bf16_f32 v98, v12, v13\n\t"
    "v_smfmac_f32_16x16x64_bf16 v[8:11], a[124:127], v[48:55], v191 abid:1\n\t"
    "v_cvt_pk_bf16_f32 v99, v14, v15\n\t"
    "v_smfmac_f32_16x16x64_bf16 v[8:11], a[140:143], v[64:71], v193 abid:1\n\t"
    "s_waitcnt lgkmcnt(3)\n\t"
    "ds_read_b128 v[4:7], v244 offset:0\n\t"
    "v_cvt_pk_bf16_f32 v101, v22, v23\n\t"
    "v_mfma_f32_16x16x32_bf16 v[8:11], v[80:83], v[88:91], v[8:11]\n\t"
    "ds_read_b128 v[80:83], v245 offset:6144\n\t"
    "v_smfmac_f32_16x16x64_bf16 v[24:27], v[116:119], v[40:47], v209\n\t"
    "ds_read_b32 v89, v76 offset:4\n\t"
    "v_smfmac_f32_16x16x64_bf16 v[24:27], v[136:139], v[48:55], v211 abid:1\n\t"
    "v_cvt_pk_bf16_f32 v100, v20, v21\n\t"
    "v_smfmac_f32_16x16x64_bf16 v[24:27], v[156:159], v[64:71], v214\n\t"
    "ds_read_b32 v91, v77 offset:4\n\t"
    "v_cvt_pk_bf16_f32 v102, v8, v9\n\t"
    "v_smfmac_f32_16x16x64_bf16 v[16:19], v[120:123], v[40:47], v209 abid:1\n\t"
    "ds_read_b128 v[12:15], v244 offset:64\n\t"
    "v_cvt_pk_bf16_f32 v103, v10, v11\n\t"
    "v_smfmac_f32_16x16x64_bf16 v[16:19], v[140:143], v[48:55], v212\n\t"
    "ds_read_b128 v[44:47], v236 offset:128\n\t"
    "v_cndmask_b32_dpp v8, v100, v96, vcc quad_perm:[0,0,2,2] row_mask:0xf bank_mask:0xf\n\t"
    "v_smfmac_f32_16x16x64_bf16 v[16:19], v[160:163], v[64:71], v214 abid:1\n\t"
    "ds_read_b128 v[20:23], v244 offset:128\n\t"
    "v_smfmac_f32_16x16x64_bf16 v[32:35], v[144:147], v[48:55], v212 abid:1\n\t"
    "v_cndmask_b32_dpp v9, v101, v97, vcc quad_perm:[0,0,2,2] row_mask:0xf bank_mask:0xf\n\t"
    "v_smfmac_f32_16x16x64_bf16 v[32:35], v[164:167], v[64:71], v215\n\t"
    "s_waitcnt lgkmcnt(5)\n\t"
    "ds_read_b128 v[40:43], v217 offset:128\n\t"
    "v_cndmask_b32_dpp v11, v103, v99, vcc quad_perm:[0,0,2,2] row_mask:0xf bank_mask:0xf\n\t"
    "v_mfma_f32_16x16x32_bf16 v[32:35], v[80:83], v[92:95], v[32:35]\n\t"
    "ds_read_b128 v[80:83], v245 offset:2048\n\t"
    "v_cndmask_b32_dpp v10, v102, v98, vcc quad_perm:[0,0,2,2] row_mask:0xf bank_mask:0xf\n\t"
    "v_smfmac_f32_16x16x64_bf16 v[36:39], v[148:151], v[48:55], v213\n\t"
    "s_not_b64 vcc, s[18:19]\n\t"
    "ds_read_b32 v88, v222 offset:4\n\t"
    "v_cndmask_b32_dpp v102, v98, v102, vcc quad_perm:[1,1,3,3] row_mask:0xf bank_mask:0xf\n\t"
    "v_smfmac_f32_16x16x64_bf16 v[36:39], v[168:171], v[64:71], v215 abid:1\n\t"
    "ds_read_b32 v95, v87 offset:4\n\t"
    "v_cndmask_b32_dpp v101, v97, v101, vcc quad_perm:[1,1,3,3] row_mask:0xf bank_mask:0xf\n\t"
    "v_smfmac_f32_16x16x64_bf16 v[28:31], v[152:155], v[48:55], v213 abid:1\n\t"
    "buffer_store_dwordx4 v[8:11], v241, s[44:47], s65 offen offset:0 sc1\n\t"
    "ds_read_b128 v[52:55], v236 offset:256\n\t"
    "v_cndmask_b32_dpp v103, v99, v103, vcc quad_perm:[1,1,3,3] row_mask:0xf bank_mask:0xf\n\t"
    "v_cvt_pk_bf16_f32 v99, v18, v19\n\t"
    "v_smfmac_f32_16x16x64_bf16 v[28:31], v[172:175], v[64:71], v216\n\t"
    "ds_read_b32 v90, v223 offset:4\n\t"
    "v_smfmac_f32_16x16x64_bf16 v[4:7], a[0:3], v[56:63], v176\n\t"
    "s_waitcnt lgkmcnt(5)\n\t"
    "s_add_u32 s66, s57, s65\n\t"
    "ds_read_b128 v[8:11], v244 offset:192\n\t"
    "v_cvt_pk_bf16_f32 v97, v26, v27\n\t"
    "v_smfmac_f32_16x16x64_bf16 v[4:7], a[16:19], v[40:47], v178\n\t"
    ".Lpad1_%=:\n\t"
    "s_cbranch_execz .Lpad2_%=\n\t"
    "ds_read_b128 v[48:51], v217 offset:256\n\t"
    "v_cndmask_b32_dpp v100, v96, v100, vcc quad_perm:[1,1,3,3] row_mask:0xf bank_mask:0xf\n\t"
    "v_smfmac_f32_16x16x64_bf16 v[12:15], a[4:7], v[56:63], v176 abid:1\n\t"
    "s_mov_b64 vcc, s[18:19]\n\t"
    "ds_read_b32 v92, v228 offset:4\n\t"
    "v_cvt_pk_bf16_f32 v96, v24, v25\n\t"
    "v_smfmac_f32_16x16x64_bf16 v[12:15], a[20:23], v[40:47], v178 abid:1\n\t"
    "ds_read_b128 v[24:27], v244 offset:256\n\t"
    "v_cvt_pk_bf16_f32 v98, v16, v17\n\t"
    "v_smfmac_f32_16x16x64_bf16 v[20:23], a[8:11], v[56:63], v177\n\t"
    "buffer_store_dwordx4 v[100:103], v241, s[44:47], s65 offen offset:1152 sc1\n\t"
    "ds_read_b128 v[16:19], v244 offset:320\n\t"
    "v_cvt_pk_bf16_f32 v28, v28, v29\n\t"
    "v_smfmac_f32_16x16x64_bf16 v[20:23], a[24:27], v[40:47], v179\n\t"
    "s_waitcnt lgkmcnt(5)\n\t"
    "ds_read_b32 v93, v86 offset:4\n\t"
    "v_cvt_pk_bf16_f32 v100, v32, v33\n\t"
    "v_mfma_f32_16x16x32_bf16 v[20:23], v[80:83], v[88:91], v[20:23]\n\t"
    "s_waitcnt lgkmcnt(5)\n\t"
    "ds_read_b128 v[68:71], v236 offset:384\n\t"
    "v_cvt_pk_bf16_f32 v101, v34, v35\n\t"
    "v_cvt_pk_bf16_f32 v102, v36, v37\n\t"
    "v_smfmac_f32_16x16x64_bf16 v[8:11], a[12:15], v[56:63], v177 abid:1\n\t"
    "ds_read_b32 v94, v229 offset:4\n\t"
    "v_cvt_pk_bf16_f32 v103, v38, v39\n\t"
    "v_smfmac_f32_16x16x64_bf16 v[8:11], a[28:31], v[40:47], v179 abid:1\n\t"
    "s_add_u32 m0, s52, 13824\n\t"
    "s_waitcnt lgkmcnt(6)\n\t"
    "buffer_load_dwordx4 v238, s[40:43], s68 offen offset:1536 nt lds\n\t"
    "v_smfmac_f32_16x16x64_bf16 v[8:11], a[44:47], v[48:55], v181 abid:1\n\t"
    "s_waitcnt lgkmcnt(4)\n\t"
    "ds_read_b128 v[80:83], v245 offset:5120\n\t"
    "v_cndmask_b32_dpp v38, v102, v98, vcc quad_perm:[0,0,2,2] row_mask:0xf bank_mask:0xf\n\t"
    "v_cndmask_b32_dpp v39, v103, v99, vcc quad_perm:[0,0,2,2] row_mask:0xf bank_mask:0xf\n\t"
    "v_add_u32_sdwa v89, v248, v234 dst_sel:DWORD dst_unused:UNUSED_PAD src0_sel:DWORD src1_sel:WORD_1\n\t"
    "v_smfmac_f32_16x16x64_bf16 v[24:27], a[144:147], v[56:63], v194\n\t"
    "ds_read_b128 v[32:35], v244 offset:384\n\t"
    "v_cndmask_b32_dpp v37, v101, v97, vcc quad_perm:[0,0,2,2] row_mask:0xf bank_mask:0xf\n\t"
    "v_smfmac_f32_16x16x64_bf16 v[24:27], a[164:167], v[40:47], v196 abid:1\n\t"
    "ds_read_b128 v[64:67], v217 offset:384\n\t"
    "v_cndmask_b32_dpp v36, v100, v96, vcc quad_perm:[0,0,2,2] row_mask:0xf bank_mask:0xf\n\t"
    "v_smfmac_f32_16x16x64_bf16 v[24:27], a[184:187], v[48:55], v199\n\t"
    "s_waitcnt lgkmcnt(6)\n\t"
    "s_not_b64 vcc, s[18:19]\n\t"
    "buffer_store_dwordx4 v[36:39], v241, s[44:47], s65 offen offset:128 sc1\n\t"
    "v_cndmask_b32_dpp v100, v96, v100, vcc quad_perm:[1,1,3,3] row_mask:0xf bank_mask:0xf\n\t"
    "v_cndmask_b32_dpp v101, v97, v101, vcc quad_perm:[1,1,3,3] row_mask:0xf bank_mask:0xf\n\t"
    "v_cvt_pk_bf16_f32 v29, v30, v31\n\t"
    "v_add_u32_sdwa v91, v248, v235 dst_sel:DWORD dst_unused:UNUSED_PAD src0_sel:DWORD src1_sel:WORD_1\n\t"
    "v_smfmac_f32_16x16x64_bf16 v[16:19], a[148:151], v[56:63], v194 abid:1\n\t"
    "ds_read_b32 v89, v89 offset:4\n\t"
    "ds_read_b32 v91, v91 offset:4\n\t"
    "v_smfmac_f32_16x16x64_bf16 v[16:19], a[168:171], v[40:47], v197\n\t"
    "ds_read_b128 v[36:39], v244 offset:448\n\t"
    "v_smfmac_f32_16x16x64_bf16 v[16:19], a[188:191], v[48:55], v199 abid:1\n\t"
    "s_waitcnt lgkmcnt(5)\n\t"
    "ds_write_b64 v242, v[28:29] offset:512\n\t"
    "v_mfma_f32_16x16x32_bf16 v[16:19], v[80:83], v[92:95], v[16:19]\n\t"
    "s_waitcnt lgkmcnt(5)\n\t"
    "v_cndmask_b32_dpp v102, v98, v102, vcc quad_perm:[1,1,3,3] row_mask:0xf bank_mask:0xf\n\t"
    "v_smfmac_f32_16x16x64_bf16 v[32:35], a[152:155], v[56:63], v195\n\t"
    "ds_read_b128 v[28:31], v244 offset:512\n\t"
    "v_cndmask_b32_dpp v103, v99, v103, vcc quad_perm:[1,1,3,3] row_mask:0xf bank_mask:0xf\n\t"
    "v_smfmac_f32_16x16x64_bf16 v[32:35], a[172:175], v[40:47], v197 abid:1\n\t"
    "buffer_store_dwordx4 v[100:103], v241, s[44:47], s65 offen offset:1280 sc1\n\t"
    "ds_read_b128 v[80:83], v245 offset:8192\n\t"
    "v_add_u32_sdwa v88, v248, v234 dst_sel:DWORD dst_unused:UNUSED_PAD src0_sel:DWORD src1_sel:WORD_0\n\t"
    "v_add_u32_sdwa v90, v248, v235 dst_sel:DWORD dst_unused:UNUSED_PAD src0_sel:DWORD src1_sel:WORD_0\n\t"
    "v_smfmac_f32_16x16x64_bf16 v[32:35], a[192:195], v[48:55], v200\n\t"
    "s_waitcnt lgkmcnt(6)\n\t"
    "ds_read_b32 v88, v88 offset:4\n\t"
    "ds_read_b32 v90, v90 offset:4\n\t"
    "v_smfmac_f32_16x16x64_bf16 v[32:35], a[212:215], v[64:71], v202 abid:1\n\t"
    "s_waitcnt lgkmcnt(5)\n\t"
    "v_smfmac_f32_16x16x64_bf16 v[36:39], a[156:159], v[56:63], v195 abid:1\n\t"
    "ds_read_b32 v93, v74 offset:4\n\t"
    "v_smfmac_f32_16x16x64_bf16 v[36:39], a[176:179], v[40:47], v198\n\t"
    "v_smfmac_f32_16x16x64_bf16 v[36:39], a[196:199], v[48:55], v200 abid:1\n\t"
    "ds_read_b32 v92, v220 offset:4\n\t"
    "ds_read_b32 v95, v75 offset:4\n\t"
    "v_smfmac_f32_16x16x64_bf16 v[36:39], a[216:219], v[64:71], v203\n\t"
    "s_waitcnt lgkmcnt(6)\n\t"
    "buffer_load_dwordx4 v239, s[40:43], s68 offen offset:2560 nt lds\n\t"
    "v_smfmac_f32_16x16x64_bf16 v[28:31], a[160:163], v[56:63], v196\n\t"
    "ds_read_b128 v[56:59], v217 offset:512\n\t"
    "ds_read_b128 v[60:63], v236 offset:512\n\t"
    "v_smfmac_f32_16x16x64_bf16 v[28:31], a[180:183], v[40:47], v198 abid:1\n\t"
    "v_smfmac_f32_16x16x64_bf16 v[28:31], a[200:203], v[48:55], v201\n\t"
    "ds_read_b32 v94, v221 offset:4\n\t"
    "v_smfmac_f32_16x16x64_bf16 v[28:31], a[220:223], v[64:71], v203 abid:1\n\t"
    "s_waitcnt lgkmcnt(6)\n\t"
    "s_mov_b64 vcc, s[18:19]\n\t"
    "ds_read_b128 v[40:43], v217 offset:640\n\t"
    "v_mfma_f32_16x16x32_bf16 v[28:31], v[80:83], v[88:91], v[28:31]\n\t"
    "ds_read_b128 v[80:83], v245 offset:1024\n\t"
    "v_add_u32_sdwa v91, v248, v247 dst_sel:DWORD dst_unused:UNUSED_PAD src0_sel:DWORD src1_sel:WORD_1\n\t"
    "v_smfmac_f32_16x16x64_bf16 v[4:7], a[32:35], v[48:55], v180\n\t"
    "ds_read_u16 v91, v91 offset:0\n\t"
    "v_add_u32_sdwa v88, v248, v246 dst_sel:DWORD dst_unused:UNUSED_PAD src0_sel:DWORD src1_sel:WORD_0\n\t"
    "v_smfmac_f32_16x16x64_bf16 v[4:7], a[48:51], v[64:71], v182\n\t"
    "s_waitcnt lgkmcnt(4)\n\t"
    "ds_read_u16 v88, v88 offset:0\n\t"
    "v_add_u32_sdwa v89, v248, v246 dst_sel:DWORD dst_unused:UNUSED_PAD src0_sel:DWORD src1_sel:WORD_1\n\t"
    "v_smfmac_f32_16x16x64_bf16 v[4:7], a[64:67], v[56:63], v184\n\t"
    "ds_read_u16 v89, v89 offset:0\n\t"
    "v_add_u32_sdwa v90, v248, v247 dst_sel:DWORD dst_unused:UNUSED_PAD src0_sel:DWORD src1_sel:WORD_0\n\t"
    "v_smfmac_f32_16x16x64_bf16 v[12:15], a[36:39], v[48:55], v180 abid:1\n\t"
    "s_waitcnt vmcnt(19)\n\t"
    "s_barrier\n\t"
    "ds_read_u16 v90, v90 offset:0\n\t"
    "v_smfmac_f32_16x16x64_bf16 v[12:15], a[52:55], v[64:71], v182 abid:1\n\t"
    "ds_read_b128 v[96:99], v243 offset:2560\n\t"
    "v_smfmac_f32_16x16x64_bf16 v[12:15], a[68:71], v[56:63], v184 abid:1\n\t"
    "s_waitcnt lgkmcnt(5)\n\t"
    "ds_read_b128 v[44:47], v236 offset:640\n\t"
    "v_mfma_f32_16x16x32_bf16 v[12:15], v[80:83], v[92:95], v[12:15]\n\t"
    "ds_read_b32 v95, v85 offset:4\n\t"
    "v_smfmac_f32_16x16x64_bf16 v[20:23], a[40:43], v[48:55], v181\n\t"
    "ds_read_b128 v[80:83], v245 offset:4096\n\t"
    "v_smfmac_f32_16x16x64_bf16 v[20:23], a[56:59], v[64:71], v183\n\t"
    "ds_read_b32 v93, v84 offset:4\n\t"
    "v_smfmac_f32_16x16x64_bf16 v[20:23], a[72:75], v[56:63], v185\n\t"
    "s_waitcnt lgkmcnt(5)\n\t"
    "ds_read_b128 v[52:55], v236 offset:768\n\t"
    "s_set_gpr_idx_on s4, gpr_idx(SRC2,DST)\n\t"
    "v_mfma_f32_16x16x32_bf16 v[0:3], v[250:253], v[88:91], v[0:3]\n\t"
    "s_set_gpr_idx_off\n\t"
    "s_waitcnt lgkmcnt(5)\n\t"
    "s_mov_b32 exec_hi, 0\n\t"
    "buffer_store_dwordx4 v[96:99], v240, s[44:47], s66 offen offset:0 sc1\n\t"
    "s_mov_b32 exec_hi, -1\n\t"
    "ds_read_b32 v92, v226 offset:4\n\t"
    "v_smfmac_f32_16x16x64_bf16 v[8:11], a[60:63], v[64:71], v183 abid:1\n\t"
    "ds_read_b32 v94, v227 offset:4\n\t"
    "v_smfmac_f32_16x16x64_bf16 v[8:11], a[76:79], v[56:63], v185 abid:1\n\t"
    "s_waitcnt lgkmcnt(6)\n\t"
    "v_smfmac_f32_16x16x64_bf16 v[8:11], a[92:95], v[40:47], v187 abid:1\n\t"
    "ds_read_b128 v[48:51], v217 offset:768\n\t"
    "s_set_gpr_idx_on s5, gpr_idx(SRC2,DST)\n\t"
    "v_mfma_f32_16x16x32_bf16 v[0:3], v[252:255], v[88:91], v[0:3]\n\t"
    "s_set_gpr_idx_off\n\t"
    "s_add_u32 m0, s52, 13824\n\t"
    "s_nop 0\n\t"
    "buffer_load_dwordx4 v239, s[40:43], s68 offen offset:3584 nt lds\n\t"
    "s_cmp_lg_u32 s22, 0\n\t"
    "s_cbranch_scc1 .Lsl17_%=\n\t"
    ".Lslr17_%=:\n\t"
    "v_smfmac_f32_16x16x64_bf16 v[24:27], a[204:207], v[64:71], v201 abid:1\n\t"
    "ds_read_b32 v88, v232 offset:4\n\t"
    "v_smfmac_f32_16x16x64_bf16 v[24:27], a[224:227], v[56:63], v204\n\t"
    "s_memrealtime s[84:85]\n\t"
    "ds_read_b32 v89, v106 offset:4\n\t"
    "v_smfmac_f32_16x16x64_bf16 v[24:27], a[244:247], v[40:47], v206 abid:1\n\t"
    "s_waitcnt lgkmcnt(3)\n\t"
    "ds_read_b32 v91, v107 offset:4\n\t"
    "v_mfma_f32_16x16x32_bf16 v[24:27], v[80:83], v[92:95], v[24:27]\n\t"
    "ds_read_b32 v90, v233 offset:4\n\t"
    "v_smfmac_f32_16x16x64_bf16 v[16:19], a[208:211], v[64:71], v202\n\t"
    "ds_read_b32 v95, v73 offset:4\n\t"
    "v_smfmac_f32_16x16x64_bf16 v[16:19], a[228:231], v[56:63], v204 abid:1\n\t"
    "v_smfmac_f32_16x16x64_bf16 v[16:19], a[248:251], v[40:47], v207\n\t"
    "s_sub_i32 s87, s87, s99\n\t"
    "ds_read_b128 v[80:83], v245 offset:7168\n\t"
    "v_smfmac_f32_16x16x64_bf16 v[32:35], a[232:235], v[56:63], v205\n\t"
    "ds_read_b32 v92, v218 offset:4\n\t"
    "v_smfmac_f32_16x16x64_bf16 v[32:35], a[252:255], v[40:47], v207 abid:1\n\t"
    "s_waitcnt lgkmcnt(7)\n\t"
    "s_sub_i32 s98, s98, s99\n\t"
    "ds_read_b32 v94, v219 offset:4\n\t"
    "v_smfmac_f32_16x16x64_bf16 v[32:35], v[124:127], v[48:55], v210\n\t"
    "s_add_i32 s86, s87, 2258\n\t"
    "ds_read_b32 v93, v72 offset:4\n\t"
    "v_smfmac_f32_16x16x64_bf16 v[36:39], a[236:239], v[56:63], v205 abid:1\n\t"
    "s_add_i32 s98, s98, 68\n\t"
    "ds_read_b128 v[68:71], v236 offset:896\n\t"
    "v_smfmac_f32_16x16x64_bf16 v[36:39], v[108:111], v[40:47], v208\n\t"
    "s_max_i32 s86, s86, s98\n\t"
    "v_smfmac_f32_16x16x64_bf16 v[36:39], v[128:131], v[48:55], v210 abid:1\n\t"
    "s_waitcnt lgkmcnt(4)\n\t"
    "ds_read_b128 v[64:67], v217 offset:896\n\t"
    "v_mfma_f32_16x16x32_bf16 v[36:39], v[80:83], v[88:91], v[36:39]\n\t"
    "s_sub_u32 s89, s84, s24\n\t"
    "ds_read_b128 v[80:83], v245 offset:0\n\t"
    "v_smfmac_f32_16x16x64_bf16 v[28:31], a[240:243], v[56:63], v206\n\t"
    "s_cmp_lt_i32 s89, s86\n\t"
    "s_cbranch_scc1 .Lpc12_%=\n\t"
    ".Lpc12r_%=:\n\t"
    "ds_read_b128 v[56:59], v217 offset:1024\n\t"
    "ds_read_b128 v[60:63], v236 offset:1024\n\t"
    "v_smfmac_f32_16x16x64_bf16 v[28:31], v[112:115], v[40:47], v208 abid:1\n\t"
    "v_smfmac_f32_16x16x64_bf16 v[28:31], v[132:135], v[48:55], v211\n\t"
    "s_bitcmp1_b32 s54, 12\n\t"
    "s_mov_b32 s67, 56623104\n\t"
    "s_cmov_b32 s67, 56696832\n\t"
    "s_max_i32 s98, s89, s86\n\t"
    "v_smfmac_f32_16x16x64_bf16 v[4:7], a[80:83], v[40:47], v186\n\t"
    "ds_read_b32 v88, v224 offset:4\n\t"
    "v_smfmac_f32_16x16x64_bf16 v[4:7], a[96:99], v[48:55], v188\n\t"
    "s_waitcnt lgkmcnt(4)\n\t"
    "ds_read_b32 v90, v225 offset:4\n\t"
    "v_smfmac_f32_16x16x64_bf16 v[4:7], a[112:115], v[64:71], v190\n\t"
    "s_waitcnt lgkmcnt(2)\n\t"
    "v_smfmac_f32_16x16x64_bf16 v[4:7], a[128:131], v[56:63], v192\n\t"
    "ds_read_b32 v91, v79 offset:4\n\t"
    "v_mfma_f32_16x16x32_bf16 v[4:7], v[80:83], v[92:95], v[4:7]\n\t"
    "ds_read_b32 v89, v78 offset:4\n\t"
    "v_smfmac_f32_16x16x64_bf16 v[12:15], a[84:87], v[40:47], v186 abid:1\n\t"
    "ds_read_b128 v[80:83], v245 offset:3072\n\t"
    "v_smfmac_f32_16x16x64_bf16 v[12:15], a[100:103], v[48:55], v188 abid:1\n\t"
    "v_smfmac_f32_16x16x64_bf16 v[12:15], a[116:119], v[64:71], v190 abid:1\n\t"
    "ds_read_b32 v94, v231 offset:4\n\t"
    "v_cvt_pk_bf16_f32 v100, v4, v5\n\t"
    "v_smfmac_f32_16x16x64_bf16 v[12:15], a[132:135], v[56:63], v192 abid:1\n\t"
    "ds_read_b32 v92, v230 offset:4\n\t"
    "v_cvt_pk_bf16_f32 v101, v6, v7\n\t"
    "v_smfmac_f32_16x16x64_bf16 v[20:23], a[88:91], v[40:47], v187\n\t"
    "ds_read_b128 v[44:47], v236 offset:4608\n\t"
    "v_smfmac_f32_16x16x64_bf16 v[20:23], a[104:107], v[48:55], v189\n\t"
    "v_smfmac_f32_16x16x64_bf16 v[20:23], a[120:123], v[64:71], v191\n\t"
    "ds_read_b32 v93, v104 offset:4\n\t"
    "v_smfmac_f32_16x16x64_bf16 v[20:23], a[136:139], v[56:63], v193\n\t"
    "ds_read_b128 v[40:43], v217 offset:4608\n\t"
    "v_cvt_pk_bf16_f32 v102, v12, v13\n\t"
    "v_smfmac_f32_16x16x64_bf16 v[8:11], a[108:111], v[48:55], v189 abid:1\n\t"
    "ds_read_b128 v[4:7], v244 offset:0\n\t"
    "v_cvt_pk_bf16_f32 v103, v14, v15\n\t"
    "v_smfmac_f32_16x16x64_bf16 v[8:11], a[124:127], v[64:71], v191 abid:1\n\t"
    "v_smfmac_f32_16x16x64_bf16 v[8:11], a[140:143], v[56:63], v193 abid:1\n\t"
    "s_waitcnt lgkmcnt(6)\n\t"
    "ds_read_b128 v[12:15], v244 offset:64\n\t"
    "v_mfma_f32_16x16x32_bf16 v[8:11], v[80:83], v[88:91], v[8:11]\n\t"
    "ds_read_b128 v[80:83], v245 offset:6144\n\t"
    "v_cvt_pk_bf16_f32 v97, v22, v23\n\t"
    "v_smfmac_f32_16x16x64_bf16 v[24:27], v[116:119], v[48:55], v209\n\t"
    "ds_read_b32 v89, v76 offset:4612\n\t"
    "v_cvt_pk_bf16_f32 v96, v20, v21\n\t"
    "v_smfmac_f32_16x16x64_bf16 v[24:27], v[136:139], v[64:71], v211 abid:1\n\t"
    "ds_read_b32 v95, v105 offset:4\n\t"
    "v_smfmac_f32_16x16x64_bf16 v[24:27], v[156:159], v[56:63], v214\n\t"
    "ds_read_b32 v90, v223 offset:4612\n\t"
    "v_cvt_pk_bf16_f32 v98, v8, v9\n\t"
    "v_smfmac_f32_16x16x64_bf16 v[16:19], v[120:123], v[48:55], v209 abid:1\n\t"
    "v_cvt_pk_bf16_f32 v99, v10, v11\n\t"
    "v_smfmac_f32_16x16x64_bf16 v[16:19], v[140:143], v[64:71], v212\n\t"
    "ds_read_b128 v[20:23], v244 offset:128\n\t"
    "v_cndmask_b32_dpp v10, v98, v102, vcc quad_perm:[0,0,2,2] row_mask:0xf bank_mask:0xf\n\t"
    "v_smfmac_f32_16x16x64_bf16 v[16:19], v[160:163], v[56:63], v214 abid:1\n\t"
    "ds_read_b128 v[48:51], v217 offset:4736\n\t"
    "v_cndmask_b32_dpp v8, v96, v100, vcc quad_perm:[0,0,2,2] row_mask:0xf bank_mask:0xf\n\t"
    "v_cndmask_b32_dpp v11, v99, v103, vcc quad_perm:[0,0,2,2] row_mask:0xf bank_mask:0xf\n\t"
    "v_smfmac_f32_16x16x64_bf16 v[32:35], v[144:147], v[64:71], v212 abid:1\n\t"
    "v_cndmask_b32_dpp v9, v97, v101, vcc quad_perm:[0,0,2,2] row_mask:0xf bank_mask:0xf\n\t"
    "v_smfmac_f32_16x16x64_bf16 v[32:35], v[164:167], v[56:63], v215\n\t"
    "s_waitcnt lgkmcnt(3)\n\t"
    "s_not_b64 vcc, s[18:19]\n\t"
    "ds_read_b128 v[52:55], v236 offset:4736\n\t"
    "v_cndmask_b32_dpp v96, v100, v96, vcc quad_perm:[1,1,3,3] row_mask:0xf bank_mask:0xf\n\t"
    "v_mfma_f32_16x16x32_bf16 v[32:35], v[80:83], v[92:95], v[32:35]\n\t"
    "ds_read_b128 v[80:83], v245 offset:2048\n\t"
    "v_cvt_pk_bf16_f32 v100, v24, v25\n\t"
    "v_smfmac_f32_16x16x64_bf16 v[36:39], v[148:151], v[64:71], v213\n\t"
    "ds_read_b32 v88, v222 offset:4612\n\t"
    "v_smfmac_f32_16x16x64_bf16 v[36:39], v[168:171], v[56:63], v215 abid:1\n\t"
    "v_cndmask_b32_dpp v97, v101, v97, vcc quad_perm:[1,1,3,3] row_mask:0xf bank_mask:0xf\n\t"
    "v_smfmac_f32_16x16x64_bf16 v[28:31], v[152:155], v[64:71], v213 abid:1\n\t"
    "buffer_store_dwordx4 v[8:11], v241, s[44:47], s67 offen offset:0 sc1\n\t"
    "ds_read_b32 v91, v77 offset:4612\n\t"
    "v_cndmask_b32_dpp v98, v102, v98, vcc quad_perm:[1,1,3,3] row_mask:0xf bank_mask:0xf\n\t"
    "v_smfmac_f32_16x16x64_bf16 v[28:31], v[172:175], v[56:63], v216\n\t"
    "ds_read_b128 v[8:11], v244 offset:192\n\t"
    "v_cvt_pk_bf16_f32 v102, v16, v17\n\t"
    "v_smfmac_f32_16x16x64_bf16 v[4:7], a[0:3], v[40:47], v176\n\t"
    "s_waitcnt lgkmcnt(4)\n\t"
    "ds_read_b32 v92, v228 offset:4612\n\t"
    "v_smfmac_f32_16x16x64_bf16 v[4:7], a[16:19], v[48:55], v178\n\t"
    "ds_read_b128 v[68:71], v236 offset:4864\n\t"
    "v_cndmask_b32_dpp v99, v103, v99, vcc quad_perm:[1,1,3,3] row_mask:0xf bank_mask:0xf\n\t"
    "v_smfmac_f32_16x16x64_bf16 v[12:15], a[4:7], v[40:47], v176 abid:1\n\t"
    "s_mov_b64 vcc, s[18:19]\n\t"
    "ds_read_b128 v[64:67], v217 offset:4864\n\t"
    "v_cvt_pk_bf16_f32 v101, v26, v27\n\t"
    "v_smfmac_f32_16x16x64_bf16 v[12:15], a[20:23], v[48:55], v178 abid:1\n\t"
    "ds_read_b128 v[24:27], v244 offset:256\n\t"
    "v_cvt_pk_bf16_f32 v103, v18, v19\n\t"
    "v_smfmac_f32_16x16x64_bf16 v[20:23], a[8:11], v[40:47], v177\n\t"
    "buffer_store_dwordx4 v[96:99], v241, s[44:47], s67 offen offset:1152 sc1\n\t"
    "ds_read_b32 v95, v87 offset:4612\n\t"
    "v_cvt_pk_bf16_f32 v28, v28, v29\n\t"
    "v_cvt_pk_bf16_f32 v29, v30, v31\n\t"
    "v_smfmac_f32_16x16x64_bf16 v[20:23], a[24:27], v[48:55], v179\n\t"
    "s_waitcnt lgkmcnt(6)\n\t"
    "ds_read_b128 v[16:19], v244 offset:320\n\t"
    "v_cvt_pk_bf16_f32 v98, v36, v37\n\t"
    "v_mfma_f32_16x16x32_bf16 v[20:23], v[80:83], v[88:91], v[20:23]\n\t"
    "s_waitcnt lgkmcnt(6)\n\t"
    "ds_read_b128 v[80:83], v245 offset:5120\n\t"
    "v_cvt_pk_bf16_f32 v97, v34, v35\n\t"
    "v_cvt_pk_bf16_f32 v99, v38, v39\n\t"
    "v_smfmac_f32_16x16x64_bf16 v[8:11], a[12:15], v[40:47], v177 abid:1\n\t"
    "v_cvt_pk_bf16_f32 v96, v32, v33\n\t"
    "v_smfmac_f32_16x16x64_bf16 v[8:11], a[28:31], v[48:55], v179 abid:1\n\t"
    "s_waitcnt lgkmcnt(4)\n\t"
    "ds_read_b32 v94, v229 offset:4612\n\t"
    "v_cndmask_b32_dpp v39, v99, v103, vcc quad_perm:[0,0,2,2] row_mask:0xf bank_mask:0xf\n\t"
    "v_smfmac_f32_16x16x64_bf16 v[8:11], a[44:47], v[64:71], v181 abid:1\n\t"
    "s_waitcnt lgkmcnt(4)\n\t"
    "ds_read_b32 v93, v86 offset:4612\n\t"
    "v_cndmask_b32_dpp v36, v96, v100, vcc quad_perm:[0,0,2,2] row_mask:0xf bank_mask:0xf\n\t"
    "v_cndmask_b32_dpp v37, v97, v101, vcc quad_perm:[0,0,2,2] row_mask:0xf bank_mask:0xf\n\t"
    "v_cndmask_b32_dpp v38, v98, v102, vcc quad_perm:[0,0,2,2] row_mask:0xf bank_mask:0xf\n\t"
    "v_smfmac_f32_16x16x64_bf16 v[24:27], a[144:147], v[40:47], v194\n\t"
    "s_not_b64 vcc, s[18:19]\n\t"
    "buffer_store_dwordx4 v[36:39], v241, s[44:47], s67 offen offset:128 sc1\n\t"
    "ds_read_b128 v[32:35], v244 offset:384\n\t"
    "v_cndmask_b32_dpp v98, v102, v98, vcc quad_perm:[1,1,3,3] row_mask:0xf bank_mask:0xf\n\t"
    "v_cndmask_b32_dpp v99, v103, v99, vcc quad_perm:[1,1,3,3] row_mask:0xf bank_mask:0xf\n\t"
    "v_smfmac_f32_16x16x64_bf16 v[24:27], a[164:167], v[48:55], v196 abid:1\n\t"
    "ds_read_b128 v[36:39], v244 offset:448\n\t"
    "v_cndmask_b32_dpp v97, v101, v97, vcc quad_perm:[1,1,3,3] row_mask:0xf bank_mask:0xf\n\t"
    "v_smfmac_f32_16x16x64_bf16 v[24:27], a[184:187], v[64:71], v199\n\t"
    "s_waitcnt lgkmcnt(5)\n\t"
    "ds_read_b128 v[56:59], v217 offset:4992\n\t"
    "v_cndmask_b32_dpp v96, v100, v96, vcc quad_perm:[1,1,3,3] row_mask:0xf bank_mask:0xf\n\t"
    "v_smfmac_f32_16x16x64_bf16 v[16:19], a[148:151], v[40:47], v194 abid:1\n\t"
    "ds_read_b128 v[60:63], v236 offset:4992\n\t"
    "v_add_u32_sdwa v91, v248, v235 dst_sel:DWORD dst_unused:UNUSED_PAD src0_sel:DWORD src1_sel:WORD_1\n\t"
    "v_smfmac_f32_16x16x64_bf16 v[16:19], a[168:171], v[48:55], v197\n\t"
    "ds_read_b32 v91, v91 offset:4612\n\t"
    "v_add_u32_sdwa v90, v248, v235 dst_sel:DWORD dst_unused:UNUSED_PAD src0_sel:DWORD src1_sel:WORD_0\n\t"
    "v_smfmac_f32_16x16x64_bf16 v[16:19], a[188:191], v[64:71], v199 abid:1\n\t"
    "s_waitcnt lgkmcnt(5)\n\t"
    "v_add_u32_sdwa v88, v248, v234 dst_sel:DWORD dst_unused:UNUSED_PAD src0_sel:DWORD src1_sel:WORD_0\n\t"
    "v_mfma_f32_16x16x32_bf16 v[16:19], v[80:83], v[92:95], v[16:19]\n\t"
    "s_waitcnt lgkmcnt(4)\n\t"
    "buffer_store_dwordx4 v[96:99], v241, s[44:47], s67 offen offset:1280 sc1\n\t"
    "ds_write_b64 v242, v[28:29] offset:0\n\t"
    "v_add_u32_sdwa v89, v248, v234 dst_sel:DWORD dst_unused:UNUSED_PAD src0_sel:DWORD src1_sel:WORD_1\n\t"
    "v_smfmac_f32_16x16x64_bf16 v[32:35], a[152:155], v[40:47], v195\n\t"
    "ds_read_b128 v[80:83], v245 offset:8192\n\t"
    "v_smfmac_f32_16x16x64_bf16 v[32:35], a[172:175], v[48:55], v197 abid:1\n\t"
    "ds_read_b128 v[28:31], v244 offset:512\n\t"
    "v_smfmac_f32_16x16x64_bf16 v[32:35], a[192:195], v[64:71], v200\n\t"
    "s_waitcnt lgkmcnt(4)\n\t"
    "v_smfmac_f32_16x16x64_bf16 v[32:35], a[212:215], v[56:63], v202 abid:1\n\t"
    "ds_read_b32 v90, v90 offset:4612\n\t"
    "v_smfmac_f32_16x16x64_bf16 v[36:39], a[156:159], v[40:47], v195 abid:1\n\t"
    "ds_read_b32 v88, v88 offset:4612\n\t"
    "ds_read_b32 v89, v89 offset:4612\n\t"
    "v_smfmac_f32_16x16x64_bf16 v[36:39], a[176:179], v[48:55], v198\n\t"
    "ds_read_b32 v95, v75 offset:4612\n\t"
    "v_smfmac_f32_16x16x64_bf16 v[36:39], a[196:199], v[64:71], v200 abid:1\n\t"
    "v_smfmac_f32_16x16x64_bf16 v[36:39], a[216:219], v[56:63], v203\n\t"
    "s_waitcnt lgkmcnt(4)\n\t"
    "ds_read_b32 v93, v74 offset:4612\n\t"
    "v_smfmac_f32_16x16x64_bf16 v[28:31], a[160:163], v[40:47], v196\n\t"
    "ds_read_b128 v[40:43], v217 offset:5120\n\t"
    "ds_read_b128 v[44:47], v236 offset:5120\n\t"
    "v_smfmac_f32_16x16x64_bf16 v[28:31], a[180:183], v[48:55], v198 abid:1\n\t"
    "v_smfmac_f32_16x16x64_bf16 v[28:31], a[200:203], v[64:71], v201\n\t"
    "ds_read_b32 v94, v221 offset:4612\n\t"
    "v_smfmac_f32_16x16x64_bf16 v[28:31], a[220:223], v[56:63], v203 abid:1\n\t"
    "s_waitcnt lgkmcnt(5)\n\t"
    "ds_read_b32 v92, v220 offset:4612\n\t"
    "v_mfma_f32_16x16x32_bf16 v[28:31], v[80:83], v[88:91], v[28:31]\n\t"
    "ds_read_b128 v[80:83], v245 offset:1024\n\t"
    "v_add_u32_sdwa v91, v248, v247 dst_sel:DWORD dst_unused:UNUSED_PAD src0_sel:DWORD src1_sel:WORD_1\n\t"
    "v_smfmac_f32_16x16x64_bf16 v[4:7], a[32:35], v[64:71], v180\n\t"
    "ds_read_u16 v91, v91 offset:4608\n\t"
    "v_add_u32_sdwa v90, v248, v247 dst_sel:DWORD dst_unused:UNUSED_PAD src0_sel:DWORD src1_sel:WORD_0\n\t"
    "v_smfmac_f32_16x16x64_bf16 v[4:7], a[48:51], v[56:63], v182\n\t"
    "s_waitcnt lgkmcnt(4)\n\t"
    "ds_read_u16 v90, v90 offset:4608\n\t"
    "v_add_u32_sdwa v88, v248, v246 dst_sel:DWORD dst_unused:UNUSED_PAD src0_sel:DWORD src1_sel:WORD_0\n\t"
    "v_smfmac_f32_16x16x64_bf16 v[4:7], a[64:67], v[40:47], v184\n\t"
    "ds_read_u16 v88, v88 offset:4608\n\t"
    "v_add_u32_sdwa v89, v248, v246 dst_sel:DWORD dst_unused:UNUSED_PAD src0_sel:DWORD src1_sel:WORD_1\n\t"
    "v_smfmac_f32_16x16x64_bf16 v[12:15], a[36:39], v[64:71], v180 abid:1\n\t"
    "s_waitcnt vmcnt(15)\n\t"
    "s_barrier\n\t"
    "ds_read_u16 v89, v89 offset:4608\n\t"
    "v_smfmac_f32_16x16x64_bf16 v[12:15], a[52:55], v[56:63], v182 abid:1\n\t"
    "ds_read_b128 v[100:103], v243 offset:2048\n\t"
    "v_smfmac_f32_16x16x64_bf16 v[12:15], a[68:71], v[40:47], v184 abid:1\n\t"
    "s_waitcnt lgkmcnt(5)\n\t"
    "ds_read_b128 v[52:55], v236 offset:5248\n\t"
    "v_mfma_f32_16x16x32_bf16 v[12:15], v[80:83], v[92:95], v[12:15]\n\t"
    "ds_read_b128 v[48:51], v217 offset:5248\n\t"
    "v_smfmac_f32_16x16x64_bf16 v[20:23], a[40:43], v[64:71], v181\n\t"
    "s_add_u32 s65, s57, s67\n\t"
    "ds_read_b32 v95, v85 offset:4612\n\t"
    "v_smfmac_f32_16x16x64_bf16 v[20:23], a[56:59], v[56:63], v183\n\t"
    "ds_read_b32 v92, v226 offset:4612\n\t"
    "v_smfmac_f32_16x16x64_bf16 v[20:23], a[72:75], v[40:47], v185\n\t"
    "s_waitcnt lgkmcnt(5)\n\t"
    "ds_read_b128 v[80:83], v245 offset:4096\n\t"
    "s_set_gpr_idx_on s4, gpr_idx(SRC2,DST)\n\t"
    "v_mfma_f32_16x16x32_bf16 v[0:3], v[250:253], v[88:91], v[0:3]\n\t"
    "s_set_gpr_idx_off\n\t"
    "s_waitcnt lgkmcnt(5)\n\t"
    "s_mov_b32 exec_hi, 0\n\t"
    "buffer_store_dwordx4 v[100:103], v240, s[44:47], s65 offen offset:0 sc1\n\t"
    "s_mov_b32 exec_hi, -1\n\t"
    "ds_read_b32 v94, v227 offset:4612\n\t"
    "v_smfmac_f32_16x16x64_bf16 v[8:11], a[60:63], v[56:63], v183 abid:1\n\t"
    "ds_read_b32 v93, v84 offset:4612\n\t"
    "v_smfmac_f32_16x16x64_bf16 v[8:11], a[76:79], v[40:47], v185 abid:1\n\t"
    "s_waitcnt lgkmcnt(5)\n\t"
    "ds_read_b128 v[64:67], v217 offset:5376\n\t"
    "v_smfmac_f32_16x16x64_bf16 v[8:11], a[92:95], v[48:55], v187 abid:1\n\t"
    "ds_read_b128 v[68:71], v236 offset:5376\n\t"
    "s_set_gpr_idx_on s5, gpr_idx(SRC2,DST)\n\t"
    "v_mfma_f32_16x16x32_bf16 v[0:3], v[252:255], v[88:91], v[0:3]\n\t"
    "s_set_gpr_idx_off\n\t"
    "s_cmp_lg_u32 s22, 0\n\t"
    "s_cbranch_scc1 .Lsl18_%=\n\t"
    ".Lslr18_%=:\n\t"
    "v_smfmac_f32_16x16x64_bf16 v[24:27], a[204:207], v[56:63], v201 abid:1\n\t"
    "ds_read_b32 v90, v233 offset:4612\n\t"
    "v_smfmac_f32_16x16x64_bf16 v[24:27], a[224:227], v[40:47], v204\n\t"
    "s_memrealtime s[84:85]\n\t"
    "ds_read_b32 v88, v232 offset:4612\n\t"
    "v_smfmac_f32_16x16x64_bf16 v[24:27], a[244:247], v[48:55], v206 abid:1\n\t"
    "s_waitcnt lgkmcnt(4)\n\t"
    "ds_read_b32 v89, v106 offset:4612\n\t"
    "v_mfma_f32_16x16x32_bf16 v[24:27], v[80:83], v[92:95], v[24:27]\n\t"
    "ds_read_b128 v[80:83], v245 offset:7168\n\t"
    "v_smfmac_f32_16x16x64_bf16 v[16:19], a[208:211], v[56:63], v202\n\t"
    "ds_read_b32 v91, v107 offset:4612\n\t"
    "v_smfmac_f32_16x16x64_bf16 v[16:19], a[228:231], v[40:47], v204 abid:1\n\t"
    "ds_read_b32 v94, v219 offset:4612\n\t"
    "v_smfmac_f32_16x16x64_bf16 v[16:19], a[248:251], v[48:55], v207\n\t"
    "s_sub_i32 s87, s87, s99\n\t"
    "ds_read_b32 v93, v72 offset:4612\n\t"
    "v_smfmac_f32_16x16x64_bf16 v[32:35], a[232:235], v[40:47], v205\n\t"
    "v_smfmac_f32_16x16x64_bf16 v[32:35], a[252:255], v[48:55], v207 abid:1\n\t"
    "s_waitcnt lgkmcnt(7)\n\t"
    "s_sub_i32 s98, s98, s99\n\t"
    "ds_read_b128 v[60:63], v236 offset:5504\n\t"
    "v_smfmac_f32_16x16x64_bf16 v[32:35], v[124:127], v[64:71], v210\n\t"
    "s_add_i32 s86, s87, 2368\n\t"
    "ds_read_b32 v95, v73 offset:4612\n\t"
    "v_smfmac_f32_16x16x64_bf16 v[36:39], a[236:239], v[40:47], v205 abid:1\n\t"
    "s_add_i32 s98, s98, 60\n\t"
    "ds_read_b32 v92, v218 offset:4612\n\t"
    "v_smfmac_f32_16x16x64_bf16 v[36:39], v[108:111], v[48:55], v208\n\t"
    "s_max_i32 s86, s86, s98\n\t"
    "ds_read_b128 v[56:59], v217 offset:5504\n\t"
    "v_smfmac_f32_16x16x64_bf16 v[36:39], v[128:131], v[64:71], v210 abid:1\n\t"
    "s_waitcnt lgkmcnt(6)\n\t"
    "s_mov_b64 vcc, s[18:19]\n\t"
    "v_mfma_f32_16x16x32_bf16 v[36:39], v[80:83], v[88:91], v[36:39]\n\t"
    "s_sub_u32 s89, s84, s24\n\t"
    "ds_read_b128 v[80:83], v245 offset:0\n\t"
    "v_smfmac_f32_16x16x64_bf16 v[28:31], a[240:243], v[40:47], v206\n\t"
    "s_cmp_lt_i32 s89, s86\n\t"
    "s_cbranch_scc1 .Lpc13_%=\n\t"
    ".Lpc13r_%=:\n\t"
    "ds_read_b128 v[40:43], v217 offset:5632\n\t"
    "ds_read_b128 v[44:47], v236 offset:5632\n\t"
    "v_smfmac_f32_16x16x64_bf16 v[28:31], v[112:115], v[48:55], v208 abid:1\n\t"
    "v_smfmac_f32_16x16x64_bf16 v[28:31], v[132:135], v[64:71], v211\n\t"
    "s_max_i32 s98, s89, s86\n\t"
    "v_smfmac_f32_16x16x64_bf16 v[4:7], a[80:83], v[48:55], v186\n\t"
    "ds_read_b32 v91, v79 offset:4612\n\t"
    "v_smfmac_f32_16x16x64_bf16 v[4:7], a[96:99], v[64:71], v188\n\t"
    "s_waitcnt lgkmcnt(4)\n\t"
    "ds_read_b32 v90, v225 offset:4612\n\t"
    "v_smfmac_f32_16x16x64_bf16 v[4:7], a[112:115], v[56:63], v190\n\t"
    "s_waitcnt lgkmcnt(2)\n\t"
    "ds_read_b32 v89, v78 offset:4612\n\t"
    "v_smfmac_f32_16x16x64_bf16 v[4:7], a[128:131], v[40:47], v192\n\t"
    "v_mfma_f32_16x16x32_bf16 v[4:7], v[80:83], v[92:95], v[4:7]\n\t"
    "v_smfmac_f32_16x16x64_bf16 v[12:15], a[84:87], v[48:55], v186 abid:1\n\t"
    "ds_read_b32 v93, v104 offset:4612\n\t"
    "v_smfmac_f32_16x16x64_bf16 v[12:15], a[100:103], v[64:71], v188 abid:1\n\t"
    "ds_read_b128 v[80:83], v245 offset:3072\n\t"
    "v_smfmac_f32_16x16x64_bf16 v[12:15], a[116:119], v[56:63], v190 abid:1\n\t"
    "ds_read_b32 v94, v231 offset:4612\n\t"
    "v_cvt_pk_bf16_f32 v97, v6, v7\n\t"
    "v_smfmac_f32_16x16x64_bf16 v[12:15], a[132:135], v[40:47], v192 abid:1\n\t"
    "s_bitcmp1_b32 s54, 13\n\t"
    "s_mov_b32 s66, 61341696\n\t"
    "s_cmov_b32 s66, 61415424\n\t"
    "v_smfmac_f32_16x16x64_bf16 v[20:23], a[88:91], v[48:55], v187\n\t"
    "ds_read_b32 v88, v224 offset:4612\n\t"
    "v_cvt_pk_bf16_f32 v96, v4, v5\n\t"
    "v_smfmac_f32_16x16x64_bf16 v[20:23], a[104:107], v[64:71], v189\n\t"
    "ds_read_b128 v[4:7], v244 offset:0\n\t"
    "v_smfmac_f32_16x16x64_bf16 v[20:23], a[120:123], v[56:63], v191\n\t"
    "ds_read_b128 v[48:51], v217 offset:9216\n\t"
    "v_smfmac_f32_16x16x64_bf16 v[20:23], a[136:139], v[40:47], v193\n\t"
    "ds_read_b128 v[52:55], v236 offset:9216\n\t"
    "v_cvt_pk_bf16_f32 v99, v14, v15\n\t"
    "v_smfmac_f32_16x16x64_bf16 v[8:11], a[108:111], v[64:71], v189 abid:1\n\t"
    "ds_read_b32 v95, v105 offset:4612\n\t"
    "s_cmp_eq_u32 s82, 1008\n\t"
    "s_cbranch_scc1 .Lepoch_%=\n\t"
    ".Lepochr_%=:\n\t"
    "v_smfmac_f32_16x16x64_bf16 v[8:11], a[124:127], v[56:63], v191 abid:1\n\t"
    "v_cvt_pk_bf16_f32 v98, v12, v13\n\t"
    "v_smfmac_f32_16x16x64_bf16 v[8:11], a[140:143], v[40:47], v193 abid:1\n\t"
    "s_waitcnt lgkmcnt(4)\n\t"
    "ds_read_b32 v92, v230 offset:4612\n\t"
    "v_mfma_f32_16x16x32_bf16 v[8:11], v[80:83], v[88:91], v[8:11]\n\t"
    "ds_read_b128 v[80:83], v245 offset:6144\n\t"
    "v_cvt_pk_bf16_f32 v101, v22, v23\n\t"
    "v_smfmac_f32_16x16x64_bf16 v[24:27], v[116:119], v[64:71], v209\n\t"
    "ds_read_b32 v90, v223 offset:9220\n\t"
    "v_smfmac_f32_16x16x64_bf16 v[24:27], v[136:139], v[56:63], v211 abid:1\n\t"
    "ds_read_b32 v91, v77 offset:9220\n\t"
    "v_cvt_pk_bf16_f32 v100, v20, v21\n\t"
    "v_smfmac_f32_16x16x64_bf16 v[24:27], v[156:159], v[40:47], v214\n\t"
    "ds_read_b32 v88, v222 offset:9220\n\t"
    "v_cvt_pk_bf16_f32 v102, v8, v9\n\t"
    "v_cvt_pk_bf16_f32 v103, v10, v11\n\t"
    "v_smfmac_f32_16x16x64_bf16 v[16:19], v[120:123], v[64:71], v209 abid:1\n\t"
    "ds_read_b32 v89, v76 offset:9220\n\t"
    "v_cndmask_b32_dpp v8, v100, v96, vcc quad_perm:[0,0,2,2] row_mask:0xf bank_mask:0xf\n\t"
    "v_smfmac_f32_16x16x64_bf16 v[16:19], v[140:143], v[56:63], v212\n\t"
    "v_cndmask_b32_dpp v10, v102, v98, vcc quad_perm:[0,0,2,2] row_mask:0xf bank_mask:0xf\n\t"
    "v_smfmac_f32_16x16x64_bf16 v[16:19], v[160:163], v[40:47], v214 abid:1\n\t"
    "ds_read_b128 v[12:15], v244 offset:64\n\t"
    "v_cndmask_b32_dpp v11, v103, v99, vcc quad_perm:[0,0,2,2] row_mask:0xf bank_mask:0xf\n\t"
    "v_smfmac_f32_16x16x64_bf16 v[32:35], v[144:147], v[56:63], v212 abid:1\n\t"
    "ds_read_b128 v[68:71], v236 offset:9344\n\t"
    "v_cndmask_b32_dpp v9, v101, v97, vcc quad_perm:[0,0,2,2] row_mask:0xf bank_mask:0xf\n\t"
    "v_smfmac_f32_16x16x64_bf16 v[32:35], v[164:167], v[40:47], v215\n\t"
    "s_waitcnt lgkmcnt(6)\n\t"
    "ds_read_b128 v[64:67], v217 offset:9344\n\t"
    "v_mfma_f32_16x16x32_bf16 v[32:35], v[80:83], v[92:95], v[32:35]\n\t"
    "s_not_b64 vcc, s[18:19]\n\t"
    "ds_read_b32 v92, v228 offset:9220\n\t"
    "v_cndmask_b32_dpp v101, v97, v101, vcc quad_perm:[1,1,3,3] row_mask:0xf bank_mask:0xf\n\t"
    "v_smfmac_f32_16x16x64_bf16 v[36:39], v[148:151], v[56:63], v213\n\t"
    "ds_read_b128 v[20:23], v244 offset:128\n\t"
    "v_cndmask_b32_dpp v102, v98, v102, vcc quad_perm:[1,1,3,3] row_mask:0xf bank_mask:0xf\n\t"
    "v_smfmac_f32_16x16x64_bf16 v[36:39], v[168:171], v[40:47], v215 abid:1\n\t"
    "ds_read_b128 v[80:83], v245 offset:2048\n\t"
    "v_cndmask_b32_dpp v100, v96, v100, vcc quad_perm:[1,1,3,3] row_mask:0xf bank_mask:0xf\n\t"
    "v_smfmac_f32_16x16x64_bf16 v[28:31], v[152:155], v[56:63], v213 abid:1\n\t"
    "buffer_store_dwordx4 v[8:11], v241, s[44:47], s66 offen offset:0 sc1\n\t"
    "ds_read_b128 v[60:63], v236 offset:9472\n\t"
    "v_cvt_pk_bf16_f32 v97, v26, v27\n\t"
    "v_cvt_pk_bf16_f32 v98, v16, v17\n\t"
    "v_smfmac_f32_16x16x64_bf16 v[28:31], v[172:175], v[40:47], v216\n\t"
    "ds_read_b128 v[8:11], v244 offset:192\n\t"
    "v_cvt_pk_bf16_f32 v96, v24, v25\n\t"
    "v_smfmac_f32_16x16x64_bf16 v[4:7], a[0:3], v[48:55], v176\n\t"
    "s_waitcnt lgkmcnt(5)\n\t"
    "v_smfmac_f32_16x16x64_bf16 v[4:7], a[16:19], v[64:71], v178\n\t"
    "ds_read_b128 v[56:59], v217 offset:9472\n\t"
    "v_cndmask_b32_dpp v103, v99, v103, vcc quad_perm:[1,1,3,3] row_mask:0xf bank_mask:0xf\n\t"
    "v_smfmac_f32_16x16x64_bf16 v[12:15], a[4:7], v[48:55], v176 abid:1\n\t"
    "ds_read_b128 v[24:27], v244 offset:256\n\t"
    "v_cvt_pk_bf16_f32 v99, v18, v19\n\t"
    "v_smfmac_f32_16x16x64_bf16 v[12:15], a[20:23], v[64:71], v178 abid:1\n\t"
    "s_waitcnt lgkmcnt(5)\n\t"
    "s_mov_b64 vcc, s[18:19]\n\t"
    "ds_read_b128 v[40:43], v217 offset:9600\n\t"
    "v_smfmac_f32_16x16x64_bf16 v[20:23], a[8:11], v[48:55], v177\n\t"
    "buffer_store_dwordx4 v[100:103], v241, s[44:47], s66 offen offset:1152 sc1\n\t"
    "ds_read_b128 v[16:19], v244 offset:320\n\t"
    "v_cvt_pk_bf16_f32 v28, v28, v29\n\t"
    "v_smfmac_f32_16x16x64_bf16 v[20:23], a[24:27], v[64:71], v179\n\t"
    "s_waitcnt lgkmcnt(6)\n\t"
    ".Lpad2_%=:\n\t"
    "s_cbranch_execz .Lpad3_%=\n\t"
    "v_cvt_pk_bf16_f32 v101, v34, v35\n\t"
    "v_mfma_f32_16x16x32_bf16 v[20:23], v[80:83], v[88:91], v[20:23]\n\t"
    "s_waitcnt lgkmcnt(4)\n\t"
    "ds_read_b32 v94, v229 offset:9220\n\t"
    "v_cvt_pk_bf16_f32 v100, v32, v33\n\t"
    "v_cvt_pk_bf16_f32 v102, v36, v37\n\t"
    "v_cvt_pk_bf16_f32 v103, v38, v39\n\t"
    "v_smfmac_f32_16x16x64_bf16 v[8:11], a[12:15], v[48:55], v177 abid:1\n\t"
    "s_add_u32 s67, s57, s66\n\t"
    "ds_read_b32 v95, v87 offset:9220\n\t"
    "v_cndmask_b32_dpp v37, v101, v97, vcc quad_perm:[0,0,2,2] row_mask:0xf bank_mask:0xf\n\t"
    "v_smfmac_f32_16x16x64_bf16 v[8:11], a[28:31], v[64:71], v179 abid:1\n\t"
    "s_waitcnt lgkmcnt(5)\n\t"
    "ds_read_b128 v[80:83], v245 offset:5120\n\t"
    "v_cndmask_b32_dpp v36, v100, v96, vcc quad_perm:[0,0,2,2] row_mask:0xf bank_mask:0xf\n\t"
    "v_smfmac_f32_16x16x64_bf16 v[8:11], a[44:47], v[56:63], v181 abid:1\n\t"
    "s_waitcnt lgkmcnt(5)\n\t"
    "ds_read_b32 v93, v86 offset:9220\n\t"
    "v_cndmask_b32_dpp v38, v102, v98, vcc quad_perm:[0,0,2,2] row_mask:0xf bank_mask:0xf\n\t"
    "v_cndmask_b32_dpp v39, v103, v99, vcc quad_perm:[0,0,2,2] row_mask:0xf bank_mask:0xf\n\t"
    "v_smfmac_f32_16x16x64_bf16 v[24:27], a[144:147], v[48:55], v194\n\t"
    "s_not_b64 vcc, s[18:19]\n\t"
    "buffer_store_dwordx4 v[36:39], v241, s[44:47], s66 offen offset:128 sc1\n\t"
    "ds_read_b128 v[32:35], v244 offset:384\n\t"
    "v_cndmask_b32_dpp v100, v96, v100, vcc quad_perm:[1,1,3,3] row_mask:0xf bank_mask:0xf\n\t"
    "v_cvt_pk_bf16_f32 v29, v30, v31\n\t"
    "v_smfmac_f32_16x16x64_bf16 v[24:27], a[164:167], v[64:71], v196 abid:1\n\t"
    "ds_read_b128 v[44:47], v236 offset:9600\n\t"
    "v_add_u32_sdwa v91, v248, v235 dst_sel:DWORD dst_unused:UNUSED_PAD src0_sel:DWORD src1_sel:WORD_1\n\t"
    "v_smfmac_f32_16x16x64_bf16 v[24:27], a[184:187], v[56:63], v199\n\t"
    "s_waitcnt lgkmcnt(6)\n\t"
    "ds_read_b128 v[36:39], v244 offset:448\n\t"
    "v_cndmask_b32_dpp v101, v97, v101, vcc quad_perm:[1,1,3,3] row_mask:0xf bank_mask:0xf\n\t"
    "v_smfmac_f32_16x16x64_bf16 v[16:19], a[148:151], v[48:55], v194 abid:1\n\t"
    "v_add_u32_sdwa v88, v248, v234 dst_sel:DWORD dst_unused:UNUSED_PAD src0_sel:DWORD src1_sel:WORD_0\n\t"
    "v_smfmac_f32_16x16x64_bf16 v[16:19], a[168:171], v[64:71], v197\n\t"
    "ds_read_b32 v88, v88 offset:9220\n\t"
    "v_cndmask_b32_dpp v102, v98, v102, vcc quad_perm:[1,1,3,3] row_mask:0xf bank_mask:0xf\n\t"
    "v_smfmac_f32_16x16x64_bf16 v[16:19], a[188:191], v[56:63], v199 abid:1\n\t"
    "s_waitcnt lgkmcnt(4)\n\t"
    "ds_read_b32 v91, v91 offset:9220\n\t"
    "v_cndmask_b32_dpp v103, v99, v103, vcc quad_perm:[1,1,3,3] row_mask:0xf bank_mask:0xf\n\t"
    "v_mfma_f32_16x16x32_bf16 v[16:19], v[80:83], v[92:95], v[16:19]\n\t"
    "s_waitcnt lgkmcnt(4)\n\t"
    "buffer_store_dwordx4 v[100:103], v241, s[44:47], s66 offen offset:1280 sc1\n\t"
    "ds_write_b64 v242, v[28:29] offset:512\n\t"
    "v_add_u32_sdwa v89, v248, v234 dst_sel:DWORD dst_unused:UNUSED_PAD src0_sel:DWORD src1_sel:WORD_1\n\t"
    "v_smfmac_f32_16x16x64_bf16 v[32:35], a[152:155], v[48:55], v195\n\t"
    "v_smfmac_f32_16x16x64_bf16 v[32:35], a[172:175], v[64:71], v197 abid:1\n\t"
    "ds_read_b128 v[28:31], v244 offset:512\n\t"
    "v_add_u32_sdwa v90, v248, v235 dst_sel:DWORD dst_unused:UNUSED_PAD src0_sel:DWORD src1_sel:WORD_0\n\t"
    "v_smfmac_f32_16x16x64_bf16 v[32:35], a[192:195], v[56:63], v200\n\t"
    "s_waitcnt lgkmcnt(5)\n\t"
    "ds_read_b128 v[80:83], v245 offset:8192\n\t"
    "v_smfmac_f32_16x16x64_bf16 v[32:35], a[212:215], v[40:47], v202 abid:1\n\t"
    "s_waitcnt lgkmcnt(5)\n\t"
    "ds_read_b32 v95, v75 offset:9220\n\t"
    "v_smfmac_f32_16x16x64_bf16 v[36:39], a[156:159], v[48:55], v195 abid:1\n\t"
    "ds_read_b32 v89, v89 offset:9220\n\t"
    "v_smfmac_f32_16x16x64_bf16 v[36:39], a[176:179], v[64:71], v198\n\t"
    "ds_read_b32 v90, v90 offset:9220\n\t"
    "v_smfmac_f32_16x16x64_bf16 v[36:39], a[196:199], v[56:63], v200 abid:1\n\t"
    "ds_read_b32 v94, v221 offset:9220\n\t"
    "v_smfmac_f32_16x16x64_bf16 v[36:39], a[216:219], v[40:47], v203\n\t"
    "s_waitcnt lgkmcnt(5)\n\t"
    "v_smfmac_f32_16x16x64_bf16 v[28:31], a[160:163], v[48:55], v196\n\t"
    "ds_read_b128 v[48:51], v217 offset:9728\n\t"
    "ds_read_b128 v[52:55], v236 offset:9728\n\t"
    "v_smfmac_f32_16x16x64_bf16 v[28:31], a[180:183], v[64:71], v198 abid:1\n\t"
    "ds_read_b32 v92, v220 offset:9220\n\t"
    "v_smfmac_f32_16x16x64_bf16 v[28:31], a[200:203], v[56:63], v201\n\t"
    "v_smfmac_f32_16x16x64_bf16 v[28:31], a[220:223], v[40:47], v203 abid:1\n\t"
    "s_waitcnt lgkmcnt(4)\n\t"
    "ds_read_b32 v93, v74 offset:9220\n\t"
    "v_mfma_f32_16x16x32_bf16 v[28:31], v[80:83], v[88:91], v[28:31]\n\t"
    "ds_read_b128 v[80:83], v245 offset:1024\n\t"
    "v_add_u32_sdwa v90, v248, v247 dst_sel:DWORD dst_unused:UNUSED_PAD src0_sel:DWORD src1_sel:WORD_0\n\t"
    "v_smfmac_f32_16x16x64_bf16 v[4:7], a[32:35], v[56:63], v180\n\t"
    "ds_read_u16 v90, v90 offset:9216\n\t"
    "v_add_u32_sdwa v89, v248, v246 dst_sel:DWORD dst_unused:UNUSED_PAD src0_sel:DWORD src1_sel:WORD_1\n\t"
    "v_smfmac_f32_16x16x64_bf16 v[4:7], a[48:51], v[40:47], v182\n\t"
    "s_waitcnt lgkmcnt(4)\n\t"
    "ds_read_u16 v89, v89 offset:9216\n\t"
    "v_add_u32_sdwa v91, v248, v247 dst_sel:DWORD dst_unused:UNUSED_PAD src0_sel:DWORD src1_sel:WORD_1\n\t"
    "v_smfmac_f32_16x16x64_bf16 v[4:7], a[64:67], v[48:55], v184\n\t"
    "v_add_u32_sdwa v88, v248, v246 dst_sel:DWORD dst_unused:UNUSED_PAD src0_sel:DWORD src1_sel:WORD_0\n\t"
    "v_smfmac_f32_16x16x64_bf16 v[12:15], a[36:39], v[56:63], v180 abid:1\n\t"
    "s_waitcnt vmcnt(9)\n\t"
    "s_barrier\n\t"
    "ds_read_u16 v88, v88 offset:9216\n\t"
    "ds_read_u16 v91, v91 offset:9216\n\t"
    "v_smfmac_f32_16x16x64_bf16 v[12:15], a[52:55], v[40:47], v182 abid:1\n\t"
    "ds_read_b128 v[96:99], v243 offset:2560\n\t"
    "v_smfmac_f32_16x16x64_bf16 v[12:15], a[68:71], v[48:55], v184 abid:1\n\t"
    "s_waitcnt lgkmcnt(5)\n\t"
    "ds_read_b128 v[68:71], v236 offset:9856\n\t"
    "v_mfma_f32_16x16x32_bf16 v[12:15], v[80:83], v[92:95], v[12:15]\n\t"
    "ds_read_b128 v[64:67], v217 offset:9856\n\t"
    "v_smfmac_f32_16x16x64_bf16 v[20:23], a[40:43], v[56:63], v181\n\t"
    "ds_read_b128 v[80:83], v245 offset:4096\n\t"
    "v_smfmac_f32_16x16x64_bf16 v[20:23], a[56:59], v[40:47], v183\n\t"
    "v_smfmac_f32_16x16x64_bf16 v[20:23], a[72:75], v[48:55], v185\n\t"
    "s_waitcnt lgkmcnt(4)\n\t"
    "ds_read_b32 v94, v227 offset:9220\n\t"
    "s_set_gpr_idx_on s4, gpr_idx(SRC2,DST)\n\t"
    "v_mfma_f32_16x16x32_bf16 v[0:3], v[250:253], v[88:91], v[0:3]\n\t"
    "s_set_gpr_idx_off\n\t"
    "s_waitcnt lgkmcnt(4)\n\t"
    "s_mov_b32 exec_hi, 0\n\t"
    "buffer_store_dwordx4 v[96:99], v240, s[44:47], s67 offen offset:0 sc1\n\t"
    "s_mov_b32 exec_hi, -1\n\t"
    "ds_read_b32 v92, v226 offset:9220\n\t"
    "v_smfmac_f32_16x16x64_bf16 v[8:11], a[60:63], v[40:47], v183 abid:1\n\t"
    "ds_read_b32 v93, v84 offset:9220\n\t"
    "ds_read_b32 v95, v85 offset:9220\n\t"
    "v_smfmac_f32_16x16x64_bf16 v[8:11], a[76:79], v[48:55], v185 abid:1\n\t"
    "s_waitcnt lgkmcnt(5)\n\t"
    "v_smfmac_f32_16x16x64_bf16 v[8:11], a[92:95], v[64:71], v187 abid:1\n\t"
    "ds_read_b128 v[56:59], v217 offset:9984\n\t"
    "s_set_gpr_idx_on s5, gpr_idx(SRC2,DST)\n\t"
    "v_mfma_f32_16x16x32_bf16 v[0:3], v[252:255], v[88:91], v[0:3]\n\t"
    "s_set_gpr_idx_off\n\t"
    "s_cmp_lg_u32 s22, 0\n\t"
    "s_cbranch_scc1 .Lsl19_%=\n\t"
    ".Lslr19_%=:\n\t"
    "v_smfmac_f32_16x16x64_bf16 v[24:27], a[204:207], v[40:47], v201 abid:1\n\t"
    "ds_read_b32 v88, v232 offset:9220\n\t"
    "ds_read_b32 v90, v233 offset:9220\n\t"
    "v_smfmac_f32_16x16x64_bf16 v[24:27], a[224:227], v[48:55], v204\n\t"
    "s_memrealtime s[84:85]\n\t"
    "v_smfmac_f32_16x16x64_bf16 v[24:27], a[244:247], v[64:71], v206 abid:1\n\t"
    "s_waitcnt lgkmcnt(3)\n\t"
    "ds_read_b128 v[60:63], v236 offset:9984\n\t"
    "v_mfma_f32_16x16x32_bf16 v[24:27], v[80:83], v[92:95], v[24:27]\n\t"
    "s_mov_b32 s77, 268368\n\t"
    "ds_read_b128 v[80:83], v245 offset:7168\n\t"
    "v_smfmac_f32_16x16x64_bf16 v[16:19], a[208:211], v[40:47], v202\n\t"
    "s_mov_b32 s76, 3238002688\n\t"
    "ds_read_b128 v[40:43], v217 offset:10112\n\t"
    "v_smfmac_f32_16x16x64_bf16 v[16:19], a[228:231], v[48:55], v204 abid:1\n\t"
    "s_lshr_b64 s[80:81], s[76:77], s69\n\t"
    "ds_read_b32 v91, v107 offset:9220\n\t"
    "v_smfmac_f32_16x16x64_bf16 v[16:19], a[248:251], v[64:71], v207\n\t"
    "s_sub_i32 s87, s87, s99\n\t"
    "ds_read_b32 v89, v106 offset:9220\n\t"
    "v_smfmac_f32_16x16x64_bf16 v[32:35], a[232:235], v[48:55], v205\n\t"
    "ds_read_b128 v[44:47], v236 offset:10112\n\t"
    "v_smfmac_f32_16x16x64_bf16 v[32:35], a[252:255], v[64:71], v207 abid:1\n\t"
    "s_waitcnt lgkmcnt(5)\n\t"
    "s_sub_i32 s98, s98, s99\n\t"
    "ds_read_b32 v92, v218 offset:9220\n\t"
    "v_smfmac_f32_16x16x64_bf16 v[32:35], v[124:127], v[56:63], v210\n\t"
    "s_add_i32 s86, s87, 2475\n\t"
    "ds_read_b32 v95, v73 offset:9220\n\t"
    "v_smfmac_f32_16x16x64_bf16 v[36:39], a[236:239], v[48:55], v205 abid:1\n\t"
    "s_add_i32 s98, s98, 57\n\t"
    "v_smfmac_f32_16x16x64_bf16 v[36:39], v[108:111], v[64:71], v208\n\t"
    "s_max_i32 s86, s86, s98\n\t"
    "ds_read_b32 v93, v72 offset:9220\n\t"
    "v_smfmac_f32_16x16x64_bf16 v[36:39], v[128:131], v[56:63], v210 abid:1\n\t"
    "s_waitcnt lgkmcnt(4)\n\t"
    "ds_read_b32 v94, v219 offset:9220\n\t"
    "v_mfma_f32_16x16x32_bf16 v[36:39], v[80:83], v[88:91], v[36:39]\n\t"
    "s_sub_u32 s89, s84, s24\n\t"
    "ds_read_b128 v[80:83], v245 offset:0\n\t"
    "v_smfmac_f32_16x16x64_bf16 v[28:31], a[240:243], v[48:55], v206\n\t"
    "s_cmp_lt_i32 s89, s86\n\t"
    "s_cbranch_scc1 .Lpc14_%=\n\t"
    ".Lpc14r_%=:\n\t"
    "ds_read_b128 v[48:51], v217 offset:10240\n\t"
    "v_smfmac_f32_16x16x64_bf16 v[28:31], v[112:115], v[64:71], v208 abid:1\n\t"
    "ds_read_b128 v[52:55], v236 offset:10240\n\t"
    "v_smfmac_f32_16x16x64_bf16 v[28:31], v[132:135], v[56:63], v211\n\t"
    "s_bitcmp1_b32 s54, 14\n\t"
    "s_mov_b32 s65, 66060288\n\t"
    "s_cmov_b32 s65, 66134016\n\t"
    "s_max_i32 s98, s89, s86\n\t"
    "v_smfmac_f32_16x16x64_bf16 v[4:7], a[80:83], v[64:71], v186\n\t"
    "ds_read_b32 v89, v78 offset:9220\n\t"
    "v_smfmac_f32_16x16x64_bf16 v[4:7], a[96:99], v[56:63], v188\n\t"
    "s_waitcnt lgkmcnt(8)\n\t"
    "ds_read_b32 v91, v79 offset:9220\n\t"
    "v_smfmac_f32_16x16x64_bf16 v[4:7], a[112:115], v[40:47], v190\n\t"
    "s_waitcnt lgkmcnt(2)\n\t"
    "v_smfmac_f32_16x16x64_bf16 v[4:7], a[128:131], v[48:55], v192\n\t"
    "ds_read_b32 v90, v225 offset:9220\n\t"
    "v_mfma_f32_16x16x32_bf16 v[4:7], v[80:83], v[92:95], v[4:7]\n\t"
    "ds_read_b32 v92, v230 offset:9220\n\t"
    "v_smfmac_f32_16x16x64_bf16 v[12:15], a[84:87], v[64:71], v186 abid:1\n\t"
    "ds_read_b32 v88, v224 offset:9220\n\t"
    "v_smfmac_f32_16x16x64_bf16 v[12:15], a[100:103], v[56:63], v188 abid:1\n\t"
    "v_smfmac_f32_16x16x64_bf16 v[12:15], a[116:119], v[40:47], v190 abid:1\n\t"
    "ds_read_b128 v[80:83], v245 offset:3072\n\t"
    "v_cvt_pk_bf16_f32 v101, v6, v7\n\t"
    "v_smfmac_f32_16x16x64_bf16 v[12:15], a[132:135], v[48:55], v192 abid:1\n\t"
    "ds_read_b32 v95, v105 offset:9220\n\t"
    "v_smfmac_f32_16x16x64_bf16 v[20:23], a[88:91], v[64:71], v187\n\t"
    "v_cvt_pk_bf16_f32 v100, v4, v5\n\t"
    "v_smfmac_f32_16x16x64_bf16 v[20:23], a[104:107], v[56:63], v189\n\t"
    "s_mov_b64 vcc, s[18:19]\n\t"
    "ds_read_b128 v[64:67], v217 offset:13824\n\t"
    "v_smfmac_f32_16x16x64_bf16 v[20:23], a[120:123], v[40:47], v191\n\t"
    "ds_read_b32 v93, v104 offset:9220\n\t"
    "v_smfmac_f32_16x16x64_bf16 v[20:23], a[136:139], v[48:55], v193\n\t"
    "s_bitcmp1_b32 s54, 15\n\t"
    "s_mov_b32 s67, 70778880\n\t"
    "s_cmov_b32 s67, 70852608\n\t"
    "v_smfmac_f32_16x16x64_bf16 v[8:11], a[108:111], v[56:63], v189 abid:1\n\t"
    "ds_read_b128 v[4:7], v244 offset:256\n\t"
    "v_cvt_pk_bf16_f32 v102, v12, v13\n\t"
    "v_smfmac_f32_16x16x64_bf16 v[8:11], a[124:127], v[40:47], v191 abid:1\n\t"
    "ds_read_b32 v94, v231 offset:9220\n\t"
    "v_smfmac_f32_16x16x64_bf16 v[8:11], a[140:143], v[48:55], v193 abid:1\n\t"
    "s_waitcnt lgkmcnt(5)\n\t"
    "ds_read_b128 v[68:71], v236 offset:13824\n\t"
    "v_cvt_pk_bf16_f32 v96, v20, v21\n\t"
    "v_mfma_f32_16x16x32_bf16 v[8:11], v[80:83], v[88:91], v[8:11]\n\t"
    "ds_read_b32 v90, v231 offset:13828\n\t"
    "v_cvt_pk_bf16_f32 v97, v22, v23\n\t"
    "v_smfmac_f32_16x16x64_bf16 v[24:27], v[116:119], v[56:63], v209\n\t"
    "ds_read_b32 v89, v104 offset:13828\n\t"
    "v_smfmac_f32_16x16x64_bf16 v[24:27], v[136:139], v[40:47], v211 abid:1\n\t"
    "s_mov_b32 s77, 268368\n\t"
    "ds_read_b128 v[80:83], v245 offset:6144\n\t"
    "v_smfmac_f32_16x16x64_bf16 v[24:27], v[156:159], v[48:55], v214\n\t"
    "ds_read_b32 v91, v105 offset:13828\n\t"
    "v_cvt_pk_bf16_f32 v98, v8, v9\n\t"
    "v_smfmac_f32_16x16x64_bf16 v[16:19], v[120:123], v[56:63], v209 abid:1\n\t"
    "ds_read_b128 v[60:63], v236 offset:13952\n\t"
    "v_cvt_pk_bf16_f32 v103, v14, v15\n\t"
    "v_smfmac_f32_16x16x64_bf16 v[16:19], v[140:143], v[40:47], v212\n\t"
    "ds_read_b128 v[56:59], v217 offset:13952\n\t"
    "v_cndmask_b32_dpp v8, v96, v100, vcc quad_perm:[0,0,2,2] row_mask:0xf bank_mask:0xf\n\t"
    "v_smfmac_f32_16x16x64_bf16 v[16:19], v[160:163], v[48:55], v214 abid:1\n\t"
    "v_cvt_pk_bf16_f32 v99, v10, v11\n\t"
    "v_smfmac_f32_16x16x64_bf16 v[32:35], v[144:147], v[40:47], v212 abid:1\n\t"
    "ds_read_b128 v[20:23], v244 offset:384\n\t"
    "v_cndmask_b32_dpp v9, v97, v101, vcc quad_perm:[0,0,2,2] row_mask:0xf bank_mask:0xf\n\t"
    "v_smfmac_f32_16x16x64_bf16 v[32:35], v[164:167], v[48:55], v215\n\t"
    "s_waitcnt lgkmcnt(4)\n\t"
    "ds_read_b128 v[12:15], v244 offset:320\n\t"
    "v_cndmask_b32_dpp v11, v99, v103, vcc quad_perm:[0,0,2,2] row_mask:0xf bank_mask:0xf\n\t"
    "v_mfma_f32_16x16x32_bf16 v[32:35], v[80:83], v[92:95], v[32:35]\n\t"
    "ds_read_b128 v[80:83], v245 offset:6144\n\t"
    "v_cndmask_b32_dpp v10, v98, v102, vcc quad_perm:[0,0,2,2] row_mask:0xf bank_mask:0xf\n\t"
    "v_smfmac_f32_16x16x64_bf16 v[36:39], v[148:151], v[40:47], v213\n\t"
    "s_not_b64 vcc, s[18:19]\n\t"
    "ds_read_b32 v88, v230 offset:13828\n\t"
    "v_cndmask_b32_dpp v98, v102, v98, vcc quad_perm:[1,1,3,3] row_mask:0xf bank_mask:0xf\n\t"
    "v_smfmac_f32_16x16x64_bf16 v[36:39], v[168:171], v[48:55], v215 abid:1\n\t"
    "v_cndmask_b32_dpp v99, v103, v99, vcc quad_perm:[1,1,3,3] row_mask:0xf bank_mask:0xf\n\t"
    "v_smfmac_f32_16x16x64_bf16 v[28:31], v[152:155], v[40:47], v213 abid:1\n\t"
    "buffer_store_dwordx4 v[8:11], v241, s[44:47], s65 offen offset:0 sc1\n\t"
    "ds_read_b32 v95, v87 offset:13828\n\t"
    "v_cndmask_b32_dpp v96, v100, v96, vcc quad_perm:[1,1,3,3] row_mask:0xf bank_mask:0xf\n\t"
    "v_cndmask_b32_dpp v97, v101, v97, vcc quad_perm:[1,1,3,3] row_mask:0xf bank_mask:0xf\n\t"
    "v_cvt_pk_bf16_f32 v100, v24, v25\n\t"
    "v_smfmac_f32_16x16x64_bf16 v[28:31], v[172:175], v[48:55], v216\n\t"
    "ds_read_b128 v[44:47], v236 offset:14080\n\t"
    "v_cvt_pk_bf16_f32 v101, v26, v27\n\t"
    "v_smfmac_f32_16x16x64_bf16 v[4:7], a[144:147], v[64:71], v194\n\t"
    "s_waitcnt lgkmcnt(6)\n\t"
    "s_add_u32 s66, s57, s65\n\t"
    "ds_read_b128 v[24:27], v244 offset:448\n\t"
    "v_cvt_pk_bf16_f32 v103, v18, v19\n\t"
    "v_smfmac_f32_16x16x64_bf16 v[4:7], a[164:167], v[56:63], v196 abid:1\n\t"
    "s_waitcnt lgkmcnt(5)\n\t"
    "s_mov_b64 vcc, s[18:19]\n\t"
    "ds_read_b32 v92, v228 offset:13828\n\t"
    "v_cvt_pk_bf16_f32 v102, v16, v17\n\t"
    "v_smfmac_f32_16x16x64_bf16 v[12:15], a[148:151], v[64:71], v194 abid:1\n\t"
    "ds_read_b128 v[16:19], v244 offset:512\n\t"
    "v_cvt_pk_bf16_f32 v28, v28, v29\n\t"
    "v_smfmac_f32_16x16x64_bf16 v[12:15], a[168:171], v[56:63], v197\n\t"
    "ds_read_b128 v[40:43], v217 offset:14080\n\t"
    "v_cvt_pk_bf16_f32 v29, v30, v31\n\t"
    "v_smfmac_f32_16x16x64_bf16 v[20:23], a[152:155], v[64:71], v195\n\t"
    "buffer_store_dwordx4 v[96:99], v241, s[44:47], s65 offen offset:1152 sc1\n\t"
    "ds_read_b128 v[52:55], v236 offset:14208\n\t"
    "ds_read_b32 v94, v229 offset:13828\n\t"
    "v_smfmac_f32_16x16x64_bf16 v[20:23], a[172:175], v[56:63], v197 abid:1\n\t"
    "s_waitcnt lgkmcnt(8)\n\t"
    "ds_read_b128 v[48:51], v217 offset:14208\n\t"
    "v_cvt_pk_bf16_f32 v98, v36, v37\n\t"
    "v_mfma_f32_16x16x32_bf16 v[20:23], v[80:83], v[88:91], v[20:23]\n\t"
    "s_waitcnt lgkmcnt(6)\n\t"
    "ds_read_b128 v[80:83], v245 offset:5120\n\t"
    "v_cvt_pk_bf16_f32 v97, v34, v35\n\t"
    "v_cvt_pk_bf16_f32 v99, v38, v39\n\t"
    "v_add_u32_sdwa v90, v248, v247 dst_sel:DWORD dst_unused:UNUSED_PAD src0_sel:DWORD src1_sel:WORD_0\n\t"
    "v_add_u32_sdwa v91, v248, v247 dst_sel:DWORD dst_unused:UNUSED_PAD src0_sel:DWORD src1_sel:WORD_1\n\t"
    "v_smfmac_f32_16x16x64_bf16 v[24:27], a[156:159], v[64:71], v195 abid:1\n\t"
    "ds_read_u16 v90, v90 offset:13824\n\t"
    "v_cvt_pk_bf16_f32 v96, v32, v33\n\t"
    "v_smfmac_f32_16x16x64_bf16 v[24:27], a[176:179], v[56:63], v198\n\t"
    "s_waitcnt lgkmcnt(6)\n\t"
    "ds_read_b32 v93, v86 offset:13828\n\t"
    "ds_read_u16 v91, v91 offset:13824\n\t"
    "v_cndmask_b32_dpp v37, v97, v101, vcc quad_perm:[0,0,2,2] row_mask:0xf bank_mask:0xf\n\t"
    "v_cndmask_b32_dpp v38, v98, v102, vcc quad_perm:[0,0,2,2] row_mask:0xf bank_mask:0xf\n\t"
    "v_cndmask_b32_dpp v39, v99, v103, vcc quad_perm:[0,0,2,2] row_mask:0xf bank_mask:0xf\n\t"
    "v_add_u32_sdwa v89, v248, v246 dst_sel:DWORD dst_unused:UNUSED_PAD src0_sel:DWORD src1_sel:WORD_1\n\t"
    "v_smfmac_f32_16x16x64_bf16 v[16:19], a[160:163], v[64:71], v196\n\t"
    "ds_read_u16 v89, v89 offset:13824\n\t"
    "v_cndmask_b32_dpp v36, v96, v100, vcc quad_perm:[0,0,2,2] row_mask:0xf bank_mask:0xf\n\t"
    "v_smfmac_f32_16x16x64_bf16 v[16:19], a[180:183], v[56:63], v198 abid:1\n\t"
    "s_waitcnt lgkmcnt(8)\n\t"
    "s_not_b64 vcc, s[18:19]\n\t"
    "buffer_store_dwordx4 v[36:39], v241, s[44:47], s65 offen offset:128 sc1\n\t"
    "ds_write_b64 v242, v[28:29] offset:0\n\t"
    "v_add_u32_sdwa v88, v248, v246 dst_sel:DWORD dst_unused:UNUSED_PAD src0_sel:DWORD src1_sel:WORD_0\n\t"
    "v_smfmac_f32_16x16x64_bf16 v[4:7], a[184:187], v[40:47], v199\n\t"
    "s_waitcnt lgkmcnt(6)\n\t"
    "ds_read_u16 v88, v88 offset:13824\n\t"
    "v_cndmask_b32_dpp v96, v100, v96, vcc quad_perm:[1,1,3,3] row_mask:0xf bank_mask:0xf\n\t"
    "v_smfmac_f32_16x16x64_bf16 v[4:7], a[204:207], v[48:55], v201 abid:1\n\t"
    "s_and_b32 s80, s80, 63\n\t"
    "ds_read_b128 v[64:67], v217 offset:14336\n\t"
    "v_cndmask_b32_dpp v98, v102, v98, vcc quad_perm:[1,1,3,3] row_mask:0xf bank_mask:0xf\n\t"
    "v_smfmac_f32_16x16x64_bf16 v[12:15], a[188:191], v[40:47], v199 abid:1\n\t"
    "ds_read_b128 v[56:59], v217 offset:14464\n\t"
    "v_cndmask_b32_dpp v99, v103, v99, vcc quad_perm:[1,1,3,3] row_mask:0xf bank_mask:0xf\n\t"
    "v_smfmac_f32_16x16x64_bf16 v[12:15], a[208:211], v[48:55], v202\n\t"
    "s_waitcnt lgkmcnt(6)\n\t"
    "ds_read_b128 v[60:63], v236 offset:14464\n\t"
    "v_cndmask_b32_dpp v97, v101, v97, vcc quad_perm:[1,1,3,3] row_mask:0xf bank_mask:0xf\n\t"
    "v_mfma_f32_16x16x32_bf16 v[12:15], v[80:83], v[92:95], v[12:15]\n\t"
    "ds_read_b128 v[80:83], v245 offset:4096\n\t"
    "v_smfmac_f32_16x16x64_bf16 v[20:23], a[192:195], v[40:47], v200\n\t"
    "s_mov_b32 s76, 3238002688\n\t"
    "buffer_store_dwordx4 v[96:99], v241, s[44:47], s65 offen offset:1280 sc1\n\t"
    "ds_read_b128 v[68:71], v236 offset:14336\n\t"
    "ds_read_b32 v94, v227 offset:13828\n\t"
    "v_smfmac_f32_16x16x64_bf16 v[20:23], a[212:215], v[48:55], v202 abid:1\n\t"
    "s_waitcnt lgkmcnt(6)\n\t"
    "s_lshl_b32 s80, s80, 2\n\t"
    "ds_read_b32 v92, v226 offset:13828\n\t"
    "s_set_gpr_idx_on s80, gpr_idx(SRC2,DST)\n\t"
    "v_mfma_f32_16x16x32_bf16 v[0:3], v[250:253], v[88:91], v[0:3]\n\t"
    "s_set_gpr_idx_off\n\t"
    "s_barrier\n\t"
    "ds_read_b128 v[100:103], v243 offset:2048\n\t"
    "v_smfmac_f32_16x16x64_bf16 v[24:27], a[196:199], v[40:47], v200 abid:1\n\t"
    "s_lshr_b64 s[80:81], s[76:77], s70\n\t"
    "ds_read_b32 v93, v84 offset:13828\n\t"
    "ds_read_b32 v95, v85 offset:13828\n\t"
    "v_smfmac_f32_16x16x64_bf16 v[24:27], a[216:219], v[48:55], v203\n\t"
    "s_and_b32 s80, s80, 63\n\t"
    "s_lshl_b32 s80, s80, 2\n\t"
    "s_set_gpr_idx_on s80, gpr_idx(SRC2,DST)\n\t"
    "v_mfma_f32_16x16x32_bf16 v[0:3], v[252:255], v[88:91], v[0:3]\n\t"
    "s_set_gpr_idx_off\n\t"
    "s_cmp_lg_u32 s22, 0\n\t"
    "s_cbranch_scc1 .Lsl20_%=\n\t"
    ".Lslr20_%=:\n\t"
    "v_smfmac_f32_16x16x64_bf16 v[16:19], a[200:203], v[40:47], v201\n\t"
    "ds_read_b128 v[44:47], v236 offset:14592\n\t"
    "v_add_u32_sdwa v89, v248, v234 dst_sel:DWORD dst_unused:UNUSED_PAD src0_sel:DWORD src1_sel:WORD_1\n\t"
    "v_smfmac_f32_16x16x64_bf16 v[16:19], a[220:223], v[48:55], v203 abid:1\n\t"
    "s_waitcnt lgkmcnt(6)\n\t"
    "ds_read_b128 v[48:51], v217 offset:14720\n\t"
    "v_add_u32_sdwa v88, v248, v234 dst_sel:DWORD dst_unused:UNUSED_PAD src0_sel:DWORD src1_sel:WORD_0\n\t"
    "v_smfmac_f32_16x16x64_bf16 v[4:7], a[224:227], v[64:71], v204\n\t"
    "s_add_u32 s65, s57, s67\n\t"
    "ds_read_b32 v88, v88 offset:13828\n\t"
    "v_add_u32_sdwa v90, v248, v235 dst_sel:DWORD dst_unused:UNUSED_PAD src0_sel:DWORD src1_sel:WORD_0\n\t"
    "v_smfmac_f32_16x16x64_bf16 v[4:7], a[244:247], v[56:63], v206 abid:1\n\t"
    "s_waitcnt lgkmcnt(3)\n\t"
    "s_mov_b64 vcc, s[18:19]\n\t"
    "s_mov_b32 exec_hi, 0\n\t"
    "buffer_store_dwordx4 v[100:103], v240, s[44:47], s66 offen offset:0 sc1\n\t"
    "s_mov_b32 exec_hi, -1\n\t"
    "ds_read_b32 v89, v89 offset:13828\n\t"
    "v_add_u32_sdwa v91, v248, v235 dst_sel:DWORD dst_unused:UNUSED_PAD src0_sel:DWORD src1_sel:WORD_1\n\t"
    "v_mfma_f32_16x16x32_bf16 v[4:7], v[80:83], v[92:95], v[4:7]\n\t"
    "ds_read_b128 v[40:43], v217 offset:14592\n\t"
    "v_smfmac_f32_16x16x64_bf16 v[12:15], a[228:231], v[64:71], v204 abid:1\n\t"
    "s_mov_b32 s77, 0\n\t"
    "ds_read_b128 v[80:83], v245 offset:8192\n\t"
    "v_smfmac_f32_16x16x64_bf16 v[12:15], a[248:251], v[56:63], v207\n\t"
    "s_mov_b32 s76, 2388482\n\t"
    "ds_read_b32 v90, v90 offset:13828\n\t"
    "ds_read_b32 v91, v91 offset:13828\n\t"
    "v_smfmac_f32_16x16x64_bf16 v[20:23], a[232:235], v[64:71], v205\n\t"
    "ds_read_b32 v92, v232 offset:13828\n\t"
    "v_smfmac_f32_16x16x64_bf16 v[20:23], a[252:255], v[56:63], v207 abid:1\n\t"
    "ds_read_b128 v[52:55], v236 offset:14720\n\t"
    "v_smfmac_f32_16x16x64_bf16 v[24:27], a[236:239], v[64:71], v205 abid:1\n\t"
    "v_smfmac_f32_16x16x64_bf16 v[24:27], v[108:111], v[56:63], v208\n\t"
    "ds_read_b128 v[8:11], v244 offset:0\n\t"
    "v_smfmac_f32_16x16x64_bf16 v[16:19], a[240:243], v[64:71], v206\n\t"
    "ds_read_b128 v[64:67], v217 offset:14848\n\t"
    "ds_read_b128 v[68:71], v236 offset:14848\n\t"
    "v_smfmac_f32_16x16x64_bf16 v[16:19], v[112:115], v[56:63], v208 abid:1\n\t"
    "s_waitcnt lgkmcnt(5)\n\t"
    "v_mfma_f32_16x16x32_bf16 v[16:19], v[80:83], v[88:91], v[16:19]\n\t"
    "ds_read_b32 v93, v106 offset:13828\n\t"
    "ds_read_b32 v94, v233 offset:13828\n\t"
    "v_smfmac_f32_16x16x64_bf16 v[4:7], v[116:119], v[40:47], v209\n\t"
    "s_waitcnt lgkmcnt(5)\n\t"
    "s_lshr_b64 s[80:81], s[76:77], s69\n\t"
    "v_smfmac_f32_16x16x64_bf16 v[4:7], v[136:139], v[48:55], v211 abid:1\n\t"
    "s_waitcnt lgkmcnt(2)\n\t"
    "ds_read_b32 v95, v107 offset:13828\n\t"
    "v_add_u32_sdwa v91, v248, v247 dst_sel:DWORD dst_unused:UNUSED_PAD src0_sel:DWORD src1_sel:WORD_1\n\t"
    "v_smfmac_f32_16x16x64_bf16 v[4:7], v[156:159], v[64:71], v214\n\t"
    "s_and_b32 s80, s80, 63\n\t"
    "ds_read_b128 v[32:35], v244 offset:64\n\t"
    "v_smfmac_f32_16x16x64_bf16 v[12:15], v[120:123], v[40:47], v209 abid:1\n\t"
    "ds_read_b128 v[36:39], v244 offset:192\n\t"
    "v_smfmac_f32_16x16x64_bf16 v[12:15], v[140:143], v[48:55], v212\n\t"
    "ds_read_b128 v[56:59], v217 offset:13824\n\t"
    "v_smfmac_f32_16x16x64_bf16 v[12:15], v[160:163], v[64:71], v214 abid:1\n\t"
    "ds_read_b128 v[80:83], v245 offset:7168\n\t"
    "v_add_u32_sdwa v89, v248, v246 dst_sel:DWORD dst_unused:UNUSED_PAD src0_sel:DWORD src1_sel:WORD_1\n\t"
    "v_smfmac_f32_16x16x64_bf16 v[20:23], v[124:127], v[40:47], v210\n\t"
    "ds_read_u16 v91, v91 offset:13824\n\t"
    "v_add_u32_sdwa v88, v248, v246 dst_sel:DWORD dst_unused:UNUSED_PAD src0_sel:DWORD src1_sel:WORD_0\n\t"
    "v_smfmac_f32_16x16x64_bf16 v[20:23], v[144:147], v[48:55], v212 abid:1\n\t"
    "v_add_u32_sdwa v90, v248, v247 dst_sel:DWORD dst_unused:UNUSED_PAD src0_sel:DWORD src1_sel:WORD_0\n\t"
    "v_smfmac_f32_16x16x64_bf16 v[20:23], v[164:167], v[64:71], v215\n\t"
    "ds_read_b128 v[28:31], v244 offset:128\n\t"
    "v_cvt_pk_bf16_f32 v96, v4, v5\n\t"
    "v_smfmac_f32_16x16x64_bf16 v[24:27], v[128:131], v[40:47], v210 abid:1\n\t"
    "ds_read_b128 v[60:63], v236 offset:13824\n\t"
    "v_cvt_pk_bf16_f32 v97, v6, v7\n\t"
    "v_smfmac_f32_16x16x64_bf16 v[24:27], v[148:151], v[48:55], v213\n\t"
    "ds_read_u16 v88, v88 offset:13824\n\t"
    "v_smfmac_f32_16x16x64_bf16 v[24:27], v[168:171], v[64:71], v215 abid:1\n\t"
    "s_waitcnt lgkmcnt(4)\n\t"
    "v_cvt_pk_bf16_f32 v99, v14, v15\n\t"
    "v_mfma_f32_16x16x32_bf16 v[24:27], v[80:83], v[92:95], v[24:27]\n\t"
    "s_lshl_b32 s80, s80, 2\n\t"
    "ds_read_b32 v92, v218 offset:13828\n\t"
    "ds_read_b32 v94, v219 offset:13828\n\t"
    "v_smfmac_f32_16x16x64_bf16 v[16:19], v[132:135], v[40:47], v211\n\t"
    "ds_read_b128 v[40:43], v217 offset:13952\n\t"
    "ds_read_b128 v[44:47], v236 offset:13952\n\t"
    "v_smfmac_f32_16x16x64_bf16 v[16:19], v[152:155], v[48:55], v213 abid:1\n\t"
    "s_mov_b32 s77, 0\n\t"
    "ds_read_b128 v[80:83], v245 offset:0\n\t"
    "v_cvt_pk_bf16_f32 v101, v22, v23\n\t"
    "v_smfmac_f32_16x16x64_bf16 v[16:19], v[172:175], v[64:71], v216\n\t"
    "s_waitcnt lgkmcnt(6)\n\t"
    "s_mov_b32 s76, 2388482\n\t"
    "ds_read_b32 v93, v72 offset:13828\n\t"
    "ds_read_b32 v95, v73 offset:13828\n\t"
    "v_cvt_pk_bf16_f32 v100, v20, v21\n\t"
    "v_smfmac_f32_16x16x64_bf16 v[8:11], a[0:3], v[56:63], v176\n\t"
    "ds_read_b128 v[48:51], v217 offset:14080\n\t"
    "v_cvt_pk_bf16_f32 v102, v24, v25\n\t"
    "v_smfmac_f32_16x16x64_bf16 v[32:35], a[4:7], v[56:63], v176 abid:1\n\t"
    "ds_read_b128 v[52:55], v236 offset:14080\n\t"
    "v_smfmac_f32_16x16x64_bf16 v[28:31], a[8:11], v[56:63], v177\n\t"
    "ds_read_u16 v89, v89 offset:13824\n\t"
    "v_cvt_pk_bf16_f32 v98, v12, v13\n\t"
    "v_smfmac_f32_16x16x64_bf16 v[36:39], a[12:15], v[56:63], v177 abid:1\n\t"
    "s_waitcnt lgkmcnt(6)\n\t"
    "ds_read_u16 v90, v90 offset:13824\n\t"
    "v_cndmask_b32_dpp v25, v101, v97, vcc quad_perm:[0,0,2,2] row_mask:0xf bank_mask:0xf\n\t"
    "v_smfmac_f32_16x16x64_bf16 v[8:11], a[16:19], v[40:47], v178\n\t"
    "ds_read_b128 v[60:63], v236 offset:14336\n\t"
    "v_cvt_pk_bf16_f32 v103, v26, v27\n\t"
    "v_smfmac_f32_16x16x64_bf16 v[32:35], a[20:23], v[40:47], v178 abid:1\n\t"
    "s_waitcnt lgkmcnt(5)\n\t"
    "ds_read_b128 v[64:67], v217 offset:14208\n\t"
    "v_cndmask_b32_dpp v24, v100, v96, vcc quad_perm:[0,0,2,2] row_mask:0xf bank_mask:0xf\n\t"
    "v_mfma_f32_16x16x32_bf16 v[8:11], v[80:83], v[92:95], v[8:11]\n\t"
    "ds_read_b128 v[68:71], v236 offset:14208\n\t"
    "v_cndmask_b32_dpp v26, v102, v98, vcc quad_perm:[0,0,2,2] row_mask:0xf bank_mask:0xf\n\t"
    "v_smfmac_f32_16x16x64_bf16 v[28:31], a[24:27], v[40:47], v179\n\t"
    "ds_read_b32 v92, v220 offset:13828\n\t"
    "v_cndmask_b32_dpp v27, v103, v99, vcc quad_perm:[0,0,2,2] row_mask:0xf bank_mask:0xf\n\t"
    "v_smfmac_f32_16x16x64_bf16 v[36:39], a[28:31], v[40:47], v179 abid:1\n\t"
    "s_waitcnt lgkmcnt(6)\n\t"
    "s_not_b64 vcc, s[18:19]\n\t"
    "buffer_store_dwordx4 v[24:27], v241, s[44:47], s67 offen offset:128 sc1\n\t"
    "ds_read_b32 v93, v74 offset:13828\n\t"
    "v_cndmask_b32_dpp v101, v97, v101, vcc quad_perm:[1,1,3,3] row_mask:0xf bank_mask:0xf\n\t"
    "v_smfmac_f32_16x16x64_bf16 v[8:11], a[32:35], v[48:55], v180\n\t"
    "ds_read_b128 v[80:83], v245 offset:1024\n\t"
    "v_cndmask_b32_dpp v102, v98, v102, vcc quad_perm:[1,1,3,3] row_mask:0xf bank_mask:0xf\n\t"
    "v_smfmac_f32_16x16x64_bf16 v[32:35], a[36:39], v[48:55], v180 abid:1\n\t"
    "s_waitcnt lgkmcnt(6)\n\t"
    "ds_read_b32 v94, v221 offset:13828\n\t"
    "ds_read_b32 v95, v75 offset:13828\n\t"
    "s_set_gpr_idx_on s80, gpr_idx(SRC2,DST)\n\t"
    "v_mfma_f32_16x16x32_bf16 v[0:3], v[250:253], v[88:91], v[0:3]\n\t"
    "s_set_gpr_idx_off\n\t"
    "v_smfmac_f32_16x16x64_bf16 v[28:31], a[40:43], v[48:55], v181\n\t"
    "ds_read_b128 v[40:43], v217 offset:14464\n\t"
    "v_cndmask_b32_dpp v100, v96, v100, vcc quad_perm:[1,1,3,3] row_mask:0xf bank_mask:0xf\n\t"
    "v_smfmac_f32_16x16x64_bf16 v[36:39], a[44:47], v[48:55], v181 abid:1\n\t"
    "s_waitcnt lgkmcnt(6)\n\t"
    "ds_read_b128 v[56:59], v217 offset:14336\n\t"
    "v_cndmask_b32_dpp v103, v99, v103, vcc quad_perm:[1,1,3,3] row_mask:0xf bank_mask:0xf\n\t"
    "v_smfmac_f32_16x16x64_bf16 v[8:11], a[48:51], v[64:71], v182\n\t"
    "buffer_store_dwordx4 v[100:103], v241, s[44:47], s67 offen offset:1280 sc1\n\t"
    "ds_read_b128 v[44:47], v236 offset:14464\n\t"
    "ds_read_b128 v[48:51], v217 offset:14592\n\t"
    "v_cvt_pk_bf16_f32 v16, v16, v17\n\t"
    "v_smfmac_f32_16x16x64_bf16 v[32:35], a[52:55], v[64:71], v182 abid:1\n\t"
    "s_lshr_b64 s[80:81], s[76:77], s70\n\t"
    "v_smfmac_f32_16x16x64_bf16 v[28:31], a[56:59], v[64:71], v183\n\t"
    "s_waitcnt lgkmcnt(4)\n\t"
    "ds_read_b128 v[52:55], v236 offset:14592\n\t"
    "v_mfma_f32_16x16x32_bf16 v[32:35], v[80:83], v[92:95], v[32:35]\n\t"
    "ds_read_b32 v95, v77 offset:13828\n\t"
    "v_cvt_pk_bf16_f32 v17, v18, v19\n\t"
    "v_smfmac_f32_16x16x64_bf16 v[36:39], a[60:63], v[64:71], v183 abid:1\n\t"
    "s_waitcnt lgkmcnt(4)\n\t"
    "s_and_b32 s80, s80, 63\n\t"
    "ds_write_b64 v242, v[16:17] offset:512\n\t"
    "v_smfmac_f32_16x16x64_bf16 v[8:11], a[64:67], v[56:63], v184\n\t"
    "ds_read_b32 v92, v222 offset:13828\n\t"
    "v_smfmac_f32_16x16x64_bf16 v[32:35], a[68:71], v[56:63], v184 abid:1\n\t"
    "ds_read_b32 v93, v76 offset:13828\n\t"
    "ds_read_b32 v94, v223 offset:13828\n\t"
    "v_smfmac_f32_16x16x64_bf16 v[28:31], a[72:75], v[56:63], v185\n\t"
    "s_lshl_b32 s80, s80, 2\n\t"
    "ds_read_b128 v[80:83], v245 offset:2048\n\t"
    "s_set_gpr_idx_on s80, gpr_idx(SRC2,DST)\n\t"
    "v_mfma_f32_16x16x32_bf16 v[0:3], v[252:255], v[88:91], v[0:3]\n\t"
    "s_set_gpr_idx_off\n\t"
    "s_cmp_lg_u32 s22, 0\n\t"
    "s_cbranch_scc1 .Lsl21_%=\n\t"
    ".Lslr21_%=:\n\t"
    "v_smfmac_f32_16x16x64_bf16 v[36:39], a[76:79], v[56:63], v185 abid:1\n\t"
    "s_waitcnt lgkmcnt(8)\n\t"
    "ds_read_b128 v[68:71], v236 offset:14720\n\t"
    "v_smfmac_f32_16x16x64_bf16 v[8:11], a[80:83], v[40:47], v186\n\t"
    "ds_read_b128 v[64:67], v217 offset:14720\n\t"
    "v_smfmac_f32_16x16x64_bf16 v[32:35], a[84:87], v[40:47], v186 abid:1\n\t"
    "ds_read_b32 v90, v225 offset:13828\n\t"
    "v_smfmac_f32_16x16x64_bf16 v[28:31], a[88:91], v[40:47], v187\n\t"
    "ds_read_b128 v[56:59], v217 offset:14848\n\t"
    "v_smfmac_f32_16x16x64_bf16 v[36:39], a[92:95], v[40:47], v187 abid:1\n\t"
    "s_waitcnt lgkmcnt(4)\n\t"
    "ds_read_b32 v89, v78 offset:13828\n\t"
    "v_mfma_f32_16x16x32_bf16 v[28:31], v[80:83], v[92:95], v[28:31]\n\t"
    "s_barrier\n\t"
    "ds_read_b128 v[96:99], v243 offset:2560\n\t"
    "v_smfmac_f32_16x16x64_bf16 v[8:11], a[96:99], v[48:55], v188\n\t"
    "ds_read_b128 v[60:63], v236 offset:14848\n\t"
    "v_smfmac_f32_16x16x64_bf16 v[32:35], a[100:103], v[48:55], v188 abid:1\n\t"
    "ds_read_b32 v91, v79 offset:13828\n\t"
    "v_smfmac_f32_16x16x64_bf16 v[28:31], a[104:107], v[48:55], v189\n\t"
    "ds_read_b128 v[80:83], v245 offset:3072\n\t"
    "ds_read_b32 v88, v224 offset:13828\n\t"
    "v_smfmac_f32_16x16x64_bf16 v[36:39], a[108:111], v[48:55], v189 abid:1\n\t"
    "s_waitcnt lgkmcnt(8)\n\t"
    "v_smfmac_f32_16x16x64_bf16 v[8:11], a[112:115], v[64:71], v190\n\t"
    "v_smfmac_f32_16x16x64_bf16 v[32:35], a[116:119], v[64:71], v190 abid:1\n\t"
    "v_smfmac_f32_16x16x64_bf16 v[28:31], a[120:123], v[64:71], v191\n\t"
    "s_waitcnt lgkmcnt(4)\n\t"
    "s_mov_b64 vcc, s[18:19]\n\t"
    "s_mov_b32 exec_hi, 0\n\t"
    "buffer_store_dwordx4 v[96:99], v240, s[44:47], s65 offen offset:0 sc1\n\t"
    "s_mov_b32 exec_hi, -1\n\t"
    "v_smfmac_f32_16x16x64_bf16 v[36:39], a[124:127], v[64:71], v191 abid:1\n\t"
    "s_waitcnt lgkmcnt(3)\n\t"
    "v_smfmac_f32_16x16x64_bf16 v[8:11], a[128:131], v[56:63], v192\n\t"
    "s_waitcnt lgkmcnt(0)\n\t"
    "v_mfma_f32_16x16x32_bf16 v[36:39], v[80:83], v[88:91], v[36:39]\n\t"
    "v_smfmac_f32_16x16x64_bf16 v[32:35], a[132:135], v[56:63], v192 abid:1\n\t"
    "v_smfmac_f32_16x16x64_bf16 v[28:31], a[136:139], v[56:63], v193\n\t"
    "v_smfmac_f32_16x16x64_bf16 v[36:39], a[140:143], v[56:63], v193 abid:1\n\t"
    "s_nop 3\n\t"
    "s_bitcmp1_b32 s54, 15\n\t"
    "s_mov_b32 s66, 70778880\n\t"
    "s_cmov_b32 s66, 70852608\n\t"
    "v_cvt_pk_bf16_f32 v100, v8, v9\n\t"
    "v_cvt_pk_bf16_f32 v101, v10, v11\n\t"
    "v_cvt_pk_bf16_f32 v102, v32, v33\n\t"
    ".Lpad3_%=:\n\t"
    "s_cbranch_execz .Lpad4_%=\n\t"
    "v_cvt_pk_bf16_f32 v103, v34, v35\n\t"
    "s_nop 3\n\t"
    "v_cvt_pk_bf16_f32 v96, v28, v29\n\t"
    "v_cvt_pk_bf16_f32 v97, v30, v31\n\t"
    "s_nop 3\n\t"
    "v_cvt_pk_bf16_f32 v98, v36, v37\n\t"
    "v_cvt_pk_bf16_f32 v99, v38, v39\n\t"
    "v_cndmask_b32_dpp v36, v96, v100, vcc quad_perm:[0,0,2,2] row_mask:0xf bank_mask:0xf\n\t"
    "v_cndmask_b32_dpp v37, v97, v101, vcc quad_perm:[0,0,2,2] row_mask:0xf bank_mask:0xf\n\t"
    "v_cndmask_b32_dpp v38, v98, v102, vcc quad_perm:[0,0,2,2] row_mask:0xf bank_mask:0xf\n\t"
    "v_cndmask_b32_dpp v39, v99, v103, vcc quad_perm:[0,0,2,2] row_mask:0xf bank_mask:0xf\n\t"
    "s_not_b64 vcc, s[18:19]\n\t"
    "buffer_store_dwordx4 v[36:39], v241, s[44:47], s66 offen offset:0 sc1\n\t"
    "s_nop 1\n\t"
    "v_cndmask_b32_dpp v96, v100, v96, vcc quad_perm:[1,1,3,3] row_mask:0xf bank_mask:0xf\n\t"
    "v_cndmask_b32_dpp v97, v101, v97, vcc quad_perm:[1,1,3,3] row_mask:0xf bank_mask:0xf\n\t"
    "v_cndmask_b32_dpp v98, v102, v98, vcc quad_perm:[1,1,3,3] row_mask:0xf bank_mask:0xf\n\t"
    "v_cndmask_b32_dpp v99, v103, v99, vcc quad_perm:[1,1,3,3] row_mask:0xf bank_mask:0xf\n\t"
    "buffer_store_dwordx4 v[96:99], v241, s[44:47], s66 offen offset:1152 sc1\n\t"
    "s_nop 1\n\t"
    "s_barrier\n\t"
    ".Lpad4_%=:\n\t"
    "s_cbranch_execz .Lwarmr_%=\n\t"
    "spmm_code_end:\n\t"
    : "+{v0}"(tid0)
    : "{s[34:35]}"(ka), "{s62}"(blockIdx.x), "s"(lds0)
    : "v1", "v2", "v3", "v4", "v5", "v6", "v7", "v8", "v9", "v10", "v11", "v12", "v13", "v14", "v15", "v16",
      "v17", "v18", "v19", "v20", "v21", "v22", "v23", "v24", "v25", "v26", "v27", "v28", "v29", "v30", "v31", "v32",
      "v33", "v34", "v35", "v36", "v37", "v38", "v39", "v40", "v41", "v42", "v43", "v44", "v45", "v46", "v47", "v48",
      "v49", "v50", "v51", "v52", "v53", "v54", "v55", "v56", "v57", "v58", "v59", "v60", "v61", "v62", "v63", "v64",
      "v65", "v66", "v67", "v68", "v69", "v70", "v71", "v72", "v73", "v74", "v75", "v76", "v77", "v78", "v79", "v80",
      "v81", "v82", "v83", "v84", "v85", "v86", "v87", "v88", "v89", "v90", "v91", "v92", "v93", "v94", "v95", "v96",
      "v97", "v98", "v99", "v100", "v101", "v102", "v103", "v104", "v105", "v106", "v107", "v108", "v109", "v110", "v111", "v112",
      "v113", "v114", "v115", "v116", "v117", "v118", "v119", "v120", "v121", "v122", "v123", "v124", "v125", "v126", "v127", "v128",
      "v129", "v130", "v131", "v132", "v133", "v134", "v135", "v136", "v137", "v138", "v139", "v140", "v141", "v142", "v143", "v144",
      "v145", "v146", "v147", "v148", "v149", "v150", "v151", "v152", "v153", "v154", "v155", "v156", "v157", "v158", "v159", "v160",
      "v161", "v162", "v163", "v164", "v165", "v166", "v167", "v168", "v169", "v170", "v171", "v172", "v173", "v174", "v175", "v176",
      "v177", "v178", "v179", "v180", "v181", "v182", "v183", "v184", "v185", "v186", "v187", "v188", "v189", "v190", "v191", "v192",
      "v193", "v194", "v195", "v196", "v197", "v198", "v199", "v200", "v201", "v202", "v203", "v204", "v205", "v206", "v207", "v208",
      "v209", "v210", "v211", "v212", "v213", "v214", "v215", "v216", "v217", "v218", "v219", "v220", "v221", "v222", "v223", "v224",
      "v225", "v226", "v227", "v228", "v229", "v230", "v231", "v232", "v233", "v234", "v235", "v236", "v237", "v238", "v239", "v240",
      "v241", "v242", "v243", "v244", "v245", "v246", "v247", "v248", "v249", "v250", "v251", "v252", "v253", "v254", "v255", "a0",
      "a1", "a2", "a3", "a4", "a5", "a6", "a7", "a8", "a9", "a10", "a11", "a12", "a13", "a14", "a15", "a16",
      "a17", "a18", "a19", "a20", "a21", "a22", "a23", "a24", "a25", "a26", "a27", "a28", "a29", "a30", "a31", "a32",
      "a33", "a34", "a35", "a36", "a37", "a38", "a39", "a40", "a41", "a42", "a43", "a44", "a45", "a46", "a47", "a48",
      "a49", "a50", "a51", "a52", "a53", "a54", "a55", "a56", "a57", "a58", "a59", "a60", "a61", "a62", "a63", "a64",
      "a65", "a66", "a67", "a68", "a69", "a70", "a71", "a72", "a73", "a74", "a75", "a76", "a77", "a78", "a79", "a80",
      "a81", "a82", "a83", "a84", "a85", "a86", "a87", "a88", "a89", "a90", "a91", "a92", "a93", "a94", "a95", "a96",
      "a97", "a98", "a99", "a100", "a101", "a102", "a103", "a104", "a105", "a106", "a107", "a108", "a109", "a110", "a111", "a112",
      "a113", "a114", "a115", "a116", "a117", "a118", "a119", "a120", "a121", "a122", "a123", "a124", "a125", "a126", "a127", "a128",
      "a129", "a130", "a131", "a132", "a133", "a134", "a135", "a136", "a137", "a138", "a139", "a140", "a141", "a142", "a143", "a144",
      "a145", "a146", "a147", "a148", "a149", "a150", "a151", "a152", "a153", "a154", "a155", "a156", "a157", "a158", "a159", "a160",
      "a161", "a162", "a163", "a164", "a165", "a166", "a167", "a168", "a169", "a170", "a171", "a172", "a173", "a174", "a175", "a176",
      "a177", "a178", "a179", "a180", "a181", "a182", "a183", "a184", "a185", "a186", "a187", "a188", "a189", "a190", "a191", "a192",
      "a193", "a194", "a195", "a196", "a197", "a198", "a199", "a200", "a201", "a202", "a203", "a204", "a205", "a206", "a207", "a208",
      "a209", "a210", "a211", "a212", "a213", "a214", "a215", "a216", "a217", "a218", "a219", "a220", "a221", "a222", "a223", "a224",
      "a225", "a226", "a227", "a228", "a229", "a230", "a231", "a232", "a233", "a234", "a235", "a236", "a237", "a238", "a239", "a240",
      "a241", "a242", "a243", "a244", "a245", "a246", "a247", "a248", "a249", "a250", "a251", "a252", "a253", "a254", "a255", "s4",
      "s5", "s6", "s7", "s8", "s9", "s10", "s11", "s12", "s13", "s14", "s15", "s16", "s17", "s18", "s19", "s20",
      "s21", "s22", "s23", "s24", "s25", "s26", "s27", "s28", "s29", "s30", "s31", "s36", "s37", "s38", "s39", "s40",
      "s41", "s42", "s43", "s44", "s45", "s46", "s47", "s48", "s49", "s50", "s51", "s52", "s53", "s54", "s55", "s56",
      "s57", "s58", "s59", "s60", "s61", "s63", "s64", "s65", "s66", "s67", "s68", "s69", "s70", "s71", "s72", "s73",
      "s74", "s75", "s76", "s77", "s78", "s79", "s80", "s81", "s82", "s83", "s84", "s85", "s86", "s87", "s88", "s89",
      "s90", "s91", "s92", "s93", "s94", "s95", "s96", "s97", "s98", "s99", "vcc", "scc", "memory");
}

}

extern "C" void kernel_launch(void* const* d_in, const int* in_sizes, int n_in, void* d_out, int out_size,
                              void* d_ws, size_t ws_size, hipStream_t stream) {
  const uint16_t* x = (const uint16_t*)d_in[0];
  const uint16_t* w = (const uint16_t*)d_in[1];
  const uint16_t* b = (const uint16_t*)d_in[2];
  if (in_sizes[0] != NBLK * NTILE * TROWS * DM) return;
  spmm_main<<<NBLK, 256, 0, stream>>>(x, w, b, (uint16_t*)d_out, (char*)d_ws);
}
```

```cpp
#include <hip/hip_runtime.h>
#include <hip/hip_bf16.h>
#include <stdint.h>

namespace {
constexpr int DM    = 576;
constexpr int TROWS = 16;
constexpr int NBLK  = 256;
constexpr int NTILE = 16;
constexpr int LDS_END = 163776;

__global__ __launch_bounds__(256, 1) void spmm_main(const uint16_t* x, const uint16_t* w,
                                                    const uint16_t* b, uint16_t* out,
                                                    char* ws) {
  __shared__ __attribute__((aligned(16))) char smem[LDS_END];
  const unsigned lds0 = (unsigned)(size_t)(__attribute__((address_space(3))) char*)smem;
  int tid0 = threadIdx.x;
  const unsigned long long ka = (unsigned long long)__builtin_amdgcn_kernarg_segment_ptr();
  asm volatile(
    "s_load_dwordx8 s[8:15], s[34:35], 0\n\t"
    "s_load_dwordx2 s[60:61], s[34:35], 32\n\t"
    "s_memrealtime s[24:25]\n\t"
    "spmm_code_begin:\n\t"
    "s_cmp_lt_u32 s62, 224\n\t"
    "s_cbranch_scc1 .Lnocp_%=\n\t"
    "s_getpc_b64 s[40:41]\n\t"
    "s_bfe_u32 s16, s62, 0x10003\n\t"
    "s_lshl_b32 s16, s16, 15\n\t"
    "s_mov_b32 m0, 150512\n\t"
    "v_lshl_add_u32 v1, v0, 7, s16\n\t"
    "v_min_u32 v1, spmm_code_end-spmm_code_begin-24, v1\n\t"
    "global_load_lds_dword v1, s[40:41]\n\t"
    ".Lnocp_%=:\n\t"
    "v_lshrrev_b32 v2, 6, v0\n\t"
    "v_and_b32 v1, 63, v0\n\t"
    "v_readfirstlane_b32 s53, v2\n\t"
    "v_lshlrev_b32 v92, 4, v1\n\t"
    "v_and_b32 v2, 15, v1\n\t"
    "v_lshrrev_b32 v3, 4, v1\n\t"
    "s_bitcmp1_b32 s62, 2\n\t"
    "s_cbranch_scc1 .Lnone_%=\n\t"
    "s_lshr_b32 s16, s62, 3\n\t"
    "s_bfe_u32 s17, s62, 0x10001\n\t"
    "s_lshl1_add_u32 s16, s16, s17\n\t"
    "s_and_b32 s17, s62, 1\n\t"
    "s_lshl_b32 s17, s17, 6\n\t"
    "s_add_u32 s16, s16, s17\n\t"
    "s_and_b32 s17, s62, 4\n\t"
    "s_lshl_b32 s17, s17, 5\n\t"
    "s_add_u32 s63, s16, s17\n\t"
    "s_cmp_lt_u32 s63, 36\n\t"
    "s_cbranch_scc1 .Ls2_%=\n\t"
    "s_cmp_ge_u32 s63, 118\n\t"
    "s_cbranch_scc1 .Lnone_%=\n\t"
    "s_sub_u32 s20, s63, 36\n\t"
    "s_mov_b32 s21, s53\n\t"
    "s_and_b32 s22, s20, 0x7e\n\t"
    "s_and_b32 s23, s20, 1\n\t"
    "v_lshrrev_b32 v4, 5, v1\n\t"
    "v_add_u32 v4, s22, v4\n\t"
    "v_min_u32 v4, 0x50, v4\n\t"
    "v_subrev_u32 v5, 36, v4\n\t"
    "v_mul_u32_u24 v6, 0xcd, v5\n\t"
    "v_lshrrev_b32 v6, 10, v6\n\t"
    "v_mad_i32_i24 v5, v6, -5, v5\n\t"
    "v_add_u32 v5, 4, v5\n\t"
    "v_cmp_gt_u32 vcc, 36, v4\n\t"
    "v_lshrrev_b32 v7, 2, v4\n\t"
    "v_and_b32 v8, 3, v4\n\t"
    "v_cndmask_b32 v6, v6, v7, vcc\n\t"
    "v_cndmask_b32 v5, v5, v8, vcc\n\t"
    "v_and_b32 v7, 4, v5\n\t"
    "v_and_b32 v8, 3, v5\n\t"
    "v_lshlrev_b32 v7, 5, v7\n\t"
    "v_lshl_add_u32 v7, v8, 3, v7\n\t"
    "v_and_b32 v8, 12, v2\n\t"
    "v_mad_u32_u24 v7, v8, 6, v7\n\t"
    "s_lshl_b32 s16, s21, 8\n\t"
    "s_lshl_b32 s17, s21, 5\n\t"
    "s_add_u32 s17, s17, 1024\n\t"
    "v_cmp_gt_u32 vcc, 8, v5\n\t"
    "v_add_u32 v7, s16, v7\n\t"
    "v_mov_b32 v8, s17\n\t"
    "v_cndmask_b32 v7, v8, v7, vcc\n\t"
    "v_lshl_add_u32 v7, v2, 1, v7\n\t"
    "s_mul_i32 s16, s23, 9216\n\t"
    "s_add_u32 s16, s16, 4096\n\t"
    "s_mov_b32 s17, 73728\n\t"
    "v_mad_u32_u24 v7, v6, s17, v7\n\t"
    "v_and_b32 v8, 16, v1\n\t"
    "v_mul_u32_u24 v8, 2304, v8\n\t"
    "v_add3_u32 v7, v7, v8, s16\n\t"
    "v_add_u32 v8, 18432, v7\n\t"
    "s_waitcnt lgkmcnt(0)\n\t"
    "global_load_ushort v16, v7, s[10:11] offset:-4096\n\t"
    "global_load_ushort v17, v7, s[10:11] offset:-2944\n\t"
    "global_load_ushort v18, v7, s[10:11] offset:-1792\n\t"
    "global_load_ushort v19, v7, s[10:11] offset:-640\n\t"
    "global_load_ushort v20, v7, s[10:11] offset:512\n\t"
    "global_load_ushort v21, v7, s[10:11] offset:1664\n\t"
    "global_load_ushort v22, v7, s[10:11] offset:2816\n\t"
    "global_load_ushort v23, v7, s[10:11] offset:3968\n\t"
    "global_load_ushort v24, v8, s[10:11] offset:-4096\n\t"
    "global_load_ushort v25, v8, s[10:11] offset:-2944\n\t"
    "global_load_ushort v26, v8, s[10:11] offset:-1792\n\t"
    "global_load_ushort v27, v8, s[10:11] offset:-640\n\t"
    "global_load_ushort v28, v8, s[10:11] offset:512\n\t"
    "global_load_ushort v29, v8, s[10:11] offset:1664\n\t"
    "global_load_ushort v30, v8, s[10:11] offset:2816\n\t"
    "global_load_ushort v31, v8, s[10:11] offset:3968\n\t"
    "s_branch .Ls1t_%=\n\t"
    ".Ls2_%=:\n\t"
    "s_mul_i32 s21, s63, 57\n\t"
    "s_lshr_b32 s21, s21, 9\n\t"
    "s_mul_i32 s20, s21, 9\n\t"
    "s_sub_u32 s20, s63, s20\n\t"
    "s_cmp_eq_u32 s53, 3\n\t"
    "s_cbranch_scc1 .Ls2w3_%=\n\t"
    "s_lshr_b32 s16, s20, 2\n\t"
    "s_lshl_b32 s16, s16, 7\n\t"
    "s_and_b32 s17, s20, 3\n\t"
    "s_lshl3_add_u32 s16, s17, s16\n\t"
    "s_lshl_b32 s17, s21, 8\n\t"
    "s_add_u32 s16, s16, s17\n\t"
    "s_lshl_b32 s17, s21, 5\n\t"
    "s_add_u32 s17, s17, 1024\n\t"
    "s_cmp_lt_u32 s20, 8\n\t"
    "s_cselect_b32 s16, s16, s17\n\t"
    "s_cselect_b32 s17, 5, 3\n\t"
    "v_lshrrev_b32 v4, 2, v0\n\t"
    "v_and_b32 v5, 3, v0\n\t"
    "v_mul_u32_u24 v7, 4608, v4\n\t"
    "v_lshlrev_b32 v6, s17, v5\n\t"
    "v_add3_u32 v7, v7, v6, s16\n\t"
    "v_add_u32 v8, 221184, v7\n\t"
    "v_add_u32 v9, 442368, v7\n\t"
    "s_waitcnt lgkmcnt(0)\n\t"
    "global_load_dwordx2 v[16:17], v7, s[10:11] offset:0\n\t"
    "global_load_dwordx2 v[18:19], v7, s[10:11] offset:1152\n\t"
    "global_load_dwordx2 v[20:21], v7, s[10:11] offset:2304\n\t"
    "global_load_dwordx2 v[22:23], v7, s[10:11] offset:3456\n\t"
    "global_load_dwordx2 v[24:25], v8, s[10:11] offset:0\n\t"
    "global_load_dwordx2 v[26:27], v8, s[10:11] offset:1152\n\t"
    "global_load_dwordx2 v[28:29], v8, s[10:11] offset:2304\n\t"
    "global_load_dwordx2 v[30:31], v8, s[10:11] offset:3456\n\t"
    "global_load_dwordx2 v[32:33], v9, s[10:11] offset:0\n\t"
    "global_load_dwordx2 v[34:35], v9, s[10:11] offset:1152\n\t"
    "global_load_dwordx2 v[36:37], v9, s[10:11] offset:2304\n\t"
    "global_load_dwordx2 v[38:39], v9, s[10:11] offset:3456\n\t"
    "s_lshl_b32 s59, s53, 3\n\t"
    "s_lshr_b32 s59, 0x64753120, s59\n\t"
    "s_lshl_b32 s64, s59, 4\n\t"
    "s_and_b32 s64, s64, 0x70\n\t"
    "s_and_b32 s59, s59, 0x70\n\t"
    "v_cmp_gt_u32 vcc, 48, v1\n\t"
    "v_xor_b32 v237, s64, v92\n\t"
    "v_xor_b32 v239, s59, v92\n\t"
    "v_cndmask_b32 v238, v239, v237, vcc\n\t"
    "s_and_b32 s58, s62, 7\n\t"
    "s_lshl_b32 s58, s58, 5\n\t"
    "s_lshr_b32 s52, s62, 3\n\t"
    "s_add_u32 s58, s58, s52\n\t"
    "s_mul_i32 s58, s58, 18432\n\t"
    "s_mul_i32 s52, s53, 4608\n\t"
    "s_add_u32 s59, s58, s52\n\t"
    "s_mul_i32 s52, s53, 27648\n\t"
    "s_sub_u32 s42, 75497472, s59\n\t"
    "s_mov_b32 s43, 0x20000\n\t"
    "s_mov_b32 s68, 0\n\t"
    "s_mov_b32 s36, s52\n\t"
    "s_mov_b32 s18, 0x7fff7fff\n\t"
    "s_add_u32 s40, s8, s59\n\t"
    "s_addc_u32 s41, s9, 0\n\t"
    "v_min_u32 v44, 35, v1\n\t"
    "v_and_b32 v45, 3, v44\n\t"
    "v_and_b32 v46, 16, v44\n\t"
    "v_lshlrev_b32 v46, 3, v46\n\t"
    "v_and_b32 v47, 12, v44\n\t"
    "v_lshl_add_u32 v46, v47, 1, v46\n\t"
    "v_lshl_add_u32 v46, v45, 5, v46\n\t"
    "s_lshl_b32 s16, s53, 8\n\t"
    "s_lshl_b32 s17, s53, 5\n\t"
    "s_add_u32 s17, s17, 1024\n\t"
    "v_cmp_gt_u32 vcc, 32, v44\n\t"
    "v_add_u32 v46, s16, v46\n\t"
    "v_lshl_add_u32 v47, v45, 3, s17\n\t"
    "v_cndmask_b32 v198, v47, v46, vcc\n\t"
    "global_load_dwordx2 v[196:197], v198, s[12:13]\n\t"
    "s_add_u32 s16, s60, 917504\n\t"
    "s_addc_u32 s17, s61, 0\n\t"
    "s_load_dwordx2 s[30:31], s[16:17], 0\n\t"
    "s_branch .Ls2t_%=\n\t"
    ".Lnone_%=:\n\t"
    "s_lshl_b32 s59, s53, 3\n\t"
    "s_lshr_b32 s59, 0x64753120, s59\n\t"
    "s_lshl_b32 s64, s59, 4\n\t"
    "s_and_b32 s64, s64, 0x70\n\t"
    "s_and_b32 s59, s59, 0x70\n\t"
    "v_cmp_gt_u32 vcc, 48, v1\n\t"
    "v_xor_b32 v237, s64, v92\n\t"
    "v_xor_b32 v239, s59, v92\n\t"
    "v_cndmask_b32 v238, v239, v237, vcc\n\t"
    "s_and_b32 s58, s62, 7\n\t"
    "s_lshl_b32 s58, s58, 5\n\t"
    "s_lshr_b32 s52, s62, 3\n\t"
    "s_add_u32 s58, s58, s52\n\t"
    "s_mul_i32 s58, s58, 18432\n\t"
    "s_mul_i32 s52, s53, 4608\n\t"
    "s_add_u32 s59, s58, s52\n\t"
    "s_mul_i32 s52, s53, 27648\n\t"
    "s_sub_u32 s42, 75497472, s59\n\t"
    "s_mov_b32 s43, 0x20000\n\t"
    "s_mov_b32 s68, 0\n\t"
    "s_mov_b32 s36, s52\n\t"
    "s_mov_b32 m0, s52\n\t"
    "s_waitcnt lgkmcnt(0)\n\t"
    "s_add_u32 s40, s8, s59\n\t"
    "s_addc_u32 s41, s9, 0\n\t"
    "s_mov_b32 exec_hi, 0\n\t"
    "buffer_load_dwordx4 v237, s[40:43], s68 offen offset:0 nt lds\n\t"
    "s_mov_b32 exec_hi, -1\n\t"
    "buffer_load_dwordx4 v237, s[40:43], s68 offen offset:512 nt lds\n\t"
    "buffer_load_dwordx4 v238, s[40:43], s68 offen offset:1536 nt lds\n\t"
    "buffer_load_dwordx4 v239, s[40:43], s68 offen offset:2560 nt lds\n\t"
    "buffer_load_dwordx4 v239, s[40:43], s68 offen offset:3584 nt lds\n\t"
    "s_add_u32 s36, s36, 4608\n\t"
    "s_add_u32 s68, s68, 4718592\n\t"
    "v_min_u32 v44, 35, v1\n\t"
    "v_and_b32 v45, 3, v44\n\t"
    "v_and_b32 v46, 16, v44\n\t"
    "v_lshlrev_b32 v46, 3, v46\n\t"
    "v_and_b32 v47, 12, v44\n\t"
    "v_lshl_add_u32 v46, v47, 1, v46\n\t"
    "v_lshl_add_u32 v46, v45, 5, v46\n\t"
    "s_lshl_b32 s16, s53, 8\n\t"
    "s_lshl_b32 s17, s53, 5\n\t"
    "s_add_u32 s17, s17, 1024\n\t"
    "v_cmp_gt_u32 vcc, 32, v44\n\t"
    "v_add_u32 v46, s16, v46\n\t"
    "v_lshl_add_u32 v47, v45, 3, s17\n\t"
    "v_cndmask_b32 v198, v47, v46, vcc\n\t"
    "global_load_dwordx2 v[196:197], v198, s[12:13]\n\t"
    "s_add_u32 s16, s60, 917504\n\t"
    "s_addc_u32 s17, s61, 0\n\t"
    "s_load_dwordx2 s[30:31], s[16:17], 0\n\t"
    "s_bitcmp1_b32 s62, 7\n\t"
    "s_cselect_b64 s[72:73], s[14:15], s[8:9]\n\t"
    "s_bfe_u32 s70, s62, 0x40003\n\t"
    "s_bfe_u32 s71, s62, 0x10001\n\t"
    "s_lshl1_add_u32 s70, s70, s71\n\t"
    "s_mul_i32 s75, s70, 28\n\t"
    "s_lshr_b32 s75, s75, 6\n\t"
    "s_add_u32 s71, s71, 10\n\t"
    "s_cmp_lt_u32 s70, 23\n\t"
    "s_cselect_b32 s71, s75, s71\n\t"
    "s_lshl_b32 s71, s71, 5\n\t"
    "s_add_u32 s71, s71, 208\n\t"
    "s_lshl2_add_u32 s75, s62, s53\n\t"
    "s_and_b32 s75, s75, 15\n\t"
    "s_add_u32 s71, s71, s75\n\t"
    "s_lshl_b32 s71, s71, 17\n\t"
    "s_mul_i32 s75, s24, 0x9e3779b1\n\t"
    "s_lshr_b32 s75, s75, 12\n\t"
    "s_lshl_b32 s70, s70, 7\n\t"
    "s_xor_b32 s75, s75, s70\n\t"
    "s_and_b32 s75, s75, 0x1df80\n\t"
    "s_add_u32 s71, s71, s75\n\t"
    "s_lshl_b32 s75, s53, 13\n\t"
    "s_xor_b32 s75, s71, s75\n\t"
    "s_xor_b32 s70, s75, 0x1000\n\t"
    "s_branch .Lnone2_%=\n\t"
    ".Ls1t_%=:\n\t"
    "s_lshl_b32 s59, s53, 3\n\t"
    "s_lshr_b32 s59, 0x64753120, s59\n\t"
    "s_lshl_b32 s64, s59, 4\n\t"
    "s_and_b32 s64, s64, 0x70\n\t"
    "s_and_b32 s59, s59, 0x70\n\t"
    "v_cmp_gt_u32 vcc, 48, v1\n\t"
    "v_xor_b32 v237, s64, v92\n\t"
    "v_xor_b32 v239, s59, v92\n\t"
    "v_cndmask_b32 v238, v239, v237, vcc\n\t"
    "s_and_b32 s58, s62, 7\n\t"
    "s_lshl_b32 s58, s58, 5\n\t"
    "s_lshr_b32 s52, s62, 3\n\t"
    "s_add_u32 s58, s58, s52\n\t"
    "s_mul_i32 s58, s58, 18432\n\t"
    "s_mul_i32 s52, s53, 4608\n\t"
    "s_add_u32 s59, s58, s52\n\t"
    "s_mul_i32 s52, s53, 27648\n\t"
    "s_sub_u32 s42, 75497472, s59\n\t"
    "s_mov_b32 s43, 0x20000\n\t"
    "s_mov_b32 s68, 0\n\t"
    "s_mov_b32 s36, s52\n\t"
    "s_mov_b32 s18, 0x7fff7fff\n\t"
    "s_add_u32 s40, s8, s59\n\t"
    "s_addc_u32 s41, s9, 0\n\t"
    "v_min_u32 v44, 35, v1\n\t"
    "v_and_b32 v45, 3, v44\n\t"
    "v_and_b32 v46, 16, v44\n\t"
    "v_lshlrev_b32 v46, 3, v46\n\t"
    "v_and_b32 v47, 12, v44\n\t"
    "v_lshl_add_u32 v46, v47, 1, v46\n\t"
    "v_lshl_add_u32 v46, v45, 5, v46\n\t"
    "s_lshl_b32 s16, s53, 8\n\t"
    "s_lshl_b32 s17, s53, 5\n\t"
    "s_add_u32 s17, s17, 1024\n\t"
    "v_cmp_gt_u32 vcc, 32, v44\n\t"
    "v_add_u32 v46, s16, v46\n\t"
    "v_lshl_add_u32 v47, v45, 3, s17\n\t"
    "v_cndmask_b32 v198, v47, v46, vcc\n\t"
    "global_load_dwordx2 v[196:197], v198, s[12:13]\n\t"
    "s_add_u32 s16, s60, 917504\n\t"
    "s_addc_u32 s17, s61, 0\n\t"
    "s_load_dwordx2 s[30:31], s[16:17], 0\n\t"
    "s_barrier\n\t"
    "s_mov_b32 s37, 1\n\t"
    "s_call_b64 s[26:27], .Lxt_%=\n\t"
    "s_call_b64 s[28:29], .Lconst_%=\n\t"
    "s_mul_i32 s16, s21, 192512\n\t"
    "s_lshl_b32 s17, s23, 9\n\t"
    "s_add_u32 s16, s16, s17\n\t"
    "v_and_b32 v9, 0x1f0, v92\n\t"
    "v_add_u32 v9, s16, v9\n\t"
    "v_lshl_add_u32 v10, v4, 10, v9\n\t"
    "s_lshr_b32 s17, s20, 3\n\t"
    "s_lshl_b32 s17, s17, 10\n\t"
    "s_and_b32 s16, s20, 6\n\t"
    "s_lshl1_add_u32 s17, s16, s17\n\t"
    "s_add_u32 s17, s17, 82944\n\t"
    "v_add_u32 v9, s17, v9\n\t"
    "s_mov_b32 s16, 1289021588\n\t"
    "s_mov_b32 s17, 1145325716\n\t"
    "s_mov_b32 s19, 0x20002000\n\t"
    "s_movk_i32 s22, 0x202\n\t"
    "v_mov_b32 v11, 0x01000100\n\t"
    "s_mul_i32 s23, s21, 82\n\t"
    "s_add_u32 s23, s23, s20\n\t"
    "s_add_u32 s23, s23, 256\n\t"
    "s_waitcnt vmcnt(5)\n\t"
    "s_memrealtime s[4:5]\n\t"
    "v_lshl_or_b32 v16, v17, 16, v16\n\t"
    "v_lshl_or_b32 v20, v21, 16, v20\n\t"
    "v_lshl_or_b32 v24, v25, 16, v24\n\t"
    "v_lshl_or_b32 v28, v29, 16, v28\n\t"
    "v_lshl_or_b32 v18, v19, 16, v18\n\t"
    "v_lshl_or_b32 v22, v23, 16, v22\n\t"
    "v_lshl_or_b32 v26, v27, 16, v26\n\t"
    "v_lshl_or_b32 v30, v31, 16, v30\n\t"
    "v_and_b32 v17, s18, v16\n\t"
    "v_and_b32 v21, s18, v20\n\t"
    "v_and_b32 v25, s18, v24\n\t"
    "v_and_b32 v29, s18, v28\n\t"
    "v_and_b32 v19, s18, v18\n\t"
    "v_and_b32 v23, s18, v22\n\t"
    "v_and_b32 v27, s18, v26\n\t"
    "v_and_b32 v31, s18, v30\n\t"
    "v_pk_max_u16 v40, v17, v19\n\t"
    "v_pk_max_u16 v48, v21, v23\n\t"
    "v_pk_max_u16 v56, v25, v27\n\t"
    "v_pk_max_u16 v64, v29, v31\n\t"
    "v_pk_min_u16 v41, v17, v19\n\t"
    "v_pk_min_u16 v49, v21, v23\n\t"
    "v_pk_min_u16 v57, v25, v27\n\t"
    "v_pk_min_u16 v65, v29, v31\n\t"
    "v_pk_min_u16 v40, v40, v40 op_sel:[0,1] op_sel_hi:[1,0]\n\t"
    "v_pk_min_u16 v48, v48, v48 op_sel:[0,1] op_sel_hi:[1,0]\n\t"
    "v_pk_min_u16 v56, v56, v56 op_sel:[0,1] op_sel_hi:[1,0]\n\t"
    "v_pk_min_u16 v64, v64, v64 op_sel:[0,1] op_sel_hi:[1,0]\n\t"
    "v_pk_max_u16 v41, v41, v41 op_sel:[0,1] op_sel_hi:[1,0]\n\t"
    "v_pk_max_u16 v49, v49, v49 op_sel:[0,1] op_sel_hi:[1,0]\n\t"
    "v_pk_max_u16 v57, v57, v57 op_sel:[0,1] op_sel_hi:[1,0]\n\t"
    "v_pk_max_u16 v65, v65, v65 op_sel:[0,1] op_sel_hi:[1,0]\n\t"
    "v_pk_max_u16 v40, v40, v41\n\t"
    "v_pk_max_u16 v48, v48, v49\n\t"
    "v_pk_max_u16 v56, v56, v57\n\t"
    "v_pk_max_u16 v64, v64, v65\n\t"
    "v_pk_sub_i16 v17, v17, v40\n\t"
    "v_pk_sub_i16 v21, v21, v48\n\t"
    "v_pk_sub_i16 v25, v25, v56\n\t"
    "v_pk_sub_i16 v29, v29, v64\n\t"
    "v_pk_sub_i16 v19, v19, v40\n\t"
    "v_pk_sub_i16 v23, v23, v48\n\t"
    "v_pk_sub_i16 v27, v27, v56\n\t"
    "v_pk_sub_i16 v31, v31, v64\n\t"
    "v_lshrrev_b32 v17, 2, v17\n\t"
    "v_lshrrev_b32 v21, 2, v21\n\t"
    "v_lshrrev_b32 v25, 2, v25\n\t"
    "v_lshrrev_b32 v29, 2, v29\n\t"
    "v_and_b32 v19, 0x80008000, v19\n\t"
    "v_and_b32 v23, 0x80008000, v23\n\t"
    "v_and_b32 v27, 0x80008000, v27\n\t"
    "v_and_b32 v31, 0x80008000, v31\n\t"
    "v_and_or_b32 v17, v17, s19, v19\n\t"
    "v_and_or_b32 v21, v21, s19, v23\n\t"
    "v_and_or_b32 v25, v25, s19, v27\n\t"
    "v_and_or_b32 v29, v29, s19, v31\n\t"
    "v_lshl_or_b32 v17, v17, 15, v17\n\t"
    "v_lshl_or_b32 v21, v21, 15, v21\n\t"
    "v_lshl_or_b32 v25, v25, 15, v25\n\t"
    "v_lshl_or_b32 v29, v29, 15, v29\n\t"
    "v_lshrrev_b32 v17, 26, v17\n\t"
    "v_lshrrev_b32 v21, 26, v21\n\t"
    "v_lshrrev_b32 v25, 26, v25\n\t"
    "v_lshrrev_b32 v29, 26, v29\n\t"
    "v_lshrrev_b64 v[42:43], v17, s[16:17]\n\t"
    "v_lshrrev_b64 v[50:51], v21, s[16:17]\n\t"
    "v_lshrrev_b64 v[58:59], v25, s[16:17]\n\t"
    "v_lshrrev_b64 v[66:67], v29, s[16:17]\n\t"
    "v_and_b32 v42, 15, v42\n\t"
    "v_and_b32 v50, 15, v50\n\t"
    "v_and_b32 v58, 15, v58\n\t"
    "v_and_b32 v66, 15, v66\n\t"
    "v_lshl_or_b32 v17, v42, 14, v42\n\t"
    "v_lshl_or_b32 v21, v50, 14, v50\n\t"
    "v_lshl_or_b32 v25, v58, 14, v58\n\t"
    "v_lshl_or_b32 v29, v66, 14, v66\n\t"
    "v_and_b32 v17, 0x30003, v17\n\t"
    "v_and_b32 v21, 0x30003, v21\n\t"
    "v_and_b32 v25, 0x30003, v25\n\t"
    "v_and_b32 v29, 0x30003, v29\n\t"
    "v_mad_u32_u24 v17, v17, s22, v11\n\t"
    "v_mad_u32_u24 v21, v21, s22, v11\n\t"
    "v_mad_u32_u24 v25, v25, s22, v11\n\t"
    "v_mad_u32_u24 v29, v29, s22, v11\n\t"
    "v_perm_b32 v12, v18, v16, v17\n\t"
    "v_perm_b32 v13, v22, v20, v21\n\t"
    "v_perm_b32 v14, v26, v24, v25\n\t"
    "v_perm_b32 v15, v30, v28, v29\n\t"
    "global_store_dwordx4 v10, v[12:15], s[60:61] sc1\n\t"
    "v_lshl_or_b32 v42, v50, 4, v42\n\t"
    "v_lshl_or_b32 v58, v66, 4, v58\n\t"
    "v_lshl_or_b32 v4, v58, 8, v42\n\t"
    "v_mov_b32 v5, v4\n\t"
    "s_nop 1\n\t"
    "v_permlane32_swap_b32 v4, v5\n\t"
    "s_nop 0\n\t"
    "v_lshl_or_b32 v4, v5, 16, v4\n\t"
    "s_mov_b32 exec_hi, 0\n\t"
    "global_store_dword v9, v4, s[60:61] sc1\n\t"
    "s_mov_b32 exec_hi, -1\n\t"
    "s_barrier\n\t"
    "s_mov_b32 s37, 1\n\t"
    "s_call_b64 s[26:27], .Lxt_%=\n\t"
    "s_mov_b32 s22, 0\n\t"
    "s_waitcnt vmcnt(5)\n\t"
    "s_barrier\n\t"
    "s_cmp_lg_u32 s53, 0\n\t"
    "s_cbranch_scc1 .Ls1j_%=\n\t"
    "s_add_u32 s23, s20, 256\n\t"
    "s_xor_b32 s16, s30, s23\n\t"
    "s_or_b32 s17, s31, s22\n\t"
    "s_lshl_b32 s23, s23, 7\n\t"
    "s_add_u32 s23, s23, 786432\n\t"
    "v_mov_b32 v4, s16\n\t"
    "v_mov_b32 v5, s17\n\t"
    "v_mul_u32_u24 v8, 82048, v1\n\t"
    "v_add_u32 v8, s23, v8\n\t"
    "s_waitcnt lgkmcnt(0)\n\t"
    "s_sub_u32 s4, s4, 145\n\t"
    "s_sub_i32 s4, s4, s24\n\t"
    "s_max_i32 s4, s4, 0\n\t"
    "s_add_u32 s4, s4, s24\n\t"
    "v_mov_b32 v6, s4\n\t"
    "s_waitcnt vmcnt(63)\n\t"
    "s_mov_b64 exec, 0x1\n\t"
    "global_store_dwordx3 v8, v[4:6], s[60:61] sc1\n\t"
    "s_mov_b64 exec, -1\n\t"
    ".Ls1j_%=:\n\t"
    "s_mov_b32 s37, 2\n\t"
    "s_call_b64 s[26:27], .Lxu_%=\n\t"
    "s_branch .Ljoin_%=\n\t"
    ".Lxt_%=:\n\t"
    "s_mov_b32 m0, s36\n\t"
    "s_mov_b32 exec_hi, 0\n\t"
    "buffer_load_dwordx4 v237, s[40:43], s68 offen offset:0 nt lds\n\t"
    "s_mov_b32 exec_hi, -1\n\t"
    "buffer_load_dwordx4 v237, s[40:43], s68 offen offset:512 nt lds\n\t"
    "buffer_load_dwordx4 v238, s[40:43], s68 offen offset:1536 nt lds\n\t"
    "buffer_load_dwordx4 v239, s[40:43], s68 offen offset:2560 nt lds\n\t"
    "buffer_load_dwordx4 v239, s[40:43], s68 offen offset:3584 nt lds\n\t"
    "s_add_u32 s36, s36, 4608\n\t"
    "s_add_u32 s68, s68, 4718592\n\t"
    "s_sub_u32 s37, s37, 1\n\t"
    "s_cmp_lg_u32 s37, 0\n\t"
    "s_cbranch_scc1 .Lxt_%=\n\t"
    "s_setpc_b64 s[26:27]\n\t"
    ".Lxu_%=:\n\t"
    "s_mov_b32 m0, s36\n\t"
    "s_waitcnt vmcnt(4)\n\t"
    "s_mov_b32 exec_hi, 0\n\t"
    "buffer_load_dwordx4 v237, s[40:43], s68 offen offset:0 nt lds\n\t"
    "s_mov_b32 exec_hi, -1\n\t"
    "s_waitcnt vmcnt(4)\n\t"
    "buffer_load_dwordx4 v237, s[40:43], s68 offen offset:512 nt lds\n\t"
    "s_waitcnt vmcnt(4)\n\t"
    "buffer_load_dwordx4 v238, s[40:43], s68 offen offset:1536 nt lds\n\t"
    "s_waitcnt vmcnt(4)\n\t"
    "buffer_load_dwordx4 v239, s[40:43], s68 offen offset:2560 nt lds\n\t"
    "s_waitcnt vmcnt(4)\n\t"
    "buffer_load_dwordx4 v239, s[40:43], s68 offen offset:3584 nt lds\n\t"
    "s_add_u32 s36, s36, 4608\n\t"
    "s_add_u32 s68, s68, 4718592\n\t"
    "s_sub_u32 s37, s37, 1\n\t"
    "s_cmp_lg_u32 s37, 0\n\t"
    "s_cbranch_scc1 .Lxu_%=\n\t"
    "s_setpc_b64 s[26:27]\n\t"
    ".Lxs_%=:\n\t"
    "s_mov_b32 m0, s36\n\t"
    "s_waitcnt vmcnt(4)\n\t"
    "s_mov_b32 exec_hi, 0\n\t"
    "buffer_load_dwordx4 v237, s[40:43], s68 offen offset:0 nt lds\n\t"
    "s_mov_b32 exec_hi, -1\n\t"
    "s_waitcnt vmcnt(4)\n\t"
    "buffer_load_dwordx4 v237, s[40:43], s68 offen offset:512 nt lds\n\t"
    "buffer_load_dwordx4 v238, s[40:43], s68 offen offset:1536 nt lds\n\t"
    "buffer_load_dwordx4 v239, s[40:43], s68 offen offset:2560 nt lds\n\t"
    "s_memtime s[76:77]\n\t"
    "s_waitcnt lgkmcnt(0)\n\t"
    "s_load_dword s74, s[72:73], s75\n\t"
    "s_waitcnt lgkmcnt(0)\n\t"
    "s_memtime s[78:79]\n\t"
    "s_xor_b32 s75, s75, 0x2000\n\t"
    "s_waitcnt lgkmcnt(0)\n\t"
    "s_sub_u32 s81, s78, s76\n\t"
    "s_cmp_lt_u32 s81, 320\n\t"
    "s_cselect_b32 s81, -1, s81\n\t"
    "s_waitcnt vmcnt(4)\n\t"
    "buffer_load_dwordx4 v239, s[40:43], s68 offen offset:3584 nt lds\n\t"
    "s_add_u32 s36, s36, 4608\n\t"
    "s_add_u32 s68, s68, 4718592\n\t"
    "s_setpc_b64 s[26:27]\n\t"
    ".Lconst_%=:\n\t"
    "s_sub_u32 s57, s59, s58\n\t"
    "s_lshl_b32 s64, s53, 8\n\t"
    "s_add_u32 s59, s58, s64\n\t"
    "s_add_u32 s44, s14, s59\n\t"
    "s_addc_u32 s45, s15, 0\n\t"
    "s_sub_u32 s46, 75497472, s59\n\t"
    "s_mov_b32 s47, 0x20000\n\t"
    "s_sub_u32 s57, s57, s64\n\t"
    "s_add_u32 s57, s57, 1024\n\t"
    "s_mul_i32 s59, s53, 192512\n\t"
    "s_add_u32 s48, s60, s59\n\t"
    "s_addc_u32 s49, s61, 0\n\t"
    "s_mov_b32 s50, 192512\n\t"
    "s_mov_b32 s51, 0x20000\n\t"
    "v_lshrrev_b32 v96, 2, v2\n\t"
    "v_mul_u32_u24 v96, 23040, v96\n\t"
    "s_movk_i32 s59, 1152\n\t"
    "v_mad_u32_u24 v248, v2, s59, v96\n\t"
    "v_lshlrev_b32 v96, 1, v2\n\t"
    "v_and_b32 v96, 28, v96\n\t"
    "s_mov_b32 s59, 0x64753120\n\t"
    "v_bfe_u32 v96, s59, v96, 3\n\t"
    "v_lshlrev_b32 v249, 4, v96\n\t"
    "v_lshlrev_b32 v96, 5, v3\n\t"
    "v_xad_u32 v217, v96, v249, v248\n\t"
    "v_xor_b32 v236, 16, v217\n\t"
    "s_mul_i32 s83, s53, 4080\n\t"
    "s_add_u32 s83, s83, 147456\n\t"
    "v_lshl_add_u32 v244, v3, 4, s83\n\t"
    "s_mul_i32 s88, s53, 9216\n\t"
    "s_add_u32 s88, s88, 110592\n\t"
    "v_add_u32 v245, s88, v92\n\t"
    "s_mul_i32 s59, s53, 4080\n\t"
    "s_add_u32 s59, s59, 148464\n\t"
    "v_and_b32 v96, 7, v2\n\t"
    "v_xor_b32 v96, v96, v3\n\t"
    "v_lshlrev_b32 v96, 4, v96\n\t"
    "v_lshl_add_u32 v96, v2, 7, v96\n\t"
    "v_add_u32 v241, s59, v96\n\t"
    "v_xor_b32 v96, 64, v96\n\t"
    "v_add_u32 v95, s59, v96\n\t"
    "v_and_b32 v96, 14, v2\n\t"
    "v_mul_u32_u24 v241, 1152, v96\n\t"
    "v_lshl_add_u32 v241, v3, 5, v241\n\t"
    "v_and_b32 v96, 1, v2\n\t"
    "v_lshl_add_u32 v241, v96, 4, v241\n\t"
    "v_lshrrev_b32 v96, 3, v1\n\t"
    "v_and_b32 v97, 7, v1\n\t"
    "v_mul_u32_u24 v240, 1152, v96\n\t"
    "v_lshl_add_u32 v240, v97, 4, v240\n\t"
    "v_xor_b32 v97, v97, v96\n\t"
    "v_lshlrev_b32 v97, 4, v97\n\t"
    "v_lshl_add_u32 v97, v96, 7, v97\n\t"
    "v_add_u32 v243, s59, v97\n\t"
    "v_lshrrev_b32 v96, 2, v2\n\t"
    "v_mul_u32_u24 v242, 4080, v96\n\t"
    "v_and_b32 v96, 3, v2\n\t"
    "v_lshl_add_u32 v242, v96, 7, v242\n\t"
    "s_lshl_b32 s64, s53, 1\n\t"
    "v_lshrrev_b32 v97, 1, v3\n\t"
    "v_or_b32 v97, s64, v97\n\t"
    "v_xor_b32 v97, v97, v96\n\t"
    "v_lshl_add_u32 v242, v97, 4, v242\n\t"
    "v_and_b32 v96, 1, v3\n\t"
    "v_lshl_add_u32 v242, v96, 3, v242\n\t"
    "v_add_u32 v242, 150512, v242\n\t"
    "v_lshl_or_b32 v96, v249, 16, v249\n\t"
    "v_lshl_or_b32 v97, v248, 16, v248\n\t"
    "v_mov_b32 v246, v96\n\t"
    "v_mov_b32 v247, v96\n\t"
    "v_mov_b32 v94, 0xeeee\n\t"
    "v_mov_b64 v[250:251], 0\n\t"
    "v_mov_b64 v[252:253], 0\n\t"
    "v_mov_b32 v93, 0x555555\n\t"
    "v_mov_b64 v[254:255], 0\n\t"
    "s_mov_b32 s56, 18432\n\t"
    "s_lshl2_add_u32 s82, s62, s53\n\t"
    "v_mov_b64 v[100:101], 0\n\t"
    "v_mov_b64 v[102:103], 0\n\t"
    "ds_write_b128 v245, v[100:103] offset:0\n\t"
    "ds_write_b128 v245, v[100:103] offset:1024\n\t"
    "ds_write_b128 v245, v[100:103] offset:2048\n\t"
    "ds_write_b128 v245, v[100:103] offset:3072\n\t"
    "ds_write_b128 v245, v[100:103] offset:4096\n\t"
    "ds_write_b128 v245, v[100:103] offset:5120\n\t"
    "ds_write_b128 v245, v[100:103] offset:6144\n\t"
    "ds_write_b128 v245, v[100:103] offset:7168\n\t"
    "ds_write_b128 v245, v[100:103] offset:8192\n\t"
    "s_waitcnt lgkmcnt(0)\n\t"
    "s_mul_hi_u32 s16, s30, 1284865837\n\t"
    "s_mul_i32 s17, s30, 1481765933\n\t"
    "s_add_u32 s16, s16, s17\n\t"
    "s_mul_i32 s17, s31, 1284865837\n\t"
    "s_add_u32 s16, s16, s17\n\t"
    "s_mul_i32 s30, s30, 1284865837\n\t"
    "s_add_u32 s30, s30, 4150755663\n\t"
    "s_addc_u32 s31, s16, 335903614\n\t"
    "s_andn2_b32 s31, s31, 3\n\t"
    "s_setpc_b64 s[28:29]\n\t"
    ".Ls2t_%=:\n\t"
    "s_call_b64 s[28:29], .Lconst_%=\n\t"
    "v_lshlrev_b32 v10, 4, v0\n\t"
    "v_add_u32 v10, 147456, v10\n\t"
    "s_mov_b32 s16, 0x05040100\n\t"
    "s_mov_b32 s17, 0x07060302\n\t"
    "s_mov_b32 s4, 0x11111111\n\t"
    "s_mov_b32 s5, s4\n\t"
    "s_mov_b32 s6, 0xf0f0f0f0\n\t"
    "s_mov_b32 s7, s6\n\t"
    "s_lshl3_add_u32 s19, s53, 156736\n\t"
    "v_mov_b32 v11, s19\n\t"
    "v_mad_u32_u24 v4, v5, 48, v4\n\t"
    "s_waitcnt vmcnt(9)\n\t"
    "s_barrier\n\t"
    "s_mov_b32 s37, 2\n\t"
    "s_call_b64 s[26:27], .Lxt_%=\n\t"
    "v_and_b32 v48, s18, v16\n\t"
    "v_and_b32 v64, s18, v17\n\t"
    "v_and_b32 v49, s18, v18\n\t"
    "v_and_b32 v65, s18, v19\n\t"
    "v_and_b32 v50, s18, v20\n\t"
    "v_and_b32 v66, s18, v21\n\t"
    "v_and_b32 v51, s18, v22\n\t"
    "v_and_b32 v67, s18, v23\n\t"
    "v_pk_max_u16 v52, v48, v49\n\t"
    "v_pk_max_u16 v68, v64, v65\n\t"
    "v_pk_min_u16 v53, v48, v49\n\t"
    "v_pk_min_u16 v69, v64, v65\n\t"
    "v_pk_max_u16 v54, v50, v51\n\t"
    "v_pk_max_u16 v70, v66, v67\n\t"
    "v_pk_min_u16 v55, v50, v51\n\t"
    "v_pk_min_u16 v71, v66, v67\n\t"
    "v_pk_min_u16 v54, v52, v54\n\t"
    "v_pk_min_u16 v70, v68, v70\n\t"
    "v_pk_max_u16 v55, v53, v55\n\t"
    "v_pk_max_u16 v71, v69, v71\n\t"
    "v_pk_min_u16 v52, v52, v50\n\t"
    "v_pk_min_u16 v68, v68, v66\n\t"
    "v_pk_max_u16 v54, v54, v55\n\t"
    "v_pk_max_u16 v70, v70, v71\n\t"
    "v_pk_max_u16 v52, v53, v52\n\t"
    "v_pk_max_u16 v68, v69, v68\n\t"
    "v_pk_min_u16 v53, v53, v50\n\t"
    "v_pk_min_u16 v69, v69, v66\n\t"
    "v_pk_min_u16 v52, v52, v51\n\t"
    "v_pk_min_u16 v68, v68, v67\n\t"
    "v_pk_sub_i16 v53, v53, v54\n\t"
    "v_pk_sub_i16 v69, v69, v70\n\t"
    "v_pk_sub_i16 v52, v52, v54\n\t"
    "v_pk_sub_i16 v68, v68, v70\n\t"
    "v_pk_ashrrev_i16 v53, 15, v53 op_sel_hi:[0,1]\n\t"
    "v_pk_ashrrev_i16 v69, 15, v69 op_sel_hi:[0,1]\n\t"
    "v_pk_ashrrev_i16 v52, 15, v52 op_sel_hi:[0,1]\n\t"
    "v_pk_ashrrev_i16 v68, 15, v68 op_sel_hi:[0,1]\n\t"
    "v_bfi_b32 v53, v53, 0, v20\n\t"
    "v_bfi_b32 v69, v69, 0, v21\n\t"
    "v_bfi_b32 v52, v52, 0, v22\n\t"
    "v_bfi_b32 v68, v68, 0, v23\n\t"
    "v_perm_b32 v80, v52, v53, s16\n\t"
    "v_perm_b32 v82, v68, v69, s16\n\t"
    "v_perm_b32 v81, v52, v53, s17\n\t"
    "v_perm_b32 v83, v68, v69, s17\n\t"
    "ds_write_b128 v10, v[80:83] offset:0\n\t"
    "v_or3_b32 v12, v52, v53, v68\n\t"
    "v_bitop3_b32 v12, v12, v69, s18 bitop3:0xa8\n\t"
    "v_cmp_ne_u32 s[94:95], 0, v12\n\t"
    "s_waitcnt vmcnt(15)\n\t"
    "v_and_b32 v48, s18, v24\n\t"
    "v_and_b32 v64, s18, v25\n\t"
    "v_and_b32 v49, s18, v26\n\t"
    "v_and_b32 v65, s18, v27\n\t"
    "v_and_b32 v50, s18, v28\n\t"
    "v_and_b32 v66, s18, v29\n\t"
    "v_and_b32 v51, s18, v30\n\t"
    "v_and_b32 v67, s18, v31\n\t"
    "v_pk_max_u16 v52, v48, v49\n\t"
    "v_pk_max_u16 v68, v64, v65\n\t"
    "v_pk_min_u16 v53, v48, v49\n\t"
    "v_pk_min_u16 v69, v64, v65\n\t"
    "v_pk_max_u16 v54, v50, v51\n\t"
    "v_pk_max_u16 v70, v66, v67\n\t"
    "v_pk_min_u16 v55, v50, v51\n\t"
    "v_pk_min_u16 v71, v66, v67\n\t"
    "v_pk_min_u16 v54, v52, v54\n\t"
    "v_pk_min_u16 v70, v68, v70\n\t"
    "v_pk_max_u16 v55, v53, v55\n\t"
    "v_pk_max_u16 v71, v69, v71\n\t"
    "v_pk_min_u16 v52, v52, v50\n\t"
    "v_pk_min_u16 v68, v68, v66\n\t"
    "v_pk_max_u16 v54, v54, v55\n\t"
    "v_pk_max_u16 v70, v70, v71\n\t"
    "v_pk_max_u16 v52, v53, v52\n\t"
    "v_pk_max_u16 v68, v69, v68\n\t"
    "v_pk_min_u16 v53, v53, v50\n\t"
    "v_pk_min_u16 v69, v69, v66\n\t"
    "v_pk_min_u16 v52, v52, v51\n\t"
    "v_pk_min_u16 v68, v68, v67\n\t"
    "v_pk_sub_i16 v53, v53, v54\n\t"
    "v_pk_sub_i16 v69, v69, v70\n\t"
    "v_pk_sub_i16 v52, v52, v54\n\t"
    "v_pk_sub_i16 v68, v68, v70\n\t"
    "v_pk_ashrrev_i16 v53, 15, v53 op_sel_hi:[0,1]\n\t"
    "v_pk_ashrrev_i16 v69, 15, v69 op_sel_hi:[0,1]\n\t"
    "v_pk_ashrrev_i16 v52, 15, v52 op_sel_hi:[0,1]\n\t"
    "v_pk_ashrrev_i16 v68, 15, v68 op_sel_hi:[0,1]\n\t"
    "v_bfi_b32 v53, v53, 0, v28\n\t"
    "v_bfi_b32 v69, v69, 0, v29\n\t"
    "v_bfi_b32 v52, v52, 0, v30\n\t"
    "v_bfi_b32 v68, v68, 0, v31\n\t"
    "v_perm_b32 v84, v52, v53, s16\n\t"
    "v_perm_b32 v86, v68, v69, s16\n\t"
    "v_perm_b32 v85, v52, v53, s17\n\t"
    "v_perm_b32 v87, v68, v69, s17\n\t"
    "ds_write_b128 v10, v[84:87] offset:3072\n\t"
    "v_or3_b32 v12, v52, v53, v68\n\t"
    "v_bitop3_b32 v12, v12, v69, s18 bitop3:0xa8\n\t"
    "v_cmp_ne_u32 s[96:97], 0, v12\n\t"
    "s_waitcnt vmcnt(11)\n\t"
    "v_and_b32 v48, s18, v32\n\t"
    "v_and_b32 v64, s18, v33\n\t"
    "v_and_b32 v49, s18, v34\n\t"
    "v_and_b32 v65, s18, v35\n\t"
    "v_and_b32 v50, s18, v36\n\t"
    "v_and_b32 v66, s18, v37\n\t"
    "v_and_b32 v51, s18, v38\n\t"
    "v_and_b32 v67, s18, v39\n\t"
    "v_pk_max_u16 v52, v48, v49\n\t"
    "v_pk_max_u16 v68, v64, v65\n\t"
    "v_pk_min_u16 v53, v48, v49\n\t"
    "v_pk_min_u16 v69, v64, v65\n\t"
    "v_pk_max_u16 v54, v50, v51\n\t"
    "v_pk_max_u16 v70, v66, v67\n\t"
    "v_pk_min_u16 v55, v50, v51\n\t"
    "v_pk_min_u16 v71, v66, v67\n\t"
    "v_pk_min_u16 v54, v52, v54\n\t"
    "v_pk_min_u16 v70, v68, v70\n\t"
    "v_pk_max_u16 v55, v53, v55\n\t"
    "v_pk_max_u16 v71, v69, v71\n\t"
    "v_pk_min_u16 v52, v52, v50\n\t"
    "v_pk_min_u16 v68, v68, v66\n\t"
    "v_pk_max_u16 v54, v54, v55\n\t"
    "v_pk_max_u16 v70, v70, v71\n\t"
    "v_pk_max_u16 v52, v53, v52\n\t"
    "v_pk_max_u16 v68, v69, v68\n\t"
    "v_pk_min_u16 v53, v53, v50\n\t"
    "v_pk_min_u16 v69, v69, v66\n\t"
    "v_pk_min_u16 v52, v52, v51\n\t"
    "v_pk_min_u16 v68, v68, v67\n\t"
    "v_pk_sub_i16 v53, v53, v54\n\t"
    "v_pk_sub_i16 v69, v69, v70\n\t"
    "v_pk_sub_i16 v52, v52, v54\n\t"
    "v_pk_sub_i16 v68, v68, v70\n\t"
    "v_pk_ashrrev_i16 v53, 15, v53 op_sel_hi:[0,1]\n\t"
    "v_pk_ashrrev_i16 v69, 15, v69 op_sel_hi:[0,1]\n\t"
    "v_pk_ashrrev_i16 v52, 15, v52 op_sel_hi:[0,1]\n\t"
    "v_pk_ashrrev_i16 v68, 15, v68 op_sel_hi:[0,1]\n\t"
    "v_bfi_b32 v53, v53, 0, v36\n\t"
    "v_bfi_b32 v69, v69, 0, v37\n\t"
    "v_bfi_b32 v52, v52, 0, v38\n\t"
    "v_bfi_b32 v68, v68, 0, v39\n\t"
    "v_perm_b32 v88, v52, v53, s16\n\t"
    "v_perm_b32 v90, v68, v69, s16\n\t"
    "v_perm_b32 v89, v52, v53, s17\n\t"
    "v_perm_b32 v91, v68, v69, s17\n\t"
    "ds_write_b128 v10, v[88:91] offset:6144\n\t"
    "v_or3_b32 v12, v52, v53, v68\n\t"
    "v_bitop3_b32 v12, v12, v69, s18 bitop3:0xa8\n\t"
    "v_cmp_ne_u32 s[98:99], 0, v12\n\t"
    "s_nop 0\n\t"
    "s_lshr_b64 s[22:23], s[94:95], 1\n\t"
    "s_or_b64 s[94:95], s[94:95], s[22:23]\n\t"
    "s_lshr_b64 s[22:23], s[94:95], 2\n\t"
    "s_or_b64 s[94:95], s[94:95], s[22:23]\n\t"
    "s_and_b64 s[94:95], s[94:95], s[4:5]\n\t"
    "s_lshr_b64 s[22:23], s[96:97], 1\n\t"
    "s_or_b64 s[96:97], s[96:97], s[22:23]\n\t"
    "s_lshr_b64 s[22:23], s[96:97], 2\n\t"
    "s_or_b64 s[96:97], s[96:97], s[22:23]\n\t"
    "s_and_b64 s[96:97], s[96:97], s[4:5]\n\t"
    "s_lshr_b64 s[22:23], s[98:99], 1\n\t"
    "s_or_b64 s[98:99], s[98:99], s[22:23]\n\t"
    "s_lshr_b64 s[22:23], s[98:99], 2\n\t"
    "s_or_b64 s[98:99], s[98:99], s[22:23]\n\t"
    "s_and_b64 s[98:99], s[98:99], s[4:5]\n\t"
    "s_lshl_b64 s[96:97], s[96:97], 1\n\t"
    "s_lshl_b64 s[98:99], s[98:99], 2\n\t"
    "s_or_b64 s[94:95], s[94:95], s[96:97]\n\t"
    "s_or_b64 s[94:95], s[94:95], s[98:99]\n\t"
    "v_mov_b32 v12, s94\n\t"
    "v_mov_b32 v13, s95\n\t"
    "ds_write_b64 v11, v[12:13]\n\t"
    "v_mov_b32 v11, 156736\n\t"
    "s_waitcnt lgkmcnt(0)\n\t"
    "s_barrier\n\t"
    "ds_read_b128 v[12:15], v11\n\t"
    "ds_read_b64 v[16:17], v11 offset:16\n\t"
    "s_mov_b32 s37, 1\n\t"
    "s_call_b64 s[26:27], .Lxt_%=\n\t"
    "s_andn2_b64 s[22:23], s[94:95], s[6:7]\n\t"
    "s_and_b64 s[96:97], s[94:95], s[6:7]\n\t"
    "v_mbcnt_lo_u32_b32 v18, s22, 0\n\t"
    "v_mbcnt_lo_u32_b32 v19, s96, 0\n\t"
    "v_mbcnt_hi_u32_b32 v18, s23, v18\n\t"
    "v_mbcnt_hi_u32_b32 v19, s97, v19\n\t"
    "v_lshlrev_b32 v20, 6, v4\n\t"
    "v_lshlrev_b32 v21, 3, v4\n\t"
    "s_waitcnt lgkmcnt(0)\n\t"
    "v_readfirstlane_b32 s70, v12\n\t"
    "v_readfirstlane_b32 s71, v13\n\t"
    "v_readfirstlane_b32 s72, v14\n\t"
    "v_readfirstlane_b32 s73, v15\n\t"
    "v_readfirstlane_b32 s74, v16\n\t"
    "v_readfirstlane_b32 s75, v17\n\t"
    "s_andn2_b64 s[22:23], s[70:71], s[6:7]\n\t"
    "s_bcnt1_i32_b64 s76, s[22:23]\n\t"
    "s_and_b64 s[22:23], s[70:71], s[6:7]\n\t"
    "s_bcnt1_i32_b64 s79, s[22:23]\n\t"
    "s_andn2_b64 s[22:23], s[72:73], s[6:7]\n\t"
    "s_bcnt1_i32_b64 s77, s[22:23]\n\t"
    "s_and_b64 s[22:23], s[72:73], s[6:7]\n\t"
    "s_bcnt1_i32_b64 s80, s[22:23]\n\t"
    "s_andn2_b64 s[22:23], s[74:75], s[6:7]\n\t"
    "s_bcnt1_i32_b64 s78, s[22:23]\n\t"
    "s_and_b64 s[22:23], s[74:75], s[6:7]\n\t"
    "s_bcnt1_i32_b64 s81, s[22:23]\n\t"
    "s_add_u32 s77, s77, s76\n\t"
    "s_add_u32 s78, s78, s77\n\t"
    "s_add_u32 s80, s80, s79\n\t"
    "s_add_u32 s81, s81, s80\n\t"
    "s_cmp_eq_u32 s53, 0\n\t"
    "s_cselect_b32 s76, 0, s76\n\t"
    "s_cselect_b32 s79, 0, s79\n\t"
    "s_cmp_eq_u32 s53, 2\n\t"
    "s_cselect_b32 s76, s77, s76\n\t"
    "s_cselect_b32 s79, s80, s79\n\t"
    "s_sub_i32 s70, 16, s81\n\t"
    "s_max_i32 s70, s70, 8\n\t"
    "s_min_i32 s70, s70, s78\n\t"
    "s_sub_i32 s71, 16, s70\n\t"
    "s_min_i32 s71, s71, s81\n\t"
    "s_sub_u32 s72, s78, s70\n\t"
    "s_sub_u32 s73, s81, s71\n\t"
    "s_add_u32 s73, s73, s72\n\t"
    "v_add_u32 v18, s76, v18\n\t"
    "v_add_u32 v19, s79, v19\n\t"
    "v_subrev_u32 v22, s70, v18\n\t"
    "v_subrev_u32 v23, s71, v19\n\t"
    "v_sub_u32 v19, 15, v19\n\t"
    "v_cndmask_b32 v24, v22, v23, s[6:7]\n\t"
    "v_add_u32 v23, s72, v23\n\t"
    "v_cndmask_b32 v18, v18, v19, s[6:7]\n\t"
    "v_cndmask_b32 v22, v22, v23, s[6:7]\n\t"
    "v_cmp_gt_i32 vcc, 0, v24\n\t"
    "v_sub_u32 v19, 15, v18\n\t"
    "v_min_u32 v19, v18, v19\n\t"
    "v_lshrrev_b32 v18, 3, v18\n\t"
    "v_lshrrev_b32 v23, 2, v19\n\t"
    "v_lshl_add_u32 v18, v23, 1, v18\n\t"
    "v_and_b32 v19, 3, v19\n\t"
    "v_lshl_add_u32 v23, v18, 2, v19\n\t"
    "v_mov_b32 v25, v23\n\t"
    "s_mov_b32 s16, 156768\n\t"
    "v_lshl_add_u32 v23, v23, 2, s16\n\t"
    "v_lshl_add_u32 v25, v25, 2, s16\n\t"
    "v_min_u32 v22, 15, v22\n\t"
    "v_lshl_add_u32 v22, v22, 2, s16\n\t"
    "s_and_b64 exec, s[94:95], vcc\n\t"
    "ds_write_b32 v25, v20\n\t"
    "ds_write_b32 v23, v21 offset:64\n\t"
    "s_andn2_b64 exec, s[94:95], vcc\n\t"
    "ds_write_b32 v22, v20 offset:128\n\t"
    "s_mov_b64 exec, -1\n\t"
    ".Ls2b_%=:\n\t"
    "s_waitcnt lgkmcnt(0)\n\t"
    "s_barrier\n\t"
    "s_cmp_lg_u32 s53, 0\n\t"
    "s_cbranch_scc1 .Ls2j_%=\n\t"
    "v_lshl_add_u32 v10, v3, 4, s16\n\t"
    "ds_read_b128 v[12:15], v10\n\t"
    "ds_read_b128 v[16:19], v10 offset:64\n\t"
    "s_mov_b32 s17, 147456\n\t"
    "v_lshl_add_u32 v11, v2, 2, s17\n\t"
    "s_mul_i32 s19, s21, 192512\n\t"
    "v_add_u32 v26, s19, v92\n\t"
    "s_mul_i32 s17, s20, 224\n\t"
    "s_add_u32 s17, s17, 99328\n\t"
    "v_add_u32 v27, s17, v26\n\t"
    "s_lshl_b32 s17, s20, 10\n\t"
    "v_add_u32 v30, s17, v92\n\t"
    "s_lshr_b32 s17, s20, 1\n\t"
    "s_lshl_b32 s17, s17, 6\n\t"
    "s_and_b32 s22, s20, 1\n\t"
    "s_lshl3_add_u32 s17, s22, s17\n\t"
    "s_add_u32 s17, s17, 108976\n\t"
    "s_add_u32 s17, s17, s19\n\t"
    "v_lshl_add_u32 v28, v3, 4, s17\n\t"
    "s_mov_b32 s4, 0x10001\n\t"
    "s_mov_b32 s5, s4\n\t"
    "s_mov_b32 s23, s63\n\t"
    "s_mov_b32 s22, 0\n\t"
    "s_waitcnt lgkmcnt(1)\n\t"
    "v_add_u32 v12, v11, v12\n\t"
    "v_add_u32 v13, v11, v13\n\t"
    "v_add_u32 v14, v11, v14\n\t"
    "v_add_u32 v15, v11, v15\n\t"
    "ds_read_b32 v12, v12\n\t"
    "ds_read_b32 v13, v13\n\t"
    "ds_read_b32 v14, v14\n\t"
    "ds_read_b32 v15, v15\n\t"
    "s_waitcnt lgkmcnt(4)\n\t"
    "v_lshl_or_b32 v16, v17, 16, v16\n\t"
    "v_lshl_or_b32 v17, v19, 16, v18\n\t"
    "s_mov_b64 exec, s[4:5]\n\t"
    "global_store_dwordx2 v28, v[16:17], s[60:61] sc1\n\t"
    "s_mov_b64 exec, -1\n\t"
    "s_cmp_lg_u32 s73, 0\n\t"
    "s_cbranch_scc1 .Ls2x_%=\n\t"
    ".Ls2r_%=:\n\t"
    "s_waitcnt lgkmcnt(0)\n\t"
    "v_and_b32 v32, s18, v12\n\t"
    "v_and_b32 v33, s18, v13\n\t"
    "v_and_b32 v34, s18, v14\n\t"
    "v_and_b32 v35, s18, v15\n\t"
    "v_cmp_ne_u32 s[70:71], 0, v32\n\t"
    "v_cmp_ne_u32 s[72:73], 0, v33\n\t"
    "v_cmp_ne_u32 s[74:75], 0, v34\n\t"
    "v_cmp_ne_u32 s[76:77], 0, v35\n\t"
    "v_mov_b32 v40, v12\n\t"
    "v_add_u32 v41, 0, v30\n\t"
    "v_mov_b32 v42, v13\n\t"
    "v_add_u32 v43, 4, v30\n\t"
    "v_mov_b32 v44, v14\n\t"
    "v_add_u32 v45, 8, v30\n\t"
    "v_mov_b32 v46, v15\n\t"
    "v_add_u32 v47, 12, v30\n\t"
    "s_bcnt1_i32_b64 s78, s[70:71]\n\t"
    "s_bcnt1_i32_b64 s79, s[72:73]\n\t"
    "s_bcnt1_i32_b64 s80, s[74:75]\n\t"
    "s_bcnt1_i32_b64 s81, s[76:77]\n\t"
    "s_add_u32 s79, s79, s78\n\t"
    "s_add_u32 s80, s80, s79\n\t"
    "s_add_u32 s81, s81, s80\n\t"
    "s_cmp_gt_u32 s81, 28\n\t"
    "s_cbranch_scc1 .Ls2h_%=\n\t"
    "v_mbcnt_lo_u32_b32 v32, s70, 0\n\t"
    "v_mbcnt_lo_u32_b32 v33, s72, 0\n\t"
    "v_mbcnt_lo_u32_b32 v34, s74, 0\n\t"
    "v_mbcnt_lo_u32_b32 v35, s76, 0\n\t"
    "v_mbcnt_hi_u32_b32 v32, s71, v32\n\t"
    "v_mbcnt_hi_u32_b32 v33, s73, v33\n\t"
    "v_mbcnt_hi_u32_b32 v34, s75, v34\n\t"
    "v_mbcnt_hi_u32_b32 v35, s77, v35\n\t"
    "v_add_u32 v33, s78, v33\n\t"
    "v_add_u32 v34, s79, v34\n\t"
    "v_add_u32 v35, s80, v35\n\t"
    "v_lshl_add_u32 v32, v32, 3, s16\n\t"
    "v_lshl_add_u32 v33, v33, 3, s16\n\t"
    "v_lshl_add_u32 v34, v34, 3, s16\n\t"
    "v_lshl_add_u32 v35, v35, 3, s16\n\t"
    "s_mov_b64 exec, s[70:71]\n\t"
    "ds_write_b64 v32, v[40:41] offset:256\n\t"
    "s_mov_b64 exec, s[72:73]\n\t"
    "ds_write_b64 v33, v[42:43] offset:256\n\t"
    "s_mov_b64 exec, s[74:75]\n\t"
    "ds_write_b64 v34, v[44:45] offset:256\n\t"
    "s_mov_b64 exec, s[76:77]\n\t"
    "ds_write_b64 v35, v[46:47] offset:256\n\t"
    "s_mov_b64 exec, 0x3fff\n\t"
    "s_cmp_eq_u32 s20, 8\n\t"
    "s_cselect_b32 exec_lo, 0xffff, exec_lo\n\t"
    "v_add_u32 v10, s16, v92\n\t"
    "ds_read_b128 v[12:15], v10 offset:256\n\t"
    "s_waitcnt lgkmcnt(0)\n\t"
    "global_store_dwordx4 v27, v[12:15], s[60:61] sc1\n\t"
    "s_mov_b64 exec, -1\n\t"
    "s_barrier\n\t"
    "s_mov_b32 s37, 1\n\t"
    "s_call_b64 s[26:27], .Lxt_%=\n\t"
    "s_xor_b32 s16, s30, s23\n\t"
    "s_or_b32 s17, s31, s22\n\t"
    "s_lshl_b32 s23, s23, 7\n\t"
    "s_add_u32 s23, s23, 786432\n\t"
    "v_mov_b32 v4, s16\n\t"
    "v_mov_b32 v5, s17\n\t"
    "v_mul_u32_u24 v8, 82048, v1\n\t"
    "v_add_u32 v8, s23, v8\n\t"
    "v_mov_b32 v6, s24\n\t"
    "s_waitcnt vmcnt(5)\n\t"
    "s_mov_b64 exec, 0x1\n\t"
    "global_store_dwordx3 v8, v[4:6], s[60:61] sc1\n\t"
    "s_mov_b64 exec, -1\n\t"
    "s_branch .Ls2k_%=\n\t"
    ".Ls2j_%=:\n\t"
    "s_barrier\n\t"
    "s_mov_b32 s37, 1\n\t"
    "s_call_b64 s[26:27], .Lxt_%=\n\t"
    ".Ls2k_%=:\n\t"
    "s_branch .Ljoin_%=\n\t"
    ".Ls2w3_%=:\n\t"
    "s_waitcnt lgkmcnt(0)\n\t"
    "s_lshl_b32 s59, s53, 3\n\t"
    "s_lshr_b32 s59, 0x64753120, s59\n\t"
    "s_lshl_b32 s64, s59, 4\n\t"
    "s_and_b32 s64, s64, 0x70\n\t"
    "s_and_b32 s59, s59, 0x70\n\t"
    "v_cmp_gt_u32 vcc, 48, v1\n\t"
    "v_xor_b32 v237, s64, v92\n\t"
    "v_xor_b32 v239, s59, v92\n\t"
    "v_cndmask_b32 v238, v239, v237, vcc\n\t"
    "s_and_b32 s58, s62, 7\n\t"
    "s_lshl_b32 s58, s58, 5\n\t"
    "s_lshr_b32 s52, s62, 3\n\t"
    "s_add_u32 s58, s58, s52\n\t"
    "s_mul_i32 s58, s58, 18432\n\t"
    "s_mul_i32 s52, s53, 4608\n\t"
    "s_add_u32 s59, s58, s52\n\t"
    "s_mul_i32 s52, s53, 27648\n\t"
    "s_sub_u32 s42, 75497472, s59\n\t"
    "s_mov_b32 s43, 0x20000\n\t"
    "s_mov_b32 s68, 0\n\t"
    "s_mov_b32 s36, s52\n\t"
    "s_mov_b32 s18, 0x7fff7fff\n\t"
    "s_add_u32 s40, s8, s59\n\t"
    "s_addc_u32 s41, s9, 0\n\t"
    "v_min_u32 v44, 35, v1\n\t"
    "v_and_b32 v45, 3, v44\n\t"
    "v_and_b32 v46, 16, v44\n\t"
    "v_lshlrev_b32 v46, 3, v46\n\t"
    "v_and_b32 v47, 12, v44\n\t"
    "v_lshl_add_u32 v46, v47, 1, v46\n\t"
    "v_lshl_add_u32 v46, v45, 5, v46\n\t"
    "s_lshl_b32 s16, s53, 8\n\t"
    "s_lshl_b32 s17, s53, 5\n\t"
    "s_add_u32 s17, s17, 1024\n\t"
    "v_cmp_gt_u32 vcc, 32, v44\n\t"
    "v_add_u32 v46, s16, v46\n\t"
    "v_lshl_add_u32 v47, v45, 3, s17\n\t"
    "v_cndmask_b32 v198, v47, v46, vcc\n\t"
    "global_load_dwordx2 v[196:197], v198, s[12:13]\n\t"
    "s_add_u32 s16, s60, 917504\n\t"
    "s_addc_u32 s17, s61, 0\n\t"
    "s_load_dwordx2 s[30:31], s[16:17], 0\n\t"
    "s_call_b64 s[28:29], .Lconst_%=\n\t"
    "s_barrier\n\t"
    "s_mov_b32 s37, 2\n\t"
    "s_call_b64 s[26:27], .Lxt_%=\n\t"
    "s_mov_b32 s16, 156672\n\t"
    "v_lshl_add_u32 v10, v2, 2, s16\n\t"
    "v_mov_b32 v11, 0\n\t"
    "v_mov_b32 v12, 9216\n\t"
    "v_and_b32 v13, 4, v2\n\t"
    "v_lshlrev_b32 v13, 1, v13\n\t"
    "v_mov_b32 v14, 1073741824\n\t"
    "ds_write_b32 v10, v11\n\t"
    "ds_write_b32 v10, v12 offset:96\n\t"
    "ds_write_b32 v10, v13 offset:160\n\t"
    "ds_write_b32 v10, v12 offset:224\n\t"
    "ds_write_b32 v10, v14 offset:288\n\t"
    "s_mov_b32 s16, 157024\n\t"
    "v_lshl_add_u32 v10, v1, 2, s16\n\t"
    "v_mov_b32 v11, 0x40000\n\t"
    "ds_write_b32 v10, v11\n\t"
    "s_waitcnt lgkmcnt(0)\n\t"
    "s_barrier\n\t"
    "s_mov_b32 s37, 1\n\t"
    "s_call_b64 s[26:27], .Lxt_%=\n\t"
    "s_branch .Ls2b_%=\n\t"
    ".Lnone2_%=:\n\t"
    "s_call_b64 s[28:29], .Lconst_%=\n\t"
    "s_and_b32 s16, s62, 5\n\t"
    "s_cmp_eq_u32 s16, 4\n\t"
    "s_cbranch_scc0 .Lnopq_%=\n\t"
    "s_load_dword s74, s[72:73], s70\n\t"
    "s_mov_b32 s37, 1\n\t"
    "s_call_b64 s[26:27], .Lxu_%=\n\t"
    "s_call_b64 s[26:27], .Lxs_%=\n\t"
    "s_mov_b32 s80, s81\n\t"
    "s_mov_b32 s37, 1\n\t"
    "s_call_b64 s[26:27], .Lxu_%=\n\t"
    "s_branch .Ljoin_%=\n\t"
    ".Lnopq_%=:\n\t"
    "s_mov_b32 s37, 3\n\t"
    "s_call_b64 s[26:27], .Lxu_%=\n\t"
    ".Ljoin_%=:\n\t"
    "s_bfe_u32 s16, s62, 0x50003\n\t"
    "s_mul_i32 s16, s16, 0\n\t"
    "s_lshr_b32 s16, s16, 5\n\t"
    "s_sub_u32 s87, 0, s16\n\t"
    "s_sub_i32 s23, 35, s87\n\t"
    "s_sub_i32 s23, 15, s87\n\t"
    "s_and_b32 s59, s62, 0\n\t"
    "s_mul_i32 s59, s59, 82048\n\t"
    "s_add_u32 s59, s59, 786432\n\t"
    "v_lshl_add_u32 v44, v1, 2, s53\n\t"
    "v_min_u32 v44, 81, v44\n\t"
    "v_add_u32 v44, 256, v44\n\t"
    "v_lshl_add_u32 v43, v44, 7, s59\n\t"
    "v_xor_b32 v44, s30, v44\n\t"
    "v_mov_b32 v45, s31\n\t"
    "global_load_dwordx3 v[40:42], v43, s[60:61] sc1\n\t"
    "s_mov_b32 s69, 0\n\t"
    "s_mov_b32 s70, 0\n\t"
    "s_and_b32 s16, s62, 5\n\t"
    "s_cmp_eq_u32 s16, 4\n\t"
    "s_cbranch_scc0 .Lnopr_%=\n\t"
    "s_mov_b32 s37, 1\n\t"
    "s_call_b64 s[26:27], .Lxt_%=\n\t"
    "s_memtime s[76:77]\n\t"
    "s_waitcnt lgkmcnt(0)\n\t"
    "s_load_dword s74, s[72:73], s75\n\t"
    "s_waitcnt lgkmcnt(0)\n\t"
    "s_memtime s[78:79]\n\t"
    "s_xor_b32 s75, s75, 0x2000\n\t"
    "s_waitcnt lgkmcnt(0)\n\t"
    "s_sub_u32 s81, s78, s76\n\t"
    "s_cmp_lt_u32 s81, 320\n\t"
    "s_cselect_b32 s81, -1, s81\n\t"
    "s_bitcmp1_b32 s75, 13\n\t"
    "s_cselect_b32 s76, s81, s80\n\t"
    "s_cselect_b32 s77, s80, s81\n\t"
    "s_lshl3_add_u32 s78, s53, 149504\n\t"
    "v_mov_b32 v4, s78\n\t"
    "v_mov_b32 v6, s76\n\t"
    "v_mov_b32 v7, s77\n\t"
    "ds_write_b64 v4, v[6:7]\n\t"
    "s_waitcnt lgkmcnt(0)\n\t"
    ".Lnopr_%=:\n\t"
    "s_waitcnt vmcnt(16)\n\t"
    "v_lshlrev_b32 v4, 16, v196\n\t"
    "v_and_b32 v5, 0xffff0000, v196\n\t"
    "v_lshlrev_b32 v6, 16, v197\n\t"
    "v_and_b32 v7, 0xffff0000, v197\n\t"
    "v_add_u32 v8, s83, v92\n\t"
    "s_mov_b32 exec_hi, 15\n\t"
    "ds_write_b128 v8, v[4:7]\n\t"
    "s_mov_b32 exec_hi, -1\n\t"
    "s_and_b32 s16, s62, 3\n\t"
    "s_lshr_b32 s17, s62, 5\n\t"
    "s_lshl3_add_u32 s16, s16, s17\n\t"
    "s_mul_i32 s17, s16, 147456\n\t"
    "s_lshl_b32 s16, s62, 1\n\t"
    "s_and_b32 s16, s16, 48\n\t"
    "s_mov_b32 s38, 0xfeb87410\n\t"
    "s_mov_b32 s39, 0xdca96532\n\t"
    "s_lshr_b64 s[38:39], s[38:39], s16\n\t"
    "s_mov_b32 s39, s38\n\t"
    "s_sub_i32 s38, s17, s58\n\t"
    "s_mov_b32 s54, 0\n\t"
    "v_min_u32 v165, 10, v1\n\t"
    "s_mov_b32 s16, 4718592\n\t"
    "s_add_u32 s17, s17, 598016\n\t"
    "v_mov_b32 v164, s17\n\t"
    "v_mad_u32_u24 v165, v165, s16, v164\n\t"
    "v_lshrrev_b32 v165, 21, v165\n\t"
    "s_mov_b32 s16, 876032\n\t"
    "v_lshl_add_u32 v164, v1, 3, s16\n\t"
    "s_mul_i32 s58, s53, 9\n\t"
    "s_add_u32 s58, s58, 0\n\t"
    "v_min_u32 v89, 8, v1\n\t"
    "v_add_u32 v89, s58, v89\n\t"
    "v_lshl_add_u32 v88, v89, 7, s59\n\t"
    "v_xor_b32 v89, s30, v89\n\t"
    "s_mov_b64 exec, 0\n\t"
    "s_branch .Lpad0_%=\n\t"
    ".Lwarmr_%=:\n\t"
    "s_mov_b64 exec, -1\n\t"
    "s_and_b32 s16, s62, 5\n\t"
    "s_cmp_eq_u32 s16, 4\n\t"
    "s_cbranch_scc1 .Lprd_%=\n\t"
    "s_mov_b32 s37, 1\n\t"
    "s_call_b64 s[26:27], .Lxu_%=\n\t"
    ".Lprd_%=:\n\t"
    "s_waitcnt vmcnt(5)\n\t"
    "s_mov_b32 s58, 0x40000\n\t"
    ".Lpa_%=:\n\t"
    "v_cmp_ne_u64 vcc, v[40:41], v[44:45]\n\t"
    "s_mov_b32 s84, s24\n\t"
    "s_cbranch_vccz .Lmain_%=\n\t"
    "s_sub_u32 s58, s58, 1\n\t"
    "s_cbranch_scc1 .Lmain_%=\n\t"
    "s_sleep 1\n\t"
    "global_load_dwordx3 v[40:42], v43, s[60:61] sc1\n\t"
    "s_waitcnt vmcnt(0)\n\t"
    "s_branch .Lpa_%=\n\t"
    ".Lxinit_%=:\n\t"
    "s_cmp_eq_u32 s71, 0\n\t"
    "s_cbranch_scc1 .Lxi1_%=\n\t"
    "s_ff1_i32_b32 s59, s71\n\t"
    "s_bitset0_b32 s71, s59\n\t"
    "v_mov_b32 v250, 1073741824\n\t"
    "v_mov_b32 v251, 1073741824\n\t"
    "s_and_b32 s64, s59, 15\n\t"
    "s_mul_i32 s64, s64, 6\n\t"
    "s_and_b32 s59, s59, 16\n\t"
    "s_cselect_b32 s80, 0, -1\n\t"
    "s_lshl1_add_u32 s59, s59, s83\n\t"
    "s_lshl3_add_u32 s59, s64, s59\n\t"
    "v_bfe_u32 v1, v245, 8, 2\n\t"
    "v_lshl_add_u32 v1, v1, 3, s59\n\t"
    "s_mov_b32 exec_hi, s80\n\t"
    "ds_read_b64 v[250:251], v1 offset:576\n\t"
    "s_mov_b32 exec_hi, -1\n\t"
    "s_mov_b32 s69, s64\n\t"
    "s_cmp_eq_u32 s71, 0\n\t"
    "s_cbranch_scc1 .Lxi0_%=\n\t"
    "s_ff1_i32_b32 s59, s71\n\t"
    "s_bitset0_b32 s71, s59\n\t"
    "v_mov_b32 v254, 1073741824\n\t"
    "v_mov_b32 v255, 1073741824\n\t"
    "s_and_b32 s64, s59, 15\n\t"
    "s_mul_i32 s64, s64, 6\n\t"
    "s_and_b32 s59, s59, 16\n\t"
    "s_cselect_b32 s80, 0, -1\n\t"
    "s_lshl1_add_u32 s59, s59, s83\n\t"
    "s_lshl3_add_u32 s59, s64, s59\n\t"
    "v_bfe_u32 v1, v245, 8, 2\n\t"
    "v_lshl_add_u32 v1, v1, 3, s59\n\t"
    "s_mov_b32 exec_hi, s80\n\t"
    "ds_read_b64 v[254:255], v1 offset:576\n\t"
    "s_mov_b32 exec_hi, -1\n\t"
    "s_mov_b32 s70, s64\n\t"
    "s_waitcnt lgkmcnt(0)\n\t"
    "v_lshrrev_b32 v2, 15, v254\n\t"
    "v_and_b32 v2, 0x7fe, v2\n\t"
    "v_xor_b32 v2, v2, v249\n\t"
    "v_lshrrev_b32 v3, 15, v255\n\t"
    "v_and_b32 v3, 0x7fe, v3\n\t"
    "v_xor_b32 v3, v3, v249\n\t"
    "v_lshl_or_b32 v247, v3, 16, v2\n\t"
    "v_bfe_u32 v1, v245, 4, 4\n\t"
    "v_lshrrev_b32 v2, 26, v254\n\t"
    "v_lshrrev_b32 v3, 26, v255\n\t"
    "v_cmp_eq_u32 s[20:21], v2, v1\n\t"
    "v_cmp_eq_u32 s[80:81], v3, v1\n\t"
    "v_and_b32 v254, 0xffff, v254\n\t"
    "v_and_b32 v255, 0xffff, v255\n\t"
    "v_cndmask_b32 v254, 0, v254, s[20:21]\n\t"
    "v_cndmask_b32 v255, 0, v255, s[80:81]\n\t"
    ".Lxi0_%=:\n\t"
    "s_waitcnt lgkmcnt(0)\n\t"
    "v_lshrrev_b32 v2, 15, v250\n\t"
    "v_and_b32 v2, 0x7fe, v2\n\t"
    "v_xor_b32 v2, v2, v249\n\t"
    "v_lshrrev_b32 v3, 15, v251\n\t"
    "v_and_b32 v3, 0x7fe, v3\n\t"
    "v_xor_b32 v3, v3, v249\n\t"
    "v_lshl_or_b32 v246, v3, 16, v2\n\t"
    "v_bfe_u32 v1, v245, 4, 4\n\t"
    "v_lshrrev_b32 v2, 26, v250\n\t"
    "v_lshrrev_b32 v3, 26, v251\n\t"
    "v_cmp_eq_u32 s[20:21], v2, v1\n\t"
    "v_cmp_eq_u32 s[80:81], v3, v1\n\t"
    "v_and_b32 v250, 0xffff, v250\n\t"
    "v_and_b32 v251, 0xffff, v251\n\t"
    "v_cndmask_b32 v250, 0, v250, s[20:21]\n\t"
    "v_cndmask_b32 v251, 0, v251, s[80:81]\n\t"
    ".Lxi1_%=:\n\t"
    "s_or_b32 s58, s71, s73\n\t"
    "s_cselect_b32 s22, 1, 0\n\t"
    "s_branch .Lxinitr_%=\n\t"
    ".Lslow_%=:\n\t"
    "s_and_b32 s58, s71, s74\n\t"
    ".Lsr0_%=:\n\t"
    "s_cbranch_scc0 .Lsh0_%=\n\t"
    "s_ff1_i32_b32 s59, s58\n\t"
    "s_bitset0_b32 s58, s59\n\t"
    "v_mov_b32 v2, 1073741824\n\t"
    "v_mov_b32 v3, 1073741824\n\t"
    "s_and_b32 s64, s59, 15\n\t"
    "s_mul_i32 s64, s64, 6\n\t"
    "s_and_b32 s59, s59, 16\n\t"
    "s_cselect_b32 s80, 0, -1\n\t"
    "s_lshl1_add_u32 s59, s59, s83\n\t"
    "s_lshl3_add_u32 s59, s64, s59\n\t"
    "v_bfe_u32 v1, v245, 8, 2\n\t"
    "v_lshl_add_u32 v1, v1, 3, s59\n\t"
    "s_mov_b32 exec_hi, s80\n\t"
    "ds_read_b64 v[2:3], v1 offset:576\n\t"
    "s_mov_b32 exec_hi, -1\n\t"
    "v_add_u32 v89, s75, v248\n\t"
    "s_waitcnt lgkmcnt(0)\n\t"
    "v_lshrrev_b32 v88, 15, v2\n\t"
    "v_and_b32 v88, 0x7fe, v88\n\t"
    "v_xad_u32 v88, v88, v249, v89\n\t"
    "v_lshrrev_b32 v90, 15, v3\n\t"
    "v_and_b32 v90, 0x7fe, v90\n\t"
    "v_xad_u32 v90, v90, v249, v89\n\t"
    "v_bfe_u32 v1, v245, 4, 4\n\t"
    "v_lshrrev_b32 v0, 26, v3\n\t"
    "v_cmp_ne_u32 s[20:21], v0, v1\n\t"
    "v_lshrrev_b32 v0, 26, v2\n\t"
    "v_cmp_ne_u32 s[80:81], v0, v1\n\t"
    "v_mov_b32 v1, 0x44\n\t"
    "v_mov_b32 v0, 0x54\n\t"
    "v_cndmask_b32 v0, v1, v0, s[20:21]\n\t"
    "v_addc_co_u32 v0, s[20:21], 0, v0, s[80:81]\n\t"
    "ds_read_u16 v88, v88\n\t"
    "ds_read_u16 v90, v90\n\t"
    "s_lshr_b64 s[80:81], s[76:77], s64\n\t"
    "s_and_b32 s80, s80, 63\n\t"
    "s_lshl_b32 s80, s80, 2\n\t"
    "s_set_gpr_idx_on s80, gpr_idx(DST)\n\t"
    "s_waitcnt lgkmcnt(0)\n\t"
    "v_smfmac_f32_16x16x32_bf16 v[0:3], v[2:3], v[88:91], v0\n\t"
    "s_set_gpr_idx_off\n\t"
    "s_cmp_lg_u32 s58, 0\n\t"
    "s_branch .Lsr0_%=\n\t"
    ".Lsh0_%=:\n\t"
    "s_and_b32 s58, s73, s74\n\t"
    ".Lsh1_%=:\n\t"
    "s_cbranch_scc0 .Lsh3_%=\n\t"
    "s_ff1_i32_b32 s59, s58\n\t"
    "s_bitset0_b32 s58, s59\n\t"
    "s_mul_i32 s64, s59, 6\n\t"
    "s_lshr_b64 s[80:81], s[76:77], s64\n\t"
    "s_and_b32 s80, s80, 63\n\t"
    "s_lshl_b32 s80, s80, 2\n\t"
    "s_mul_i32 s59, s59, 9216\n\t"
    "s_add_u32 s59, s59, 109568\n\t"
    "s_mov_b32 s64, 0\n\t"
    "v_mov_b32 v0, 0xeeee\n\t"
    ".Lsh2_%=:\n\t"
    "v_bfe_u32 v1, v245, 1, 9\n\t"
    "buffer_load_dwordx2 v[2:3], v1, s[48:51], s59 offen sc1\n\t"
    "v_bfe_u32 v1, v245, 8, 2\n\t"
    "v_lshl_add_u32 v1, v1, 4, s64\n\t"
    "v_xad_u32 v1, v1, v249, v248\n\t"
    "v_add_u32 v1, s75, v1\n\t"
    "ds_read_b128 v[88:91], v1\n\t"
    "s_set_gpr_idx_on s80, gpr_idx(DST)\n\t"
    "s_waitcnt vmcnt(0) lgkmcnt(0)\n\t"
    "v_smfmac_f32_16x16x32_bf16 v[0:3], v[2:3], v[88:91], v0\n\t"
    "s_set_gpr_idx_off\n\t"
    "s_add_u32 s59, s59, 512\n\t"
    "s_add_u32 s64, s64, 64\n\t"
    "s_cmp_lt_u32 s64, 1152\n\t"
    "s_cbranch_scc1 .Lsh2_%=\n\t"
    "s_cmp_lg_u32 s58, 0\n\t"
    "s_branch .Lsh1_%=\n\t"
    ".Lsh3_%=:\n\t"
    "s_nop 3\n\t"
    "s_setpc_b64 s[78:79]\n\t"
    ".Lsl0_%=:\n\t"
    "s_or_b32 s58, s71, s73\n\t"
    "s_and_b32 s58, s58, 983055\n\t"
    "s_cbranch_scc0 .Lslr0_%=\n\t"
    "s_mov_b32 s74, 983055\n\t"
    "s_mov_b32 s75, 0\n\t"
    "s_call_b64 s[78:79], .Lslow_%=\n\t"
    "s_branch .Lslr0_%=\n\t"
    ".Lsl1_%=:\n\t"
    "s_or_b32 s58, s71, s73\n\t"
    "s_and_b32 s58, s58, 983055\n\t"
    "s_cbranch_scc0 .Lslr1_%=\n\t"
    "s_mov_b32 s74, 983055\n\t"
    "s_mov_b32 s75, 4608\n\t"
    "s_call_b64 s[78:79], .Lslow_%=\n\t"
    "s_branch .Lslr1_%=\n\t"
    ".Lsl2_%=:\n\t"
    "s_or_b32 s58, s71, s73\n\t"
    "s_and_b32 s58, s58, 983055\n\t"
    "s_cbranch_scc0 .Lslr2_%=\n\t"
    "s_mov_b32 s74, 983055\n\t"
    "s_mov_b32 s75, 9216\n\t"
    "s_call_b64 s[78:79], .Lslow_%=\n\t"
    "s_branch .Lslr2_%=\n\t"
    ".Lsl3_%=:\n\t"
    "s_or_b32 s58, s71, s73\n\t"
    "s_and_b32 s58, s58, 983055\n\t"
    "s_cbranch_scc0 .Lslr3_%=\n\t"
    "s_mov_b32 s74, 983055\n\t"
    "s_mov_b32 s75, 13824\n\t"
    "s_call_b64 s[78:79], .Lslow_%=\n\t"
    "s_branch .Lslr3_%=\n\t"
    ".Lsl4_%=:\n\t"
    "s_or_b32 s58, s71, s73\n\t"
    "s_and_b32 s58, s58, 983055\n\t"
    "s_cbranch_scc0 .Lslr4_%=\n\t"
    "s_mov_b32 s74, 983055\n\t"
    "s_mov_b32 s75, 18432\n\t"
    "s_call_b64 s[78:79], .Lslow_%=\n\t"
    "s_branch .Lslr4_%=\n\t"
    ".Lsl5_%=:\n\t"
    "s_or_b32 s58, s71, s73\n\t"
    "s_and_b32 s58, s58, 32506352\n\t"
    "s_cbranch_scc0 .Lslr5_%=\n\t"
    "s_mov_b32 s74, 32506352\n\t"
    "s_mov_b32 s75, 0\n\t"
    "s_call_b64 s[78:79], .Lslow_%=\n\t"
    "s_branch .Lslr5_%=\n\t"
    ".Lsl6_%=:\n\t"
    "s_or_b32 s58, s71, s73\n\t"
    "s_and_b32 s58, s58, 32506352\n\t"
    "s_cbranch_scc0 .Lslr6_%=\n\t"
    "s_mov_b32 s74, 32506352\n\t"
    "s_mov_b32 s75, 4608\n\t"
    "s_call_b64 s[78:79], .Lslow_%=\n\t"
    "s_branch .Lslr6_%=\n\t"
    ".Lsl7_%=:\n\t"
    "s_or_b32 s58, s71, s73\n\t"
    "s_and_b32 s58, s58, 32506352\n\t"
    "s_cbranch_scc0 .Lslr7_%=\n\t"
    "s_mov_b32 s74, 32506352\n\t"
    "s_mov_b32 s75, 9216\n\t"
    "s_call_b64 s[78:79], .Lslow_%=\n\t"
    "s_branch .Lslr7_%=\n\t"
    ".Lsl8_%=:\n\t"
    "s_or_b32 s58, s71, s73\n\t"
    "s_and_b32 s58, s58, 32506352\n\t"
    "s_cbranch_scc0 .Lslr8_%=\n\t"
    "s_mov_b32 s74, 32506352\n\t"
    "s_mov_b32 s75, 13824\n\t"
    "s_call_b64 s[78:79], .Lslow_%=\n\t"
    "s_branch .Lslr8_%=\n\t"
    ".Lsl9_%=:\n\t"
    "s_or_b32 s58, s71, s73\n\t"
    "s_and_b32 s58, s58, 32506352\n\t"
    "s_cbranch_scc0 .Lslr9_%=\n\t"
    "s_mov_b32 s74, 32506352\n\t"
    "s_mov_b32 s75, 18432\n\t"
    "s_call_b64 s[78:79], .Lslow_%=\n\t"
    "s_branch .Lslr9_%=\n\t"
    ".Lsl10_%=:\n\t"
    "s_mov_b32 s74, 33489407\n\t"
    "s_mov_b32 s75, 23040\n\t"
    "s_call_b64 s[78:79], .Lslow_%=\n\t"
    "s_branch .Lslr10_%=\n\t"
    ".Lsl11_%=:\n\t"
    "s_mov_b32 s74, 33489407\n\t"
    "s_mov_b32 s75, 0\n\t"
    "s_call_b64 s[78:79], .Lslow_%=\n\t"
    "s_branch .Lslr11_%=\n\t"
    ".Lsl12_%=:\n\t"
    "s_mov_b32 s74, 33489407\n\t"
    "s_mov_b32 s75, 4608\n\t"
    "s_call_b64 s[78:79], .Lslow_%=\n\t"
    "s_branch .Lslr12_%=\n\t"
    ".Lsl13_%=:\n\t"
    "s_mov_b32 s74, 33489407\n\t"
    "s_mov_b32 s75, 9216\n\t"
    "s_call_b64 s[78:79], .Lslow_%=\n\t"
    "s_branch .Lslr13_%=\n\t"
    ".Lsl14_%=:\n\t"
    "s_mov_b32 s74, 33489407\n\t"
    "s_mov_b32 s75, 13824\n\t"
    "s_call_b64 s[78:79], .Lslow_%=\n\t"
    "s_branch .Lslr14_%=\n\t"
    ".Lsl15_%=:\n\t"
    "s_mov_b32 s74, 33489407\n\t"
    "s_mov_b32 s75, 18432\n\t"
    "s_call_b64 s[78:79], .Lslow_%=\n\t"
    "s_branch .Lslr15_%=\n\t"
    ".Lsl16_%=:\n\t"
    "s_mov_b32 s74, 33489407\n\t"
    "s_mov_b32 s75, 23040\n\t"
    "s_call_b64 s[78:79], .Lslow_%=\n\t"
    "s_branch .Lslr16_%=\n\t"
    ".Lsl17_%=:\n\t"
    "s_mov_b32 s74, 33489407\n\t"
    "s_mov_b32 s75, 0\n\t"
    "s_call_b64 s[78:79], .Lslow_%=\n\t"
    "s_branch .Lslr17_%=\n\t"
    ".Lsl18_%=:\n\t"
    "s_mov_b32 s74, 33489407\n\t"
    "s_mov_b32 s75, 4608\n\t"
    "s_call_b64 s[78:79], .Lslow_%=\n\t"
    "s_branch .Lslr18_%=\n\t"
    ".Lsl19_%=:\n\t"
    "s_mov_b32 s74, 33489407\n\t"
    "s_mov_b32 s75, 9216\n\t"
    "s_call_b64 s[78:79], .Lslow_%=\n\t"
    "s_branch .Lslr19_%=\n\t"
    ".Lsl20_%=:\n\t"
    "s_or_b32 s58, s71, s73\n\t"
    "s_and_b32 s58, s58, 32506352\n\t"
    "s_cbranch_scc0 .Lslr20_%=\n\t"
    "s_mov_b32 s74, 32506352\n\t"
    "s_mov_b32 s75, 13824\n\t"
    "s_call_b64 s[78:79], .Lslow_%=\n\t"
    "s_branch .Lslr20_%=\n\t"
    ".Lsl21_%=:\n\t"
    "s_or_b32 s58, s71, s73\n\t"
    "s_and_b32 s58, s58, 983055\n\t"
    "s_cbranch_scc0 .Lslr21_%=\n\t"
    "s_mov_b32 s74, 983055\n\t"
    "s_mov_b32 s75, 13824\n\t"
    "s_call_b64 s[78:79], .Lslow_%=\n\t"
    "s_branch .Lslr21_%=\n\t"
    ".Ls2x_%=:\n\t"
    "s_cmp_gt_u32 s73, 12\n\t"
    "s_cbranch_scc1 .Ls2h_%=\n\t"
    "v_lshl_add_u32 v10, v3, 2, s16\n\t"
    "ds_read_b32 v50, v10 offset:128\n\t"
    "ds_read_b32 v51, v10 offset:144\n\t"
    "ds_read_b32 v52, v10 offset:160\n\t"
    "s_mov_b32 s70, 0\n\t"
    "v_lshlrev_b32 v29, 26, v2\n\t"
    "s_waitcnt lgkmcnt(0)\n\t"
    "v_add_u32 v54, v11, v50\n\t"
    "v_add_u32 v55, v11, v51\n\t"
    "v_add_u32 v56, v11, v52\n\t"
    "ds_read_b32 v54, v54\n\t"
    "ds_read_b32 v55, v55\n\t"
    "ds_read_b32 v56, v56\n\t"
    "v_lshl_or_b32 v50, v50, 12, v29\n\t"
    "v_lshl_or_b32 v51, v51, 12, v29\n\t"
    "v_lshl_or_b32 v52, v52, 12, v29\n\t"
    "s_waitcnt lgkmcnt(0)\n\t"
    "v_bfe_u32 v24, v54, 0, 16\n\t"
    "v_and_b32 v25, 0x7fff, v24\n\t"
    "v_cmp_ne_u32 vcc, 0, v25\n\t"
    "v_or_b32 v24, v50, v24\n\t"
    "v_or_b32 v24, 131072, v24\n\t"
    "v_mbcnt_lo_u32_b32 v25, vcc_lo, 0\n\t"
    "v_mbcnt_hi_u32_b32 v25, vcc_hi, v25\n\t"
    "v_add_u32 v25, s70, v25\n\t"
    "v_min_u32 v25, 15, v25\n\t"
    "v_lshl_add_u32 v25, v25, 2, s16\n\t"
    "s_bcnt1_i32_b64 s71, vcc\n\t"
    "s_add_u32 s70, s70, s71\n\t"
    "s_mov_b64 exec, vcc\n\t"
    "ds_write_b32 v25, v24 offset:192\n\t"
    "s_mov_b64 exec, -1\n\t"
    "v_bfe_u32 v24, v54, 16, 16\n\t"
    "v_and_b32 v25, 0x7fff, v24\n\t"
    "v_cmp_ne_u32 vcc, 0, v25\n\t"
    "v_or_b32 v24, v50, v24\n\t"
    "v_or_b32 v24, 196608, v24\n\t"
    "v_mbcnt_lo_u32_b32 v25, vcc_lo, 0\n\t"
    "v_mbcnt_hi_u32_b32 v25, vcc_hi, v25\n\t"
    "v_add_u32 v25, s70, v25\n\t"
    "v_min_u32 v25, 15, v25\n\t"
    "v_lshl_add_u32 v25, v25, 2, s16\n\t"
    "s_bcnt1_i32_b64 s71, vcc\n\t"
    "s_add_u32 s70, s70, s71\n\t"
    "s_mov_b64 exec, vcc\n\t"
    "ds_write_b32 v25, v24 offset:192\n\t"
    "s_mov_b64 exec, -1\n\t"
    "s_cmp_le_u32 s73, 4\n\t"
    "s_cbranch_scc1 .Ls2xe_%=\n\t"
    "v_bfe_u32 v24, v55, 0, 16\n\t"
    "v_and_b32 v25, 0x7fff, v24\n\t"
    "v_cmp_ne_u32 vcc, 0, v25\n\t"
    "v_or_b32 v24, v51, v24\n\t"
    "v_or_b32 v24, 131072, v24\n\t"
    "v_mbcnt_lo_u32_b32 v25, vcc_lo, 0\n\t"
    "v_mbcnt_hi_u32_b32 v25, vcc_hi, v25\n\t"
    "v_add_u32 v25, s70, v25\n\t"
    "v_min_u32 v25, 15, v25\n\t"
    "v_lshl_add_u32 v25, v25, 2, s16\n\t"
    "s_bcnt1_i32_b64 s71, vcc\n\t"
    "s_add_u32 s70, s70, s71\n\t"
    "s_mov_b64 exec, vcc\n\t"
    "ds_write_b32 v25, v24 offset:192\n\t"
    "s_mov_b64 exec, -1\n\t"
    "v_bfe_u32 v24, v55, 16, 16\n\t"
    "v_and_b32 v25, 0x7fff, v24\n\t"
    "v_cmp_ne_u32 vcc, 0, v25\n\t"
    "v_or_b32 v24, v51, v24\n\t"
    "v_or_b32 v24, 196608, v24\n\t"
    "v_mbcnt_lo_u32_b32 v25, vcc_lo, 0\n\t"
    "v_mbcnt_hi_u32_b32 v25, vcc_hi, v25\n\t"
    "v_add_u32 v25, s70, v25\n\t"
    "v_min_u32 v25, 15, v25\n\t"
    "v_lshl_add_u32 v25, v25, 2, s16\n\t"
    "s_bcnt1_i32_b64 s71, vcc\n\t"
    "s_add_u32 s70, s70, s71\n\t"
    "s_mov_b64 exec, vcc\n\t"
    "ds_write_b32 v25, v24 offset:192\n\t"
    "s_mov_b64 exec, -1\n\t"
    "s_cmp_le_u32 s73, 8\n\t"
    "s_cbranch_scc1 .Ls2xe_%=\n\t"
    "v_bfe_u32 v24, v56, 0, 16\n\t"
    "v_and_b32 v25, 0x7fff, v24\n\t"
    "v_cmp_ne_u32 vcc, 0, v25\n\t"
    "v_or_b32 v24, v52, v24\n\t"
    "v_or_b32 v24, 131072, v24\n\t"
    "v_mbcnt_lo_u32_b32 v25, vcc_lo, 0\n\t"
    "v_mbcnt_hi_u32_b32 v25, vcc_hi, v25\n\t"
    "v_add_u32 v25, s70, v25\n\t"
    "v_min_u32 v25, 15, v25\n\t"
    "v_lshl_add_u32 v25, v25, 2, s16\n\t"
    "s_bcnt1_i32_b64 s71, vcc\n\t"
    "s_add_u32 s70, s70, s71\n\t"
    "s_mov_b64 exec, vcc\n\t"
    "ds_write_b32 v25, v24 offset:192\n\t"
    "s_mov_b64 exec, -1\n\t"
    "v_bfe_u32 v24, v56, 16, 16\n\t"
    "v_and_b32 v25, 0x7fff, v24\n\t"
    "v_cmp_ne_u32 vcc, 0, v25\n\t"
    "v_or_b32 v24, v52, v24\n\t"
    "v_or_b32 v24, 196608, v24\n\t"
    "v_mbcnt_lo_u32_b32 v25, vcc_lo, 0\n\t"
    "v_mbcnt_hi_u32_b32 v25, vcc_hi, v25\n\t"
    "v_add_u32 v25, s70, v25\n\t"
    "v_min_u32 v25, 15, v25\n\t"
    "v_lshl_add_u32 v25, v25, 2, s16\n\t"
    "s_bcnt1_i32_b64 s71, vcc\n\t"
    "s_add_u32 s70, s70, s71\n\t"
    "s_mov_b64 exec, vcc\n\t"
    "ds_write_b32 v25, v24 offset:192\n\t"
    "s_mov_b64 exec, -1\n\t"
    ".Ls2xe_%=:\n\t"
    "s_cmp_gt_u32 s70, 12\n\t"
    "s_cbranch_scc1 .Ls2h_%=\n\t"
    "v_add_u32 v10, s16, v92\n\t"
    "ds_read_b128 v[20:23], v10 offset:192\n\t"
    "s_mul_i32 s17, s20, 48\n\t"
    "s_add_u32 s17, s17, 108544\n\t"
    "v_add_u32 v10, s17, v26\n\t"
    "s_cmp_gt_u32 s70, 8\n\t"
    "s_cselect_b32 s22, 3, 1\n\t"
    "s_waitcnt lgkmcnt(0)\n\t"
    "s_mov_b64 exec, 7\n\t"
    "global_store_dwordx4 v10, v[20:23], s[60:61] sc1\n\t"
    "s_mov_b64 exec, -1\n\t"
    "s_branch .Ls2r_%=\n\t"
    ".Ls2h_%=:\n\t"
    "s_waitcnt lgkmcnt(0)\n\t"
    "v_mov_b64 v[12:13], 0\n\t"
    "v_mov_b64 v[14:15], 0\n\t"
    "s_mov_b32 s22, 2\n\t"
    "v_lshl_add_u32 v10, v3, 7, v11\n\t"
    "s_mul_i32 s17, s20, 9216\n\t"
    "s_add_u32 s17, s17, 109568\n\t"
    "s_add_u32 s17, s17, s19\n\t"
    "v_lshl_add_u32 v24, v1, 3, s17\n\t"
    "s_mov_b32 s70, 18\n\t"
    ".Ls2hl_%=:\n\t"
    "ds_read2_b32 v[20:21], v10 offset1:16\n\t"
    "v_add_u32 v10, 512, v10\n\t"
    "s_waitcnt lgkmcnt(0)\n\t"
    "global_store_dwordx2 v24, v[20:21], s[60:61] sc1\n\t"
    "v_add_u32 v24, 512, v24\n\t"
    "s_waitcnt vmcnt(0)\n\t"
    "s_sub_u32 s70, s70, 1\n\t"
    "s_cmp_lg_u32 s70, 0\n\t"
    "s_cbranch_scc1 .Ls2hl_%=\n\t"
    "s_branch .Ls2r_%=\n\t"
    ".Lepq_%=:\n\t"
    "s_add_u32 s96, s60, 868352\n\t"
    "s_addc_u32 s97, s61, 0\n\t"
    "s_mov_b32 m0, 18432\n\t"
    "v_and_b32 v0, 0x3ff, v245\n\t"
    "global_load_lds_dwordx4 v0, s[96:97] sc1\n\t"
    "s_branch .Lepqr_%=\n\t"
    ".Lepoch_%=:\n\t"
    "s_mov_b64 s[94:95], vcc\n\t"
    "s_mov_b32 s58, 0x40000\n\t"
    "v_and_b32 v0, 0x3ff, v245\n\t"
    "v_add_u32 v0, 18432, v0\n\t"
    "s_waitcnt vmcnt(13)\n\t"
    "ds_read_b128 v[0:3], v0\n\t"
    "s_waitcnt lgkmcnt(0)\n\t"
    "s_branch .Lepc_%=\n\t"
    ".Lepl_%=:\n\t"
    "v_and_b32 v0, 0x3ff, v245\n\t"
    "global_load_dwordx4 v[0:3], v0, s[96:97] sc1\n\t"
    "s_waitcnt vmcnt(0)\n\t"
    ".Lepc_%=:\n\t"
    "v_xor_b32 v0, s30, v0\n\t"
    "v_xor_b32 v1, s30, v1\n\t"
    "v_xor_b32 v2, s30, v2\n\t"
    "v_xor_b32 v3, s30, v3\n\t"
    "v_or3_b32 v0, v0, v1, v2\n\t"
    "v_or_b32 v0, v0, v3\n\t"
    "v_cmp_ne_u32 vcc, 0, v0\n\t"
    "s_cbranch_vccz .Lepok_%=\n\t"
    "s_sub_u32 s58, s58, 1\n\t"
    "s_cbranch_scc1 .Lepok_%=\n\t"
    "s_sleep 8\n\t"
    "s_branch .Lepl_%=\n\t"
    ".Lepok_%=:\n\t"
    "v_mov_b32 v2, s30\n\t"
    "v_mov_b32 v3, s31\n\t"
    "v_mov_b32 v1, 917504\n\t"
    "s_mov_b64 exec, 1\n\t"
    "global_store_dwordx2 v1, v[2:3], s[60:61] sc1\n\t"
    "s_mov_b64 exec, -1\n\t"
    "s_mov_b64 vcc, s[94:95]\n\t"
    "s_branch .Lepochr_%=\n\t"
    ".Lprp_%=:\n\t"
    "v_mov_b32 v0, 149504\n\t"
    "ds_read_b128 v[100:103], v0\n\t"
    "ds_read_b128 v[0:3], v0 offset:16\n\t"
    "s_lshr_b32 s76, s62, 3\n\t"
    "s_bfe_u32 s77, s62, 0x10001\n\t"
    "s_lshl1_add_u32 s76, s76, s77\n\t"
    "s_lshl3_add_u32 s77, s76, 876032\n\t"
    "s_xor_b32 s76, s77, s30\n\t"
    "s_waitcnt lgkmcnt(0)\n\t"
    "v_min3_u32 v100, v100, v102, v0\n\t"
    "v_min3_u32 v101, v101, v103, v1\n\t"
    "v_min_u32 v100, v100, v2\n\t"
    "v_min_u32 v101, v101, v3\n\t"
    "v_cmp_lt_u32 vcc, v100, v101\n\t"
    "v_mov_b32 v2, s76\n\t"
    "v_mov_b32 v1, s77\n\t"
    "v_mov_b32 v3, s31\n\t"
    "v_addc_co_u32 v3, vcc, 0, v3, vcc\n\t"
    "s_mov_b64 exec, 1\n\t"
    "global_store_dwordx2 v1, v[2:3], s[60:61] sc1\n\t"
    "s_mov_b64 exec, -1\n\t"
    "s_branch .Lprpr_%=\n\t"
    ".Lprl_%=:\n\t"
    "s_sub_u32 s58, s58, 1\n\t"
    "s_cbranch_scc1 .Lprok_%=\n\t"
    "s_sleep 1\n\t"
    "global_load_dwordx2 v[166:167], v164, s[60:61] sc1\n\t"
    "s_waitcnt vmcnt(0)\n\t"
    "s_branch .Lprc_%=\n\t"
    ".Lpal_%=:\n\t"
    "s_bitcmp1_b32 s62, 3\n\t"
    "s_cselect_b32 s96, s59, s97\n\t"
    "s_xor_b32 s96, s96, 22369621\n\t"
    "v_bfe_u32 v166, s96, v165, 1\n\t"
    "v_cmp_eq_u32 s[96:97], 1, v166\n\t"
    "s_nop 1\n\t"
    "s_lshl_b32 s96, s96, 5\n\t"
    "s_xor_b32 s54, s54, s96\n\t"
    "s_lshr_b32 s58, s62, 2\n\t"
    "s_xor_b32 s58, s58, s62\n\t"
    "s_bitcmp1_b32 s58, 2\n\t"
    "s_cselect_b32 s58, -1, 0\n\t"
    "s_xor_b32 s54, s54, s58\n\t"
    "s_lshl_b32 s59, s53, 2\n\t"
    "s_lshr_b32 s58, s39, s59\n\t"
    "s_and_b32 s58, s58, 15\n\t"
    "s_sub_i32 s58, s58, s53\n\t"
    "s_mul_i32 s58, s58, 4608\n\t"
    "s_add_i32 s58, s58, s38\n\t"
    "s_ashr_i32 s59, s58, 31\n\t"
    "s_sub_u32 s42, s42, s58\n\t"
    "s_add_u32 s40, s40, s58\n\t"
    "s_addc_u32 s41, s41, s59\n\t"
    "s_branch .Lpalr_%=\n\t"
    ".Lpsw_%=:\n\t"
    "s_lshl_b32 s59, s53, 2\n\t"
    "s_lshr_b32 s58, s39, s59\n\t"
    "s_and_b32 s58, s58, 15\n\t"
    "s_sub_i32 s58, s58, s53\n\t"
    "s_mul_i32 s58, s58, 4608\n\t"
    "s_add_u32 s57, s57, s58\n\t"
    "s_ashr_i32 s59, s38, 31\n\t"
    "s_sub_u32 s46, s46, s38\n\t"
    "s_add_u32 s44, s44, s38\n\t"
    "s_addc_u32 s45, s45, s59\n\t"
    "v_bfe_u32 v101, v245, 6, 2\n\t"
    "v_lshlrev_b32 v102, 2, v101\n\t"
    "v_bfe_u32 v102, s39, v102, 4\n\t"
    "v_sub_u32 v102, v102, v101\n\t"
    "s_movk_i32 s58, 4608\n\t"
    "v_mad_i32_i24 v241, v102, s58, v241\n\t"
    "s_branch .Lpswr_%=\n\t"
    ".Ls2l_%=:\n\t"
    "s_sub_u32 s58, s58, 1\n\t"
    "s_cbranch_scc1 .Ls2ok_%=\n\t"
    "s_sleep 1\n\t"
    "global_load_dwordx2 v[90:91], v88, s[60:61] sc1\n\t"
    "s_waitcnt vmcnt(0)\n\t"
    "s_branch .Ls2c_%=\n\t"
    ".Lpc0_%=:\n\t"
    "s_memrealtime s[84:85]\n\t"
    "s_waitcnt lgkmcnt(0)\n\t"
    "s_sub_u32 s89, s84, s24\n\t"
    "s_sub_u32 s89, s89, 14\n\t"
    "s_cmp_lt_i32 s89, s86\n\t"
    "s_cbranch_scc1 .Lpc0_%=\n\t"
    "s_branch .Lpc0r_%=\n\t"
    ".Lpc1_%=:\n\t"
    "s_memrealtime s[84:85]\n\t"
    "s_waitcnt lgkmcnt(0)\n\t"
    "s_sub_u32 s89, s84, s24\n\t"
    "s_sub_u32 s89, s89, 14\n\t"
    "s_cmp_lt_i32 s89, s86\n\t"
    "s_cbranch_scc1 .Lpc1_%=\n\t"
    "s_branch .Lpc1r_%=\n\t"
    ".Lpc2_%=:\n\t"
    "s_memrealtime s[84:85]\n\t"
    "s_waitcnt lgkmcnt(0)\n\t"
    "s_sub_u32 s89, s84, s24\n\t"
    "s_sub_u32 s89, s89, 14\n\t"
    "s_cmp_lt_i32 s89, s86\n\t"
    "s_cbranch_scc1 .Lpc2_%=\n\t"
    "s_branch .Lpc2r_%=\n\t"
    ".Lpc3_%=:\n\t"
    "s_memrealtime s[84:85]\n\t"
    "s_waitcnt lgkmcnt(0)\n\t"
    "s_sub_u32 s89, s84, s24\n\t"
    "s_sub_u32 s89, s89, 14\n\t"
    "s_cmp_lt_i32 s89, s86\n\t"
    "s_cbranch_scc1 .Lpc3_%=\n\t"
    "s_branch .Lpc3r_%=\n\t"
    ".Lpc4_%=:\n\t"
    "s_memrealtime s[84:85]\n\t"
    "s_waitcnt lgkmcnt(0)\n\t"
    "s_sub_u32 s89, s84, s24\n\t"
    "s_sub_u32 s89, s89, 14\n\t"
    "s_cmp_lt_i32 s89, s86\n\t"
    "s_cbranch_scc1 .Lpc4_%=\n\t"
    "s_branch .Lpc4r_%=\n\t"
    ".Lpc5_%=:\n\t"
    "s_memrealtime s[84:85]\n\t"
    "s_waitcnt lgkmcnt(0)\n\t"
    "s_sub_u32 s89, s84, s24\n\t"
    "s_sub_u32 s89, s89, 14\n\t"
    "s_cmp_lt_i32 s89, s86\n\t"
    "s_cbranch_scc1 .Lpc5_%=\n\t"
    "s_branch .Lpc5r_%=\n\t"
    ".Lpc6_%=:\n\t"
    "s_memrealtime s[84:85]\n\t"
    "s_waitcnt lgkmcnt(0)\n\t"
    "s_sub_u32 s89, s84, s24\n\t"
    "s_sub_u32 s89, s89, 14\n\t"
    "s_cmp_lt_i32 s89, s86\n\t"
    "s_cbranch_scc1 .Lpc6_%=\n\t"
    "s_branch .Lpc6r_%=\n\t"
    ".Lpc7_%=:\n\t"
    "s_memrealtime s[84:85]\n\t"
    "s_waitcnt lgkmcnt(0)\n\t"
    "s_sub_u32 s89, s84, s24\n\t"
    "s_sub_u32 s89, s89, 14\n\t"
    "s_cmp_lt_i32 s89, s86\n\t"
    "s_cbranch_scc1 .Lpc7_%=\n\t"
    "s_branch .Lpc7r_%=\n\t"
    ".Lpc8_%=:\n\t"
    "s_memrealtime s[84:85]\n\t"
    "s_waitcnt lgkmcnt(0)\n\t"
    "s_sub_u32 s89, s84, s24\n\t"
    "s_sub_u32 s89, s89, 14\n\t"
    "s_cmp_lt_i32 s89, s86\n\t"
    "s_cbranch_scc1 .Lpc8_%=\n\t"
    "s_branch .Lpc8r_%=\n\t"
    ".Lpc9_%=:\n\t"
    "s_memrealtime s[84:85]\n\t"
    "s_waitcnt lgkmcnt(0)\n\t"
    "s_sub_u32 s89, s84, s24\n\t"
    "s_sub_u32 s89, s89, 14\n\t"
    "s_cmp_lt_i32 s89, s86\n\t"
    "s_cbranch_scc1 .Lpc9_%=\n\t"
    "s_branch .Lpc9r_%=\n\t"
    ".Lpc10_%=:\n\t"
    "s_memrealtime s[84:85]\n\t"
    "s_waitcnt lgkmcnt(0)\n\t"
    "s_sub_u32 s89, s84, s24\n\t"
    "s_sub_u32 s89, s89, 14\n\t"
    "s_cmp_lt_i32 s89, s86\n\t"
    "s_cbranch_scc1 .Lpc10_%=\n\t"
    "s_branch .Lpc10r_%=\n\t"
    ".Lpc11_%=:\n\t"
    "s_memrealtime s[84:85]\n\t"
    "s_waitcnt lgkmcnt(0)\n\t"
    "s_sub_u32 s89, s84, s24\n\t"
    "s_sub_u32 s89, s89, 14\n\t"
    "s_cmp_lt_i32 s89, s86\n\t"
    "s_cbranch_scc1 .Lpc11_%=\n\t"
    "s_branch .Lpc11r_%=\n\t"
    ".Lpc12_%=:\n\t"
    "s_memrealtime s[84:85]\n\t"
    "s_waitcnt lgkmcnt(0)\n\t"
    "s_sub_u32 s89, s84, s24\n\t"
    "s_sub_u32 s89, s89, 14\n\t"
    "s_cmp_lt_i32 s89, s86\n\t"
    "s_cbranch_scc1 .Lpc12_%=\n\t"
    "s_branch .Lpc12r_%=\n\t"
    ".Lpc13_%=:\n\t"
    "s_memrealtime s[84:85]\n\t"
    "s_waitcnt lgkmcnt(0)\n\t"
    "s_sub_u32 s89, s84, s24\n\t"
    "s_sub_u32 s89, s89, 14\n\t"
    "s_cmp_lt_i32 s89, s86\n\t"
    "s_cbranch_scc1 .Lpc13_%=\n\t"
    "s_branch .Lpc13r_%=\n\t"
    ".Lpc14_%=:\n\t"
    "s_memrealtime s[84:85]\n\t"
    "s_waitcnt lgkmcnt(0)\n\t"
    "s_sub_u32 s89, s84, s24\n\t"
    "s_sub_u32 s89, s89, 14\n\t"
    "s_cmp_lt_i32 s89, s86\n\t"
    "s_cbranch_scc1 .Lpc14_%=\n\t"
    "s_branch .Lpc14r_%=\n\t"
    ".p2align 12\n\t"
    ".Lmain_%=:\n\t"
    "ds_read_b128 v[12:15], v244 offset:128\n\t"
    "ds_read_b128 v[4:7], v244 offset:0\n\t"
    "ds_read_b128 v[24:27], v244 offset:64\n\t"
    "v_readlane_b32 s16, v42, 1\n\t"
    "v_readlane_b32 s17, v42, 1\n\t"
    "s_cmp_lt_u32 s53, 2\n\t"
    "s_cselect_b32 s16, s16, s17\n\t"
    "s_sub_i32 s16, s24, s16\n\t"
    "s_max_i32 s16, s16, -150\n\t"
    "s_min_i32 s16, s16, 150\n\t"
    "s_sub_u32 s87, s87, s16\n\t"
    "s_barrier\n\t"
    "s_cmp_lg_u32 s53, 0\n\t"
    "s_cbranch_scc1 .Lnoarr_%=\n\t"
    "s_lshl2_add_u32 s58, s62, 868352\n\t"
    "v_mov_b32 v2, s30\n\t"
    "v_mov_b32 v1, s58\n\t"
    "s_mov_b64 exec, 1\n\t"
    "global_store_dword v1, v2, s[60:61] sc1\n\t"
    "s_mov_b64 exec, -1\n\t"
    ".Lnoarr_%=:\n\t"
    "s_mov_b32 s55, 81920\n\t"
    "buffer_load_dwordx4 v[176:179], v92, s[48:51], s55 offen offset:1024 sc1\n\t"
    "s_mov_b32 s55, 0\n\t"
    "buffer_load_dwordx4 a[0:3], v92, s[48:51], s55 offen offset:0 sc1\n\t"
    "buffer_load_dwordx4 a[4:7], v92, s[48:51], s55 offen offset:1024 sc1\n\t"
    "buffer_load_dwordx4 a[8:11], v92, s[48:51], s55 offen offset:2048 sc1\n\t"
    "buffer_load_dwordx4 a[12:15], v92, s[48:51], s55 offen offset:3072 sc1\n\t"
    "s_mov_b32 s55, 4096\n\t"
    "buffer_load_dwordx4 a[16:19], v92, s[48:51], s55 offen offset:0 sc1\n\t"
    "buffer_load_dwordx4 a[20:23], v92, s[48:51], s55 offen offset:1024 sc1\n\t"
    "ds_read_b128 v[16:19], v244 offset:192\n\t"
    "ds_read_b128 v[36:39], v244 offset:0\n\t"
    "ds_read_b128 v[52:55], v236 offset:4608\n\t"
    "ds_read_b128 v[28:31], v244 offset:128\n\t"
    "ds_read_b128 v[60:63], v236 offset:128\n\t"
    "ds_read_b128 v[40:43], v217 offset:0\n\t"
    "ds_read_b128 v[48:51], v217 offset:4608\n\t"
    "ds_read_b128 v[44:47], v236 offset:0\n\t"
    "ds_read_b128 v[8:11], v244 offset:64\n\t"
    "ds_read_b128 v[20:23], v244 offset:0\n\t"
    "ds_read_b128 v[56:59], v217 offset:128\n\t"
    "ds_read_b128 v[68:71], v236 offset:4736\n\t"
    "s_waitcnt lgkmcnt(4)\n\t"
    "buffer_load_dwordx4 a[24:27], v92, s[48:51], s55 offen offset:2048 sc1\n\t"
    "buffer_load_dwordx4 a[28:31], v92, s[48:51], s55 offen offset:3072 sc1\n\t"
    "s_mov_b32 s55, 81920\n\t"
    "buffer_load_dwordx4 v[180:183], v92, s[48:51], s55 offen offset:2048 sc1\n\t"
    "s_mov_b32 s55, 8192\n\t"
    "buffer_load_dwordx4 a[32:35], v92, s[48:51], s55 offen offset:0 sc1\n\t"
    "buffer_load_dwordx4 a[36:39], v92, s[48:51], s55 offen offset:1024 sc1\n\t"
    "buffer_load_dwordx4 a[40:43], v92, s[48:51], s55 offen offset:2048 sc1\n\t"
    "buffer_load_dwordx4 a[44:47], v92, s[48:51], s55 offen offset:3072 sc1\n\t"
    "s_and_b32 s76, s82, 23\n\t"
    "s_cmp_eq_u32 s76, 16\n\t"
    "s_cbranch_scc1 .Lprp_%=\n\t"
    ".Lprpr_%=:\n\t"
    "s_waitcnt vmcnt(12)\n\t"
    "v_smfmac_f32_16x16x64_bf16 v[4:7], a[0:3], v[40:47], v176\n\t"
    "s_waitcnt lgkmcnt(3)\n\t"
    "ds_read_b128 v[32:35], v244 offset:192\n\t"
    "s_waitcnt vmcnt(11)\n\t"
    "v_smfmac_f32_16x16x64_bf16 v[8:11], a[4:7], v[40:47], v176 abid:1\n\t"
    "s_mov_b32 s55, 12288\n\t"
    "buffer_load_dwordx4 a[48:51], v92, s[48:51], s55 offen offset:0 sc1\n\t"
    "s_waitcnt vmcnt(11)\n\t"
    "v_smfmac_f32_16x16x64_bf16 v[12:15], a[8:11], v[40:47], v177\n\t"
    "ds_read_b128 v[64:67], v217 offset:4736\n\t"
    "s_waitcnt vmcnt(10)\n\t"
    "v_smfmac_f32_16x16x64_bf16 v[16:19], a[12:15], v[40:47], v177 abid:1\n\t"
    "s_waitcnt lgkmcnt(4)\n\t"
    "buffer_load_dwordx4 a[52:55], v92, s[48:51], s55 offen offset:1024 sc1\n\t"
    "v_smfmac_f32_16x16x64_bf16 v[20:23], a[0:3], v[48:55], v176\n\t"
    "buffer_load_dwordx4 a[56:59], v92, s[48:51], s55 offen offset:2048 sc1\n\t"
    "v_smfmac_f32_16x16x64_bf16 v[24:27], a[4:7], v[48:55], v176 abid:1\n\t"
    "buffer_load_dwordx4 a[60:63], v92, s[48:51], s55 offen offset:3072 sc1\n\t"
    "v_smfmac_f32_16x16x64_bf16 v[28:31], a[8:11], v[48:55], v177\n\t"
    "s_waitcnt lgkmcnt(1)\n\t"
    "v_smfmac_f32_16x16x64_bf16 v[32:35], a[12:15], v[48:55], v177 abid:1\n\t"
    "ds_read_b128 v[44:47], v236 offset:256\n\t"
    "ds_read_b128 v[48:51], v217 offset:4864\n\t"
    "s_waitcnt vmcnt(12)\n\t"
    "v_smfmac_f32_16x16x64_bf16 v[4:7], a[16:19], v[56:63], v178\n\t"
    "ds_read_b128 v[40:43], v217 offset:256\n\t"
    "s_waitcnt vmcnt(11)\n\t"
    "v_smfmac_f32_16x16x64_bf16 v[8:11], a[20:23], v[56:63], v178 abid:1\n\t"
    "s_mov_b32 s55, 81920\n\t"
    "buffer_load_dwordx4 v[184:187], v92, s[48:51], s55 offen offset:3072 sc1\n\t"
    "s_waitcnt vmcnt(11)\n\t"
    "v_smfmac_f32_16x16x64_bf16 v[12:15], a[24:27], v[56:63], v179\n\t"
    "s_mov_b32 s55, 16384\n\t"
    "buffer_load_dwordx4 a[64:67], v92, s[48:51], s55 offen offset:0 sc1\n\t"
    "s_waitcnt vmcnt(11)\n\t"
    "v_smfmac_f32_16x16x64_bf16 v[16:19], a[28:31], v[56:63], v179 abid:1\n\t"
    "s_waitcnt lgkmcnt(3)\n\t"
    "buffer_load_dwordx4 a[68:71], v92, s[48:51], s55 offen offset:1024 sc1\n\t"
    "v_smfmac_f32_16x16x64_bf16 v[20:23], a[16:19], v[64:71], v178\n\t"
    "buffer_load_dwordx4 a[72:75], v92, s[48:51], s55 offen offset:2048 sc1\n\t"
    "ds_read_b128 v[52:55], v236 offset:4864\n\t"
    "v_smfmac_f32_16x16x64_bf16 v[24:27], a[20:23], v[64:71], v178 abid:1\n\t"
    "buffer_load_dwordx4 a[76:79], v92, s[48:51], s55 offen offset:3072 sc1\n\t"
    "v_smfmac_f32_16x16x64_bf16 v[28:31], a[24:27], v[64:71], v179\n\t"
    "ds_read_b128 v[56:59], v217 offset:384\n\t"
    "v_smfmac_f32_16x16x64_bf16 v[32:35], a[28:31], v[64:71], v179 abid:1\n\t"
    "s_waitcnt lgkmcnt(2)\n\t"
    "ds_read_b128 v[60:63], v236 offset:384\n\t"
    "s_waitcnt vmcnt(12)\n\t"
    "v_smfmac_f32_16x16x64_bf16 v[4:7], a[32:35], v[40:47], v180\n\t"
    "ds_read_b128 v[64:67], v217 offset:4992\n\t"
    "s_waitcnt vmcnt(11)\n\t"
    "v_smfmac_f32_16x16x64_bf16 v[8:11], a[36:39], v[40:47], v180 abid:1\n\t"
    "s_mov_b32 s55, 20480\n\t"
    "buffer_load_dwordx4 a[80:83], v92, s[48:51], s55 offen offset:0 sc1\n\t"
    "s_waitcnt vmcnt(11)\n\t"
    "v_smfmac_f32_16x16x64_bf16 v[12:15], a[40:43], v[40:47], v181\n\t"
    "ds_read_b128 v[68:71], v236 offset:4992\n\t"
    "s_waitcnt vmcnt(10)\n\t"
    "v_smfmac_f32_16x16x64_bf16 v[16:19], a[44:47], v[40:47], v181 abid:1\n\t"
    "s_waitcnt lgkmcnt(4)\n\t"
    "s_mov_b32 s76, 1060993\n\t"
    "buffer_load_dwordx4 a[84:87], v92, s[48:51], s55 offen offset:1024 sc1\n\t"
    "v_smfmac_f32_16x16x64_bf16 v[20:23], a[32:35], v[48:55], v180\n\t"
    "buffer_load_dwordx4 a[88:91], v92, s[48:51], s55 offen offset:2048 sc1\n\t"
    "v_smfmac_f32_16x16x64_bf16 v[24:27], a[36:39], v[48:55], v180 abid:1\n\t"
    "global_load_dwordx2 v[90:91], v88, s[60:61] sc1\n\t"
    "buffer_load_dwordx4 a[92:95], v92, s[48:51], s55 offen offset:3072 sc1\n\t"
    "v_smfmac_f32_16x16x64_bf16 v[28:31], a[40:43], v[48:55], v181\n\t"
    "ds_read_b128 v[40:43], v217 offset:512\n\t"
    "v_smfmac_f32_16x16x64_bf16 v[32:35], a[44:47], v[48:55], v181 abid:1\n\t"
    "s_waitcnt lgkmcnt(3)\n\t"
    "s_mov_b32 s65, 0\n\t"
    "ds_read_b128 v[44:47], v236 offset:512\n\t"
    "ds_read_b128 v[52:55], v236 offset:5120\n\t"
    "s_waitcnt vmcnt(13)\n\t"
    "v_smfmac_f32_16x16x64_bf16 v[4:7], a[48:51], v[56:63], v182\n\t"
    "ds_read_b128 v[48:51], v217 offset:5120\n\t"
    "s_waitcnt vmcnt(12)\n\t"
    "v_smfmac_f32_16x16x64_bf16 v[8:11], a[52:55], v[56:63], v182 abid:1\n\t"
    "s_mov_b32 s55, 86016\n\t"
    "buffer_load_dwordx4 v[188:191], v92, s[48:51], s55 offen offset:0 sc1\n\t"
    "s_waitcnt vmcnt(12)\n\t"
    "v_smfmac_f32_16x16x64_bf16 v[12:15], a[56:59], v[56:63], v183\n\t"
    "s_mov_b32 s55, 24576\n\t"
    "buffer_load_dwordx4 a[96:99], v92, s[48:51], s55 offen offset:0 sc1\n\t"
    "s_waitcnt vmcnt(12)\n\t"
    "v_smfmac_f32_16x16x64_bf16 v[16:19], a[60:63], v[56:63], v183 abid:1\n\t"
    "s_waitcnt lgkmcnt(4)\n\t"
    "buffer_load_dwordx4 a[100:103], v92, s[48:51], s55 offen offset:1024 sc1\n\t"
    "v_smfmac_f32_16x16x64_bf16 v[20:23], a[48:51], v[64:71], v182\n\t"
    "buffer_load_dwordx4 a[104:107], v92, s[48:51], s55 offen offset:2048 sc1\n\t"
    "v_smfmac_f32_16x16x64_bf16 v[24:27], a[52:55], v[64:71], v182 abid:1\n\t"
    "buffer_load_dwordx4 a[108:111], v92, s[48:51], s55 offen offset:3072 sc1\n\t"
    "v_smfmac_f32_16x16x64_bf16 v[28:31], a[56:59], v[64:71], v183\n\t"
    "ds_read_b128 v[60:63], v236 offset:640\n\t"
    "v_smfmac_f32_16x16x64_bf16 v[32:35], a[60:63], v[64:71], v183 abid:1\n\t"
    "s_waitcnt lgkmcnt(3)\n\t"
    "ds_read_b128 v[56:59], v217 offset:640\n\t"
    "s_waitcnt vmcnt(13)\n\t"
    "v_smfmac_f32_16x16x64_bf16 v[4:7], a[64:67], v[40:47], v184\n\t"
    "ds_read_b128 v[64:67], v217 offset:5248\n\t"
    "s_waitcnt vmcnt(12)\n\t"
    "v_smfmac_f32_16x16x64_bf16 v[8:11], a[68:71], v[40:47], v184 abid:1\n\t"
    "s_mov_b32 s55, 28672\n\t"
    "buffer_load_dwordx4 a[112:115], v92, s[48:51], s55 offen offset:0 sc1\n\t"
    "s_waitcnt vmcnt(12)\n\t"
    "v_smfmac_f32_16x16x64_bf16 v[12:15], a[72:75], v[40:47], v185\n\t"
    "ds_read_b128 v[68:71], v236 offset:5248\n\t"
    "s_waitcnt vmcnt(11)\n\t"
    "v_smfmac_f32_16x16x64_bf16 v[16:19], a[76:79], v[40:47], v185 abid:1\n\t"
    "s_waitcnt lgkmcnt(4)\n\t"
    "s_mov_b32 s77, 0\n\t"
    "buffer_load_dwordx4 a[116:119], v92, s[48:51], s55 offen offset:1024 sc1\n\t"
    "v_smfmac_f32_16x16x64_bf16 v[20:23], a[64:67], v[48:55], v184\n\t"
    "buffer_load_dwordx4 a[120:123], v92, s[48:51], s55 offen offset:2048 sc1\n\t"
    "v_smfmac_f32_16x16x64_bf16 v[24:27], a[68:71], v[48:55], v184 abid:1\n\t"
    "buffer_load_dwordx4 a[124:127], v92, s[48:51], s55 offen offset:3072 sc1\n\t"
    "v_smfmac_f32_16x16x64_bf16 v[28:31], a[72:75], v[48:55], v185\n\t"
    "ds_read_b128 v[44:47], v236 offset:768\n\t"
    "v_smfmac_f32_16x16x64_bf16 v[32:35], a[76:79], v[48:55], v185 abid:1\n\t"
    "s_waitcnt lgkmcnt(3)\n\t"
    "ds_read_b128 v[48:51], v217 offset:5376\n\t"
    "s_waitcnt vmcnt(13)\n\t"
    "v_smfmac_f32_16x16x64_bf16 v[4:7], a[80:83], v[56:63], v186\n\t"
    "s_mov_b32 s55, 86016\n\t"
    "buffer_load_dwordx4 v[192:195], v92, s[48:51], s55 offen offset:1024 sc1\n\t"
    "s_waitcnt vmcnt(13)\n\t"
    "v_smfmac_f32_16x16x64_bf16 v[8:11], a[84:87], v[56:63], v186 abid:1\n\t"
    "ds_read_b128 v[40:43], v217 offset:768\n\t"
    "ds_read_b128 v[52:55], v236 offset:5376\n\t"
    "s_waitcnt vmcnt(12)\n\t"
    "v_smfmac_f32_16x16x64_bf16 v[12:15], a[88:91], v[56:63], v187\n\t"
    "s_waitcnt vmcnt(11)\n\t"
    "s_mov_b32 s58, 0x40000\n\t"
    ".Ls2c_%=:\n\t"
    "v_xor_b32 v1, v90, v89\n\t"
    "v_xor_b32 v2, s31, v91\n\t"
    "v_lshrrev_b32 v3, 2, v2\n\t"
    "v_or_b32 v1, v1, v3\n\t"
    "v_cmp_ne_u32 vcc, 0, v1\n\t"
    "s_cbranch_vccnz .Ls2l_%=\n\t"
    ".Ls2ok_%=:\n\t"
    "s_add_u32 m0, s83, 576\n\t"
    "s_mov_b32 s55, 108544\n\t"
    "s_mov_b32 exec_hi, 0x7fff\n\t"
    "buffer_load_dwordx4 v92, s[48:51], s55 offen sc1 lds\n\t"
    "s_mov_b64 exec, -1\n\t"
    "v_and_b32 v2, 3, v2\n\t"
    "v_and_b32 v3, 1, v2\n\t"
    "v_cmp_eq_u32 vcc, 2, v2\n\t"
    "v_cmp_eq_u32 s[80:81], 1, v3\n\t"
    "v_cmp_eq_u32 s[96:97], 3, v2\n\t"
    "s_nop 1\n\t"
    "s_and_b32 s73, vcc_lo, 0x1ff\n\t"
    "s_and_b32 s71, s80, 0x1ff\n\t"
    "s_and_b32 s96, s96, 0x1ff\n\t"
    "s_lshl_b32 s96, s96, 16\n\t"
    "s_or_b32 s71, s71, s96\n\t"
    "s_mov_b32 s55, 98304\n\t"
    "buffer_load_dwordx4 v[168:171], v92, s[48:51], s55 offen offset:1024 sc1\n\t"
    "buffer_load_dwordx4 v[172:175], v92, s[48:51], s55 offen offset:2048 sc1\n\t"
    "s_mov_b32 s55, 32768\n\t"
    "buffer_load_dwordx4 a[128:131], v92, s[48:51], s55 offen offset:0 sc1\n\t"
    "s_waitcnt vmcnt(14)\n\t"
    "v_smfmac_f32_16x16x64_bf16 v[16:19], a[92:95], v[56:63], v187 abid:1\n\t"
    "s_waitcnt lgkmcnt(4)\n\t"
    "buffer_load_dwordx4 a[132:135], v92, s[48:51], s55 offen offset:1024 sc1\n\t"
    "v_smfmac_f32_16x16x64_bf16 v[20:23], a[80:83], v[64:71], v186\n\t"
    "buffer_load_dwordx4 a[136:139], v92, s[48:51], s55 offen offset:2048 sc1\n\t"
    "v_smfmac_f32_16x16x64_bf16 v[24:27], a[84:87], v[64:71], v186 abid:1\n\t"
    "buffer_load_dwordx4 a[140:143], v92, s[48:51], s55 offen offset:3072 sc1\n\t"
    "v_smfmac_f32_16x16x64_bf16 v[28:31], a[88:91], v[64:71], v187\n\t"
    "ds_read_b128 v[56:59], v217 offset:896\n\t"
    "v_smfmac_f32_16x16x64_bf16 v[32:35], a[92:95], v[64:71], v187 abid:1\n\t"
    "s_waitcnt lgkmcnt(2)\n\t"
    "ds_read_b128 v[60:63], v236 offset:896\n\t"
    "ds_read_b128 v[68:71], v236 offset:5504\n\t"
    "s_waitcnt vmcnt(15)\n\t"
    "v_smfmac_f32_16x16x64_bf16 v[4:7], a[96:99], v[40:47], v188\n\t"
    "s_mov_b32 s55, 36864\n\t"
    "buffer_load_dwordx4 a[144:147], v92, s[48:51], s55 offen offset:0 sc1\n\t"
    "s_waitcnt vmcnt(15)\n\t"
    "v_smfmac_f32_16x16x64_bf16 v[8:11], a[100:103], v[40:47], v188 abid:1\n\t"
    "s_mov_b32 s66, 4718592\n\t"
    "ds_read_b128 v[64:67], v217 offset:5504\n\t"
    "s_waitcnt vmcnt(14)\n\t"
    "v_smfmac_f32_16x16x64_bf16 v[12:15], a[104:107], v[40:47], v189\n\t"
    "s_mov_b32 s55, 86016\n\t"
    "buffer_load_dwordx4 v[196:199], v92, s[48:51], s55 offen offset:2048 sc1\n\t"
    "s_waitcnt vmcnt(14)\n\t"
    "v_smfmac_f32_16x16x64_bf16 v[16:19], a[108:111], v[40:47], v189 abid:1\n\t"
    "s_waitcnt lgkmcnt(4)\n\t"
    "s_mov_b32 s55, 40960\n\t"
    "buffer_load_dwordx4 a[164:167], v92, s[48:51], s55 offen offset:1024 sc1\n\t"
    "ds_read_b128 v[40:43], v217 offset:1024\n\t"
    "v_smfmac_f32_16x16x64_bf16 v[20:23], a[96:99], v[48:55], v188\n\t"
    "ds_read_b128 v[44:47], v236 offset:1024\n\t"
    "v_smfmac_f32_16x16x64_bf16 v[24:27], a[100:103], v[48:55], v188 abid:1\n\t"
    "s_mov_b32 s55, 36864\n\t"
    "buffer_load_dwordx4 a[148:151], v92, s[48:51], s55 offen offset:1024 sc1\n\t"
    "v_smfmac_f32_16x16x64_bf16 v[28:31], a[104:107], v[48:55], v189\n\t"
    "s_mov_b32 s55, 40960\n\t"
    "buffer_load_dwordx4 a[168:171], v92, s[48:51], s55 offen offset:2048 sc1\n\t"
    "s_waitcnt vmcnt(11)\n\t"
    "ds_read_b128 v[218:221], v244 offset:1008\n\t"
    "ds_read_b128 v[222:225], v244 offset:1072\n\t"
    "s_waitcnt vmcnt(9)\n\t"
    "v_add_u32 v173, s88, v173\n\t"
    "v_smfmac_f32_16x16x64_bf16 v[32:35], a[108:111], v[48:55], v189 abid:1\n\t"
    "s_waitcnt lgkmcnt(6)\n\t"
    "ds_read_b128 v[230:233], v244 offset:1200\n\t"
    "v_add_u32 v175, s88, v175\n\t"
    "v_smfmac_f32_16x16x64_bf16 v[4:7], a[112:115], v[56:63], v190\n\t"
    "s_mov_b32 s55, 36864\n\t"
    "buffer_load_dwordx4 a[152:155], v92, s[48:51], s55 offen offset:2048 sc1\n\t"
    "ds_read_b128 v[48:51], v217 offset:5632\n\t"
    "s_mov_b32 s22, 0\n\t"
    "s_mov_b32 s18, 0x55555555\n\t"
    "s_mov_b32 s19, 0x55555555\n\t"
    "s_or_b32 s58, s71, s73\n\t"
    "s_cbranch_scc1 .Lxinit_%=\n\t"
    ".Lxinitr_%=:\n\t"
    "v_smfmac_f32_16x16x64_bf16 v[8:11], a[116:119], v[56:63], v190 abid:1\n\t"
    "s_lshr_b64 s[80:81], s[76:77], s69\n\t"
    "s_mov_b32 s55, 40960\n\t"
    "buffer_load_dwordx4 a[172:175], v92, s[48:51], s55 offen offset:3072 sc1\n\t"
    "ds_read_b64 v[234:235], v244 offset:1264\n\t"
    "ds_write_b32 v175, v174\n\t"
    "v_add_u32 v171, s88, v171\n\t"
    "v_add_u32_sdwa v89, v248, v246 dst_sel:DWORD dst_unused:UNUSED_PAD src0_sel:DWORD src1_sel:WORD_1\n\t"
    "v_add_u32_sdwa v91, v248, v247 dst_sel:DWORD dst_unused:UNUSED_PAD src0_sel:DWORD src1_sel:WORD_1\n\t"
    "v_smfmac_f32_16x16x64_bf16 v[12:15], a[120:123], v[56:63], v191\n\t"
    "s_and_b32 s80, s80, 63\n\t"
    "s_mov_b32 s55, 36864\n\t"
    "buffer_load_dwordx4 a[156:159], v92, s[48:51], s55 offen offset:3072 sc1\n\t"
    "v_add_u32_sdwa v88, v248, v246 dst_sel:DWORD dst_unused:UNUSED_PAD src0_sel:DWORD src1_sel:WORD_0\n\t"
    "v_smfmac_f32_16x16x64_bf16 v[16:19], a[124:127], v[56:63], v191 abid:1\n\t"
    "s_waitcnt lgkmcnt(8)\n\t"
    "s_lshl_b32 s80, s80, 2\n\t"
    "ds_read_b128 v[226:229], v244 offset:1136\n\t"
    "v_add_u32_sdwa v90, v248, v247 dst_sel:DWORD dst_unused:UNUSED_PAD src0_sel:DWORD src1_sel:WORD_0\n\t"
    "v_smfmac_f32_16x16x64_bf16 v[20:23], a[112:115], v[64:71], v190\n\t"
    "s_waitcnt lgkmcnt(5)\n\t"
    "s_mov_b32 s77, 0\n\t"
    "s_mov_b32 s55, 45056\n\t"
    "buffer_load_dwordx4 a[176:179], v92, s[48:51], s55 offen offset:0 sc1\n\t"
    "ds_read_u16 v89, v89 offset:0\n\t"
    "v_xor_b32 v219, v219, v96\n\t"
    "v_xor_b32 v222, v222, v96\n\t"
    "v_xor_b32 v223, v223, v96\n\t"
    "v_add_u32 v169, s88, v169\n\t"
    "v_smfmac_f32_16x16x64_bf16 v[24:27], a[116:119], v[64:71], v190 abid:1\n\t"
    "s_mov_b32 s55, 40960\n\t"
    "buffer_load_dwordx4 a[160:163], v92, s[48:51], s55 offen offset:0 sc1\n\t"
    "ds_read_u16 v88, v88 offset:0\n\t"
    "ds_read_u16 v90, v90 offset:0\n\t"
    "ds_read_u16 v91, v91 offset:0\n\t"
    "ds_write_b32 v169, v168\n\t"
    "v_xor_b32 v221, v221, v96\n\t"
    "v_smfmac_f32_16x16x64_bf16 v[28:31], a[120:123], v[64:71], v191\n\t"
    "s_mov_b32 s76, 1060993\n\t"
    "ds_read_b128 v[52:55], v236 offset:5632\n\t"
    "v_xor_b32 v218, v218, v96\n\t"
    "v_add_u32_sdwa v72, v248, v218 dst_sel:DWORD dst_unused:UNUSED_PAD src0_sel:DWORD src1_sel:WORD_1\n\t"
    "v_add_u32_sdwa v218, v248, v218 dst_sel:DWORD dst_unused:UNUSED_PAD src0_sel:DWORD src1_sel:WORD_0\n\t"
    "v_add_u32_sdwa v73, v248, v219 dst_sel:DWORD dst_unused:UNUSED_PAD src0_sel:DWORD src1_sel:WORD_1\n\t"
    "v_xor_b32 v220, v220, v96\n\t"
    "v_add_u32_sdwa v77, v248, v223 dst_sel:DWORD dst_unused:UNUSED_PAD src0_sel:DWORD src1_sel:WORD_1\n\t"
    "v_smfmac_f32_16x16x64_bf16 v[32:35], a[124:127], v[64:71], v191 abid:1\n\t"
    "s_mov_b32 s55, 45056\n\t"
    "buffer_load_dwordx4 a[180:183], v92, s[48:51], s55 offen offset:1024 sc1\n\t"
    "v_add_u32_sdwa v219, v248, v219 dst_sel:DWORD dst_unused:UNUSED_PAD src0_sel:DWORD src1_sel:WORD_0\n\t"
    "s_waitcnt vmcnt(14)\n\t"
    "v_smfmac_f32_16x16x64_bf16 v[4:7], a[128:131], v[40:47], v192\n\t"
    "buffer_load_dwordx4 a[184:187], v92, s[48:51], s55 offen offset:2048 sc1\n\t"
    "ds_read_b32 v57, v72 offset:4\n\t"
    "ds_write_b32 v171, v170\n\t"
    "v_xor_b32 v224, v224, v96\n\t"
    "s_waitcnt vmcnt(14)\n\t"
    "v_smfmac_f32_16x16x64_bf16 v[8:11], a[132:135], v[40:47], v192 abid:1\n\t"
    "s_waitcnt lgkmcnt(8)\n\t"
    "ds_read_b32 v58, v219 offset:4\n\t"
    "v_add_u32_sdwa v74, v248, v220 dst_sel:DWORD dst_unused:UNUSED_PAD src0_sel:DWORD src1_sel:WORD_1\n\t"
    "v_add_u32_sdwa v75, v248, v221 dst_sel:DWORD dst_unused:UNUSED_PAD src0_sel:DWORD src1_sel:WORD_1\n\t"
    "v_xor_b32 v226, v226, v96\n\t"
    "s_waitcnt vmcnt(13)\n\t"
    "v_smfmac_f32_16x16x64_bf16 v[12:15], a[136:139], v[40:47], v193\n\t"
    "s_mov_b32 s55, 86016\n\t"
    "buffer_load_dwordx4 v[200:203], v92, s[48:51], s55 offen offset:3072 sc1\n\t"
    "ds_read_b32 v56, v218 offset:4\n\t"
    "ds_read_b32 v59, v73 offset:4\n\t"
    "ds_read_b32 v67, v75 offset:4\n\t"
    "ds_write_b32 v173, v172\n\t"
    "v_add_u32_sdwa v220, v248, v220 dst_sel:DWORD dst_unused:UNUSED_PAD src0_sel:DWORD src1_sel:WORD_0\n\t"
    "v_xor_b32 v225, v225, v96\n\t"
    "v_xor_b32 v230, v230, v96\n\t"
    "v_xor_b32 v233, v233, v96\n\t"
    "s_waitcnt vmcnt(13)\n\t"
    "v_smfmac_f32_16x16x64_bf16 v[16:19], a[140:143], v[40:47], v193 abid:1\n\t"
    "s_waitcnt lgkmcnt(9)\n\t"
    "s_mov_b64 vcc, s[18:19]\n\t"
    "s_mov_b32 s55, 49152\n\t"
    "buffer_load_dwordx4 a[204:207], v92, s[48:51], s55 offen offset:3072 sc1\n\t"
    "ds_read_b128 v[80:83], v245 offset:0\n\t"
    "v_add_u32_sdwa v223, v248, v223 dst_sel:DWORD dst_unused:UNUSED_PAD src0_sel:DWORD src1_sel:WORD_0\n\t"
    "v_add_u32_sdwa v78, v248, v224 dst_sel:DWORD dst_unused:UNUSED_PAD src0_sel:DWORD src1_sel:WORD_1\n\t"
    "s_set_gpr_idx_on s80, gpr_idx(SRC2,DST)\n\t"
    "v_mfma_f32_16x16x32_bf16 v[0:3], v[250:253], v[88:91], v[0:3]\n\t"
    "s_set_gpr_idx_off\n\t"
    "s_waitcnt lgkmcnt(8)\n\t"
    "s_lshr_b64 s[80:81], s[76:77], s70\n\t"
    "s_mov_b32 s55, 45056\n\t"
    "buffer_load_dwordx4 a[188:191], v92, s[48:51], s55 offen offset:3072 sc1\n\t"
    "ds_read_b32 v43, v77 offset:4\n\t"
    "v_add_u32_sdwa v221, v248, v221 dst_sel:DWORD dst_unused:UNUSED_PAD src0_sel:DWORD src1_sel:WORD_0\n\t"
    "v_add_u32_sdwa v224, v248, v224 dst_sel:DWORD dst_unused:UNUSED_PAD src0_sel:DWORD src1_sel:WORD_0\n\t"
    "v_add_u32_sdwa v79, v248, v225 dst_sel:DWORD dst_unused:UNUSED_PAD src0_sel:DWORD src1_sel:WORD_1\n\t"
    "v_smfmac_f32_16x16x64_bf16 v[20:23], a[128:131], v[48:55], v192\n\t"
    "s_and_b32 s80, s80, 63\n\t"
    "ds_read_b32 v64, v220 offset:4\n\t"
    "ds_read_b32 v65, v74 offset:4\n\t"
    "ds_read_b32 v66, v221 offset:4\n\t"
    "ds_read_b32 v42, v223 offset:4\n\t"
    "v_add_u32_sdwa v76, v248, v222 dst_sel:DWORD dst_unused:UNUSED_PAD src0_sel:DWORD src1_sel:WORD_1\n\t"
    "v_add_u32_sdwa v222, v248, v222 dst_sel:DWORD dst_unused:UNUSED_PAD src0_sel:DWORD src1_sel:WORD_0\n\t"
    "v_xor_b32 v229, v229, v96\n\t"
    "v_xor_b32 v235, v235, v96\n\t"
    "v_smfmac_f32_16x16x64_bf16 v[24:27], a[132:135], v[48:55], v192 abid:1\n\t"
    "s_lshl_b32 s80, s80, 2\n\t"
    "s_mov_b32 s55, 53248\n\t"
    "buffer_load_dwordx4 a[208:211], v92, s[48:51], s55 offen offset:0 sc1\n\t"
    "ds_read_b32 v40, v222 offset:4\n\t"
    "ds_read_b32 v41, v76 offset:4\n\t"
    "s_set_gpr_idx_on s80, gpr_idx(SRC2,DST)\n\t"
    "v_mfma_f32_16x16x32_bf16 v[0:3], v[252:255], v[88:91], v[0:3]\n\t"
    "s_set_gpr_idx_off\n\t"
    "s_mov_b32 s55, 49152\n\t"
    "buffer_load_dwordx4 a[192:195], v92, s[48:51], s55 offen offset:0 sc1\n\t"
    "v_xor_b32 v227, v227, v96\n\t"
    "s_cmp_lg_u32 s22, 0\n\t"
    "s_cbranch_scc1 .Lsl0_%=\n\t"
    ".Lslr0_%=:\n\t"
    "v_smfmac_f32_16x16x64_bf16 v[28:31], a[136:139], v[48:55], v193\n\t"
    "s_mov_b32 s76, 2126213\n\t"
    "s_mov_b32 s77, 0\n\t"
    "ds_read_b128 v[60:63], v236 offset:9216\n\t"
    "v_add_u32_sdwa v225, v248, v225 dst_sel:DWORD dst_unused:UNUSED_PAD src0_sel:DWORD src1_sel:WORD_0\n\t"
    "v_add_u32_sdwa v88, v248, v246 dst_sel:DWORD dst_unused:UNUSED_PAD src0_sel:DWORD src1_sel:WORD_0\n\t"
    "v_add_u32_sdwa v89, v248, v246 dst_sel:DWORD dst_unused:UNUSED_PAD src0_sel:DWORD src1_sel:WORD_1\n\t"
    "v_add_u32_sdwa v90, v248, v247 dst_sel:DWORD dst_unused:UNUSED_PAD src0_sel:DWORD src1_sel:WORD_0\n\t"
    "v_add_u32_sdwa v91, v248, v247 dst_sel:DWORD dst_unused:UNUSED_PAD src0_sel:DWORD src1_sel:WORD_1\n\t"
    "v_smfmac_f32_16x16x64_bf16 v[32:35], a[140:143], v[48:55], v193 abid:1\n\t"
    "s_waitcnt lgkmcnt(8)\n\t"
    "s_lshr_b64 s[80:81], s[76:77], s69\n\t"
    "s_and_b32 s80, s80, 63\n\t"
    "s_mov_b32 s55, 53248\n\t"
    "buffer_load_dwordx4 a[212:215], v92, s[48:51], s55 offen offset:1024 sc1\n\t"
    "ds_read_u16 v88, v88 offset:4608\n\t"
    "ds_read_u16 v89, v89 offset:4608\n\t"
    "ds_read_u16 v90, v90 offset:4608\n\t"
    "ds_read_u16 v91, v91 offset:4608\n\t"
    "ds_read_b32 v48, v224 offset:4\n\t"
    "ds_read_b32 v49, v78 offset:4\n\t"
    "ds_read_b32 v50, v225 offset:4\n\t"
    "ds_read_b32 v51, v79 offset:4\n\t"
    "v_mfma_f32_16x16x32_bf16 v[4:7], v[80:83], v[56:59], v[4:7]\n\t"
    "s_waitcnt lgkmcnt(4)\n\t"
    "s_lshl_b32 s80, s80, 2\n\t"
    "s_mov_b32 s55, 49152\n\t"
    "buffer_load_dwordx4 a[196:199], v92, s[48:51], s55 offen offset:1024 sc1\n\t"
    "ds_read_b128 v[80:83], v245 offset:1024\n\t"
    "ds_read_b32 v56, v218 offset:4612\n\t"
    "ds_read_b32 v57, v72 offset:4612\n\t"
    "ds_read_b32 v58, v219 offset:4612\n\t"
    "ds_read_b32 v59, v73 offset:4612\n\t"
    "v_xor_b32 v234, v234, v96\n\t"
    "s_set_gpr_idx_on s80, gpr_idx(SRC2,DST)\n\t"
    "v_mfma_f32_16x16x32_bf16 v[0:3], v[250:253], v[88:91], v[0:3]\n\t"
    "s_set_gpr_idx_off\n\t"
    "s_waitcnt lgkmcnt(4)\n\t"
    "s_mov_b32 s76, 2126213\n\t"
    "s_mov_b32 s77, 0\n\t"
    "ds_read_b128 v[52:55], v236 offset:9472\n\t"
    "v_xor_b32 v231, v231, v96\n\t"
    "v_mfma_f32_16x16x32_bf16 v[8:11], v[80:83], v[64:67], v[8:11]\n\t"
    "s_lshr_b64 s[80:81], s[76:77], s70\n\t"
    "s_and_b32 s80, s80, 63\n\t"
    "s_mov_b32 s55, 53248\n\t"
    "buffer_load_dwordx4 a[216:219], v92, s[48:51], s55 offen offset:2048 sc1\n\t"
    "ds_read_b128 v[80:83], v245 offset:2048\n\t"
    "ds_read_b32 v64, v220 offset:4612\n\t"
    "ds_read_b32 v65, v74 offset:4612\n\t"
    "ds_read_b32 v66, v221 offset:4612\n\t"
    "ds_read_b32 v67, v75 offset:4612\n\t"
    "s_waitcnt lgkmcnt(4)\n\t"
    "v_mfma_f32_16x16x32_bf16 v[12:15], v[80:83], v[40:43], v[12:15]\n\t"
    "s_lshl_b32 s80, s80, 2\n\t"
    "s_mov_b32 s55, 49152\n\t"
    "buffer_load_dwordx4 a[200:203], v92, s[48:51], s55 offen offset:2048 sc1\n\t"
    "ds_read_b128 v[80:83], v245 offset:3072\n\t"
    "ds_read_b128 v[68:71], v236 offset:9344\n\t"
    "ds_read_b32 v40, v222 offset:4612\n\t"
    "ds_read_b32 v41, v76 offset:4612\n\t"
    "ds_read_b32 v42, v223 offset:4612\n\t"
    "ds_read_b32 v43, v77 offset:4612\n\t"
    "v_xor_b32 v232, v232, v96\n\t"
    "v_cvt_pk_bf16_f32 v100, v4, v5\n\t"
    "v_cvt_pk_bf16_f32 v101, v6, v7\n\t"
    "s_set_gpr_idx_on s80, gpr_idx(SRC2,DST)\n\t"
    "v_mfma_f32_16x16x32_bf16 v[0:3], v[252:255], v[88:91], v[0:3]\n\t"
    "s_set_gpr_idx_off\n\t"
    "s_waitcnt lgkmcnt(5)\n\t"
    "s_mov_b32 s55, 53248\n\t"
    "buffer_load_dwordx4 a[220:223], v92, s[48:51], s55 offen offset:3072 sc1\n\t"
    "ds_read_b128 v[4:7], v244 offset:64\n\t"
    "s_cmp_lg_u32 s22, 0\n\t"
    "s_cbranch_scc1 .Lsl1_%=\n\t"
    ".Lslr1_%=:\n\t"
    "v_mfma_f32_16x16x32_bf16 v[16:19], v[80:83], v[48:51], v[16:19]\n\t"
    "s_mov_b32 s76, 794697\n\t"
    "ds_read_b128 v[80:83], v245 offset:0\n\t"
    "ds_read_b32 v48, v224 offset:4612\n\t"
    "ds_read_b32 v49, v78 offset:4612\n\t"
    "ds_read_b32 v50, v225 offset:4612\n\t"
    "ds_read_b32 v51, v79 offset:4612\n\t"
    "v_cvt_pk_bf16_f32 v102, v8, v9\n\t"
    "v_cvt_pk_bf16_f32 v103, v10, v11\n\t"
    "s_waitcnt lgkmcnt(4)\n\t"
    "v_mfma_f32_16x16x32_bf16 v[20:23], v[80:83], v[56:59], v[20:23]\n\t"
    "s_mov_b32 s55, 90112\n\t"
    "buffer_load_dwordx4 v[204:207], v92, s[48:51], s55 offen offset:0 sc1\n\t"
    "ds_read_b128 v[80:83], v245 offset:1024\n\t"
    "ds_read_b128 v[56:59], v217 offset:9216\n\t"
    "ds_read_b128 v[8:11], v244 offset:128\n\t"
    "v_xor_b32 v228, v228, v96\n\t"
    "v_cvt_pk_bf16_f32 v96, v12, v13\n\t"
    "v_cvt_pk_bf16_f32 v97, v14, v15\n\t"
    "s_waitcnt lgkmcnt(2)\n\t"
    "v_mfma_f32_16x16x32_bf16 v[24:27], v[80:83], v[64:67], v[24:27]\n\t"
    "s_mov_b32 s55, 57344\n\t"
    "buffer_load_dwordx4 a[224:227], v92, s[48:51], s55 offen offset:0 sc1\n\t"
    "ds_read_b128 v[80:83], v245 offset:2048\n\t"
    "ds_read_b128 v[12:15], v244 offset:192\n\t"
    "ds_read_b128 v[64:67], v217 offset:9344\n\t"
    "s_waitcnt lgkmcnt(2)\n\t"
    "v_mfma_f32_16x16x32_bf16 v[28:31], v[80:83], v[40:43], v[28:31]\n\t"
    "ds_read_b128 v[80:83], v245 offset:3072\n\t"
    "v_cvt_pk_bf16_f32 v98, v16, v17\n\t"
    "v_add_u32_sdwa v89, v248, v246 dst_sel:DWORD dst_unused:UNUSED_PAD src0_sel:DWORD src1_sel:WORD_1\n\t"
    "v_add_u32_sdwa v90, v248, v247 dst_sel:DWORD dst_unused:UNUSED_PAD src0_sel:DWORD src1_sel:WORD_0\n\t"
    "s_waitcnt lgkmcnt(0)\n\t"
    "v_mfma_f32_16x16x32_bf16 v[32:35], v[80:83], v[48:51], v[32:35]\n\t"
    "s_mov_b32 s55, 61440\n\t"
    "buffer_load_dwordx4 a[244:247], v92, s[48:51], s55 offen offset:1024 sc1\n\t"
    "ds_read_b128 v[80:83], v245 offset:0\n\t"
    "ds_read_b32 v40, v218 offset:9220\n\t"
    "ds_read_b32 v41, v72 offset:9220\n\t"
    "ds_read_b32 v42, v219 offset:9220\n\t"
    "ds_read_b32 v43, v73 offset:9220\n\t"
    "v_smfmac_f32_16x16x64_bf16 v[36:39], a[0:3], v[56:63], v176\n\t"
    "ds_read_u16 v89, v89 offset:9216\n\t"
    "v_cndmask_b32_dpp v16, v96, v100, vcc quad_perm:[0,0,2,2] row_mask:0xf bank_mask:0xf\n\t"
    "v_add_u32_sdwa v91, v248, v247 dst_sel:DWORD dst_unused:UNUSED_PAD src0_sel:DWORD src1_sel:WORD_1\n\t"
    "v_smfmac_f32_16x16x64_bf16 v[4:7], a[4:7], v[56:63], v176 abid:1\n\t"
    "s_mov_b32 s55, 57344\n\t"
    "buffer_load_dwordx4 a[228:231], v92, s[48:51], s55 offen offset:1024 sc1\n\t"
    "v_smfmac_f32_16x16x64_bf16 v[8:11], a[8:11], v[56:63], v177\n\t"
    "ds_read_b128 v[48:51], v217 offset:9472\n\t"
    "v_add_u32_sdwa v88, v248, v246 dst_sel:DWORD dst_unused:UNUSED_PAD src0_sel:DWORD src1_sel:WORD_0\n\t"
    "v_smfmac_f32_16x16x64_bf16 v[12:15], a[12:15], v[56:63], v177 abid:1\n\t"
    "ds_read_u16 v88, v88 offset:9216\n\t"
    "ds_read_u16 v90, v90 offset:9216\n\t"
    "ds_read_u16 v91, v91 offset:9216\n\t"
    "v_cvt_pk_bf16_f32 v99, v18, v19\n\t"
    "v_smfmac_f32_16x16x64_bf16 v[36:39], a[16:19], v[64:71], v178\n\t"
    "s_mov_b32 s77, 0\n\t"
    "ds_read_b128 v[56:59], v217 offset:9600\n\t"
    "v_cndmask_b32_dpp v18, v98, v102, vcc quad_perm:[0,0,2,2] row_mask:0xf bank_mask:0xf\n\t"
    "v_smfmac_f32_16x16x64_bf16 v[4:7], a[20:23], v[64:71], v178 abid:1\n\t"
    "s_waitcnt lgkmcnt(6)\n\t"
    "s_mov_b32 s55, 61440\n\t"
    "buffer_load_dwordx4 a[248:251], v92, s[48:51], s55 offen offset:2048 sc1\n\t"
    "v_mfma_f32_16x16x32_bf16 v[36:39], v[80:83], v[40:43], v[36:39]\n\t"
    "s_lshr_b64 s[80:81], s[76:77], s69\n\t"
    "ds_read_b128 v[60:63], v236 offset:9600\n\t"
    "v_cndmask_b32_dpp v17, v97, v101, vcc quad_perm:[0,0,2,2] row_mask:0xf bank_mask:0xf\n\t"
    "v_cndmask_b32_dpp v19, v99, v103, vcc quad_perm:[0,0,2,2] row_mask:0xf bank_mask:0xf\n\t"
    "v_smfmac_f32_16x16x64_bf16 v[8:11], a[24:27], v[64:71], v179\n\t"
    "s_not_b64 vcc, s[18:19]\n\t"
    "ds_read_b32 v41, v74 offset:9220\n\t"
    "ds_read_b32 v43, v75 offset:9220\n\t"
    "v_smfmac_f32_16x16x64_bf16 v[12:15], a[28:31], v[64:71], v179 abid:1\n\t"
    "s_waitcnt lgkmcnt(7)\n\t"
    "s_and_b32 s80, s80, 63\n\t"
    "ds_read_b128 v[80:83], v245 offset:1024\n\t"
    "v_smfmac_f32_16x16x64_bf16 v[36:39], a[32:35], v[48:55], v180\n\t"
    "s_lshl_b32 s80, s80, 2\n\t"
    "buffer_store_dwordx4 v[16:19], v241, s[44:47], s65 offen offset:0 nt\n\t"
    "ds_read_b32 v40, v220 offset:9220\n\t"
    "v_cndmask_b32_dpp v97, v101, v97, vcc quad_perm:[1,1,3,3] row_mask:0xf bank_mask:0xf\n\t"
    "v_cndmask_b32_dpp v99, v103, v99, vcc quad_perm:[1,1,3,3] row_mask:0xf bank_mask:0xf\n\t"
    "v_cvt_pk_bf16_f32 v101, v22, v23\n\t"
    "v_smfmac_f32_16x16x64_bf16 v[4:7], a[36:39], v[48:55], v180 abid:1\n\t"
    "s_waitcnt lgkmcnt(6)\n\t"
    "s_mov_b32 s77, 0\n\t"
    "ds_read_b32 v42, v221 offset:9220\n\t"
    "s_set_gpr_idx_on s80, gpr_idx(SRC2,DST)\n\t"
    "v_mfma_f32_16x16x32_bf16 v[0:3], v[250:253], v[88:91], v[0:3]\n\t"
    "s_set_gpr_idx_off\n\t"
    "ds_read_b128 v[68:71], v236 offset:9728\n\t"
    "v_smfmac_f32_16x16x64_bf16 v[8:11], a[40:43], v[48:55], v181\n\t"
    "ds_read_b128 v[64:67], v217 offset:9728\n\t"
    "v_cndmask_b32_dpp v98, v102, v98, vcc quad_perm:[1,1,3,3] row_mask:0xf bank_mask:0xf\n\t"
    "v_smfmac_f32_16x16x64_bf16 v[12:15], a[44:47], v[48:55], v181 abid:1\n\t"
    "s_waitcnt lgkmcnt(7)\n\t"
    "s_mov_b32 s76, 794697\n\t"
    "global_load_dwordx2 v[166:167], v164, s[60:61] sc1\n\t"
    "v_smfmac_f32_16x16x64_bf16 v[36:39], a[48:51], v[56:63], v182\n\t"
    "s_lshr_b64 s[80:81], s[76:77], s70\n\t"
    "ds_read_b128 v[48:51], v217 offset:9856\n\t"
    "v_cvt_pk_bf16_f32 v102, v24, v25\n\t"
    "v_smfmac_f32_16x16x64_bf16 v[4:7], a[52:55], v[56:63], v182 abid:1\n\t"
    "ds_read_b128 v[52:55], v236 offset:9856\n\t"
    "v_cndmask_b32_dpp v96, v100, v96, vcc quad_perm:[1,1,3,3] row_mask:0xf bank_mask:0xf\n\t"
    "v_smfmac_f32_16x16x64_bf16 v[8:11], a[56:59], v[56:63], v183\n\t"
    "s_waitcnt lgkmcnt(4)\n\t"
    "s_and_b32 s80, s80, 63\n\t"
    "ds_read_b128 v[16:19], v244 offset:192\n\t"
    "v_cvt_pk_bf16_f32 v100, v20, v21\n\t"
    "v_mfma_f32_16x16x32_bf16 v[4:7], v[80:83], v[40:43], v[4:7]\n\t"
    "buffer_store_dwordx4 v[96:99], v241, s[44:47], s65 offen offset:1152 nt\n\t"
    "ds_read_b128 v[80:83], v245 offset:2048\n\t"
    "ds_read_b32 v41, v76 offset:9220\n\t"
    "v_cvt_pk_bf16_f32 v103, v26, v27\n\t"
    "v_smfmac_f32_16x16x64_bf16 v[12:15], a[60:63], v[56:63], v183 abid:1\n\t"
    "s_waitcnt lgkmcnt(5)\n\t"
    "ds_read_b128 v[56:59], v217 offset:9984\n\t"
    "v_cvt_pk_bf16_f32 v96, v28, v29\n\t"
    "v_cvt_pk_bf16_f32 v98, v32, v33\n\t"
    "v_smfmac_f32_16x16x64_bf16 v[36:39], a[64:67], v[64:71], v184\n\t"
    "ds_read_b32 v43, v77 offset:9220\n\t"
    "v_cvt_pk_bf16_f32 v99, v34, v35\n\t"
    "v_smfmac_f32_16x16x64_bf16 v[4:7], a[68:71], v[64:71], v184 abid:1\n\t"
    "s_mov_b64 vcc, s[18:19]\n\t"
    "ds_read_b128 v[60:63], v236 offset:9984\n\t"
    "v_cvt_pk_bf16_f32 v97, v30, v31\n\t"
    "v_cndmask_b32_dpp v32, v96, v100, vcc quad_perm:[0,0,2,2] row_mask:0xf bank_mask:0xf\n\t"
    "v_smfmac_f32_16x16x64_bf16 v[8:11], a[72:75], v[64:71], v185\n\t"
    "s_lshl_b32 s80, s80, 2\n\t"
    "s_mov_b32 s55, 57344\n\t"
    "buffer_load_dwordx4 a[232:235], v92, s[48:51], s55 offen offset:2048 sc1\n\t"
    "ds_read_b32 v40, v222 offset:9220\n\t"
    "ds_read_b32 v42, v223 offset:9220\n\t"
    "s_set_gpr_idx_on s80, gpr_idx(SRC2,DST)\n\t"
    "v_mfma_f32_16x16x32_bf16 v[0:3], v[252:255], v[88:91], v[0:3]\n\t"
    "s_set_gpr_idx_off\n\t"
    "s_cmp_lg_u32 s22, 0\n\t"
    "s_cbranch_scc1 .Lsl2_%=\n\t"
    ".Lslr2_%=:\n\t"
    "v_smfmac_f32_16x16x64_bf16 v[12:15], a[76:79], v[64:71], v185 abid:1\n\t"
    "s_waitcnt lgkmcnt(8)\n\t"
    "ds_read_b128 v[64:67], v217 offset:10112\n\t"
    "v_cndmask_b32_dpp v33, v97, v101, vcc quad_perm:[0,0,2,2] row_mask:0xf bank_mask:0xf\n\t"
    "v_cndmask_b32_dpp v34, v98, v102, vcc quad_perm:[0,0,2,2] row_mask:0xf bank_mask:0xf\n\t"
    "v_smfmac_f32_16x16x64_bf16 v[36:39], a[80:83], v[48:55], v186\n\t"
    "ds_read_b128 v[24:27], v244 offset:64\n\t"
    "v_cndmask_b32_dpp v35, v99, v103, vcc quad_perm:[0,0,2,2] row_mask:0xf bank_mask:0xf\n\t"
    "v_smfmac_f32_16x16x64_bf16 v[4:7], a[84:87], v[48:55], v186 abid:1\n\t"
    "s_not_b64 vcc, s[18:19]\n\t"
    "buffer_store_dwordx4 v[32:35], v241, s[44:47], s66 offen offset:0 nt\n\t"
    "ds_read_b128 v[28:31], v244 offset:128\n\t"
    "v_cndmask_b32_dpp v96, v100, v96, vcc quad_perm:[1,1,3,3] row_mask:0xf bank_mask:0xf\n\t"
    "v_cndmask_b32_dpp v97, v101, v97, vcc quad_perm:[1,1,3,3] row_mask:0xf bank_mask:0xf\n\t"
    "v_cndmask_b32_dpp v98, v102, v98, vcc quad_perm:[1,1,3,3] row_mask:0xf bank_mask:0xf\n\t"
    "v_smfmac_f32_16x16x64_bf16 v[8:11], a[88:91], v[48:55], v187\n\t"
    "ds_read_b128 v[68:71], v236 offset:10112\n\t"
    "v_cndmask_b32_dpp v99, v103, v99, vcc quad_perm:[1,1,3,3] row_mask:0xf bank_mask:0xf\n\t"
    "v_smfmac_f32_16x16x64_bf16 v[12:15], a[92:95], v[48:55], v187 abid:1\n\t"
    "s_waitcnt lgkmcnt(4)\n\t"
    "ds_read_b128 v[48:51], v217 offset:10240\n\t"
    "ds_read_b128 v[52:55], v236 offset:10240\n\t"
    "v_mfma_f32_16x16x32_bf16 v[8:11], v[80:83], v[40:43], v[8:11]\n\t"
    "ds_read_b32 v40, v224 offset:9220\n\t"
    "ds_read_b32 v41, v78 offset:9220\n\t"
    "v_smfmac_f32_16x16x64_bf16 v[36:39], a[96:99], v[56:63], v188\n\t"
    "s_mov_b32 s77, 0\n\t"
    "s_mov_b32 s55, 61440\n\t"
    "buffer_load_dwordx4 a[252:255], v92, s[48:51], s55 offen offset:3072 sc1\n\t"
    "v_smfmac_f32_16x16x64_bf16 v[4:7], a[100:103], v[56:63], v188 abid:1\n\t"
    "s_mov_b64 vcc, s[18:19]\n\t"
    "ds_read_b32 v42, v225 offset:9220\n\t"
    "ds_read_b32 v43, v79 offset:9220\n\t"
    "v_smfmac_f32_16x16x64_bf16 v[8:11], a[104:107], v[56:63], v189\n\t"
    "ds_read_b128 v[80:83], v245 offset:3072\n\t"
    "v_add_u32_sdwa v89, v248, v246 dst_sel:DWORD dst_unused:UNUSED_PAD src0_sel:DWORD src1_sel:WORD_1\n\t"
    "v_smfmac_f32_16x16x64_bf16 v[12:15], a[108:111], v[56:63], v189 abid:1\n\t"
    "s_waitcnt lgkmcnt(7)\n\t"
    "ds_read_b128 v[60:63], v236 offset:13824\n\t"
    "v_add_u32_sdwa v91, v248, v247 dst_sel:DWORD dst_unused:UNUSED_PAD src0_sel:DWORD src1_sel:WORD_1\n\t"
    "v_smfmac_f32_16x16x64_bf16 v[36:39], a[112:115], v[64:71], v190\n\t"
    "s_mov_b32 s76, 1077637\n\t"
    "buffer_store_dwordx4 v[96:99], v241, s[44:47], s66 offen offset:1152 nt\n\t"
    "ds_read_u16 v91, v91 offset:13824\n\t"
    "v_smfmac_f32_16x16x64_bf16 v[4:7], a[116:119], v[64:71], v190 abid:1\n\t"
    "ds_read_b128 v[56:59], v217 offset:13824\n\t"
    "v_add_u32_sdwa v88, v248, v246 dst_sel:DWORD dst_unused:UNUSED_PAD src0_sel:DWORD src1_sel:WORD_0\n\t"
    "v_smfmac_f32_16x16x64_bf16 v[8:11], a[120:123], v[64:71], v191\n\t"
    "s_mov_b32 s67, 9437184\n\t"
    "ds_read_b128 v[20:23], v244 offset:0\n\t"
    "v_add_u32_sdwa v90, v248, v247 dst_sel:DWORD dst_unused:UNUSED_PAD src0_sel:DWORD src1_sel:WORD_0\n\t"
    "v_smfmac_f32_16x16x64_bf16 v[12:15], a[124:127], v[64:71], v191 abid:1\n\t"
    "s_waitcnt lgkmcnt(9)\n\t"
    "ds_read_b128 v[64:67], v217 offset:13952\n\t"
    "ds_read_b128 v[68:71], v236 offset:13952\n\t"
    "v_smfmac_f32_16x16x64_bf16 v[36:39], a[128:131], v[48:55], v192\n\t"
    "s_waitcnt lgkmcnt(6)\n\t"
    "s_lshr_b64 s[80:81], s[76:77], s69\n\t"
    "s_mov_b32 s55, 57344\n\t"
    "buffer_load_dwordx4 a[236:239], v92, s[48:51], s55 offen offset:3072 sc1\n\t"
    "v_mfma_f32_16x16x32_bf16 v[12:15], v[80:83], v[40:43], v[12:15]\n\t"
    "s_and_b32 s80, s80, 63\n\t"
    "ds_read_b32 v40, v218 offset:13828\n\t"
    "ds_read_b32 v42, v219 offset:13828\n\t"
    "v_smfmac_f32_16x16x64_bf16 v[4:7], a[132:135], v[48:55], v192 abid:1\n\t"
    "ds_read_b128 v[80:83], v245 offset:0\n\t"
    "v_add_u32_sdwa v84, v248, v226 dst_sel:DWORD dst_unused:UNUSED_PAD src0_sel:DWORD src1_sel:WORD_1\n\t"
    "v_smfmac_f32_16x16x64_bf16 v[8:11], a[136:139], v[48:55], v193\n\t"
    "ds_read_b32 v41, v72 offset:13828\n\t"
    "v_cvt_pk_bf16_f32 v100, v36, v37\n\t"
    "v_smfmac_f32_16x16x64_bf16 v[12:15], a[140:143], v[48:55], v193 abid:1\n\t"
    "s_waitcnt lgkmcnt(6)\n\t"
    "s_mov_b32 s55, 90112\n\t"
    "buffer_load_dwordx4 v[208:211], v92, s[48:51], s55 offen offset:1024 sc1\n\t"
    "ds_read_b32 v43, v73 offset:13828\n\t"
    "v_smfmac_f32_16x16x64_bf16 v[20:23], a[0:3], v[56:63], v176\n\t"
    "s_mov_b32 s65, 14155776\n\t"
    "ds_read_b128 v[52:55], v236 offset:14080\n\t"
    "v_cvt_pk_bf16_f32 v102, v4, v5\n\t"
    "v_smfmac_f32_16x16x64_bf16 v[24:27], a[4:7], v[56:63], v176 abid:1\n\t"
    "ds_read_u16 v88, v88 offset:13824\n\t"
    "ds_read_u16 v90, v90 offset:13824\n\t"
    "v_smfmac_f32_16x16x64_bf16 v[28:31], a[8:11], v[56:63], v177\n\t"
    "ds_read_b128 v[48:51], v217 offset:14080\n\t"
    "v_cvt_pk_bf16_f32 v96, v8, v9\n\t"
    "v_cvt_pk_bf16_f32 v99, v14, v15\n\t"
    "v_smfmac_f32_16x16x64_bf16 v[16:19], a[12:15], v[56:63], v177 abid:1\n\t"
    "s_waitcnt lgkmcnt(9)\n\t"
    "s_mov_b32 s55, 65536\n\t"
    "buffer_load_dwordx4 v[108:111], v92, s[48:51], s55 offen offset:0 sc1\n\t"
    "ds_read_u16 v89, v89 offset:13824\n\t"
    "v_cvt_pk_bf16_f32 v101, v38, v39\n\t"
    "v_smfmac_f32_16x16x64_bf16 v[20:23], a[16:19], v[64:71], v178\n\t"
    "ds_read_b128 v[60:63], v236 offset:14208\n\t"
    "v_cvt_pk_bf16_f32 v103, v6, v7\n\t"
    "v_cvt_pk_bf16_f32 v97, v10, v11\n\t"
    "v_smfmac_f32_16x16x64_bf16 v[24:27], a[20:23], v[64:71], v178 abid:1\n\t"
    "s_waitcnt lgkmcnt(6)\n\t"
    "s_mov_b32 s76, 1077637\n\t"
    "ds_read_b128 v[56:59], v217 offset:14208\n\t"
    "v_cvt_pk_bf16_f32 v98, v12, v13\n\t"
    "v_mfma_f32_16x16x32_bf16 v[20:23], v[80:83], v[40:43], v[20:23]\n\t"
    "ds_read_b32 v41, v74 offset:13828\n\t"
    "v_cndmask_b32_dpp v13, v97, v101, vcc quad_perm:[0,0,2,2] row_mask:0xf bank_mask:0xf\n\t"
    "v_smfmac_f32_16x16x64_bf16 v[28:31], a[24:27], v[64:71], v179\n\t"
    "s_mov_b32 s55, 61440\n\t"
    "buffer_load_dwordx4 a[240:243], v92, s[48:51], s55 offen offset:0 sc1\n\t"
    "ds_read_b32 v40, v220 offset:13828\n\t"
    "v_smfmac_f32_16x16x64_bf16 v[16:19], a[28:31], v[64:71], v179 abid:1\n\t"
    "s_waitcnt lgkmcnt(5)\n\t"
    "s_lshl_b32 s80, s80, 2\n\t"
    "ds_read_b32 v43, v75 offset:13828\n\t"
    "v_cndmask_b32_dpp v15, v99, v103, vcc quad_perm:[0,0,2,2] row_mask:0xf bank_mask:0xf\n\t"
    "v_smfmac_f32_16x16x64_bf16 v[20:23], a[32:35], v[48:55], v180\n\t"
    "s_mov_b32 s77, 0\n\t"
    "ds_read_b128 v[80:83], v245 offset:1024\n\t"
    "v_cndmask_b32_dpp v14, v98, v102, vcc quad_perm:[0,0,2,2] row_mask:0xf bank_mask:0xf\n\t"
    "v_smfmac_f32_16x16x64_bf16 v[24:27], a[36:39], v[48:55], v180 abid:1\n\t"
    "s_waitcnt lgkmcnt(6)\n\t"
    "ds_read_b32 v42, v221 offset:13828\n\t"
    "v_cndmask_b32_dpp v12, v96, v100, vcc quad_perm:[0,0,2,2] row_mask:0xf bank_mask:0xf\n\t"
    "s_set_gpr_idx_on s80, gpr_idx(SRC2,DST)\n\t"
    "v_mfma_f32_16x16x32_bf16 v[0:3], v[250:253], v[88:91], v[0:3]\n\t"
    "s_set_gpr_idx_off\n\t"
    "s_not_b64 vcc, s[18:19]\n\t"
    "buffer_store_dwordx4 v[12:15], v241, s[44:47], s67 offen offset:0 nt\n\t"
    "ds_read_b128 v[68:71], v236 offset:14336\n\t"
    "v_cndmask_b32_dpp v97, v101, v97, vcc quad_perm:[1,1,3,3] row_mask:0xf bank_mask:0xf\n\t"
    "s_waitcnt vmcnt(10)\n\t"
    "s_mov_b32 s58, 0x40000\n\t"
    ".Lprc_%=:\n\t"
    "v_bitop3_b32 v166, v166, v164, s30 bitop3:0x96\n\t"
    "v_xor_b32 v167, s31, v167\n\t"
    "v_and_or_b32 v166, v167, -2, v166\n\t"
    "v_cmp_ne_u32 s[96:97], 0, v166\n\t"
    "s_nop 1\n\t"
    "s_cmp_lg_u64 s[96:97], 0\n\t"
    "s_cbranch_scc1 .Lprl_%=\n\t"
    ".Lprok_%=:\n\t"
    "v_cmp_eq_u32 s[96:97], 1, v167\n\t"
    "s_nop 1\n\t"
    "s_and_b32 s58, s97, 0x7fffff\n\t"
    "s_bcnt1_i32_b32 s58, s58\n\t"
    "s_cmp_ge_u32 s58, 12\n\t"
    "s_cselect_b32 s64, 0, 0x1fffff\n\t"
    "s_and_b32 s58, s97, 0xff800000\n\t"
    "s_bcnt1_i32_b32 s58, s58\n\t"
    "s_cmp_ge_u32 s58, 5\n\t"
    "s_cselect_b32 s58, 0, 0x1e00000\n\t"
    "s_or_b32 s59, s64, s58\n\t"
    "s_and_b32 s58, s96, 0x7fffff\n\t"
    "s_bcnt1_i32_b32 s58, s58\n\t"
    "s_cmp_ge_u32 s58, 12\n\t"
    "s_cselect_b32 s64, 0, 0x1fffff\n\t"
    "s_and_b32 s58, s96, 0xff800000\n\t"
    "s_bcnt1_i32_b32 s58, s58\n\t"
    "s_cmp_ge_u32 s58, 5\n\t"
    "s_cselect_b32 s58, 0, 0x1e00000\n\t"
    "s_or_b32 s97, s64, s58\n\t"
    "s_xor_b32 s58, s97, s59\n\t"
    "s_bitcmp1_b32 s58, 0\n\t"
    "s_cselect_b32 s99, 0, 7\n\t"
    "s_cbranch_scc0 .Lpal_%=\n\t"
    ".Lpalr_%=:\n\t"
    "v_smfmac_f32_16x16x64_bf16 v[28:31], a[40:43], v[48:55], v181\n\t"
    "s_lshr_b64 s[80:81], s[76:77], s70\n\t"
    "ds_read_b128 v[64:67], v217 offset:14336\n\t"
    "v_cndmask_b32_dpp v99, v103, v99, vcc quad_perm:[1,1,3,3] row_mask:0xf bank_mask:0xf\n\t"
    "v_smfmac_f32_16x16x64_bf16 v[16:19], a[44:47], v[48:55], v181 abid:1\n\t"
    "s_waitcnt lgkmcnt(7)\n\t"
    "ds_read_b128 v[52:55], v236 offset:14464\n\t"
    "v_add_u32_sdwa v105, v248, v231 dst_sel:DWORD dst_unused:UNUSED_PAD src0_sel:DWORD src1_sel:WORD_1\n\t"
    "v_smfmac_f32_16x16x64_bf16 v[20:23], a[48:51], v[56:63], v182\n\t"
    "ds_read_b128 v[8:11], v244 offset:192\n\t"
    "v_smfmac_f32_16x16x64_bf16 v[24:27], a[52:55], v[56:63], v182 abid:1\n\t"
    "s_mov_b32 s55, 65536\n\t"
    "buffer_load_dwordx4 v[112:115], v92, s[48:51], s55 offen offset:1024 sc1\n\t"
    "ds_read_b128 v[48:51], v217 offset:14464\n\t"
    "v_smfmac_f32_16x16x64_bf16 v[28:31], a[56:59], v[56:63], v183\n\t"
    "s_waitcnt lgkmcnt(5)\n\t"
    "s_and_b32 s80, s80, 63\n\t"
    "ds_read_b128 v[4:7], v244 offset:128\n\t"
    "v_add_u32_sdwa v87, v248, v229 dst_sel:DWORD dst_unused:UNUSED_PAD src0_sel:DWORD src1_sel:WORD_1\n\t"
    "v_mfma_f32_16x16x32_bf16 v[24:27], v[80:83], v[40:43], v[24:27]\n\t"
    "ds_read_b32 v40, v222 offset:13828\n\t"
    "v_cndmask_b32_dpp v96, v100, v96, vcc quad_perm:[1,1,3,3] row_mask:0xf bank_mask:0xf\n\t"
    "v_smfmac_f32_16x16x64_bf16 v[16:19], a[60:63], v[56:63], v183 abid:1\n\t"
    "s_waitcnt lgkmcnt(5)\n\t"
    "ds_read_b32 v42, v223 offset:13828\n\t"
    "v_cndmask_b32_dpp v98, v102, v98, vcc quad_perm:[1,1,3,3] row_mask:0xf bank_mask:0xf\n\t"
    "v_smfmac_f32_16x16x64_bf16 v[20:23], a[64:67], v[64:71], v184\n\t"
    "buffer_store_dwordx4 v[96:99], v241, s[44:47], s67 offen offset:1152 nt\n\t"
    "ds_read_b128 v[80:83], v245 offset:2048\n\t"
    "ds_read_b32 v41, v76 offset:13828\n\t"
    "v_smfmac_f32_16x16x64_bf16 v[24:27], a[68:71], v[64:71], v184 abid:1\n\t"
    "ds_read_b128 v[56:59], v217 offset:14592\n\t"
    "v_add_u32_sdwa v86, v248, v228 dst_sel:DWORD dst_unused:UNUSED_PAD src0_sel:DWORD src1_sel:WORD_1\n\t"
    "v_smfmac_f32_16x16x64_bf16 v[28:31], a[72:75], v[64:71], v185\n\t"
    "s_lshl_b32 s80, s80, 2\n\t"
    "ds_read_b32 v43, v77 offset:13828\n\t"
    "s_set_gpr_idx_on s80, gpr_idx(SRC2,DST)\n\t"
    "v_mfma_f32_16x16x32_bf16 v[0:3], v[252:255], v[88:91], v[0:3]\n\t"
    "s_set_gpr_idx_off\n\t"
    "ds_read_b128 v[60:63], v236 offset:14592\n\t"
    "s_cmp_lg_u32 s22, 0\n\t"
    "s_cbranch_scc1 .Lsl3_%=\n\t"
    ".Lslr3_%=:\n\t"
    "v_smfmac_f32_16x16x64_bf16 v[16:19], a[76:79], v[64:71], v185 abid:1\n\t"
    "s_waitcnt lgkmcnt(8)\n\t"
    "buffer_load_dwordx4 v[116:119], v92, s[48:51], s55 offen offset:2048 sc1\n\t"
    "v_smfmac_f32_16x16x64_bf16 v[20:23], a[80:83], v[48:55], v186\n\t"
    "ds_read_b128 v[68:71], v236 offset:14720\n\t"
    "ds_read_b128 v[32:35], v244 offset:0\n\t"
    "v_smfmac_f32_16x16x64_bf16 v[24:27], a[84:87], v[48:55], v186 abid:1\n\t"
    "ds_read_b128 v[64:67], v217 offset:14720\n\t"
    "v_add_u32_sdwa v90, v248, v247 dst_sel:DWORD dst_unused:UNUSED_PAD src0_sel:DWORD src1_sel:WORD_0\n\t"
    "v_smfmac_f32_16x16x64_bf16 v[28:31], a[88:91], v[48:55], v187\n\t"
    "ds_read_b128 v[36:39], v244 offset:64\n\t"
    "v_add_u32_sdwa v88, v248, v246 dst_sel:DWORD dst_unused:UNUSED_PAD src0_sel:DWORD src1_sel:WORD_0\n\t"
    "v_smfmac_f32_16x16x64_bf16 v[16:19], a[92:95], v[48:55], v187 abid:1\n\t"
    "s_waitcnt lgkmcnt(5)\n\t"
    "s_mov_b32 s55, 69632\n\t"
    "buffer_load_dwordx4 v[136:139], v92, s[48:51], s55 offen offset:3072 sc1\n\t"
    "v_mfma_f32_16x16x32_bf16 v[28:31], v[80:83], v[40:43], v[28:31]\n\t"
    "s_waitcnt lgkmcnt(4)\n\t"
    "s_barrier\n\t"
    "ds_read_b128 v[80:83], v245 offset:3072\n\t"
    "ds_read_b32 v41, v78 offset:13828\n\t"
    "v_smfmac_f32_16x16x64_bf16 v[20:23], a[96:99], v[56:63], v188\n\t"
    "ds_read_b128 v[48:51], v217 offset:14848\n\t"
    "ds_read_b128 v[52:55], v236 offset:14848\n\t"
    "v_smfmac_f32_16x16x64_bf16 v[24:27], a[100:103], v[56:63], v188 abid:1\n\t"
    "ds_read_b32 v42, v225 offset:13828\n\t"
    "ds_read_b32 v43, v79 offset:13828\n\t"
    "v_smfmac_f32_16x16x64_bf16 v[28:31], a[104:107], v[56:63], v189\n\t"
    "s_mov_b32 s55, 90112\n\t"
    "buffer_load_dwordx4 v[212:215], v92, s[48:51], s55 offen offset:2048 sc1\n\t"
    "ds_read_b32 v40, v224 offset:13828\n\t"
    "v_smfmac_f32_16x16x64_bf16 v[16:19], a[108:111], v[56:63], v189 abid:1\n\t"
    "s_waitcnt lgkmcnt(8)\n\t"
    "s_mov_b32 s76, 528968\n\t"
    "ds_read_b128 v[56:59], v217 offset:18432\n\t"
    "v_add_u32_sdwa v89, v248, v246 dst_sel:DWORD dst_unused:UNUSED_PAD src0_sel:DWORD src1_sel:WORD_1\n\t"
    "v_smfmac_f32_16x16x64_bf16 v[20:23], a[112:115], v[64:71], v190\n\t"
    "ds_read_b128 v[44:47], v236 offset:384\n\t"
    "v_add_u32_sdwa v91, v248, v247 dst_sel:DWORD dst_unused:UNUSED_PAD src0_sel:DWORD src1_sel:WORD_1\n\t"
    "v_smfmac_f32_16x16x64_bf16 v[24:27], a[116:119], v[64:71], v190 abid:1\n\t"
    "s_mov_b32 s77, 0\n\t"
    "ds_read_b128 v[60:63], v236 offset:18432\n\t"
    "v_add_u32_sdwa v231, v248, v231 dst_sel:DWORD dst_unused:UNUSED_PAD src0_sel:DWORD src1_sel:WORD_0\n\t"
    "v_smfmac_f32_16x16x64_bf16 v[28:31], a[120:123], v[64:71], v191\n\t"
    "s_lshr_b64 s[80:81], s[76:77], s69\n\t"
    "s_mov_b32 s55, 77824\n\t"
    "buffer_load_dwordx4 v[156:159], v92, s[48:51], s55 offen offset:0 sc1\n\t"
    "ds_read_u16 v88, v88 offset:18432\n\t"
    "v_smfmac_f32_16x16x64_bf16 v[16:19], a[124:127], v[64:71], v191 abid:1\n\t"
    "s_waitcnt lgkmcnt(7)\n\t"
    "ds_read_b128 v[68:71], v236 offset:18560\n\t"
    "v_smfmac_f32_16x16x64_bf16 v[20:23], a[128:131], v[48:55], v192\n\t"
    "s_waitcnt lgkmcnt(5)\n\t"
    "ds_read_b128 v[64:67], v217 offset:18560\n\t"
    "v_add_u32_sdwa v104, v248, v230 dst_sel:DWORD dst_unused:UNUSED_PAD src0_sel:DWORD src1_sel:WORD_1\n\t"
    "v_mfma_f32_16x16x32_bf16 v[16:19], v[80:83], v[40:43], v[16:19]\n\t"
    "s_mov_b64 vcc, s[18:19]\n\t"
    "ds_read_b128 v[80:83], v245 offset:0\n\t"
    "ds_read_b32 v43, v73 offset:18436\n\t"
    "v_smfmac_f32_16x16x64_bf16 v[24:27], a[132:135], v[48:55], v192 abid:1\n\t"
    "s_mov_b32 s55, 65536\n\t"
    "buffer_load_dwordx4 v[120:123], v92, s[48:51], s55 offen offset:3072 sc1\n\t"
    "ds_read_u16 v89, v89 offset:18432\n\t"
    "v_smfmac_f32_16x16x64_bf16 v[28:31], a[136:139], v[48:55], v193\n\t"
    "ds_read_b32 v41, v72 offset:18436\n\t"
    "v_cvt_pk_bf16_f32 v100, v20, v21\n\t"
    "v_smfmac_f32_16x16x64_bf16 v[16:19], a[140:143], v[48:55], v193 abid:1\n\t"
    "s_waitcnt lgkmcnt(7)\n\t"
    "ds_read_b32 v40, v218 offset:18436\n\t"
    "ds_read_b32 v42, v219 offset:18436\n\t"
    "v_smfmac_f32_16x16x64_bf16 v[32:35], a[0:3], v[56:63], v176\n\t"
    "ds_read_b128 v[52:55], v236 offset:18688\n\t"
    "v_cvt_pk_bf16_f32 v102, v24, v25\n\t"
    "v_smfmac_f32_16x16x64_bf16 v[36:39], a[4:7], v[56:63], v176 abid:1\n\t"
    "s_mov_b32 s55, 73728\n\t"
    "buffer_load_dwordx4 v[140:143], v92, s[48:51], s55 offen offset:0 sc1\n\t"
    "v_cvt_pk_bf16_f32 v96, v28, v29\n\t"
    "v_smfmac_f32_16x16x64_bf16 v[4:7], a[8:11], v[56:63], v177\n\t"
    "ds_read_b128 v[48:51], v217 offset:18688\n\t"
    "v_cvt_pk_bf16_f32 v98, v16, v17\n\t"
    "v_smfmac_f32_16x16x64_bf16 v[8:11], a[12:15], v[56:63], v177 abid:1\n\t"
    "s_waitcnt lgkmcnt(8)\n\t"
    "ds_read_b128 v[60:63], v236 offset:18816\n\t"
    "ds_read_u16 v90, v90 offset:18432\n\t"
    "v_smfmac_f32_16x16x64_bf16 v[32:35], a[16:19], v[64:71], v178\n\t"
    "s_and_b32 s80, s80, 63\n\t"
    "ds_read_u16 v91, v91 offset:18432\n\t"
    "v_cvt_pk_bf16_f32 v97, v30, v31\n\t"
    "v_cndmask_b32_dpp v16, v96, v100, vcc quad_perm:[0,0,2,2] row_mask:0xf bank_mask:0xf\n\t"
    "v_smfmac_f32_16x16x64_bf16 v[36:39], a[20:23], v[64:71], v178 abid:1\n\t"
    "s_waitcnt lgkmcnt(5)\n\t"
    "s_lshl_b32 s80, s80, 2\n\t"
    "s_mov_b32 s55, 77824\n\t"
    "buffer_load_dwordx4 v[160:163], v92, s[48:51], s55 offen offset:1024 sc1\n\t"
    "v_mfma_f32_16x16x32_bf16 v[32:35], v[80:83], v[40:43], v[32:35]\n\t"
    "ds_read_b32 v40, v220 offset:18436\n\t"
    "ds_read_b32 v43, v75 offset:18436\n\t"
    "v_cvt_pk_bf16_f32 v101, v22, v23\n\t"
    "v_cvt_pk_bf16_f32 v99, v18, v19\n\t"
    "v_smfmac_f32_16x16x64_bf16 v[4:7], a[24:27], v[64:71], v179\n\t"
    "s_mov_b32 s76, 528968\n\t"
    "ds_read_b128 v[56:59], v217 offset:18816\n\t"
    "v_cvt_pk_bf16_f32 v103, v26, v27\n\t"
    "v_smfmac_f32_16x16x64_bf16 v[8:11], a[28:31], v[64:71], v179 abid:1\n\t"
    "s_waitcnt lgkmcnt(6)\n\t"
    "ds_read_b128 v[80:83], v245 offset:1024\n\t"
    "v_cndmask_b32_dpp v17, v97, v101, vcc quad_perm:[0,0,2,2] row_mask:0xf bank_mask:0xf\n\t"
    "v_smfmac_f32_16x16x64_bf16 v[32:35], a[32:35], v[48:55], v180\n\t"
    "s_mov_b32 s77, 0\n\t"
    "s_mov_b32 s55, 69632\n\t"
    "buffer_load_dwordx4 v[124:127], v92, s[48:51], s55 offen offset:0 sc1\n\t"
    "ds_read_b32 v42, v221 offset:18436\n\t"
    "v_cndmask_b32_dpp v18, v98, v102, vcc quad_perm:[0,0,2,2] row_mask:0xf bank_mask:0xf\n\t"
    "v_smfmac_f32_16x16x64_bf16 v[36:39], a[36:39], v[48:55], v180 abid:1\n\t"
    "s_waitcnt lgkmcnt(5)\n\t"
    "ds_read_b32 v41, v74 offset:18436\n\t"
    "s_set_gpr_idx_on s80, gpr_idx(SRC2,DST)\n\t"
    "v_mfma_f32_16x16x32_bf16 v[0:3], v[250:253], v[88:91], v[0:3]\n\t"
    "s_set_gpr_idx_off\n\t"
    "ds_read_b128 v[68:71], v236 offset:18944\n\t"
    "v_smfmac_f32_16x16x64_bf16 v[4:7], a[40:43], v[48:55], v181\n\t"
    "ds_read_b128 v[64:67], v217 offset:18944\n\t"
    "v_cndmask_b32_dpp v19, v99, v103, vcc quad_perm:[0,0,2,2] row_mask:0xf bank_mask:0xf\n\t"
    "v_smfmac_f32_16x16x64_bf16 v[8:11], a[44:47], v[48:55], v181 abid:1\n\t"
    "s_waitcnt lgkmcnt(5)\n\t"
    "s_lshr_b64 s[80:81], s[76:77], s70\n\t"
    "buffer_store_dwordx4 v[16:19], v241, s[44:47], s65 offen offset:0 nt\n\t"
    "ds_read_b128 v[48:51], v217 offset:19072\n\t"
    "v_smfmac_f32_16x16x64_bf16 v[32:35], a[48:51], v[56:63], v182\n\t"
    "s_not_b64 vcc, s[18:19]\n\t"
    "ds_read_b128 v[52:55], v236 offset:19072\n\t"
    "v_cndmask_b32_dpp v97, v101, v97, vcc quad_perm:[1,1,3,3] row_mask:0xf bank_mask:0xf\n\t"
    "v_smfmac_f32_16x16x64_bf16 v[36:39], a[52:55], v[56:63], v182 abid:1\n\t"
    "ds_read_b128 v[24:27], v244 offset:384\n\t"
    "v_cndmask_b32_dpp v99, v103, v99, vcc quad_perm:[1,1,3,3] row_mask:0xf bank_mask:0xf\n\t"
    "v_smfmac_f32_16x16x64_bf16 v[4:7], a[56:59], v[56:63], v183\n\t"
    "s_waitcnt lgkmcnt(5)\n\t"
    "ds_read_b128 v[20:23], v244 offset:320\n\t"
    "v_add_u32_sdwa v229, v248, v229 dst_sel:DWORD dst_unused:UNUSED_PAD src0_sel:DWORD src1_sel:WORD_0\n\t"
    "v_mfma_f32_16x16x32_bf16 v[36:39], v[80:83], v[40:43], v[36:39]\n\t"
    "s_and_b32 s80, s80, 63\n\t"
    "s_mov_b32 s55, 73728\n\t"
    "buffer_load_dwordx4 v[144:147], v92, s[48:51], s55 offen offset:1024 sc1\n\t"
    "v_smfmac_f32_16x16x64_bf16 v[8:11], a[60:63], v[56:63], v183 abid:1\n\t"
    "s_waitcnt lgkmcnt(4)\n\t"
    "s_lshl_b32 s80, s80, 2\n\t"
    "ds_read_b32 v41, v76 offset:18436\n\t"
    "ds_read_b32 v43, v77 offset:18436\n\t"
    "v_smfmac_f32_16x16x64_bf16 v[32:35], a[64:67], v[64:71], v184\n\t"
    "ds_read_b32 v42, v223 offset:18436\n\t"
    "v_cndmask_b32_dpp v96, v100, v96, vcc quad_perm:[1,1,3,3] row_mask:0xf bank_mask:0xf\n\t"
    "v_smfmac_f32_16x16x64_bf16 v[36:39], a[68:71], v[64:71], v184 abid:1\n\t"
    "ds_read_b128 v[80:83], v245 offset:2048\n\t"
    "v_cndmask_b32_dpp v98, v102, v98, vcc quad_perm:[1,1,3,3] row_mask:0xf bank_mask:0xf\n\t"
    "v_smfmac_f32_16x16x64_bf16 v[4:7], a[72:75], v[64:71], v185\n\t"
    "buffer_store_dwordx4 v[96:99], v241, s[44:47], s65 offen offset:1152 nt\n\t"
    "ds_read_b128 v[60:63], v236 offset:19200\n\t"
    "ds_read_b32 v40, v222 offset:18436\n\t"
    "s_set_gpr_idx_on s80, gpr_idx(SRC2,DST)\n\t"
    "v_mfma_f32_16x16x32_bf16 v[0:3], v[252:255], v[88:91], v[0:3]\n\t"
    "s_set_gpr_idx_off\n\t"
    "ds_read_b128 v[56:59], v217 offset:19200\n\t"
    "s_cmp_lg_u32 s22, 0\n\t"
    "s_cbranch_scc1 .Lsl4_%=\n\t"
    ".Lslr4_%=:\n\t"
    "v_smfmac_f32_16x16x64_bf16 v[8:11], a[76:79], v[64:71], v185 abid:1\n\t"
    "s_waitcnt lgkmcnt(9)\n\t"
    "s_mov_b64 vcc, s[18:19]\n\t"
    "s_bitcmp1_b32 s54, 5\n\t"
    "s_mov_b32 s68, 23592960\n\t"
    "s_cmov_b32 s68, 23666688\n\t"
    "ds_read_b128 v[64:67], v217 offset:19328\n\t"
    "v_smfmac_f32_16x16x64_bf16 v[32:35], a[80:83], v[48:55], v186\n\t"
    "ds_read_b128 v[28:31], v244 offset:448\n\t"
    "v_smfmac_f32_16x16x64_bf16 v[36:39], a[84:87], v[48:55], v186 abid:1\n\t"
    "s_mov_b32 s55, 77824\n\t"
    "buffer_load_dwordx4 v[164:167], v92, s[48:51], s55 offen offset:2048 sc1\n\t"
    "v_smfmac_f32_16x16x64_bf16 v[4:7], a[88:91], v[48:55], v187\n\t"
    "ds_read_b128 v[68:71], v236 offset:19328\n\t"
    "v_add_u32_sdwa v107, v248, v233 dst_sel:DWORD dst_unused:UNUSED_PAD src0_sel:DWORD src1_sel:WORD_1\n\t"
    "v_smfmac_f32_16x16x64_bf16 v[8:11], a[92:95], v[48:55], v187 abid:1\n\t"
    "s_waitcnt lgkmcnt(4)\n\t"
    "ds_read_b128 v[52:55], v236 offset:19456\n\t"
    "v_mfma_f32_16x16x32_bf16 v[4:7], v[80:83], v[40:43], v[4:7]\n\t"
    "s_waitcnt lgkmcnt(4)\n\t"
    "s_mov_b32 s77, 269409\n\t"
    "ds_read_b128 v[48:51], v217 offset:19456\n\t"
    "ds_read_b128 v[80:83], v245 offset:3072\n\t"
    "v_smfmac_f32_16x16x64_bf16 v[32:35], a[96:99], v[56:63], v188\n\t"
    "s_mov_b32 s55, 69632\n\t"
    "buffer_load_dwordx4 v[128:131], v92, s[48:51], s55 offen offset:1024 sc1\n\t"
    "ds_read_b32 v41, v78 offset:18436\n\t"
    "v_add_u32_sdwa v230, v248, v230 dst_sel:DWORD dst_unused:UNUSED_PAD src0_sel:DWORD src1_sel:WORD_0\n\t"
    "v_smfmac_f32_16x16x64_bf16 v[36:39], a[100:103], v[56:63], v188 abid:1\n\t"
    "ds_read_b32 v40, v224 offset:18436\n\t"
    "v_smfmac_f32_16x16x64_bf16 v[4:7], a[104:107], v[56:63], v189\n\t"
    "ds_read_b32 v42, v225 offset:18436\n\t"
    "ds_read_b32 v43, v79 offset:18436\n\t"
    "v_smfmac_f32_16x16x64_bf16 v[8:11], a[108:111], v[56:63], v189 abid:1\n\t"
    "s_waitcnt lgkmcnt(7)\n\t"
    "ds_read_b128 v[12:15], v244 offset:256\n\t"
    "v_add_u32_sdwa v91, v248, v247 dst_sel:DWORD dst_unused:UNUSED_PAD src0_sel:DWORD src1_sel:WORD_1\n\t"
    "v_smfmac_f32_16x16x64_bf16 v[32:35], a[112:115], v[64:71], v190\n\t"
    "s_mov_b32 s55, 73728\n\t"
    "buffer_load_dwordx4 v[148:151], v92, s[48:51], s55 offen offset:2048 sc1\n\t"
    "v_smfmac_f32_16x16x64_bf16 v[36:39], a[116:119], v[64:71], v190 abid:1\n\t"
    "ds_read_b128 v[60:63], v236 offset:0\n\t"
    "v_add_u32_sdwa v88, v248, v246 dst_sel:DWORD dst_unused:UNUSED_PAD src0_sel:DWORD src1_sel:WORD_0\n\t"
    "v_smfmac_f32_16x16x64_bf16 v[4:7], a[120:123], v[64:71], v191\n\t"
    "ds_read_b128 v[56:59], v217 offset:0\n\t"
    "v_add_u32_sdwa v89, v248, v246 dst_sel:DWORD dst_unused:UNUSED_PAD src0_sel:DWORD src1_sel:WORD_1\n\t"
    "v_smfmac_f32_16x16x64_bf16 v[8:11], a[124:127], v[64:71], v191 abid:1\n\t"
    "s_waitcnt lgkmcnt(8)\n\t"
    "ds_read_b128 v[64:67], v217 offset:128\n\t"
    "ds_read_b128 v[68:71], v236 offset:128\n\t"
    "v_smfmac_f32_16x16x64_bf16 v[32:35], a[128:131], v[48:55], v192\n\t"
    "s_waitcnt lgkmcnt(5)\n\t"
    "s_mov_b32 s55, 77824\n\t"
    "buffer_load_dwordx4 v[168:171], v92, s[48:51], s55 offen offset:3072 sc1\n\t"
    "v_mfma_f32_16x16x32_bf16 v[8:11], v[80:83], v[40:43], v[8:11]\n\t"
    "ds_read_b32 v40, v230 offset:4\n\t"
    "v_add_u32_sdwa v228, v248, v228 dst_sel:DWORD dst_unused:UNUSED_PAD src0_sel:DWORD src1_sel:WORD_0\n\t"
    "v_smfmac_f32_16x16x64_bf16 v[36:39], a[132:135], v[48:55], v192 abid:1\n\t"
    "s_mov_b32 s76, 1124073472\n\t"
    "ds_read_b128 v[80:83], v245 offset:6144\n\t"
    "v_add_u32_sdwa v90, v248, v247 dst_sel:DWORD dst_unused:UNUSED_PAD src0_sel:DWORD src1_sel:WORD_0\n\t"
    "v_smfmac_f32_16x16x64_bf16 v[4:7], a[136:139], v[48:55], v193\n\t"
    "ds_read_b32 v42, v231 offset:4\n\t"
    "ds_read_b32 v43, v105 offset:4\n\t"
    "v_cvt_pk_bf16_f32 v100, v32, v33\n\t"
    "v_smfmac_f32_16x16x64_bf16 v[8:11], a[140:143], v[48:55], v193 abid:1\n\t"
    "s_waitcnt lgkmcnt(6)\n\t"
    "s_mov_b32 s67, 0\n\t"
    "s_mov_b32 s55, 69632\n\t"
    "buffer_load_dwordx4 v[132:135], v92, s[48:51], s55 offen offset:2048 sc1\n\t"
    "ds_read_b32 v41, v104 offset:4\n\t"
    "v_smfmac_f32_16x16x64_bf16 v[12:15], a[144:147], v[56:63], v194\n\t"
    "s_waitcnt lgkmcnt(5)\n\t"
    "s_mov_b32 s66, 18874368\n\t"
    "ds_read_b128 v[16:19], v244 offset:512\n\t"
    "v_cvt_pk_bf16_f32 v103, v38, v39\n\t"
    "v_smfmac_f32_16x16x64_bf16 v[12:15], a[164:167], v[64:71], v196 abid:1\n\t"
    "s_lshr_b64 s[80:81], s[76:77], s69\n\t"
    "ds_read_b128 v[48:51], v217 offset:256\n\t"
    "v_cvt_pk_bf16_f32 v96, v4, v5\n\t"
    "v_cvt_pk_bf16_f32 v97, v6, v7\n\t"
    "v_smfmac_f32_16x16x64_bf16 v[20:23], a[148:151], v[56:63], v194 abid:1\n\t"
    "ds_read_b128 v[52:55], v236 offset:256\n\t"
    "v_cvt_pk_bf16_f32 v99, v10, v11\n\t"
    "v_smfmac_f32_16x16x64_bf16 v[20:23], a[168:171], v[64:71], v197\n\t"
    "s_mov_b32 s55, 73728\n\t"
    "buffer_load_dwordx4 v[152:155], v92, s[48:51], s55 offen offset:3072 sc1\n\t"
    "v_cvt_pk_bf16_f32 v98, v8, v9\n\t"
    "v_smfmac_f32_16x16x64_bf16 v[24:27], a[152:155], v[56:63], v195\n\t"
    "s_mov_b32 s77, 269409\n\t"
    "ds_read_u16 v89, v89 offset:0\n\t"
    "v_cvt_pk_bf16_f32 v102, v36, v37\n\t"
    "v_smfmac_f32_16x16x64_bf16 v[24:27], a[172:175], v[64:71], v197 abid:1\n\t"
    "s_waitcnt lgkmcnt(4)\n\t"
    "s_and_b32 s80, s80, 63\n\t"
    "ds_read_u16 v91, v91 offset:0\n\t"
    "v_cvt_pk_bf16_f32 v101, v34, v35\n\t"
    "v_mfma_f32_16x16x32_bf16 v[24:27], v[80:83], v[40:43], v[24:27]\n\t"
    "s_memrealtime s[84:85]\n\t"
    "ds_read_b128 v[40:43], v217 offset:384\n\t"
    "v_cndmask_b32_dpp v8, v96, v100, vcc quad_perm:[0,0,2,2] row_mask:0xf bank_mask:0xf\n\t"
    "v_smfmac_f32_16x16x64_bf16 v[28:31], a[156:159], v[56:63], v195 abid:1\n\t"
    "s_mov_b32 s55, 90112\n\t"
    "buffer_load_dword v216, v92, s[48:51], s55 offen offset:3072 sc1\n\t"
    "v_smfmac_f32_16x16x64_bf16 v[28:31], a[176:179], v[64:71], v198\n\t"
    "s_waitcnt lgkmcnt(5)\n\t"
    "ds_read_b128 v[80:83], v245 offset:5120\n\t"
    "v_cndmask_b32_dpp v9, v97, v101, vcc quad_perm:[0,0,2,2] row_mask:0xf bank_mask:0xf\n\t"
    "v_cndmask_b32_dpp v11, v99, v103, vcc quad_perm:[0,0,2,2] row_mask:0xf bank_mask:0xf\n\t"
    "v_smfmac_f32_16x16x64_bf16 v[16:19], a[160:163], v[56:63], v196\n\t"
    "s_mov_b32 s76, 1124073472\n\t"
    "ds_read_b32 v56, v228 offset:4\n\t"
    "ds_read_b32 v57, v86 offset:4\n\t"
    "ds_read_b32 v58, v229 offset:4\n\t"
    "ds_read_b32 v59, v87 offset:4\n\t"
    "v_cndmask_b32_dpp v10, v98, v102, vcc quad_perm:[0,0,2,2] row_mask:0xf bank_mask:0xf\n\t"
    "v_smfmac_f32_16x16x64_bf16 v[16:19], a[180:183], v[64:71], v198 abid:1\n\t"
    "s_waitcnt lgkmcnt(8)\n\t"
    "s_not_b64 vcc, s[18:19]\n\t"
    "v_cndmask_b32_dpp v97, v101, v97, vcc quad_perm:[1,1,3,3] row_mask:0xf bank_mask:0xf\n\t"
    "v_smfmac_f32_16x16x64_bf16 v[12:15], a[184:187], v[48:55], v199\n\t"
    "s_waitcnt lgkmcnt(5)\n\t"
    "buffer_store_dwordx4 v[8:11], v241, s[44:47], s66 offen offset:0 nt\n\t"
    "ds_read_u16 v88, v88 offset:0\n\t"
    "ds_read_u16 v90, v90 offset:0\n\t"
    "v_add_u32_sdwa v85, v248, v227 dst_sel:DWORD dst_unused:UNUSED_PAD src0_sel:DWORD src1_sel:WORD_1\n\t"
    "v_cndmask_b32_dpp v96, v100, v96, vcc quad_perm:[1,1,3,3] row_mask:0xf bank_mask:0xf\n\t"
    "v_smfmac_f32_16x16x64_bf16 v[12:15], a[204:207], v[40:47], v201 abid:1\n\t"
    "v_add_u32_sdwa v227, v248, v227 dst_sel:DWORD dst_unused:UNUSED_PAD src0_sel:DWORD src1_sel:WORD_0\n\t"
    "v_smfmac_f32_16x16x64_bf16 v[20:23], a[188:191], v[48:55], v199 abid:1\n\t"
    "s_lshl_b32 s80, s80, 2\n\t"
    "ds_read_b128 v[4:7], v244 offset:384\n\t"
    "v_smfmac_f32_16x16x64_bf16 v[20:23], a[208:211], v[40:47], v202\n\t"
    "s_waitcnt lgkmcnt(3)\n\t"
    "s_add_i32 s86, s87, 915\n\t"
    "ds_read_b128 v[60:63], v236 offset:640\n\t"
    "v_cndmask_b32_dpp v98, v102, v98, vcc quad_perm:[1,1,3,3] row_mask:0xf bank_mask:0xf\n\t"
    "v_mfma_f32_16x16x32_bf16 v[20:23], v[80:83], v[56:59], v[20:23]\n\t"
    "s_mov_b32 s55, 81920\n\t"
    "buffer_load_dwordx4 v[172:175], v92, s[48:51], s55 offen offset:0 sc1\n\t"
    "ds_read_b128 v[56:59], v217 offset:640\n\t"
    "v_smfmac_f32_16x16x64_bf16 v[24:27], a[192:195], v[48:55], v200\n\t"
    "ds_read_b128 v[64:67], v217 offset:512\n\t"
    "v_cndmask_b32_dpp v99, v103, v99, vcc quad_perm:[1,1,3,3] row_mask:0xf bank_mask:0xf\n\t"
    "v_smfmac_f32_16x16x64_bf16 v[24:27], a[212:215], v[40:47], v202 abid:1\n\t"
    "s_waitcnt lgkmcnt(4)\n\t"
    "ds_read_b128 v[68:71], v236 offset:512\n\t"
    "s_set_gpr_idx_on s80, gpr_idx(SRC2,DST)\n\t"
    "v_mfma_f32_16x16x32_bf16 v[0:3], v[250:253], v[88:91], v[0:3]\n\t"
    "s_set_gpr_idx_off\n\t"
    "s_lshr_b64 s[80:81], s[76:77], s70\n\t"
    "s_and_b32 s80, s80, 63\n\t"
    "s_sub_u32 s89, s84, s24\n\t"
    "ds_read_b128 v[80:83], v245 offset:4096\n\t"
    "v_smfmac_f32_16x16x64_bf16 v[28:31], a[196:199], v[48:55], v200 abid:1\n\t"
    "s_cmp_lt_i32 s89, s86\n\t"
    "s_cbranch_scc1 .Lpc0_%=\n\t"
    ".Lpc0r_%=:\n\t"
    "buffer_store_dwordx4 v[96:99], v241, s[44:47], s66 offen offset:1152 nt\n\t"
    "ds_read_b128 v[8:11], v244 offset:448\n\t"
    "ds_read_b32 v94, v229 offset:4612\n\t"
    "v_add_u32_sdwa v226, v248, v226 dst_sel:DWORD dst_unused:UNUSED_PAD src0_sel:DWORD src1_sel:WORD_0\n\t"
    "v_smfmac_f32_16x16x64_bf16 v[28:31], a[216:219], v[40:47], v203\n\t"
    "s_lshl_b32 s80, s80, 2\n\t"
    "s_set_gpr_idx_on s80, gpr_idx(SRC2,DST)\n\t"
    "v_mfma_f32_16x16x32_bf16 v[0:3], v[252:255], v[88:91], v[0:3]\n\t"
    "s_set_gpr_idx_off\n\t"
    "s_max_i32 s98, s89, s86\n\t"
    "s_cmp_lg_u32 s22, 0\n\t"
    "s_cbranch_scc1 .Lsl5_%=\n\t"
    ".Lslr5_%=:\n\t"
    "v_smfmac_f32_16x16x64_bf16 v[16:19], a[200:203], v[48:55], v201\n\t"
    "s_sub_i32 s16, s89, s86\n\t"
    "s_sub_i32 s16, s16, s23\n\t"
    "ds_read_b32 v48, v226 offset:4\n\t"
    "ds_read_b32 v49, v84 offset:4\n\t"
    "ds_read_b32 v50, v227 offset:4\n\t"
    "ds_read_b32 v51, v85 offset:4\n\t"
    "v_smfmac_f32_16x16x64_bf16 v[16:19], a[220:223], v[40:47], v203 abid:1\n\t"
    "s_add_u32 m0, s52, 23040\n\t"
    "s_waitcnt lgkmcnt(7)\n\t"
    "s_max_i32 s16, s16, 0\n\t"
    "s_min_i32 s16, s16, 30\n\t"
    "s_mov_b32 exec_hi, 0\n\t"
    "buffer_load_dwordx4 v237, s[40:43], s68 offen offset:0 nt lds\n\t"
    "s_mov_b32 exec_hi, -1\n\t"
    "v_add_u32_sdwa v42, v248, v235 dst_sel:DWORD dst_unused:UNUSED_PAD src0_sel:DWORD src1_sel:WORD_0\n\t"
    "v_add_u32_sdwa v43, v248, v235 dst_sel:DWORD dst_unused:UNUSED_PAD src0_sel:DWORD src1_sel:WORD_1\n\t"
    "v_smfmac_f32_16x16x64_bf16 v[12:15], a[224:227], v[64:71], v204\n\t"
    "s_add_i32 s87, s87, s16\n\t"
    "ds_read_b32 v42, v42 offset:4\n\t"
    "v_add_u32_sdwa v40, v248, v234 dst_sel:DWORD dst_unused:UNUSED_PAD src0_sel:DWORD src1_sel:WORD_0\n\t"
    "v_smfmac_f32_16x16x64_bf16 v[12:15], a[244:247], v[56:63], v206 abid:1\n\t"
    "s_waitcnt lgkmcnt(1)\n\t"
    "s_add_i32 s23, s23, 20\n\t"
    "ds_read_b32 v43, v43 offset:4\n\t"
    "v_add_u32_sdwa v41, v248, v234 dst_sel:DWORD dst_unused:UNUSED_PAD src0_sel:DWORD src1_sel:WORD_1\n\t"
    "v_mfma_f32_16x16x32_bf16 v[12:15], v[80:83], v[48:51], v[12:15]\n\t"
    "s_mov_b32 s76, 1207959552\n\t"
    "ds_read_b128 v[80:83], v245 offset:8192\n\t"
    "ds_read_b128 v[48:51], v217 offset:768\n\t"
    "v_smfmac_f32_16x16x64_bf16 v[20:23], a[228:231], v[64:71], v204 abid:1\n\t"
    "buffer_load_dwordx4 v237, s[40:43], s68 offen offset:512 nt lds\n\t"
    "v_smfmac_f32_16x16x64_bf16 v[20:23], a[248:251], v[56:63], v207\n\t"
    "ds_read_b32 v40, v40 offset:4\n\t"
    "ds_read_b32 v41, v41 offset:4\n\t"
    "s_waitcnt vmcnt(33)\n\t"
    "v_smfmac_f32_16x16x64_bf16 v[24:27], a[232:235], v[64:71], v205\n\t"
    "ds_read_b128 v[52:55], v236 offset:768\n\t"
    "v_add_u32_sdwa v233, v248, v233 dst_sel:DWORD dst_unused:UNUSED_PAD src0_sel:DWORD src1_sel:WORD_0\n\t"
    "s_waitcnt vmcnt(31)\n\t"
    "v_smfmac_f32_16x16x64_bf16 v[24:27], a[252:255], v[56:63], v207 abid:1\n\t"
    "ds_read_b32 v88, v230 offset:4612\n\t"
    "s_waitcnt vmcnt(29)\n\t"
    "v_smfmac_f32_16x16x64_bf16 v[28:31], a[236:239], v[64:71], v205 abid:1\n\t"
    "buffer_load_dwordx4 v238, s[40:43], s68 offen offset:1536 nt lds\n\t"
    "s_waitcnt vmcnt(28)\n\t"
    "v_smfmac_f32_16x16x64_bf16 v[28:31], v[108:111], v[56:63], v208\n\t"
    "ds_read_b32 v89, v104 offset:4612\n\t"
    "v_add_u32_sdwa v106, v248, v232 dst_sel:DWORD dst_unused:UNUSED_PAD src0_sel:DWORD src1_sel:WORD_1\n\t"
    "s_waitcnt vmcnt(27)\n\t"
    "v_smfmac_f32_16x16x64_bf16 v[16:19], a[240:243], v[64:71], v206\n\t"
    "ds_read_b128 v[64:67], v217 offset:896\n\t"
    "ds_read_b128 v[68:71], v236 offset:896\n\t"
    "s_waitcnt vmcnt(25)\n\t"
    "v_smfmac_f32_16x16x64_bf16 v[16:19], v[112:115], v[56:63], v208 abid:1\n\t"
    "s_waitcnt lgkmcnt(5)\n\t"
    "s_mov_b64 vcc, s[18:19]\n\t"
    "ds_read_b128 v[56:59], v217 offset:1024\n\t"
    "ds_read_b128 v[60:63], v236 offset:1024\n\t"
    "v_mfma_f32_16x16x32_bf16 v[16:19], v[80:83], v[40:43], v[16:19]\n\t"
    "s_waitcnt lgkmcnt(6)\n\t"
    "buffer_load_dwordx4 v239, s[40:43], s68 offen offset:2560 nt lds\n\t"
    "s_waitcnt vmcnt(24)\n\t"
    "v_smfmac_f32_16x16x64_bf16 v[12:15], v[116:119], v[48:55], v209\n\t"
    "s_waitcnt lgkmcnt(2)\n\t"
    "ds_read_b32 v42, v233 offset:4\n\t"
    "s_waitcnt vmcnt(23)\n\t"
    "v_smfmac_f32_16x16x64_bf16 v[12:15], v[136:139], v[64:71], v211 abid:1\n\t"
    "s_waitcnt lgkmcnt(1)\n\t"
    "ds_read_b32 v41, v106 offset:4\n\t"
    "v_add_u32_sdwa v232, v248, v232 dst_sel:DWORD dst_unused:UNUSED_PAD src0_sel:DWORD src1_sel:WORD_0\n\t"
    "s_waitcnt vmcnt(21)\n\t"
    "v_smfmac_f32_16x16x64_bf16 v[12:15], v[156:159], v[56:63], v214\n\t"
    "ds_read_b32 v43, v107 offset:4\n\t"
    "ds_read_b32 v91, v105 offset:4612\n\t"
    "s_waitcnt vmcnt(20)\n\t"
    "v_smfmac_f32_16x16x64_bf16 v[20:23], v[120:123], v[48:55], v209 abid:1\n\t"
    "buffer_load_dwordx4 v239, s[40:43], s68 offen offset:3584 nt lds\n\t"
    "s_waitcnt vmcnt(20)\n\t"
    "v_smfmac_f32_16x16x64_bf16 v[20:23], v[140:143], v[64:71], v212\n\t"
    "s_mov_b32 s77, 198674\n\t"
    "ds_read_b128 v[80:83], v245 offset:7168\n\t"
    "s_waitcnt vmcnt(19)\n\t"
    "v_smfmac_f32_16x16x64_bf16 v[20:23], v[160:163], v[56:63], v214 abid:1\n\t"
    "ds_read_b32 v40, v232 offset:4\n\t"
    "v_cvt_pk_bf16_f32 v101, v14, v15\n\t"
    "s_waitcnt vmcnt(18)\n\t"
    "v_smfmac_f32_16x16x64_bf16 v[24:27], v[124:127], v[48:55], v210\n\t"
    "ds_read_b32 v92, v228 offset:4612\n\t"
    "s_waitcnt vmcnt(16)\n\t"
    "v_smfmac_f32_16x16x64_bf16 v[24:27], v[144:147], v[64:71], v212 abid:1\n\t"
    "s_lshr_b64 s[80:81], s[76:77], s69\n\t"
    "ds_read_b128 v[44:47], v236 offset:4608\n\t"
    "s_waitcnt vmcnt(14)\n\t"
    "v_smfmac_f32_16x16x64_bf16 v[24:27], v[164:167], v[56:63], v215\n\t"
    "s_and_b32 s80, s80, 63\n\t"
    "ds_read_b128 v[36:39], v244 offset:320\n\t"
    "v_cvt_pk_bf16_f32 v102, v20, v21\n\t"
    "s_waitcnt vmcnt(13)\n\t"
    "v_smfmac_f32_16x16x64_bf16 v[28:31], v[128:131], v[48:55], v210 abid:1\n\t"
    "ds_read_b128 v[32:35], v244 offset:256\n\t"
    "v_cvt_pk_bf16_f32 v103, v22, v23\n\t"
    "s_waitcnt vmcnt(12)\n\t"
    "v_smfmac_f32_16x16x64_bf16 v[28:31], v[148:151], v[64:71], v213\n\t"
    "ds_read_b32 v95, v87 offset:4612\n\t"
    "s_waitcnt vmcnt(11)\n\t"
    "v_smfmac_f32_16x16x64_bf16 v[28:31], v[168:171], v[56:63], v215 abid:1\n\t"
    "s_waitcnt lgkmcnt(5)\n\t"
    "ds_read_b32 v90, v231 offset:4612\n\t"
    "v_cvt_pk_bf16_f32 v97, v26, v27\n\t"
    "v_mfma_f32_16x16x32_bf16 v[28:31], v[80:83], v[40:43], v[28:31]\n\t"
    "s_mov_b32 s66, 4718592\n\t"
    "ds_read_b128 v[40:43], v217 offset:4608\n\t"
    "v_cvt_pk_bf16_f32 v100, v12, v13\n\t"
    "s_waitcnt vmcnt(10)\n\t"
    "v_smfmac_f32_16x16x64_bf16 v[16:19], v[132:135], v[48:55], v211\n\t"
    "s_mov_b32 s76, 1207959552\n\t"
    "ds_read_b128 v[48:51], v217 offset:4736\n\t"
    "ds_read_b128 v[52:55], v236 offset:4736\n\t"
    "s_waitcnt vmcnt(9)\n\t"
    "v_smfmac_f32_16x16x64_bf16 v[16:19], v[152:155], v[64:71], v213 abid:1\n\t"
    "v_cvt_pk_bf16_f32 v96, v24, v25\n\t"
    "s_waitcnt vmcnt(6)\n\t"
    "v_smfmac_f32_16x16x64_bf16 v[16:19], v[172:175], v[56:63], v216\n\t"
    "s_waitcnt lgkmcnt(2)\n\t"
    "s_bitcmp1_b32 s54, 6\n\t"
    "s_mov_b32 s68, 28311552\n\t"
    "s_cmov_b32 s68, 28385280\n\t"
    "ds_read_b128 v[80:83], v245 offset:6144\n\t"
    "v_smfmac_f32_16x16x64_bf16 v[32:35], a[144:147], v[40:47], v194\n\t"
    "s_waitcnt lgkmcnt(1)\n\t"
    "s_lshl_b32 s80, s80, 2\n\t"
    "ds_read_b128 v[64:67], v217 offset:4864\n\t"
    "v_cvt_pk_bf16_f32 v98, v28, v29\n\t"
    "v_smfmac_f32_16x16x64_bf16 v[32:35], a[164:167], v[48:55], v196 abid:1\n\t"
    "ds_read_b128 v[12:15], v244 offset:512\n\t"
    "v_cndmask_b32_dpp v29, v97, v101, vcc quad_perm:[0,0,2,2] row_mask:0xf bank_mask:0xf\n\t"
    "v_smfmac_f32_16x16x64_bf16 v[36:39], a[148:151], v[40:47], v194 abid:1\n\t"
    "s_mov_b32 s77, 198674\n\t"
    "ds_read_b128 v[68:71], v236 offset:4864\n\t"
    "v_cvt_pk_bf16_f32 v99, v30, v31\n\t"
    "v_smfmac_f32_16x16x64_bf16 v[36:39], a[168:171], v[48:55], v197\n\t"
    "ds_read_b32 v93, v86 offset:4612\n\t"
    "v_cndmask_b32_dpp v30, v98, v102, vcc quad_perm:[0,0,2,2] row_mask:0xf bank_mask:0xf\n\t"
    "v_smfmac_f32_16x16x64_bf16 v[4:7], a[152:155], v[40:47], v195\n\t"
    "ds_read_b128 v[60:63], v236 offset:4992\n\t"
    "v_cvt_pk_bf16_f32 v16, v16, v17\n\t"
    "v_smfmac_f32_16x16x64_bf16 v[4:7], a[172:175], v[48:55], v197 abid:1\n\t"
    "s_waitcnt lgkmcnt(5)\n\t"
    "s_add_u32 s65, s57, s67\n\t"
    "ds_read_b128 v[56:59], v217 offset:4992\n\t"
    "v_cndmask_b32_dpp v31, v99, v103, vcc quad_perm:[0,0,2,2] row_mask:0xf bank_mask:0xf\n\t"
    "v_mfma_f32_16x16x32_bf16 v[4:7], v[80:83], v[88:91], v[4:7]\n\t"
    "s_memrealtime s[84:85]\n\t"
    "ds_read_b128 v[80:83], v245 offset:5120\n\t"
    "v_cndmask_b32_dpp v28, v96, v100, vcc quad_perm:[0,0,2,2] row_mask:0xf bank_mask:0xf\n\t"
    "v_add_u32_sdwa v88, v248, v246 dst_sel:DWORD dst_unused:UNUSED_PAD src0_sel:DWORD src1_sel:WORD_0\n\t"
    "v_add_u32_sdwa v89, v248, v246 dst_sel:DWORD dst_unused:UNUSED_PAD src0_sel:DWORD src1_sel:WORD_1\n\t"
    "v_add_u32_sdwa v90, v248, v247 dst_sel:DWORD dst_unused:UNUSED_PAD src0_sel:DWORD src1_sel:WORD_0\n\t"
    "v_smfmac_f32_16x16x64_bf16 v[8:11], a[156:159], v[40:47], v195 abid:1\n\t"
    "s_not_b64 vcc, s[18:19]\n\t"
    "ds_read_u16 v90, v90 offset:4608\n\t"
    "v_add_u32_sdwa v91, v248, v247 dst_sel:DWORD dst_unused:UNUSED_PAD src0_sel:DWORD src1_sel:WORD_1\n\t"
    "v_smfmac_f32_16x16x64_bf16 v[8:11], a[176:179], v[48:55], v198\n\t"
    "s_waitcnt lgkmcnt(6)\n\t"
    "buffer_store_dwordx4 v[28:31], v241, s[44:47], s67 offen offset:128 sc1\n\t"
    "ds_read_u16 v88, v88 offset:4608\n\t"
    "ds_read_u16 v89, v89 offset:4608\n\t"
    "ds_read_u16 v91, v91 offset:4608\n\t"
    "v_cvt_pk_bf16_f32 v17, v18, v19\n\t"
    "v_smfmac_f32_16x16x64_bf16 v[12:15], a[160:163], v[40:47], v196\n\t"
    "ds_read_b128 v[20:23], v244 offset:256\n\t"
    "v_cndmask_b32_dpp v99, v103, v99, vcc quad_perm:[1,1,3,3] row_mask:0xf bank_mask:0xf\n\t"
    "v_smfmac_f32_16x16x64_bf16 v[12:15], a[180:183], v[48:55], v198 abid:1\n\t"
    "s_waitcnt lgkmcnt(9)\n\t"
    "ds_read_b128 v[48:51], v217 offset:5248\n\t"
    "v_cndmask_b32_dpp v96, v100, v96, vcc quad_perm:[1,1,3,3] row_mask:0xf bank_mask:0xf\n\t"
    "v_smfmac_f32_16x16x64_bf16 v[32:35], a[184:187], v[64:71], v199\n\t"
    "s_waitcnt lgkmcnt(7)\n\t"
    "ds_read_b128 v[40:43], v217 offset:5120\n\t"
    "v_cndmask_b32_dpp v97, v101, v97, vcc quad_perm:[1,1,3,3] row_mask:0xf bank_mask:0xf\n\t"
    "v_smfmac_f32_16x16x64_bf16 v[32:35], a[204:207], v[56:63], v201 abid:1\n\t"
    "ds_read_b128 v[44:47], v236 offset:5120\n\t"
    "v_smfmac_f32_16x16x64_bf16 v[36:39], a[188:191], v[64:71], v199 abid:1\n\t"
    "v_cndmask_b32_dpp v98, v102, v98, vcc quad_perm:[1,1,3,3] row_mask:0xf bank_mask:0xf\n\t"
    "v_smfmac_f32_16x16x64_bf16 v[36:39], a[208:211], v[56:63], v202\n\t"
    "s_waitcnt lgkmcnt(8)\n\t"
    "s_add_i32 s86, s87, 1011\n\t"
    "buffer_store_dwordx4 v[96:99], v241, s[44:47], s67 offen offset:1280 sc1\n\t"
    "ds_write_b64 v242, v[16:17] offset:0\n\t"
    "v_mfma_f32_16x16x32_bf16 v[36:39], v[80:83], v[92:95], v[36:39]\n\t"
    "s_add_i32 s98, s98, 96\n\t"
    "ds_read_b128 v[52:55], v236 offset:5248\n\t"
    "v_smfmac_f32_16x16x64_bf16 v[4:7], a[192:195], v[64:71], v200\n\t"
    "s_add_i32 s89, s86, s23\n\t"
    "s_min_i32 s98, s98, s89\n\t"
    "ds_read_b32 v93, v84 offset:4612\n\t"
    "ds_read_b32 v94, v227 offset:4612\n\t"
    "v_smfmac_f32_16x16x64_bf16 v[4:7], a[212:215], v[56:63], v202 abid:1\n\t"
    "s_waitcnt lgkmcnt(8)\n\t"
    "s_max_i32 s86, s86, s98\n\t"
    "ds_read_b32 v95, v85 offset:4612\n\t"
    "s_set_gpr_idx_on s80, gpr_idx(SRC2,DST)\n\t"
    "v_mfma_f32_16x16x32_bf16 v[0:3], v[250:253], v[88:91], v[0:3]\n\t"
    "s_set_gpr_idx_off\n\t"
    "s_waitcnt lgkmcnt(4)\n\t"
    "s_lshr_b64 s[80:81], s[76:77], s70\n\t"
    "s_sub_u32 s89, s84, s24\n\t"
    "s_barrier\n\t"
    "ds_read_b128 v[100:103], v243 offset:2048\n\t"
    "v_smfmac_f32_16x16x64_bf16 v[8:11], a[196:199], v[64:71], v200 abid:1\n\t"
    "s_and_b32 s80, s80, 63\n\t"
    "s_cmp_lt_i32 s89, s86\n\t"
    "s_cbranch_scc1 .Lpc1_%=\n\t"
    ".Lpc1r_%=:\n\t"
    "ds_read_b128 v[80:83], v245 offset:4096\n\t"
    "v_smfmac_f32_16x16x64_bf16 v[8:11], a[216:219], v[56:63], v203\n\t"
    "s_add_u32 m0, s52, 0\n\t"
    "s_lshl_b32 s80, s80, 2\n\t"
    "s_mov_b32 exec_hi, 0\n\t"
    "buffer_load_dwordx4 v237, s[40:43], s68 offen offset:0 nt lds\n\t"
    "s_mov_b32 exec_hi, -1\n\t"
    "ds_read_b32 v92, v226 offset:4612\n\t"
    "s_set_gpr_idx_on s80, gpr_idx(SRC2,DST)\n\t"
    "v_mfma_f32_16x16x32_bf16 v[0:3], v[252:255], v[88:91], v[0:3]\n\t"
    "s_set_gpr_idx_off\n\t"
    "s_max_i32 s98, s89, s86\n\t"
    "s_cmp_lg_u32 s22, 0\n\t"
    "s_cbranch_scc1 .Lsl6_%=\n\t"
    ".Lslr6_%=:\n\t"
    "v_smfmac_f32_16x16x64_bf16 v[12:15], a[200:203], v[64:71], v201\n\t"
    "s_sub_i32 s23, s23, 3\n\t"
    "ds_read_b128 v[64:67], v217 offset:5376\n\t"
    "v_add_u32_sdwa v91, v248, v235 dst_sel:DWORD dst_unused:UNUSED_PAD src0_sel:DWORD src1_sel:WORD_1\n\t"
    "v_smfmac_f32_16x16x64_bf16 v[12:15], a[220:223], v[56:63], v203 abid:1\n\t"
    "s_mov_b32 s76, 2231369728\n\t"
    "ds_read_b128 v[68:71], v236 offset:5376\n\t"
    "v_add_u32_sdwa v88, v248, v234 dst_sel:DWORD dst_unused:UNUSED_PAD src0_sel:DWORD src1_sel:WORD_0\n\t"
    "v_smfmac_f32_16x16x64_bf16 v[32:35], a[224:227], v[40:47], v204\n\t"
    "s_waitcnt lgkmcnt(8)\n\t"
    "ds_read_b128 v[60:63], v236 offset:5504\n\t"
    "v_add_u32_sdwa v90, v248, v235 dst_sel:DWORD dst_unused:UNUSED_PAD src0_sel:DWORD src1_sel:WORD_0\n\t"
    "v_smfmac_f32_16x16x64_bf16 v[32:35], a[244:247], v[48:55], v206 abid:1\n\t"
    "s_waitcnt lgkmcnt(3)\n\t"
    "s_mov_b32 exec_hi, 0\n\t"
    "buffer_store_dwordx4 v[100:103], v240, s[44:47], s65 offen offset:0 sc1\n\t"
    "s_mov_b32 exec_hi, -1\n\t"
    "ds_read_b32 v90, v90 offset:4612\n\t"
    "v_add_u32_sdwa v89, v248, v234 dst_sel:DWORD dst_unused:UNUSED_PAD src0_sel:DWORD src1_sel:WORD_1\n\t"
    "v_mfma_f32_16x16x32_bf16 v[32:35], v[80:83], v[92:95], v[32:35]\n\t"
    "ds_read_b32 v91, v91 offset:4612\n\t"
    "v_smfmac_f32_16x16x64_bf16 v[36:39], a[228:231], v[40:47], v204 abid:1\n\t"
    "ds_read_b32 v88, v88 offset:4612\n\t"
    "ds_read_b32 v89, v89 offset:4612\n\t"
    "v_smfmac_f32_16x16x64_bf16 v[36:39], a[248:251], v[48:55], v207\n\t"
    "ds_read_b128 v[80:83], v245 offset:8192\n\t"
    "v_smfmac_f32_16x16x64_bf16 v[4:7], a[232:235], v[40:47], v205\n\t"
    "ds_read_b128 v[56:59], v217 offset:5504\n\t"
    "v_smfmac_f32_16x16x64_bf16 v[4:7], a[252:255], v[48:55], v207 abid:1\n\t"
    "ds_read_b32 v95, v107 offset:4612\n\t"
    "v_smfmac_f32_16x16x64_bf16 v[8:11], a[236:239], v[40:47], v205 abid:1\n\t"
    "s_mov_b32 s77, 531521\n\t"
    "ds_read_b32 v94, v233 offset:4612\n\t"
    "v_smfmac_f32_16x16x64_bf16 v[8:11], v[108:111], v[48:55], v208\n\t"
    "s_add_u32 m0, s52, 0\n\t"
    "s_lshr_b64 s[80:81], s[76:77], s69\n\t"
    "buffer_load_dwordx4 v237, s[40:43], s68 offen offset:512 nt lds\n\t"
    "v_smfmac_f32_16x16x64_bf16 v[12:15], a[240:243], v[40:47], v206\n\t"
    "ds_read_b128 v[40:43], v217 offset:5632\n\t"
    "ds_read_b128 v[44:47], v236 offset:5632\n\t"
    "v_smfmac_f32_16x16x64_bf16 v[12:15], v[112:115], v[48:55], v208 abid:1\n\t"
    "s_waitcnt lgkmcnt(5)\n\t"
    "s_add_u32 s67, s57, s66\n\t"
    "ds_read_b128 v[16:19], v244 offset:384\n\t"
    "v_mfma_f32_16x16x32_bf16 v[12:15], v[80:83], v[88:91], v[12:15]\n\t"
    "ds_read_b32 v93, v106 offset:4612\n\t"
    "v_smfmac_f32_16x16x64_bf16 v[32:35], v[116:119], v[64:71], v209\n\t"
    "s_waitcnt lgkmcnt(6)\n\t"
    "ds_read_b32 v88, v230 offset:9220\n\t"
    "v_smfmac_f32_16x16x64_bf16 v[32:35], v[136:139], v[56:63], v211 abid:1\n\t"
    "s_waitcnt lgkmcnt(3)\n\t"
    "ds_read_b128 v[24:27], v244 offset:320\n\t"
    "v_smfmac_f32_16x16x64_bf16 v[32:35], v[156:159], v[40:47], v214\n\t"
    "ds_read_b32 v92, v232 offset:4612\n\t"
    "v_smfmac_f32_16x16x64_bf16 v[36:39], v[120:123], v[64:71], v209 abid:1\n\t"
    "ds_read_b128 v[80:83], v245 offset:7168\n\t"
    "v_smfmac_f32_16x16x64_bf16 v[36:39], v[140:143], v[56:63], v212\n\t"
    "ds_read_b128 v[28:31], v244 offset:448\n\t"
    "v_smfmac_f32_16x16x64_bf16 v[36:39], v[160:163], v[40:47], v214 abid:1\n\t"
    "ds_read_b32 v91, v105 offset:9220\n\t"
    "v_cvt_pk_bf16_f32 v96, v32, v33\n\t"
    "v_smfmac_f32_16x16x64_bf16 v[4:7], v[124:127], v[64:71], v210\n\t"
    "ds_read_b32 v90, v231 offset:9220\n\t"
    "v_smfmac_f32_16x16x64_bf16 v[4:7], v[144:147], v[56:63], v212 abid:1\n\t"
    "ds_read_b128 v[52:55], v236 offset:9216\n\t"
    "v_cvt_pk_bf16_f32 v97, v34, v35\n\t"
    "v_smfmac_f32_16x16x64_bf16 v[4:7], v[164:167], v[40:47], v215\n\t"
    "buffer_load_dwordx4 v238, s[40:43], s68 offen offset:1536 nt lds\n\t"
    "ds_read_b128 v[48:51], v217 offset:9216\n\t"
    "v_smfmac_f32_16x16x64_bf16 v[8:11], v[128:131], v[64:71], v210 abid:1\n\t"
    "ds_read_b32 v89, v104 offset:9220\n\t"
    "v_smfmac_f32_16x16x64_bf16 v[8:11], v[148:151], v[56:63], v213\n\t"
    "ds_read_b128 v[32:35], v244 offset:512\n\t"
    "v_cvt_pk_bf16_f32 v99, v38, v39\n\t"
    "v_smfmac_f32_16x16x64_bf16 v[8:11], v[168:171], v[40:47], v215 abid:1\n\t"
    "s_waitcnt lgkmcnt(7)\n\t"
    "s_and_b32 s80, s80, 63\n\t"
    "v_cvt_pk_bf16_f32 v101, v6, v7\n\t"
    "v_mfma_f32_16x16x32_bf16 v[8:11], v[80:83], v[92:95], v[8:11]\n\t"
    "s_mov_b64 vcc, s[18:19]\n\t"
    "ds_read_b32 v92, v228 offset:9220\n\t"
    "v_cvt_pk_bf16_f32 v98, v36, v37\n\t"
    "v_smfmac_f32_16x16x64_bf16 v[12:15], v[132:135], v[64:71], v211\n\t"
    "s_lshl_b32 s80, s80, 2\n\t"
    "ds_read_b128 v[64:67], v217 offset:9344\n\t"
    "ds_read_b128 v[68:71], v236 offset:9344\n\t"
    "v_smfmac_f32_16x16x64_bf16 v[12:15], v[152:155], v[56:63], v213 abid:1\n\t"
    "ds_read_b128 v[80:83], v245 offset:6144\n\t"
    "v_cvt_pk_bf16_f32 v100, v4, v5\n\t"
    "v_smfmac_f32_16x16x64_bf16 v[12:15], v[172:175], v[40:47], v216\n\t"
    "s_waitcnt lgkmcnt(6)\n\t"
    "ds_read_b128 v[60:63], v236 offset:9472\n\t"
    "v_smfmac_f32_16x16x64_bf16 v[20:23], a[144:147], v[48:55], v194\n\t"
    "s_waitcnt lgkmcnt(2)\n\t"
    "ds_read_b128 v[36:39], v244 offset:256\n\t"
    "v_smfmac_f32_16x16x64_bf16 v[20:23], a[164:167], v[64:71], v196 abid:1\n\t"
    "ds_read_b32 v93, v86 offset:9220\n\t"
    "v_cvt_pk_bf16_f32 v102, v8, v9\n\t"
    "v_smfmac_f32_16x16x64_bf16 v[24:27], a[148:151], v[48:55], v194 abid:1\n\t"
    "ds_read_b128 v[56:59], v217 offset:9472\n\t"
    "v_cvt_pk_bf16_f32 v103, v10, v11\n\t"
    "v_smfmac_f32_16x16x64_bf16 v[24:27], a[168:171], v[64:71], v197\n\t"
    "ds_read_b128 v[44:47], v236 offset:9600\n\t"
    "v_cndmask_b32_dpp v8, v100, v96, vcc quad_perm:[0,0,2,2] row_mask:0xf bank_mask:0xf\n\t"
    "v_cndmask_b32_dpp v9, v101, v97, vcc quad_perm:[0,0,2,2] row_mask:0xf bank_mask:0xf\n\t"
    "v_cndmask_b32_dpp v10, v102, v98, vcc quad_perm:[0,0,2,2] row_mask:0xf bank_mask:0xf\n\t"
    "v_smfmac_f32_16x16x64_bf16 v[16:19], a[152:155], v[48:55], v195\n\t"
    "buffer_load_dwordx4 v239, s[40:43], s68 offen offset:2560 nt lds\n\t"
    "v_smfmac_f32_16x16x64_bf16 v[16:19], a[172:175], v[64:71], v197 abid:1\n\t"
    "s_waitcnt lgkmcnt(5)\n\t"
    "s_mov_b32 s77, 531521\n\t"
    "ds_read_b128 v[40:43], v217 offset:9600\n\t"
    "v_cndmask_b32_dpp v11, v103, v99, vcc quad_perm:[0,0,2,2] row_mask:0xf bank_mask:0xf\n\t"
    "v_mfma_f32_16x16x32_bf16 v[16:19], v[80:83], v[88:91], v[16:19]\n\t"
    "s_memrealtime s[84:85]\n\t"
    "ds_read_b32 v94, v229 offset:9220\n\t"
    "v_add_u32_sdwa v88, v248, v246 dst_sel:DWORD dst_unused:UNUSED_PAD src0_sel:DWORD src1_sel:WORD_0\n\t"
    "v_smfmac_f32_16x16x64_bf16 v[28:31], a[156:159], v[48:55], v195 abid:1\n\t"
    "ds_read_b32 v95, v87 offset:9220\n\t"
    "ds_read_u16 v88, v88 offset:9216\n\t"
    "v_smfmac_f32_16x16x64_bf16 v[28:31], a[176:179], v[64:71], v198\n\t"
    "s_mov_b32 s76, 2231369728\n\t"
    "buffer_store_dwordx4 v[8:11], v241, s[44:47], s66 offen offset:128 sc1\n\t"
    "ds_read_b128 v[80:83], v245 offset:5120\n\t"
    "v_cvt_pk_bf16_f32 v12, v12, v13\n\t"
    "v_add_u32_sdwa v90, v248, v247 dst_sel:DWORD dst_unused:UNUSED_PAD src0_sel:DWORD src1_sel:WORD_0\n\t"
    "v_add_u32_sdwa v91, v248, v247 dst_sel:DWORD dst_unused:UNUSED_PAD src0_sel:DWORD src1_sel:WORD_1\n\t"
    "v_smfmac_f32_16x16x64_bf16 v[32:35], a[160:163], v[48:55], v196\n\t"
    "s_not_b64 vcc, s[18:19]\n\t"
    "ds_read_u16 v90, v90 offset:9216\n\t"
    "v_add_u32_sdwa v89, v248, v246 dst_sel:DWORD dst_unused:UNUSED_PAD src0_sel:DWORD src1_sel:WORD_1\n\t"
    "v_smfmac_f32_16x16x64_bf16 v[32:35], a[180:183], v[64:71], v198 abid:1\n\t"
    "s_waitcnt lgkmcnt(7)\n\t"
    "ds_read_u16 v89, v89 offset:9216\n\t"
    "ds_read_u16 v91, v91 offset:9216\n\t"
    "v_cndmask_b32_dpp v100, v96, v100, vcc quad_perm:[1,1,3,3] row_mask:0xf bank_mask:0xf\n\t"
    "v_cndmask_b32_dpp v103, v99, v103, vcc quad_perm:[1,1,3,3] row_mask:0xf bank_mask:0xf\n\t"
    "v_smfmac_f32_16x16x64_bf16 v[20:23], a[184:187], v[56:63], v199\n\t"
    "s_waitcnt lgkmcnt(7)\n\t"
    "ds_read_b128 v[64:67], v217 offset:9856\n\t"
    "v_cndmask_b32_dpp v101, v97, v101, vcc quad_perm:[1,1,3,3] row_mask:0xf bank_mask:0xf\n\t"
    "v_smfmac_f32_16x16x64_bf16 v[20:23], a[204:207], v[40:47], v201 abid:1\n\t"
    "v_cndmask_b32_dpp v102, v98, v102, vcc quad_perm:[1,1,3,3] row_mask:0xf bank_mask:0xf\n\t"
    "v_smfmac_f32_16x16x64_bf16 v[24:27], a[188:191], v[56:63], v199 abid:1\n\t"
    "ds_read_b128 v[48:51], v217 offset:9728\n\t"
    "v_cvt_pk_bf16_f32 v13, v14, v15\n\t"
    "v_smfmac_f32_16x16x64_bf16 v[24:27], a[208:211], v[40:47], v202\n\t"
    "s_waitcnt lgkmcnt(5)\n\t"
    "s_add_i32 s86, s87, 1103\n\t"
    "buffer_store_dwordx4 v[100:103], v241, s[44:47], s66 offen offset:1280 sc1\n\t"
    "ds_read_b128 v[68:71], v236 offset:9856\n\t"
    "ds_write_b64 v242, v[12:13] offset:512\n\t"
    "v_mfma_f32_16x16x32_bf16 v[24:27], v[80:83], v[92:95], v[24:27]\n\t"
    "s_add_i32 s98, s98, 92\n\t"
    "ds_read_b32 v92, v226 offset:9220\n\t"
    "ds_read_b32 v95, v85 offset:9220\n\t"
    "v_smfmac_f32_16x16x64_bf16 v[16:19], a[192:195], v[56:63], v200\n\t"
    "s_add_i32 s89, s86, s23\n\t"
    "s_min_i32 s98, s98, s89\n\t"
    "ds_read_b128 v[52:55], v236 offset:9728\n\t"
    "v_smfmac_f32_16x16x64_bf16 v[16:19], a[212:215], v[40:47], v202 abid:1\n\t"
    "s_waitcnt lgkmcnt(7)\n\t"
    "s_max_i32 s86, s86, s98\n\t"
    "ds_read_b32 v94, v227 offset:9220\n\t"
    "s_set_gpr_idx_on s80, gpr_idx(SRC2,DST)\n\t"
    "v_mfma_f32_16x16x32_bf16 v[0:3], v[250:253], v[88:91], v[0:3]\n\t"
    "s_set_gpr_idx_off\n\t"
    "s_add_u32 m0, s52, 0\n\t"
    "s_waitcnt lgkmcnt(4)\n\t"
    "s_lshr_b64 s[80:81], s[76:77], s70\n\t"
    "s_sub_u32 s89, s84, s24\n\t"
    "s_barrier\n\t"
    "buffer_load_dwordx4 v239, s[40:43], s68 offen offset:3584 nt lds\n\t"
    "ds_read_b32 v93, v84 offset:9220\n\t"
    "v_smfmac_f32_16x16x64_bf16 v[28:31], a[196:199], v[56:63], v200 abid:1\n\t"
    "s_and_b32 s80, s80, 63\n\t"
    "s_cmp_lt_i32 s89, s86\n\t"
    "s_cbranch_scc1 .Lpc2_%=\n\t"
    ".Lpc2r_%=:\n\t"
    "ds_read_b128 v[80:83], v245 offset:4096\n\t"
    "v_smfmac_f32_16x16x64_bf16 v[28:31], a[216:219], v[40:47], v203\n\t"
    "s_lshl_b32 s80, s80, 2\n\t"
    "ds_read_b128 v[96:99], v243 offset:2560\n\t"
    "s_set_gpr_idx_on s80, gpr_idx(SRC2,DST)\n\t"
    "v_mfma_f32_16x16x32_bf16 v[0:3], v[252:255], v[88:91], v[0:3]\n\t"
    "s_set_gpr_idx_off\n\t"
    "s_max_i32 s98, s89, s86\n\t"
    "s_cmp_lg_u32 s22, 0\n\t"
    "s_cbranch_scc1 .Lsl7_%=\n\t"
    ".Lslr7_%=:\n\t"
    "v_smfmac_f32_16x16x64_bf16 v[32:35], a[200:203], v[56:63], v201\n\t"
    "s_sub_i32 s23, s23, 3\n\t"
    "ds_read_b128 v[56:59], v217 offset:9984\n\t"
    "v_add_u32_sdwa v90, v248, v235 dst_sel:DWORD dst_unused:UNUSED_PAD src0_sel:DWORD src1_sel:WORD_0\n\t"
    "v_smfmac_f32_16x16x64_bf16 v[32:35], a[220:223], v[40:47], v203 abid:1\n\t"
    "s_waitcnt lgkmcnt(5)\n\t"
    "ds_read_b32 v90, v90 offset:9220\n\t"
    "v_add_u32_sdwa v89, v248, v234 dst_sel:DWORD dst_unused:UNUSED_PAD src0_sel:DWORD src1_sel:WORD_1\n\t"
    "v_smfmac_f32_16x16x64_bf16 v[20:23], a[224:227], v[48:55], v204\n\t"
    "s_mov_b32 s65, 9437184\n\t"
    "ds_read_b32 v89, v89 offset:9220\n\t"
    "v_add_u32_sdwa v91, v248, v235 dst_sel:DWORD dst_unused:UNUSED_PAD src0_sel:DWORD src1_sel:WORD_1\n\t"
    "v_smfmac_f32_16x16x64_bf16 v[20:23], a[244:247], v[64:71], v206 abid:1\n\t"
    "s_waitcnt lgkmcnt(3)\n\t"
    "s_mov_b32 exec_hi, 0\n\t"
    "buffer_store_dwordx4 v[96:99], v240, s[44:47], s67 offen offset:0 sc1\n\t"
    "s_mov_b32 exec_hi, -1\n\t"
    "ds_read_b128 v[60:63], v236 offset:9984\n\t"
    "v_add_u32_sdwa v88, v248, v234 dst_sel:DWORD dst_unused:UNUSED_PAD src0_sel:DWORD src1_sel:WORD_0\n\t"
    "v_mfma_f32_16x16x32_bf16 v[20:23], v[80:83], v[92:95], v[20:23]\n\t"
    "ds_read_b32 v91, v91 offset:9220\n\t"
    "v_smfmac_f32_16x16x64_bf16 v[24:27], a[228:231], v[48:55], v204 abid:1\n\t"
    "s_bitcmp1_b32 s54, 7\n\t"
    "s_mov_b32 s68, 33030144\n\t"
    "s_cmov_b32 s68, 33103872\n\t"
    "ds_read_b32 v88, v88 offset:9220\n\t"
    "v_smfmac_f32_16x16x64_bf16 v[24:27], a[248:251], v[64:71], v207\n\t"
    "ds_read_b128 v[80:83], v245 offset:8192\n\t"
    "ds_read_b128 v[40:43], v217 offset:10112\n\t"
    "v_smfmac_f32_16x16x64_bf16 v[16:19], a[232:235], v[48:55], v205\n\t"
    "s_add_u32 m0, s52, 4608\n\t"
    "s_mov_b32 exec_hi, 0\n\t"
    "buffer_load_dwordx4 v237, s[40:43], s68 offen offset:0 nt lds\n\t"
    "s_mov_b32 exec_hi, -1\n\t"
    "v_smfmac_f32_16x16x64_bf16 v[16:19], a[252:255], v[64:71], v207 abid:1\n\t"
    "ds_read_b128 v[44:47], v236 offset:10112\n\t"
    "v_smfmac_f32_16x16x64_bf16 v[28:31], a[236:239], v[48:55], v205 abid:1\n\t"
    "s_mov_b32 s76, 1224736768\n\t"
    "ds_read_b128 v[12:15], v244 offset:384\n\t"
    "v_smfmac_f32_16x16x64_bf16 v[28:31], v[108:111], v[64:71], v208\n\t"
    "s_mov_b32 s77, 329776\n\t"
    "v_smfmac_f32_16x16x64_bf16 v[32:35], a[240:243], v[48:55], v206\n\t"
    "s_lshr_b64 s[80:81], s[76:77], s69\n\t"
    "ds_read_b128 v[48:51], v217 offset:10240\n\t"
    "ds_read_b128 v[52:55], v236 offset:10240\n\t"
    "v_smfmac_f32_16x16x64_bf16 v[32:35], v[112:115], v[64:71], v208 abid:1\n\t"
    "s_waitcnt lgkmcnt(5)\n\t"
    "ds_read_b32 v95, v107 offset:9220\n\t"
    "v_mfma_f32_16x16x32_bf16 v[32:35], v[80:83], v[88:91], v[32:35]\n\t"
    "s_add_u32 s66, s57, s65\n\t"
    "v_smfmac_f32_16x16x64_bf16 v[20:23], v[116:119], v[56:63], v209\n\t"
    "s_waitcnt lgkmcnt(4)\n\t"
    "ds_read_b128 v[64:67], v217 offset:13824\n\t"
    "v_smfmac_f32_16x16x64_bf16 v[20:23], v[136:139], v[40:47], v211 abid:1\n\t"
    "s_waitcnt lgkmcnt(2)\n\t"
    "ds_read_b128 v[80:83], v245 offset:7168\n\t"
    "v_smfmac_f32_16x16x64_bf16 v[20:23], v[156:159], v[48:55], v214\n\t"
    "ds_read_b32 v92, v232 offset:9220\n\t"
    "ds_read_b32 v93, v106 offset:9220\n\t"
    "v_smfmac_f32_16x16x64_bf16 v[24:27], v[120:123], v[56:63], v209 abid:1\n\t"
    "v_smfmac_f32_16x16x64_bf16 v[24:27], v[140:143], v[40:47], v212\n\t"
    "ds_read_b32 v94, v233 offset:9220\n\t"
    "v_smfmac_f32_16x16x64_bf16 v[24:27], v[160:163], v[48:55], v214 abid:1\n\t"
    "buffer_load_dwordx4 v237, s[40:43], s68 offen offset:512 nt lds\n\t"
    "ds_read_b128 v[4:7], v244 offset:320\n\t"
    "v_smfmac_f32_16x16x64_bf16 v[16:19], v[124:127], v[56:63], v210\n\t"
    "s_mov_b32 s76, 1224736768\n\t"
    "ds_read_b32 v90, v231 offset:13828\n\t"
    "v_smfmac_f32_16x16x64_bf16 v[16:19], v[144:147], v[40:47], v212 abid:1\n\t"
    "ds_read_b128 v[68:71], v236 offset:13824\n\t"
    "v_smfmac_f32_16x16x64_bf16 v[16:19], v[164:167], v[48:55], v215\n\t"
    "s_and_b32 s80, s80, 63\n\t"
    "ds_read_b32 v89, v104 offset:13828\n\t"
    "v_cvt_pk_bf16_f32 v101, v22, v23\n\t"
    "v_smfmac_f32_16x16x64_bf16 v[28:31], v[128:131], v[56:63], v210 abid:1\n\t"
    "ds_read_b32 v88, v230 offset:13828\n\t"
    "v_cvt_pk_bf16_f32 v102, v24, v25\n\t"
    "v_smfmac_f32_16x16x64_bf16 v[28:31], v[148:151], v[40:47], v213\n\t"
    "s_mov_b64 vcc, s[18:19]\n\t"
    "ds_read_b128 v[8:11], v244 offset:448\n\t"
    "v_cvt_pk_bf16_f32 v103, v26, v27\n\t"
    "v_smfmac_f32_16x16x64_bf16 v[28:31], v[168:171], v[48:55], v215 abid:1\n\t"
    "s_waitcnt lgkmcnt(6)\n\t"
    "ds_read_b32 v91, v105 offset:13828\n\t"
    "v_mfma_f32_16x16x32_bf16 v[28:31], v[80:83], v[92:95], v[28:31]\n\t"
    "ds_read_b128 v[80:83], v245 offset:6144\n\t"
    "v_cvt_pk_bf16_f32 v96, v16, v17\n\t"
    "v_smfmac_f32_16x16x64_bf16 v[32:35], v[132:135], v[56:63], v211\n\t"
    "ds_read_b128 v[60:63], v236 offset:13952\n\t"
    "v_cvt_pk_bf16_f32 v100, v20, v21\n\t"
    "v_smfmac_f32_16x16x64_bf16 v[32:35], v[152:155], v[40:47], v213 abid:1\n\t"
    "ds_read_b128 v[56:59], v217 offset:13952\n\t"
    "v_cvt_pk_bf16_f32 v97, v18, v19\n\t"
    "v_smfmac_f32_16x16x64_bf16 v[32:35], v[172:175], v[48:55], v216\n\t"
    "s_waitcnt lgkmcnt(7)\n\t"
    "ds_read_b128 v[48:51], v217 offset:14208\n\t"
    "v_cvt_pk_bf16_f32 v98, v28, v29\n\t"
    "v_smfmac_f32_16x16x64_bf16 v[36:39], a[144:147], v[64:71], v194\n\t"
    "s_waitcnt lgkmcnt(1)\n\t"
    "ds_read_b128 v[20:23], v244 offset:512\n\t"
    "v_cvt_pk_bf16_f32 v99, v30, v31\n\t"
    "v_smfmac_f32_16x16x64_bf16 v[36:39], a[164:167], v[56:63], v196 abid:1\n\t"
    "ds_read_b128 v[40:43], v217 offset:14080\n\t"
    "v_cndmask_b32_dpp v30, v98, v102, vcc quad_perm:[0,0,2,2] row_mask:0xf bank_mask:0xf\n\t"
    "v_smfmac_f32_16x16x64_bf16 v[4:7], a[148:151], v[64:71], v194 abid:1\n\t"
    "s_mov_b32 s77, 329776\n\t"
    "buffer_load_dwordx4 v238, s[40:43], s68 offen offset:1536 nt lds\n\t"
    "ds_read_b32 v93, v86 offset:13828\n\t"
    "v_smfmac_f32_16x16x64_bf16 v[4:7], a[168:171], v[56:63], v197\n\t"
    "ds_read_b128 v[44:47], v236 offset:14080\n\t"
    "v_cndmask_b32_dpp v28, v96, v100, vcc quad_perm:[0,0,2,2] row_mask:0xf bank_mask:0xf\n\t"
    "v_smfmac_f32_16x16x64_bf16 v[12:15], a[152:155], v[64:71], v195\n\t"
    "ds_read_b128 v[52:55], v236 offset:14208\n\t"
    "v_cndmask_b32_dpp v31, v99, v103, vcc quad_perm:[0,0,2,2] row_mask:0xf bank_mask:0xf\n\t"
    "v_smfmac_f32_16x16x64_bf16 v[12:15], a[172:175], v[56:63], v197 abid:1\n\t"
    "ds_read_b32 v95, v87 offset:13828\n\t"
    "v_cndmask_b32_dpp v29, v97, v101, vcc quad_perm:[0,0,2,2] row_mask:0xf bank_mask:0xf\n\t"
    "v_mfma_f32_16x16x32_bf16 v[12:15], v[80:83], v[88:91], v[12:15]\n\t"
    "s_memrealtime s[84:85]\n\t"
    "ds_read_b32 v92, v228 offset:13828\n\t"
    "v_add_u32_sdwa v88, v248, v246 dst_sel:DWORD dst_unused:UNUSED_PAD src0_sel:DWORD src1_sel:WORD_0\n\t"
    "v_smfmac_f32_16x16x64_bf16 v[8:11], a[156:159], v[64:71], v195 abid:1\n\t"
    "s_not_b64 vcc, s[18:19]\n\t"
    "ds_read_b32 v94, v229 offset:13828\n\t"
    "v_add_u32_sdwa v89, v248, v246 dst_sel:DWORD dst_unused:UNUSED_PAD src0_sel:DWORD src1_sel:WORD_1\n\t"
    "v_smfmac_f32_16x16x64_bf16 v[8:11], a[176:179], v[56:63], v198\n\t"
    "s_waitcnt lgkmcnt(7)\n\t"
    "buffer_store_dwordx4 v[28:31], v241, s[44:47], s65 offen offset:128 sc1\n\t"
    "ds_read_b128 v[80:83], v245 offset:5120\n\t"
    "v_cvt_pk_bf16_f32 v32, v32, v33\n\t"
    "v_add_u32_sdwa v90, v248, v247 dst_sel:DWORD dst_unused:UNUSED_PAD src0_sel:DWORD src1_sel:WORD_0\n\t"
    "v_add_u32_sdwa v91, v248, v247 dst_sel:DWORD dst_unused:UNUSED_PAD src0_sel:DWORD src1_sel:WORD_1\n\t"
    "v_smfmac_f32_16x16x64_bf16 v[20:23], a[160:163], v[64:71], v196\n\t"
    "ds_read_u16 v89, v89 offset:13824\n\t"
    "v_cndmask_b32_dpp v97, v101, v97, vcc quad_perm:[1,1,3,3] row_mask:0xf bank_mask:0xf\n\t"
    "v_smfmac_f32_16x16x64_bf16 v[20:23], a[180:183], v[56:63], v198 abid:1\n\t"
    "s_waitcnt lgkmcnt(6)\n\t"
    "ds_read_u16 v90, v90 offset:13824\n\t"
    "ds_read_u16 v91, v91 offset:13824\n\t"
    "v_cndmask_b32_dpp v99, v103, v99, vcc quad_perm:[1,1,3,3] row_mask:0xf bank_mask:0xf\n\t"
    "v_smfmac_f32_16x16x64_bf16 v[36:39], a[184:187], v[40:47], v199\n\t"
    "s_waitcnt lgkmcnt(7)\n\t"
    "ds_read_u16 v88, v88 offset:13824\n\t"
    "v_cndmask_b32_dpp v96, v100, v96, vcc quad_perm:[1,1,3,3] row_mask:0xf bank_mask:0xf\n\t"
    "v_smfmac_f32_16x16x64_bf16 v[36:39], a[204:207], v[48:55], v201 abid:1\n\t"
    "s_lshl_b32 s80, s80, 2\n\t"
    "ds_read_b128 v[68:71], v236 offset:14336\n\t"
    "v_cndmask_b32_dpp v98, v102, v98, vcc quad_perm:[1,1,3,3] row_mask:0xf bank_mask:0xf\n\t"
    "v_smfmac_f32_16x16x64_bf16 v[4:7], a[188:191], v[40:47], v199 abid:1\n\t"
    "ds_read_b128 v[64:67], v217 offset:14336\n\t"
    "v_cvt_pk_bf16_f32 v33, v34, v35\n\t"
    "v_smfmac_f32_16x16x64_bf16 v[4:7], a[208:211], v[48:55], v202\n\t"
    "s_waitcnt lgkmcnt(6)\n\t"
    "s_add_i32 s86, s87, 1191\n\t"
    "buffer_store_dwordx4 v[96:99], v241, s[44:47], s65 offen offset:1280 sc1\n\t"
    "ds_read_b128 v[56:59], v217 offset:14464\n\t"
    "ds_write_b64 v242, v[32:33] offset:0\n\t"
    "v_mfma_f32_16x16x32_bf16 v[4:7], v[80:83], v[92:95], v[4:7]\n\t"
    "s_add_i32 s98, s98, 88\n\t"
    "ds_read_b128 v[60:63], v236 offset:14464\n\t"
    "v_smfmac_f32_16x16x64_bf16 v[12:15], a[192:195], v[40:47], v200\n\t"
    "s_add_i32 s89, s86, s23\n\t"
    "s_min_i32 s98, s98, s89\n\t"
    "ds_read_b32 v92, v226 offset:13828\n\t"
    "ds_read_b32 v93, v84 offset:13828\n\t"
    "v_smfmac_f32_16x16x64_bf16 v[12:15], a[212:215], v[48:55], v202 abid:1\n\t"
    "s_waitcnt lgkmcnt(7)\n\t"
    "s_max_i32 s86, s86, s98\n\t"
    "ds_read_b128 v[80:83], v245 offset:4096\n\t"
    "s_set_gpr_idx_on s80, gpr_idx(SRC2,DST)\n\t"
    "v_mfma_f32_16x16x32_bf16 v[0:3], v[250:253], v[88:91], v[0:3]\n\t"
    "s_set_gpr_idx_off\n\t"
    "s_add_u32 m0, s52, 4608\n\t"
    "s_waitcnt lgkmcnt(4)\n\t"
    "s_sub_u32 s89, s84, s24\n\t"
    "s_barrier\n\t"
    "buffer_load_dwordx4 v239, s[40:43], s68 offen offset:2560 nt lds\n\t"
    "ds_read_b32 v94, v227 offset:13828\n\t"
    "ds_read_b32 v95, v85 offset:13828\n\t"
    "v_smfmac_f32_16x16x64_bf16 v[8:11], a[196:199], v[40:47], v200 abid:1\n\t"
    "s_lshr_b64 s[80:81], s[76:77], s70\n\t"
    "s_cmp_lt_i32 s89, s86\n\t"
    "s_cbranch_scc1 .Lpc3_%=\n\t"
    ".Lpc3r_%=:\n\t"
    "ds_read_b128 v[100:103], v243 offset:2048\n\t"
    "v_smfmac_f32_16x16x64_bf16 v[8:11], a[216:219], v[48:55], v203\n\t"
    "s_and_b32 s80, s80, 63\n\t"
    "s_lshl_b32 s80, s80, 2\n\t"
    "s_set_gpr_idx_on s80, gpr_idx(SRC2,DST)\n\t"
    "v_mfma_f32_16x16x32_bf16 v[0:3], v[252:255], v[88:91], v[0:3]\n\t"
    "s_set_gpr_idx_off\n\t"
    "s_max_i32 s98, s89, s86\n\t"
    "s_cmp_lg_u32 s22, 0\n\t"
    "s_cbranch_scc1 .Lsl8_%=\n\t"
    ".Lslr8_%=:\n\t"
    "v_smfmac_f32_16x16x64_bf16 v[20:23], a[200:203], v[40:47], v201\n\t"
    "s_sub_i32 s23, s23, 3\n\t"
    "ds_read_b128 v[40:43], v217 offset:14592\n\t"
    "v_smfmac_f32_16x16x64_bf16 v[20:23], a[220:223], v[48:55], v203 abid:1\n\t"
    "ds_read_b128 v[44:47], v236 offset:14592\n\t"
    "v_add_u32_sdwa v89, v248, v234 dst_sel:DWORD dst_unused:UNUSED_PAD src0_sel:DWORD src1_sel:WORD_1\n\t"
    "v_smfmac_f32_16x16x64_bf16 v[36:39], a[224:227], v[64:71], v204\n\t"
    "s_waitcnt lgkmcnt(8)\n\t"
    "ds_read_b32 v89, v89 offset:13828\n\t"
    "v_smfmac_f32_16x16x64_bf16 v[36:39], a[244:247], v[56:63], v206 abid:1\n\t"
    "s_waitcnt lgkmcnt(3)\n\t"
    "s_mov_b32 exec_hi, 0\n\t"
    "buffer_store_dwordx4 v[100:103], v240, s[44:47], s66 offen offset:0 sc1\n\t"
    "s_mov_b32 exec_hi, -1\n\t"
    "ds_read_b128 v[48:51], v217 offset:14720\n\t"
    "v_add_u32_sdwa v90, v248, v235 dst_sel:DWORD dst_unused:UNUSED_PAD src0_sel:DWORD src1_sel:WORD_0\n\t"
    "v_add_u32_sdwa v91, v248, v235 dst_sel:DWORD dst_unused:UNUSED_PAD src0_sel:DWORD src1_sel:WORD_1\n\t"
    "v_mfma_f32_16x16x32_bf16 v[36:39], v[80:83], v[92:95], v[36:39]\n\t"
    "ds_read_b32 v91, v91 offset:13828\n\t"
    "v_add_u32_sdwa v88, v248, v234 dst_sel:DWORD dst_unused:UNUSED_PAD src0_sel:DWORD src1_sel:WORD_0\n\t"
    "v_smfmac_f32_16x16x64_bf16 v[4:7], a[228:231], v[64:71], v204 abid:1\n\t"
    "ds_read_b32 v88, v88 offset:13828\n\t"
    "ds_read_b32 v90, v90 offset:13828\n\t"
    "v_smfmac_f32_16x16x64_bf16 v[4:7], a[248:251], v[56:63], v207\n\t"
    "ds_read_b128 v[80:83], v245 offset:8192\n\t"
    "v_smfmac_f32_16x16x64_bf16 v[12:15], a[232:235], v[64:71], v205\n\t"
    "s_add_u32 m0, s52, 4608\n\t"
    "s_nop 0\n\t"
    "buffer_load_dwordx4 v239, s[40:43], s68 offen offset:3584 nt lds\n\t"
    "v_smfmac_f32_16x16x64_bf16 v[12:15], a[252:255], v[56:63], v207 abid:1\n\t"
    "ds_read_b128 v[52:55], v236 offset:14720\n\t"
    "v_smfmac_f32_16x16x64_bf16 v[8:11], a[236:239], v[64:71], v205 abid:1\n\t"
    "ds_read_b128 v[24:27], v244 offset:256\n\t"
    "v_smfmac_f32_16x16x64_bf16 v[8:11], v[108:111], v[56:63], v208\n\t"
    "s_bitcmp1_b32 s54, 8\n\t"
    "s_mov_b32 s68, 37748736\n\t"
    "s_cmov_b32 s68, 37822464\n\t"
    "ds_read_b32 v93, v106 offset:13828\n\t"
    "v_smfmac_f32_16x16x64_bf16 v[20:23], a[240:243], v[64:71], v206\n\t"
    "ds_read_b128 v[64:67], v217 offset:14848\n\t"
    "v_smfmac_f32_16x16x64_bf16 v[20:23], v[112:115], v[56:63], v208 abid:1\n\t"
    "s_waitcnt lgkmcnt(4)\n\t"
    "s_mov_b32 s76, 100663296\n\t"
    "ds_read_b128 v[68:71], v236 offset:14848\n\t"
    "v_mfma_f32_16x16x32_bf16 v[20:23], v[80:83], v[88:91], v[20:23]\n\t"
    "s_mov_b64 vcc, s[18:19]\n\t"
    "ds_read_b128 v[80:83], v245 offset:7168\n\t"
    "v_smfmac_f32_16x16x64_bf16 v[36:39], v[116:119], v[40:47], v209\n\t"
    "s_waitcnt lgkmcnt(5)\n\t"
    "ds_read_b32 v92, v232 offset:13828\n\t"
    "v_smfmac_f32_16x16x64_bf16 v[36:39], v[136:139], v[48:55], v211 abid:1\n\t"
    "s_waitcnt lgkmcnt(2)\n\t"
    "s_mov_b32 s77, 597121\n\t"
    "s_lshr_b64 s[80:81], s[76:77], s69\n\t"
    "ds_read_b128 v[16:19], v244 offset:320\n\t"
    "v_smfmac_f32_16x16x64_bf16 v[36:39], v[156:159], v[64:71], v214\n\t"
    "ds_read_b128 v[60:63], v236 offset:18432\n\t"
    "v_smfmac_f32_16x16x64_bf16 v[4:7], v[120:123], v[40:47], v209 abid:1\n\t"
    "ds_read_b32 v89, v104 offset:18436\n\t"
    "v_smfmac_f32_16x16x64_bf16 v[4:7], v[140:143], v[48:55], v212\n\t"
    "s_add_u32 m0, s52, 9216\n\t"
    "s_mov_b32 exec_hi, 0\n\t"
    "buffer_load_dwordx4 v237, s[40:43], s68 offen offset:0 nt lds\n\t"
    "s_mov_b32 exec_hi, -1\n\t"
    "ds_read_b32 v95, v107 offset:13828\n\t"
    "v_smfmac_f32_16x16x64_bf16 v[4:7], v[160:163], v[64:71], v214 abid:1\n\t"
    "s_mov_b32 s67, 14155776\n\t"
    "ds_read_b32 v94, v233 offset:13828\n\t"
    "v_cvt_pk_bf16_f32 v96, v36, v37\n\t"
    "v_smfmac_f32_16x16x64_bf16 v[12:15], v[124:127], v[40:47], v210\n\t"
    "ds_read_b128 v[56:59], v217 offset:18432\n\t"
    "v_smfmac_f32_16x16x64_bf16 v[12:15], v[144:147], v[48:55], v212 abid:1\n\t"
    "ds_read_b128 v[32:35], v244 offset:384\n\t"
    "v_smfmac_f32_16x16x64_bf16 v[12:15], v[164:167], v[64:71], v215\n\t"
    "s_and_b32 s80, s80, 63\n\t"
    "ds_read_b32 v90, v231 offset:18436\n\t"
    "v_cvt_pk_bf16_f32 v99, v6, v7\n\t"
    "v_smfmac_f32_16x16x64_bf16 v[8:11], v[128:131], v[40:47], v210 abid:1\n\t"
    "s_mov_b32 s77, 597121\n\t"
    "ds_read_b32 v88, v230 offset:18436\n\t"
    "v_smfmac_f32_16x16x64_bf16 v[8:11], v[148:151], v[48:55], v213\n\t"
    "ds_read_b128 v[28:31], v244 offset:448\n\t"
    "v_cvt_pk_bf16_f32 v97, v38, v39\n\t"
    "v_smfmac_f32_16x16x64_bf16 v[8:11], v[168:171], v[64:71], v215 abid:1\n\t"
    "s_waitcnt lgkmcnt(5)\n\t"
    "ds_read_b128 v[36:39], v244 offset:512\n\t"
    "v_cvt_pk_bf16_f32 v100, v12, v13\n\t"
    "v_mfma_f32_16x16x32_bf16 v[8:11], v[80:83], v[92:95], v[8:11]\n\t"
    "ds_read_b128 v[80:83], v245 offset:6144\n\t"
    "v_cvt_pk_bf16_f32 v98, v4, v5\n\t"
    "v_smfmac_f32_16x16x64_bf16 v[20:23], v[132:135], v[40:47], v211\n\t"
    "ds_read_b128 v[40:43], v217 offset:18560\n\t"
    "ds_read_b128 v[44:47], v236 offset:18560\n\t"
    "v_smfmac_f32_16x16x64_bf16 v[20:23], v[152:155], v[48:55], v213 abid:1\n\t"
    "ds_read_b32 v91, v105 offset:18436\n\t"
    "v_cvt_pk_bf16_f32 v101, v14, v15\n\t"
    "v_smfmac_f32_16x16x64_bf16 v[20:23], v[172:175], v[64:71], v216\n\t"
    "s_waitcnt lgkmcnt(9)\n\t"
    "ds_read_b32 v95, v87 offset:18436\n\t"
    "v_cvt_pk_bf16_f32 v102, v8, v9\n\t"
    "v_cvt_pk_bf16_f32 v103, v10, v11\n\t"
    "v_smfmac_f32_16x16x64_bf16 v[24:27], a[144:147], v[56:63], v194\n\t"
    "s_waitcnt lgkmcnt(2)\n\t"
    "ds_read_b128 v[48:51], v217 offset:18688\n\t"
    "v_cndmask_b32_dpp v9, v101, v97, vcc quad_perm:[0,0,2,2] row_mask:0xf bank_mask:0xf\n\t"
    "v_smfmac_f32_16x16x64_bf16 v[24:27], a[164:167], v[40:47], v196 abid:1\n\t"
    "ds_read_b128 v[52:55], v236 offset:18688\n\t"
    "v_cndmask_b32_dpp v11, v103, v99, vcc quad_perm:[0,0,2,2] row_mask:0xf bank_mask:0xf\n\t"
    "v_smfmac_f32_16x16x64_bf16 v[16:19], a[148:151], v[56:63], v194 abid:1\n\t"
    "ds_read_b128 v[64:67], v217 offset:18816\n\t"
    "v_cndmask_b32_dpp v8, v100, v96, vcc quad_perm:[0,0,2,2] row_mask:0xf bank_mask:0xf\n\t"
    "v_smfmac_f32_16x16x64_bf16 v[16:19], a[168:171], v[40:47], v197\n\t"
    "ds_read_b32 v94, v229 offset:18436\n\t"
    "v_cndmask_b32_dpp v10, v102, v98, vcc quad_perm:[0,0,2,2] row_mask:0xf bank_mask:0xf\n\t"
    "v_smfmac_f32_16x16x64_bf16 v[32:35], a[152:155], v[56:63], v195\n\t"
    "s_not_b64 vcc, s[18:19]\n\t"
    "ds_read_b32 v93, v86 offset:18436\n\t"
    "v_cvt_pk_bf16_f32 v20, v20, v21\n\t"
    "v_smfmac_f32_16x16x64_bf16 v[32:35], a[172:175], v[40:47], v197 abid:1\n\t"
    "s_waitcnt lgkmcnt(6)\n\t"
    "s_mov_b32 s76, 100663296\n\t"
    "ds_read_b128 v[68:71], v236 offset:18816\n\t"
    "v_cvt_pk_bf16_f32 v21, v22, v23\n\t"
    "v_mfma_f32_16x16x32_bf16 v[32:35], v[80:83], v[88:91], v[32:35]\n\t"
    "s_memrealtime s[84:85]\n\t"
    "ds_read_b32 v92, v228 offset:18436\n\t"
    "v_add_u32_sdwa v90, v248, v247 dst_sel:DWORD dst_unused:UNUSED_PAD src0_sel:DWORD src1_sel:WORD_0\n\t"
    "v_smfmac_f32_16x16x64_bf16 v[28:31], a[156:159], v[56:63], v195 abid:1\n\t"
    "s_add_u32 s65, s57, s67\n\t"
    "ds_read_b128 v[80:83], v245 offset:5120\n\t"
    "v_add_u32_sdwa v89, v248, v246 dst_sel:DWORD dst_unused:UNUSED_PAD src0_sel:DWORD src1_sel:WORD_1\n\t"
    "v_smfmac_f32_16x16x64_bf16 v[28:31], a[176:179], v[40:47], v198\n\t"
    "buffer_store_dwordx4 v[8:11], v241, s[44:47], s67 offen offset:128 sc1\n\t"
    "ds_write_b64 v242, v[20:21] offset:512\n\t"
    "v_cndmask_b32_dpp v103, v99, v103, vcc quad_perm:[1,1,3,3] row_mask:0xf bank_mask:0xf\n\t"
    "v_add_u32_sdwa v91, v248, v247 dst_sel:DWORD dst_unused:UNUSED_PAD src0_sel:DWORD src1_sel:WORD_1\n\t"
    "v_smfmac_f32_16x16x64_bf16 v[36:39], a[160:163], v[56:63], v196\n\t"
    "ds_read_u16 v89, v89 offset:18432\n\t"
    "v_cndmask_b32_dpp v102, v98, v102, vcc quad_perm:[1,1,3,3] row_mask:0xf bank_mask:0xf\n\t"
    "v_smfmac_f32_16x16x64_bf16 v[36:39], a[180:183], v[40:47], v198 abid:1\n\t"
    "s_waitcnt lgkmcnt(8)\n\t"
    "s_lshl_b32 s80, s80, 2\n\t"
    "ds_read_u16 v90, v90 offset:18432\n\t"
    "v_add_u32_sdwa v88, v248, v246 dst_sel:DWORD dst_unused:UNUSED_PAD src0_sel:DWORD src1_sel:WORD_0\n\t"
    "v_smfmac_f32_16x16x64_bf16 v[24:27], a[184:187], v[48:55], v199\n\t"
    "s_waitcnt lgkmcnt(5)\n\t"
    "s_sub_i32 s87, s87, s99\n\t"
    "ds_read_u16 v88, v88 offset:18432\n\t"
    "ds_read_u16 v91, v91 offset:18432\n\t"
    "v_smfmac_f32_16x16x64_bf16 v[24:27], a[204:207], v[64:71], v201 abid:1\n\t"
    "buffer_load_dwordx4 v237, s[40:43], s68 offen offset:512 nt lds\n\t"
    "v_smfmac_f32_16x16x64_bf16 v[16:19], a[188:191], v[48:55], v199 abid:1\n\t"
    "s_sub_i32 s98, s98, s99\n\t"
    "ds_read_b128 v[56:59], v217 offset:18944\n\t"
    "v_cndmask_b32_dpp v101, v97, v101, vcc quad_perm:[1,1,3,3] row_mask:0xf bank_mask:0xf\n\t"
    "v_smfmac_f32_16x16x64_bf16 v[16:19], a[208:211], v[64:71], v202\n\t"
    "s_waitcnt lgkmcnt(6)\n\t"
    "s_add_i32 s86, s87, 1275\n\t"
    "ds_read_b128 v[40:43], v217 offset:19072\n\t"
    "v_mfma_f32_16x16x32_bf16 v[16:19], v[80:83], v[92:95], v[16:19]\n\t"
    "s_add_i32 s98, s98, 84\n\t"
    "ds_read_b128 v[60:63], v236 offset:18944\n\t"
    "v_cndmask_b32_dpp v100, v96, v100, vcc quad_perm:[1,1,3,3] row_mask:0xf bank_mask:0xf\n\t"
    "v_smfmac_f32_16x16x64_bf16 v[32:35], a[192:195], v[48:55], v200\n\t"
    "s_add_i32 s89, s86, s23\n\t"
    "s_min_i32 s98, s98, s89\n\t"
    "buffer_store_dwordx4 v[100:103], v241, s[44:47], s67 offen offset:1280 sc1\n\t"
    "ds_read_b128 v[80:83], v245 offset:4096\n\t"
    "ds_read_b32 v92, v226 offset:18436\n\t"
    "ds_read_b32 v94, v227 offset:18436\n\t"
    "v_smfmac_f32_16x16x64_bf16 v[32:35], a[212:215], v[64:71], v202 abid:1\n\t"
    "s_waitcnt lgkmcnt(6)\n\t"
    "s_max_i32 s86, s86, s98\n\t"
    "ds_read_b128 v[44:47], v236 offset:19072\n\t"
    "s_set_gpr_idx_on s80, gpr_idx(SRC2,DST)\n\t"
    "v_mfma_f32_16x16x32_bf16 v[0:3], v[250:253], v[88:91], v[0:3]\n\t"
    "s_set_gpr_idx_off\n\t"
    "s_sub_u32 s89, s84, s24\n\t"
    "s_waitcnt vmcnt(23)\n\t"
    "s_barrier\n\t"
    "ds_read_b128 v[96:99], v243 offset:2560\n\t"
    "v_smfmac_f32_16x16x64_bf16 v[28:31], a[196:199], v[48:55], v200 abid:1\n\t"
    "s_cmp_lt_i32 s89, s86\n\t"
    "s_cbranch_scc1 .Lpc4_%=\n\t"
    ".Lpc4r_%=:\n\t"
    "ds_read_b32 v93, v84 offset:18436\n\t"
    "ds_read_b32 v95, v85 offset:18436\n\t"
    "v_smfmac_f32_16x16x64_bf16 v[28:31], a[216:219], v[64:71], v203\n\t"
    "s_lshr_b64 s[80:81], s[76:77], s70\n\t"
    "s_and_b32 s80, s80, 63\n\t"
    "s_lshl_b32 s80, s80, 2\n\t"
    "s_set_gpr_idx_on s80, gpr_idx(SRC2,DST)\n\t"
    "v_mfma_f32_16x16x32_bf16 v[0:3], v[252:255], v[88:91], v[0:3]\n\t"
    "s_set_gpr_idx_off\n\t"
    "s_max_i32 s98, s89, s86\n\t"
    "s_cmp_lg_u32 s22, 0\n\t"
    "s_cbranch_scc1 .Lsl9_%=\n\t"
    ".Lslr9_%=:\n\t"
    "v_smfmac_f32_16x16x64_bf16 v[36:39], a[200:203], v[48:55], v201\n\t"
    "s_sub_i32 s23, s23, 3\n\t"
    "ds_read_b128 v[48:51], v217 offset:19200\n\t"
    "v_smfmac_f32_16x16x64_bf16 v[36:39], a[220:223], v[64:71], v203 abid:1\n\t"
    "s_waitcnt lgkmcnt(8)\n\t"
    "ds_read_b128 v[52:55], v236 offset:19200\n\t"
    "v_add_u32_sdwa v91, v248, v235 dst_sel:DWORD dst_unused:UNUSED_PAD src0_sel:DWORD src1_sel:WORD_1\n\t"
    "v_smfmac_f32_16x16x64_bf16 v[24:27], a[224:227], v[56:63], v204\n\t"
    "s_waitcnt lgkmcnt(5)\n\t"
    "ds_read_b128 v[64:67], v217 offset:19328\n\t"
    "v_add_u32_sdwa v89, v248, v234 dst_sel:DWORD dst_unused:UNUSED_PAD src0_sel:DWORD src1_sel:WORD_1\n\t"
    "v_smfmac_f32_16x16x64_bf16 v[24:27], a[244:247], v[40:47], v206 abid:1\n\t"
    "s_waitcnt lgkmcnt(3)\n\t"
    "s_mov_b32 exec_hi, 0\n\t"
    "buffer_store_dwordx4 v[96:99], v240, s[44:47], s65 offen offset:0 sc1\n\t"
    "s_mov_b32 exec_hi, -1\n\t"
    "ds_read_b32 v89, v89 offset:18436\n\t"
    "v_add_u32_sdwa v88, v248, v234 dst_sel:DWORD dst_unused:UNUSED_PAD src0_sel:DWORD src1_sel:WORD_0\n\t"
    "v_add_u32_sdwa v90, v248, v235 dst_sel:DWORD dst_unused:UNUSED_PAD src0_sel:DWORD src1_sel:WORD_0\n\t"
    "v_mfma_f32_16x16x32_bf16 v[24:27], v[80:83], v[92:95], v[24:27]\n\t"
    "ds_read_b32 v91, v91 offset:18436\n\t"
    "v_smfmac_f32_16x16x64_bf16 v[16:19], a[228:231], v[56:63], v204 abid:1\n\t"
    "ds_read_b32 v88, v88 offset:18436\n\t"
    "ds_read_b32 v90, v90 offset:18436\n\t"
    "v_smfmac_f32_16x16x64_bf16 v[16:19], a[248:251], v[40:47], v207\n\t"
    "ds_read_b128 v[80:83], v245 offset:8192\n\t"
    "v_smfmac_f32_16x16x64_bf16 v[32:35], a[232:235], v[56:63], v205\n\t"
    "s_add_u32 m0, s52, 9216\n\t"
    "s_nop 0\n\t"
    "buffer_load_dwordx4 v238, s[40:43], s68 offen offset:1536 nt lds\n\t"
    "v_smfmac_f32_16x16x64_bf16 v[32:35], a[252:255], v[40:47], v207 abid:1\n\t"
    "ds_read_b128 v[68:71], v236 offset:19328\n\t"
    "v_smfmac_f32_16x16x64_bf16 v[28:31], a[236:239], v[56:63], v205 abid:1\n\t"
    "s_mov_b32 s76, 101208257\n\t"
    "ds_read_b32 v92, v232 offset:18436\n\t"
    "v_smfmac_f32_16x16x64_bf16 v[28:31], v[108:111], v[40:47], v208\n\t"
    "ds_read_b128 v[20:23], v244 offset:128\n\t"
    "v_smfmac_f32_16x16x64_bf16 v[36:39], a[240:243], v[56:63], v206\n\t"
    "ds_read_b128 v[60:63], v236 offset:19456\n\t"
    "v_smfmac_f32_16x16x64_bf16 v[36:39], v[112:115], v[40:47], v208 abid:1\n\t"
    "s_waitcnt lgkmcnt(4)\n\t"
    "ds_read_b128 v[56:59], v217 offset:19456\n\t"
    "v_mfma_f32_16x16x32_bf16 v[36:39], v[80:83], v[88:91], v[36:39]\n\t"
    "s_mov_b32 s77, 468097\n\t"
    "s_lshr_b64 s[80:81], s[76:77], s69\n\t"
    "ds_read_b32 v94, v233 offset:18436\n\t"
    "v_smfmac_f32_16x16x64_bf16 v[24:27], v[116:119], v[48:55], v209\n\t"
    "s_waitcnt lgkmcnt(5)\n\t"
    "ds_read_b128 v[80:83], v245 offset:7168\n\t"
    "v_smfmac_f32_16x16x64_bf16 v[24:27], v[136:139], v[64:71], v211 abid:1\n\t"
    "s_waitcnt lgkmcnt(2)\n\t"
    "ds_read_b128 v[40:43], v217 offset:23040\n\t"
    "v_smfmac_f32_16x16x64_bf16 v[24:27], v[156:159], v[56:63], v214\n\t"
    "ds_read_b32 v95, v107 offset:18436\n\t"
    "v_smfmac_f32_16x16x64_bf16 v[16:19], v[120:123], v[48:55], v209 abid:1\n\t"
    "ds_read_b128 v[4:7], v244 offset:0\n\t"
    "v_smfmac_f32_16x16x64_bf16 v[16:19], v[140:143], v[64:71], v212\n\t"
    "ds_read_b32 v93, v106 offset:18436\n\t"
    "v_smfmac_f32_16x16x64_bf16 v[16:19], v[160:163], v[56:63], v214 abid:1\n\t"
    "ds_read_b32 v91, v77 offset:23044\n\t"
    "v_smfmac_f32_16x16x64_bf16 v[32:35], v[124:127], v[48:55], v210\n\t"
    "ds_read_b128 v[8:11], v244 offset:192\n\t"
    "v_cvt_pk_bf16_f32 v100, v24, v25\n\t"
    "v_smfmac_f32_16x16x64_bf16 v[32:35], v[144:147], v[64:71], v212 abid:1\n\t"
    "ds_read_b32 v88, v222 offset:23044\n\t"
    "v_cvt_pk_bf16_f32 v101, v26, v27\n\t"
    "v_smfmac_f32_16x16x64_bf16 v[32:35], v[164:167], v[56:63], v215\n\t"
    "ds_read_b128 v[44:47], v236 offset:23040\n\t"
    "v_smfmac_f32_16x16x64_bf16 v[28:31], v[128:131], v[48:55], v210 abid:1\n\t"
    "s_mov_b64 vcc, s[18:19]\n\t"
    "ds_read_b32 v89, v76 offset:23044\n\t"
    "v_cvt_pk_bf16_f32 v102, v16, v17\n\t"
    "v_smfmac_f32_16x16x64_bf16 v[28:31], v[148:151], v[64:71], v213\n\t"
    "ds_read_b128 v[12:15], v244 offset:64\n\t"
    "v_smfmac_f32_16x16x64_bf16 v[28:31], v[168:171], v[56:63], v215 abid:1\n\t"
    "s_waitcnt lgkmcnt(6)\n\t"
    "buffer_load_dwordx4 v239, s[40:43], s68 offen offset:2560 nt lds\n\t"
    "v_mfma_f32_16x16x32_bf16 v[28:31], v[80:83], v[92:95], v[28:31]\n\t"
    "ds_read_b32 v90, v223 offset:23044\n\t"
    "v_cvt_pk_bf16_f32 v103, v18, v19\n\t"
    "v_smfmac_f32_16x16x64_bf16 v[36:39], v[132:135], v[48:55], v211\n\t"
    "ds_read_b128 v[48:51], v217 offset:23168\n\t"
    "ds_read_b128 v[52:55], v236 offset:23168\n\t"
    "v_smfmac_f32_16x16x64_bf16 v[36:39], v[152:155], v[64:71], v213 abid:1\n\t"
    "ds_read_b32 v93, v86 offset:23044\n\t"
    "v_cvt_pk_bf16_f32 v97, v34, v35\n\t"
    "v_smfmac_f32_16x16x64_bf16 v[36:39], v[172:175], v[56:63], v216\n\t"
    "s_waitcnt lgkmcnt(6)\n\t"
    "ds_read_b128 v[80:83], v245 offset:2048\n\t"
    "v_cvt_pk_bf16_f32 v96, v32, v33\n\t"
    "v_smfmac_f32_16x16x64_bf16 v[4:7], a[0:3], v[40:47], v176\n\t"
    "s_waitcnt lgkmcnt(2)\n\t"
    "s_and_b32 s80, s80, 63\n\t"
    "ds_read_b128 v[64:67], v217 offset:23296\n\t"
    "v_cvt_pk_bf16_f32 v98, v28, v29\n\t"
    "v_smfmac_f32_16x16x64_bf16 v[4:7], a[16:19], v[48:55], v178\n\t"
    "s_lshl_b32 s4, s80, 2\n\t"
    "ds_read_b128 v[68:71], v236 offset:23296\n\t"
    "v_cndmask_b32_dpp v28, v96, v100, vcc quad_perm:[0,0,2,2] row_mask:0xf bank_mask:0xf\n\t"
    "v_smfmac_f32_16x16x64_bf16 v[12:15], a[4:7], v[40:47], v176 abid:1\n\t"
    "ds_read_b128 v[16:19], v244 offset:320\n\t"
    "v_cvt_pk_bf16_f32 v99, v30, v31\n\t"
    "v_smfmac_f32_16x16x64_bf16 v[12:15], a[20:23], v[48:55], v178 abid:1\n\t"
    "ds_read_b128 v[24:27], v244 offset:256\n\t"
    "v_smfmac_f32_16x16x64_bf16 v[20:23], a[8:11], v[40:47], v177\n\t"
    "ds_read_b128 v[56:59], v217 offset:23424\n\t"
    "v_cndmask_b32_dpp v31, v99, v103, vcc quad_perm:[0,0,2,2] row_mask:0xf bank_mask:0xf\n\t"
    "v_smfmac_f32_16x16x64_bf16 v[20:23], a[24:27], v[48:55], v179\n\t"
    "s_waitcnt lgkmcnt(5)\n\t"
    "ds_read_b128 v[60:63], v236 offset:23424\n\t"
    "v_cvt_pk_bf16_f32 v36, v36, v37\n\t"
    "v_mfma_f32_16x16x32_bf16 v[20:23], v[80:83], v[88:91], v[20:23]\n\t"
    "ds_read_b128 v[80:83], v245 offset:5120\n\t"
    "v_cndmask_b32_dpp v29, v97, v101, vcc quad_perm:[0,0,2,2] row_mask:0xf bank_mask:0xf\n\t"
    "v_smfmac_f32_16x16x64_bf16 v[8:11], a[12:15], v[40:47], v177 abid:1\n\t"
    "s_mov_b32 s66, 18874368\n\t"
    "ds_read_b32 v94, v229 offset:23044\n\t"
    "v_cndmask_b32_dpp v30, v98, v102, vcc quad_perm:[0,0,2,2] row_mask:0xf bank_mask:0xf\n\t"
    "v_smfmac_f32_16x16x64_bf16 v[8:11], a[28:31], v[48:55], v179 abid:1\n\t"
    "s_waitcnt lgkmcnt(6)\n\t"
    "buffer_store_dwordx4 v[28:31], v241, s[44:47], s66 offen offset:128 sc1\n\t"
    "ds_read_b32 v95, v87 offset:23044\n\t"
    "v_cvt_pk_bf16_f32 v37, v38, v39\n\t"
    "v_add_u32_sdwa v88, v248, v234 dst_sel:DWORD dst_unused:UNUSED_PAD src0_sel:DWORD src1_sel:WORD_0\n\t"
    "v_add_u32_sdwa v89, v248, v234 dst_sel:DWORD dst_unused:UNUSED_PAD src0_sel:DWORD src1_sel:WORD_1\n\t"
    "v_smfmac_f32_16x16x64_bf16 v[8:11], a[44:47], v[64:71], v181 abid:1\n\t"
    "s_waitcnt lgkmcnt(5)\n\t"
    "s_not_b64 vcc, s[18:19]\n\t"
    "ds_read_b128 v[32:35], v244 offset:384\n\t"
    "v_cndmask_b32_dpp v99, v103, v99, vcc quad_perm:[1,1,3,3] row_mask:0xf bank_mask:0xf\n\t"
    "v_smfmac_f32_16x16x64_bf16 v[24:27], a[144:147], v[40:47], v194\n\t"
    "ds_read_b32 v92, v228 offset:23044\n\t"
    "v_cndmask_b32_dpp v96, v100, v96, vcc quad_perm:[1,1,3,3] row_mask:0xf bank_mask:0xf\n\t"
    "v_smfmac_f32_16x16x64_bf16 v[24:27], a[164:167], v[48:55], v196 abid:1\n\t"
    "ds_write_b64 v242, v[36:37] offset:0\n\t"
    "v_smfmac_f32_16x16x64_bf16 v[24:27], a[184:187], v[64:71], v199\n\t"
    "v_cndmask_b32_dpp v97, v101, v97, vcc quad_perm:[1,1,3,3] row_mask:0xf bank_mask:0xf\n\t"
    "v_smfmac_f32_16x16x64_bf16 v[16:19], a[148:151], v[40:47], v194 abid:1\n\t"
    "ds_read_b32 v88, v88 offset:23044\n\t"
    "v_cndmask_b32_dpp v98, v102, v98, vcc quad_perm:[1,1,3,3] row_mask:0xf bank_mask:0xf\n\t"
    "v_smfmac_f32_16x16x64_bf16 v[16:19], a[168:171], v[48:55], v197\n\t"
    "buffer_store_dwordx4 v[96:99], v241, s[44:47], s66 offen offset:1280 sc1\n\t"
    "ds_read_b128 v[36:39], v244 offset:448\n\t"
    "v_add_u32_sdwa v91, v248, v235 dst_sel:DWORD dst_unused:UNUSED_PAD src0_sel:DWORD src1_sel:WORD_1\n\t"
    "v_smfmac_f32_16x16x64_bf16 v[16:19], a[188:191], v[64:71], v199 abid:1\n\t"
    "s_waitcnt lgkmcnt(3)\n\t"
    "ds_read_b128 v[28:31], v244 offset:512\n\t"
    "v_mfma_f32_16x16x32_bf16 v[16:19], v[80:83], v[92:95], v[16:19]\n\t"
    "ds_read_b32 v92, v220 offset:23044\n\t"
    "v_add_u32_sdwa v90, v248, v235 dst_sel:DWORD dst_unused:UNUSED_PAD src0_sel:DWORD src1_sel:WORD_0\n\t"
    "v_smfmac_f32_16x16x64_bf16 v[32:35], a[152:155], v[40:47], v195\n\t"
    "ds_read_b32 v89, v89 offset:23044\n\t"
    "v_smfmac_f32_16x16x64_bf16 v[32:35], a[172:175], v[48:55], v197 abid:1\n\t"
    "buffer_load_dwordx4 v239, s[40:43], s68 offen offset:3584 nt lds\n\t"
    "v_smfmac_f32_16x16x64_bf16 v[32:35], a[192:195], v[64:71], v200\n\t"
    "ds_read_b32 v91, v91 offset:23044\n\t"
    "v_smfmac_f32_16x16x64_bf16 v[32:35], a[212:215], v[56:63], v202 abid:1\n\t"
    "s_waitcnt lgkmcnt(4)\n\t"
    "s_bitcmp1_b32 s54, 9\n\t"
    "s_mov_b32 s68, 42467328\n\t"
    "s_cmov_b32 s68, 42541056\n\t"
    "ds_read_b128 v[80:83], v245 offset:8192\n\t"
    "v_smfmac_f32_16x16x64_bf16 v[36:39], a[156:159], v[40:47], v195 abid:1\n\t"
    "ds_read_b32 v90, v90 offset:23044\n\t"
    "ds_read_b32 v95, v75 offset:23044\n\t"
    "v_smfmac_f32_16x16x64_bf16 v[36:39], a[176:179], v[48:55], v198\n\t"
    "s_add_u32 m0, s52, 13824\n\t"
    "s_mov_b32 exec_hi, 0\n\t"
    "buffer_load_dwordx4 v237, s[40:43], s68 offen offset:0 nt lds\n\t"
    "s_mov_b32 exec_hi, -1\n\t"
    "v_smfmac_f32_16x16x64_bf16 v[36:39], a[196:199], v[64:71], v200 abid:1\n\t"
    "ds_read_b32 v93, v74 offset:23044\n\t"
    "v_smfmac_f32_16x16x64_bf16 v[36:39], a[216:219], v[56:63], v203\n\t"
    "s_waitcnt lgkmcnt(7)\n\t"
    "v_smfmac_f32_16x16x64_bf16 v[28:31], a[160:163], v[40:47], v196\n\t"
    "ds_read_b128 v[40:43], v217 offset:23552\n\t"
    "ds_read_b128 v[44:47], v236 offset:23552\n\t"
    "v_smfmac_f32_16x16x64_bf16 v[28:31], a[180:183], v[48:55], v198 abid:1\n\t"
    "buffer_load_dwordx4 v237, s[40:43], s68 offen offset:512 nt lds\n\t"
    "v_smfmac_f32_16x16x64_bf16 v[28:31], a[200:203], v[64:71], v201\n\t"
    "s_lshr_b64 s[80:81], s[76:77], s70\n\t"
    "s_and_b32 s80, s80, 63\n\t"
    "ds_read_b128 v[48:51], v217 offset:23680\n\t"
    "v_smfmac_f32_16x16x64_bf16 v[28:31], a[220:223], v[56:63], v203 abid:1\n\t"
    "s_waitcnt lgkmcnt(5)\n\t"
    "ds_read_b32 v94, v221 offset:23044\n\t"
    "v_mfma_f32_16x16x32_bf16 v[28:31], v[80:83], v[88:91], v[28:31]\n\t"
    "ds_read_b128 v[80:83], v245 offset:1024\n\t"
    "v_add_u32_sdwa v91, v248, v247 dst_sel:DWORD dst_unused:UNUSED_PAD src0_sel:DWORD src1_sel:WORD_1\n\t"
    "v_smfmac_f32_16x16x64_bf16 v[4:7], a[32:35], v[64:71], v180\n\t"
    "s_mov_b64 vcc, s[18:19]\n\t"
    "ds_read_u16 v91, v91 offset:23040\n\t"
    "v_add_u32_sdwa v90, v248, v247 dst_sel:DWORD dst_unused:UNUSED_PAD src0_sel:DWORD src1_sel:WORD_0\n\t"
    "v_smfmac_f32_16x16x64_bf16 v[4:7], a[48:51], v[56:63], v182\n\t"
    "s_waitcnt lgkmcnt(4)\n\t"
    "s_add_u32 s67, s57, s66\n\t"
    "ds_read_u16 v90, v90 offset:23040\n\t"
    "v_add_u32_sdwa v89, v248, v246 dst_sel:DWORD dst_unused:UNUSED_PAD src0_sel:DWORD src1_sel:WORD_1\n\t"
    "v_smfmac_f32_16x16x64_bf16 v[4:7], a[64:67], v[40:47], v184\n\t"
    "v_add_u32_sdwa v88, v248, v246 dst_sel:DWORD dst_unused:UNUSED_PAD src0_sel:DWORD src1_sel:WORD_0\n\t"
    "v_smfmac_f32_16x16x64_bf16 v[12:15], a[36:39], v[64:71], v180 abid:1\n\t"
    "s_waitcnt vmcnt(21)\n\t"
    "s_barrier\n\t"
    "ds_read_u16 v88, v88 offset:23040\n\t"
    "ds_read_u16 v89, v89 offset:23040\n\t"
    "v_smfmac_f32_16x16x64_bf16 v[12:15], a[52:55], v[56:63], v182 abid:1\n\t"
    "ds_read_b128 v[100:103], v243 offset:2048\n\t"
    "v_smfmac_f32_16x16x64_bf16 v[12:15], a[68:71], v[40:47], v184 abid:1\n\t"
    "s_waitcnt lgkmcnt(5)\n\t"
    "s_lshl_b32 s5, s80, 2\n\t"
    "v_mfma_f32_16x16x32_bf16 v[12:15], v[80:83], v[92:95], v[12:15]\n\t"
    "ds_read_b128 v[52:55], v236 offset:23680\n\t"
    "v_smfmac_f32_16x16x64_bf16 v[20:23], a[40:43], v[64:71], v181\n\t"
    "ds_read_b32 v92, v226 offset:23044\n\t"
    "ds_read_b32 v93, v84 offset:23044\n\t"
    "v_smfmac_f32_16x16x64_bf16 v[20:23], a[56:59], v[56:63], v183\n\t"
    "ds_read_b128 v[80:83], v245 offset:4096\n\t"
    "v_smfmac_f32_16x16x64_bf16 v[20:23], a[72:75], v[40:47], v185\n\t"
    "s_waitcnt lgkmcnt(5)\n\t"
    "s_bitcmp1_b32 s54, 5\n\t"
    "s_mov_b32 s65, 23592960\n\t"
    "s_cmov_b32 s65, 23666688\n\t"
    "s_set_gpr_idx_on s4, gpr_idx(SRC2,DST)\n\t"
    "v_mfma_f32_16x16x32_bf16 v[0:3], v[250:253], v[88:91], v[0:3]\n\t"
    "s_set_gpr_idx_off\n\t"
    "s_waitcnt lgkmcnt(4)\n\t"
    "s_mov_b32 exec_hi, 0\n\t"
    "buffer_store_dwordx4 v[100:103], v240, s[44:47], s67 offen offset:0 sc1\n\t"
    "s_mov_b32 exec_hi, -1\n\t"
    "v_smfmac_f32_16x16x64_bf16 v[8:11], a[60:63], v[56:63], v183 abid:1\n\t"
    "ds_read_b32 v94, v227 offset:23044\n\t"
    "ds_read_b32 v95, v85 offset:23044\n\t"
    "v_smfmac_f32_16x16x64_bf16 v[8:11], a[76:79], v[40:47], v185 abid:1\n\t"
    "s_waitcnt lgkmcnt(5)\n\t"
    "ds_read_b128 v[64:67], v217 offset:23808\n\t"
    "v_smfmac_f32_16x16x64_bf16 v[8:11], a[92:95], v[48:55], v187 abid:1\n\t"
    "ds_read_b128 v[68:71], v236 offset:23808\n\t"
    "s_set_gpr_idx_on s5, gpr_idx(SRC2,DST)\n\t"
    "v_mfma_f32_16x16x32_bf16 v[0:3], v[252:255], v[88:91], v[0:3]\n\t"
    "s_set_gpr_idx_off\n\t"
    "s_add_u32 m0, s52, 13824\n\t"
    "s_nop 0\n\t"
    "buffer_load_dwordx4 v238, s[40:43], s68 offen offset:1536 nt lds\n\t"
    "s_cmp_lg_u32 s22, 0\n\t"
    "s_cbranch_scc1 .Lsl10_%=\n\t"
    ".Lslr10_%=:\n\t"
    "v_smfmac_f32_16x16x64_bf16 v[24:27], a[204:207], v[56:63], v201 abid:1\n\t"
    "ds_read_b32 v88, v232 offset:23044\n\t"
    "v_smfmac_f32_16x16x64_bf16 v[24:27], a[224:227], v[40:47], v204\n\t"
    "s_memrealtime s[84:85]\n\t"
    "ds_read_b32 v90, v233 offset:23044\n\t"
    "v_smfmac_f32_16x16x64_bf16 v[24:27], a[244:247], v[48:55], v206 abid:1\n\t"
    "s_waitcnt lgkmcnt(4)\n\t"
    "ds_read_b32 v89, v106 offset:23044\n\t"
    "v_mfma_f32_16x16x32_bf16 v[24:27], v[80:83], v[92:95], v[24:27]\n\t"
    "ds_read_b32 v91, v107 offset:23044\n\t"
    "v_smfmac_f32_16x16x64_bf16 v[16:19], a[208:211], v[56:63], v202\n\t"
    "v_smfmac_f32_16x16x64_bf16 v[16:19], a[228:231], v[40:47], v204 abid:1\n\t"
    "ds_read_b128 v[80:83], v245 offset:7168\n\t"
    "v_smfmac_f32_16x16x64_bf16 v[16:19], a[248:251], v[48:55], v207\n\t"
    "s_sub_i32 s87, s87, s99\n\t"
    "ds_read_b32 v93, v72 offset:23044\n\t"
    "s_cmp_lg_u32 s99, 0\n\t"
    "s_cbranch_scc1 .Lpsw_%=\n\t"
    ".Lpswr_%=:\n\t"
    "v_smfmac_f32_16x16x64_bf16 v[32:35], a[232:235], v[40:47], v205\n\t"
    "ds_read_b32 v95, v73 offset:23044\n\t"
    "v_smfmac_f32_16x16x64_bf16 v[32:35], a[252:255], v[48:55], v207 abid:1\n\t"
    "s_waitcnt lgkmcnt(7)\n\t"
    "s_sub_i32 s98, s98, s99\n\t"
    "ds_read_b32 v92, v218 offset:23044\n\t"
    "v_smfmac_f32_16x16x64_bf16 v[32:35], v[124:127], v[64:71], v210\n\t"
    "s_add_i32 s86, s87, 1409\n\t"
    "ds_read_b128 v[60:63], v236 offset:23936\n\t"
    "v_smfmac_f32_16x16x64_bf16 v[36:39], a[236:239], v[40:47], v205 abid:1\n\t"
    "s_add_i32 s98, s98, 132\n\t"
    "ds_read_b128 v[56:59], v217 offset:23936\n\t"
    "v_smfmac_f32_16x16x64_bf16 v[36:39], v[108:111], v[48:55], v208\n\t"
    "s_add_u32 s66, s57, s65\n\t"
    "s_add_i32 s89, s86, s23\n\t"
    "s_min_i32 s98, s98, s89\n\t"
    "ds_read_b32 v94, v219 offset:23044\n\t"
    "v_smfmac_f32_16x16x64_bf16 v[36:39], v[128:131], v[64:71], v210 abid:1\n\t"
    "s_waitcnt lgkmcnt(6)\n\t"
    "s_max_i32 s86, s86, s98\n\t"
    "v_mfma_f32_16x16x32_bf16 v[36:39], v[80:83], v[88:91], v[36:39]\n\t"
    "s_sub_u32 s89, s84, s24\n\t"
    "ds_read_b128 v[80:83], v245 offset:0\n\t"
    "v_smfmac_f32_16x16x64_bf16 v[28:31], a[240:243], v[40:47], v206\n\t"
    "s_cmp_lt_i32 s89, s86\n\t"
    "s_cbranch_scc1 .Lpc5_%=\n\t"
    ".Lpc5r_%=:\n\t"
    "ds_read_b128 v[40:43], v217 offset:24064\n\t"
    "ds_read_b128 v[44:47], v236 offset:24064\n\t"
    "v_smfmac_f32_16x16x64_bf16 v[28:31], v[112:115], v[48:55], v208 abid:1\n\t"
    "v_smfmac_f32_16x16x64_bf16 v[28:31], v[132:135], v[64:71], v211\n\t"
    "s_max_i32 s98, s89, s86\n\t"
    "v_smfmac_f32_16x16x64_bf16 v[4:7], a[80:83], v[48:55], v186\n\t"
    "s_sub_i32 s23, s23, 3\n\t"
    "ds_read_b32 v91, v79 offset:23044\n\t"
    "v_smfmac_f32_16x16x64_bf16 v[4:7], a[96:99], v[64:71], v188\n\t"
    "s_waitcnt lgkmcnt(5)\n\t"
    "ds_read_b32 v89, v78 offset:23044\n\t"
    "v_smfmac_f32_16x16x64_bf16 v[4:7], a[112:115], v[56:63], v190\n\t"
    "s_add_u32 m0, s52, 13824\n\t"
    "s_waitcnt lgkmcnt(2)\n\t"
    "buffer_load_dwordx4 v239, s[40:43], s68 offen offset:2560 nt lds\n\t"
    "v_smfmac_f32_16x16x64_bf16 v[4:7], a[128:131], v[40:47], v192\n\t"
    "ds_read_b32 v90, v225 offset:23044\n\t"
    "v_mfma_f32_16x16x32_bf16 v[4:7], v[80:83], v[92:95], v[4:7]\n\t"
    "v_smfmac_f32_16x16x64_bf16 v[12:15], a[84:87], v[48:55], v186 abid:1\n\t"
    "ds_read_b128 v[80:83], v245 offset:3072\n\t"
    "v_smfmac_f32_16x16x64_bf16 v[12:15], a[100:103], v[64:71], v188 abid:1\n\t"
    "ds_read_b32 v95, v105 offset:23044\n\t"
    "v_smfmac_f32_16x16x64_bf16 v[12:15], a[116:119], v[56:63], v190 abid:1\n\t"
    "ds_read_b32 v88, v224 offset:23044\n\t"
    "v_smfmac_f32_16x16x64_bf16 v[12:15], a[132:135], v[40:47], v192 abid:1\n\t"
    "ds_read_b32 v92, v230 offset:23044\n\t"
    "v_cvt_pk_bf16_f32 v96, v4, v5\n\t"
    "v_smfmac_f32_16x16x64_bf16 v[20:23], a[88:91], v[48:55], v187\n\t"
    "v_smfmac_f32_16x16x64_bf16 v[20:23], a[104:107], v[64:71], v189\n\t"
    "ds_read_b32 v94, v231 offset:23044\n\t"
    "v_smfmac_f32_16x16x64_bf16 v[20:23], a[120:123], v[56:63], v191\n\t"
    "ds_read_b128 v[48:51], v217 offset:0\n\t"
    "v_cvt_pk_bf16_f32 v97, v6, v7\n\t"
    "v_smfmac_f32_16x16x64_bf16 v[20:23], a[136:139], v[40:47], v193\n\t"
    "ds_read_b128 v[4:7], v244 offset:0\n\t"
    "v_smfmac_f32_16x16x64_bf16 v[8:11], a[108:111], v[64:71], v189 abid:1\n\t"
    "v_cvt_pk_bf16_f32 v98, v12, v13\n\t"
    "v_smfmac_f32_16x16x64_bf16 v[8:11], a[124:127], v[56:63], v191 abid:1\n\t"
    "ds_read_b32 v93, v104 offset:23044\n\t"
    "v_smfmac_f32_16x16x64_bf16 v[8:11], a[140:143], v[40:47], v193 abid:1\n\t"
    "s_waitcnt lgkmcnt(5)\n\t"
    "v_cvt_pk_bf16_f32 v99, v14, v15\n\t"
    "v_mfma_f32_16x16x32_bf16 v[8:11], v[80:83], v[88:91], v[8:11]\n\t"
    "ds_read_b32 v90, v223 offset:4\n\t"
    "v_cvt_pk_bf16_f32 v101, v22, v23\n\t"
    "v_smfmac_f32_16x16x64_bf16 v[24:27], v[116:119], v[64:71], v209\n\t"
    "ds_read_b128 v[52:55], v236 offset:0\n\t"
    "v_cvt_pk_bf16_f32 v100, v20, v21\n\t"
    "v_smfmac_f32_16x16x64_bf16 v[24:27], v[136:139], v[56:63], v211 abid:1\n\t"
    "ds_read_b128 v[80:83], v245 offset:6144\n\t"
    "v_smfmac_f32_16x16x64_bf16 v[24:27], v[156:159], v[40:47], v214\n\t"
    "v_cvt_pk_bf16_f32 v102, v8, v9\n\t"
    "v_smfmac_f32_16x16x64_bf16 v[16:19], v[120:123], v[64:71], v209 abid:1\n\t"
    "ds_read_b128 v[12:15], v244 offset:64\n\t"
    "v_cvt_pk_bf16_f32 v103, v10, v11\n\t"
    "v_smfmac_f32_16x16x64_bf16 v[16:19], v[140:143], v[56:63], v212\n\t"
    "ds_read_b128 v[64:67], v217 offset:128\n\t"
    "v_cndmask_b32_dpp v8, v100, v96, vcc quad_perm:[0,0,2,2] row_mask:0xf bank_mask:0xf\n\t"
    "v_smfmac_f32_16x16x64_bf16 v[16:19], v[160:163], v[40:47], v214 abid:1\n\t"
    "ds_read_b32 v89, v76 offset:4\n\t"
    "v_cndmask_b32_dpp v10, v102, v98, vcc quad_perm:[0,0,2,2] row_mask:0xf bank_mask:0xf\n\t"
    "v_smfmac_f32_16x16x64_bf16 v[32:35], v[144:147], v[56:63], v212 abid:1\n\t"
    "ds_read_b128 v[68:71], v236 offset:128\n\t"
    "v_cndmask_b32_dpp v9, v101, v97, vcc quad_perm:[0,0,2,2] row_mask:0xf bank_mask:0xf\n\t"
    "v_smfmac_f32_16x16x64_bf16 v[32:35], v[164:167], v[40:47], v215\n\t"
    "s_waitcnt lgkmcnt(4)\n\t"
    "ds_read_b128 v[20:23], v244 offset:128\n\t"
    "v_cndmask_b32_dpp v11, v103, v99, vcc quad_perm:[0,0,2,2] row_mask:0xf bank_mask:0xf\n\t"
    "v_mfma_f32_16x16x32_bf16 v[32:35], v[80:83], v[92:95], v[32:35]\n\t"
    "s_not_b64 vcc, s[18:19]\n\t"
    "ds_read_b32 v91, v77 offset:4\n\t"
    "v_cndmask_b32_dpp v101, v97, v101, vcc quad_perm:[1,1,3,3] row_mask:0xf bank_mask:0xf\n\t"
    "v_smfmac_f32_16x16x64_bf16 v[36:39], v[148:151], v[56:63], v213\n\t"
    "ds_read_b128 v[80:83], v245 offset:2048\n\t"
    "v_smfmac_f32_16x16x64_bf16 v[36:39], v[168:171], v[40:47], v215 abid:1\n\t"
    "ds_read_b32 v88, v222 offset:4\n\t"
    "v_cndmask_b32_dpp v103, v99, v103, vcc quad_perm:[1,1,3,3] row_mask:0xf bank_mask:0xf\n\t"
    "v_smfmac_f32_16x16x64_bf16 v[28:31], v[152:155], v[56:63], v213 abid:1\n\t"
    "buffer_store_dwordx4 v[8:11], v241, s[44:47], s65 offen offset:0 sc1\n\t"
    "ds_read_b32 v94, v229 offset:4\n\t"
    "v_cndmask_b32_dpp v100, v96, v100, vcc quad_perm:[1,1,3,3] row_mask:0xf bank_mask:0xf\n\t"
    "v_cndmask_b32_dpp v102, v98, v102, vcc quad_perm:[1,1,3,3] row_mask:0xf bank_mask:0xf\n\t"
    "v_cvt_pk_bf16_f32 v98, v16, v17\n\t"
    "v_smfmac_f32_16x16x64_bf16 v[28:31], v[172:175], v[40:47], v216\n\t"
    "ds_read_b128 v[8:11], v244 offset:192\n\t"
    "v_cvt_pk_bf16_f32 v97, v26, v27\n\t"
    "v_smfmac_f32_16x16x64_bf16 v[4:7], a[0:3], v[48:55], v176\n\t"
    "s_waitcnt lgkmcnt(6)\n\t"
    "ds_read_b128 v[56:59], v217 offset:256\n\t"
    "v_cvt_pk_bf16_f32 v96, v24, v25\n\t"
    "v_smfmac_f32_16x16x64_bf16 v[4:7], a[16:19], v[64:71], v178\n\t"
    "ds_read_b128 v[60:63], v236 offset:256\n\t"
    "v_cvt_pk_bf16_f32 v99, v18, v19\n\t"
    "v_smfmac_f32_16x16x64_bf16 v[12:15], a[4:7], v[48:55], v176 abid:1\n\t"
    "s_mov_b64 vcc, s[18:19]\n\t"
    "ds_read_b32 v95, v87 offset:4\n\t"
    "v_cvt_pk_bf16_f32 v28, v28, v29\n\t"
    "v_smfmac_f32_16x16x64_bf16 v[12:15], a[20:23], v[64:71], v178 abid:1\n\t"
    "s_waitcnt lgkmcnt(8)\n\t"
    "ds_read_b128 v[24:27], v244 offset:256\n\t"
    "v_cvt_pk_bf16_f32 v29, v30, v31\n\t"
    "v_smfmac_f32_16x16x64_bf16 v[20:23], a[8:11], v[48:55], v177\n\t"
    "buffer_store_dwordx4 v[100:103], v241, s[44:47], s65 offen offset:1152 sc1\n\t"
    "ds_read_b128 v[16:19], v244 offset:320\n\t"
    "v_smfmac_f32_16x16x64_bf16 v[20:23], a[24:27], v[64:71], v179\n\t"
    "s_waitcnt lgkmcnt(7)\n\t"
    "ds_read_b32 v93, v86 offset:4\n\t"
    "v_cvt_pk_bf16_f32 v102, v36, v37\n\t"
    "v_mfma_f32_16x16x32_bf16 v[20:23], v[80:83], v[88:91], v[20:23]\n\t"
    "s_waitcnt lgkmcnt(6)\n\t"
    "ds_read_b32 v92, v228 offset:4\n\t"
    "v_cvt_pk_bf16_f32 v100, v32, v33\n\t"
    "v_cvt_pk_bf16_f32 v101, v34, v35\n\t"
    "v_cvt_pk_bf16_f32 v103, v38, v39\n\t"
    "v_smfmac_f32_16x16x64_bf16 v[8:11], a[12:15], v[48:55], v177 abid:1\n\t"
    "ds_read_b128 v[80:83], v245 offset:5120\n\t"
    "v_cndmask_b32_dpp v38, v102, v98, vcc quad_perm:[0,0,2,2] row_mask:0xf bank_mask:0xf\n\t"
    "v_smfmac_f32_16x16x64_bf16 v[8:11], a[28:31], v[64:71], v179 abid:1\n\t"
    "s_waitcnt lgkmcnt(6)\n\t"
    "buffer_load_dwordx4 v239, s[40:43], s68 offen offset:3584 nt lds\n\t"
    "v_smfmac_f32_16x16x64_bf16 v[8:11], a[44:47], v[56:63], v181 abid:1\n\t"
    "s_waitcnt lgkmcnt(4)\n\t"
    "ds_read_b128 v[32:35], v244 offset:384\n\t"
    "v_cndmask_b32_dpp v36, v100, v96, vcc quad_perm:[0,0,2,2] row_mask:0xf bank_mask:0xf\n\t"
    "v_smfmac_f32_16x16x64_bf16 v[24:27], a[144:147], v[48:55], v194\n\t"
    "ds_write_b64 v242, v[28:29] offset:512\n\t"
    "v_cndmask_b32_dpp v39, v103, v99, vcc quad_perm:[0,0,2,2] row_mask:0xf bank_mask:0xf\n\t"
    "v_smfmac_f32_16x16x64_bf16 v[24:27], a[164:167], v[64:71], v196 abid:1\n\t"
    "v_cndmask_b32_dpp v37, v101, v97, vcc quad_perm:[0,0,2,2] row_mask:0xf bank_mask:0xf\n\t"
    "v_smfmac_f32_16x16x64_bf16 v[24:27], a[184:187], v[56:63], v199\n\t"
    "s_waitcnt lgkmcnt(5)\n\t"
    "s_not_b64 vcc, s[18:19]\n\t"
    "buffer_store_dwordx4 v[36:39], v241, s[44:47], s65 offen offset:128 sc1\n\t"
    "ds_read_b128 v[44:47], v236 offset:384\n\t"
    "v_cndmask_b32_dpp v101, v97, v101, vcc quad_perm:[1,1,3,3] row_mask:0xf bank_mask:0xf\n\t"
    "v_cndmask_b32_dpp v103, v99, v103, vcc quad_perm:[1,1,3,3] row_mask:0xf bank_mask:0xf\n\t"
    "v_add_u32_sdwa v89, v248, v234 dst_sel:DWORD dst_unused:UNUSED_PAD src0_sel:DWORD src1_sel:WORD_1\n\t"
    "v_smfmac_f32_16x16x64_bf16 v[16:19], a[148:151], v[48:55], v194 abid:1\n\t"
    "ds_read_b128 v[40:43], v217 offset:384\n\t"
    "v_add_u32_sdwa v88, v248, v234 dst_sel:DWORD dst_unused:UNUSED_PAD src0_sel:DWORD src1_sel:WORD_0\n\t"
    "v_smfmac_f32_16x16x64_bf16 v[16:19], a[168:171], v[64:71], v197\n\t"
    "ds_read_b128 v[36:39], v244 offset:448\n\t"
    "v_smfmac_f32_16x16x64_bf16 v[16:19], a[188:191], v[56:63], v199 abid:1\n\t"
    "s_waitcnt lgkmcnt(5)\n\t"
    "ds_read_b128 v[28:31], v244 offset:512\n\t"
    "v_cndmask_b32_dpp v100, v96, v100, vcc quad_perm:[1,1,3,3] row_mask:0xf bank_mask:0xf\n\t"
    "v_mfma_f32_16x16x32_bf16 v[16:19], v[80:83], v[92:95], v[16:19]\n\t"
    "s_waitcnt lgkmcnt(5)\n\t"
    "s_bitcmp1_b32 s54, 10\n\t"
    "s_mov_b32 s68, 47185920\n\t"
    "s_cmov_b32 s68, 47259648\n\t"
    "v_smfmac_f32_16x16x64_bf16 v[32:35], a[152:155], v[48:55], v195\n\t"
    "ds_read_b32 v89, v89 offset:4\n\t"
    "v_cndmask_b32_dpp v102, v98, v102, vcc quad_perm:[1,1,3,3] row_mask:0xf bank_mask:0xf\n\t"
    "v_smfmac_f32_16x16x64_bf16 v[32:35], a[172:175], v[64:71], v197 abid:1\n\t"
    "buffer_store_dwordx4 v[100:103], v241, s[44:47], s65 offen offset:1280 sc1\n\t"
    "ds_read_b128 v[80:83], v245 offset:8192\n\t"
    "ds_read_b32 v95, v75 offset:4\n\t"
    "v_smfmac_f32_16x16x64_bf16 v[32:35], a[192:195], v[56:63], v200\n\t"
    "s_waitcnt lgkmcnt(5)\n\t"
    "ds_read_b32 v88, v88 offset:4\n\t"
    "v_add_u32_sdwa v90, v248, v235 dst_sel:DWORD dst_unused:UNUSED_PAD src0_sel:DWORD src1_sel:WORD_0\n\t"
    "v_smfmac_f32_16x16x64_bf16 v[32:35], a[212:215], v[40:47], v202 abid:1\n\t"
    "s_waitcnt lgkmcnt(5)\n\t"
    "v_add_u32_sdwa v91, v248, v235 dst_sel:DWORD dst_unused:UNUSED_PAD src0_sel:DWORD src1_sel:WORD_1\n\t"
    "v_smfmac_f32_16x16x64_bf16 v[36:39], a[156:159], v[48:55], v195 abid:1\n\t"
    "ds_read_b32 v90, v90 offset:4\n\t"
    "ds_read_b32 v91, v91 offset:4\n\t"
    "v_smfmac_f32_16x16x64_bf16 v[36:39], a[176:179], v[64:71], v198\n\t"
    "s_add_u32 m0, s52, 18432\n\t"
    "s_mov_b32 exec_hi, 0\n\t"
    "buffer_load_dwordx4 v237, s[40:43], s68 offen offset:0 nt lds\n\t"
    "s_mov_b32 exec_hi, -1\n\t"
    "v_smfmac_f32_16x16x64_bf16 v[36:39], a[196:199], v[56:63], v200 abid:1\n\t"
    "ds_read_b32 v94, v221 offset:4\n\t"
    "v_smfmac_f32_16x16x64_bf16 v[36:39], a[216:219], v[40:47], v203\n\t"
    "s_waitcnt lgkmcnt(7)\n\t"
    "v_smfmac_f32_16x16x64_bf16 v[28:31], a[160:163], v[48:55], v196\n\t"
    "ds_read_b128 v[48:51], v217 offset:512\n\t"
    "ds_read_b128 v[52:55], v236 offset:512\n\t"
    "v_smfmac_f32_16x16x64_bf16 v[28:31], a[180:183], v[64:71], v198 abid:1\n\t"
    "ds_read_b128 v[64:67], v217 offset:640\n\t"
    "v_smfmac_f32_16x16x64_bf16 v[28:31], a[200:203], v[56:63], v201\n\t"
    "ds_read_b32 v93, v74 offset:4\n\t"
    "v_smfmac_f32_16x16x64_bf16 v[28:31], a[220:223], v[40:47], v203 abid:1\n\t"
    "s_waitcnt lgkmcnt(5)\n\t"
    "ds_read_b32 v92, v220 offset:4\n\t"
    "v_mfma_f32_16x16x32_bf16 v[28:31], v[80:83], v[88:91], v[28:31]\n\t"
    "ds_read_b128 v[80:83], v245 offset:1024\n\t"
    "v_add_u32_sdwa v89, v248, v246 dst_sel:DWORD dst_unused:UNUSED_PAD src0_sel:DWORD src1_sel:WORD_1\n\t"
    "v_add_u32_sdwa v90, v248, v247 dst_sel:DWORD dst_unused:UNUSED_PAD src0_sel:DWORD src1_sel:WORD_0\n\t"
    "v_smfmac_f32_16x16x64_bf16 v[4:7], a[32:35], v[56:63], v180\n\t"
    "ds_read_u16 v89, v89 offset:0\n\t"
    "v_add_u32_sdwa v91, v248, v247 dst_sel:DWORD dst_unused:UNUSED_PAD src0_sel:DWORD src1_sel:WORD_1\n\t"
    "v_smfmac_f32_16x16x64_bf16 v[4:7], a[48:51], v[40:47], v182\n\t"
    "s_waitcnt lgkmcnt(5)\n\t"
    "ds_read_u16 v90, v90 offset:0\n\t"
    "v_add_u32_sdwa v88, v248, v246 dst_sel:DWORD dst_unused:UNUSED_PAD src0_sel:DWORD src1_sel:WORD_0\n\t"
    "v_smfmac_f32_16x16x64_bf16 v[4:7], a[64:67], v[48:55], v184\n\t"
    "ds_read_u16 v88, v88 offset:0\n\t"
    "v_smfmac_f32_16x16x64_bf16 v[12:15], a[36:39], v[56:63], v180 abid:1\n\t"
    "s_waitcnt vmcnt(21)\n\t"
    "s_barrier\n\t"
    "ds_read_b128 v[96:99], v243 offset:2560\n\t"
    "ds_read_u16 v91, v91 offset:0\n\t"
    "v_smfmac_f32_16x16x64_bf16 v[12:15], a[52:55], v[40:47], v182 abid:1\n\t"
    "buffer_load_dwordx4 v237, s[40:43], s68 offen offset:512 nt lds\n\t"
    "v_smfmac_f32_16x16x64_bf16 v[12:15], a[68:71], v[48:55], v184 abid:1\n\t"
    "s_waitcnt lgkmcnt(5)\n\t"
    "ds_read_b128 v[68:71], v236 offset:640\n\t"
    "v_mfma_f32_16x16x32_bf16 v[12:15], v[80:83], v[92:95], v[12:15]\n\t"
    "ds_read_b32 v93, v84 offset:4\n\t"
    "v_smfmac_f32_16x16x64_bf16 v[20:23], a[40:43], v[56:63], v181\n\t"
    "s_mov_b64 vcc, s[18:19]\n\t"
    "ds_read_b32 v94, v227 offset:4\n\t"
    "v_smfmac_f32_16x16x64_bf16 v[20:23], a[56:59], v[40:47], v183\n\t"
    "ds_read_b32 v92, v226 offset:4\n\t"
    "v_smfmac_f32_16x16x64_bf16 v[20:23], a[72:75], v[48:55], v185\n\t"
    "s_waitcnt lgkmcnt(4)\n\t"
    "s_set_gpr_idx_on s4, gpr_idx(SRC2,DST)\n\t"
    "v_mfma_f32_16x16x32_bf16 v[0:3], v[250:253], v[88:91], v[0:3]\n\t"
    "s_set_gpr_idx_off\n\t"
    "s_mov_b32 exec_hi, 0\n\t"
    "buffer_store_dwordx4 v[96:99], v240, s[44:47], s66 offen offset:0 sc1\n\t"
    "s_mov_b32 exec_hi, -1\n\t"
    "ds_read_b128 v[80:83], v245 offset:4096\n\t"
    "v_smfmac_f32_16x16x64_bf16 v[8:11], a[60:63], v[40:47], v183 abid:1\n\t"
    "ds_read_b32 v95, v85 offset:4\n\t"
    "v_smfmac_f32_16x16x64_bf16 v[8:11], a[76:79], v[48:55], v185 abid:1\n\t"
    "s_waitcnt lgkmcnt(5)\n\t"
    "ds_read_b128 v[56:59], v217 offset:768\n\t"
    "v_smfmac_f32_16x16x64_bf16 v[8:11], a[92:95], v[64:71], v187 abid:1\n\t"
    "ds_read_b128 v[60:63], v236 offset:768\n\t"
    "s_set_gpr_idx_on s5, gpr_idx(SRC2,DST)\n\t"
    "v_mfma_f32_16x16x32_bf16 v[0:3], v[252:255], v[88:91], v[0:3]\n\t"
    "s_set_gpr_idx_off\n\t"
    "s_cmp_lg_u32 s22, 0\n\t"
    "s_cbranch_scc1 .Lsl11_%=\n\t"
    ".Lslr11_%=:\n\t"
    "v_smfmac_f32_16x16x64_bf16 v[24:27], a[204:207], v[40:47], v201 abid:1\n\t"
    "ds_read_b32 v90, v233 offset:4\n\t"
    "v_smfmac_f32_16x16x64_bf16 v[24:27], a[224:227], v[48:55], v204\n\t"
    "s_memrealtime s[84:85]\n\t"
    "ds_read_b32 v88, v232 offset:4\n\t"
    "v_smfmac_f32_16x16x64_bf16 v[24:27], a[244:247], v[64:71], v206 abid:1\n\t"
    "s_waitcnt lgkmcnt(4)\n\t"
    "ds_read_b32 v91, v107 offset:4\n\t"
    "v_mfma_f32_16x16x32_bf16 v[24:27], v[80:83], v[92:95], v[24:27]\n\t"
    "ds_read_b128 v[80:83], v245 offset:7168\n\t"
    "v_smfmac_f32_16x16x64_bf16 v[16:19], a[208:211], v[40:47], v202\n\t"
    "s_add_u32 m0, s52, 18432\n\t"
    "s_nop 0\n\t"
    "buffer_load_dwordx4 v238, s[40:43], s68 offen offset:1536 nt lds\n\t"
    "v_smfmac_f32_16x16x64_bf16 v[16:19], a[228:231], v[48:55], v204 abid:1\n\t"
    "ds_read_b32 v89, v106 offset:4\n\t"
    "ds_read_b32 v94, v219 offset:4\n\t"
    "v_smfmac_f32_16x16x64_bf16 v[16:19], a[248:251], v[64:71], v207\n\t"
    "s_sub_i32 s87, s87, s99\n\t"
    "ds_read_b32 v93, v72 offset:4\n\t"
    "v_smfmac_f32_16x16x64_bf16 v[32:35], a[232:235], v[48:55], v205\n\t"
    "v_smfmac_f32_16x16x64_bf16 v[32:35], a[252:255], v[64:71], v207 abid:1\n\t"
    "s_waitcnt lgkmcnt(7)\n\t"
    "s_sub_i32 s98, s98, s99\n\t"
    "ds_read_b128 v[40:43], v217 offset:896\n\t"
    "v_smfmac_f32_16x16x64_bf16 v[32:35], v[124:127], v[56:63], v210\n\t"
    "s_add_i32 s86, s87, 1531\n\t"
    "ds_read_b32 v92, v218 offset:4\n\t"
    "v_smfmac_f32_16x16x64_bf16 v[36:39], a[236:239], v[48:55], v205 abid:1\n\t"
    "s_add_i32 s98, s98, 118\n\t"
    "ds_read_b128 v[44:47], v236 offset:896\n\t"
    "v_smfmac_f32_16x16x64_bf16 v[36:39], v[108:111], v[64:71], v208\n\t"
    "s_add_i32 s89, s86, s23\n\t"
    "s_min_i32 s98, s98, s89\n\t"
    "v_smfmac_f32_16x16x64_bf16 v[36:39], v[128:131], v[56:63], v210 abid:1\n\t"
    "s_waitcnt lgkmcnt(5)\n\t"
    "s_max_i32 s86, s86, s98\n\t"
    "ds_read_b32 v95, v73 offset:4\n\t"
    "v_mfma_f32_16x16x32_bf16 v[36:39], v[80:83], v[88:91], v[36:39]\n\t"
    "s_sub_u32 s89, s84, s24\n\t"
    "ds_read_b128 v[80:83], v245 offset:0\n\t"
    "v_smfmac_f32_16x16x64_bf16 v[28:31], a[240:243], v[48:55], v206\n\t"
    "s_cmp_lt_i32 s89, s86\n\t"
    "s_cbranch_scc1 .Lpc6_%=\n\t"
    ".Lpc6r_%=:\n\t"
    "ds_read_b128 v[48:51], v217 offset:1024\n\t"
    "ds_read_b128 v[52:55], v236 offset:1024\n\t"
    "v_smfmac_f32_16x16x64_bf16 v[28:31], v[112:115], v[64:71], v208 abid:1\n\t"
    "v_smfmac_f32_16x16x64_bf16 v[28:31], v[132:135], v[56:63], v211\n\t"
    "s_max_i32 s98, s89, s86\n\t"
    "v_smfmac_f32_16x16x64_bf16 v[4:7], a[80:83], v[64:71], v186\n\t"
    "s_sub_i32 s23, s23, 3\n\t"
    "ds_read_b32 v91, v79 offset:4\n\t"
    "v_smfmac_f32_16x16x64_bf16 v[4:7], a[96:99], v[56:63], v188\n\t"
    "s_waitcnt lgkmcnt(5)\n\t"
    "ds_read_b32 v88, v224 offset:4\n\t"
    "ds_read_b32 v89, v78 offset:4\n\t"
    "v_smfmac_f32_16x16x64_bf16 v[4:7], a[112:115], v[40:47], v190\n\t"
    "s_waitcnt lgkmcnt(3)\n\t"
    "buffer_load_dwordx4 v239, s[40:43], s68 offen offset:2560 nt lds\n\t"
    "v_smfmac_f32_16x16x64_bf16 v[4:7], a[128:131], v[48:55], v192\n\t"
    "v_mfma_f32_16x16x32_bf16 v[4:7], v[80:83], v[92:95], v[4:7]\n\t"
    "ds_read_b32 v95, v105 offset:4\n\t"
    "v_smfmac_f32_16x16x64_bf16 v[12:15], a[84:87], v[64:71], v186 abid:1\n\t"
    "ds_read_b32 v90, v225 offset:4\n\t"
    "ds_read_b32 v93, v104 offset:4\n\t"
    "v_smfmac_f32_16x16x64_bf16 v[12:15], a[100:103], v[56:63], v188 abid:1\n\t"
    "v_smfmac_f32_16x16x64_bf16 v[12:15], a[116:119], v[40:47], v190 abid:1\n\t"
    "v_smfmac_f32_16x16x64_bf16 v[12:15], a[132:135], v[48:55], v192 abid:1\n\t"
    "ds_read_b128 v[80:83], v245 offset:3072\n\t"
    "v_smfmac_f32_16x16x64_bf16 v[20:23], a[88:91], v[64:71], v187\n\t"
    "ds_read_b32 v92, v230 offset:4\n\t"
    "v_smfmac_f32_16x16x64_bf16 v[20:23], a[104:107], v[56:63], v189\n\t"
    "ds_read_b32 v94, v231 offset:4\n\t"
    "v_cvt_pk_bf16_f32 v101, v6, v7\n\t"
    "v_smfmac_f32_16x16x64_bf16 v[20:23], a[120:123], v[40:47], v191\n\t"
    "ds_read_b128 v[64:67], v217 offset:4608\n\t"
    "v_cvt_pk_bf16_f32 v100, v4, v5\n\t"
    "v_smfmac_f32_16x16x64_bf16 v[20:23], a[136:139], v[48:55], v193\n\t"
    "s_bitcmp1_b32 s54, 6\n\t"
    "s_mov_b32 s67, 28311552\n\t"
    "s_cmov_b32 s67, 28385280\n\t"
    "v_smfmac_f32_16x16x64_bf16 v[8:11], a[108:111], v[56:63], v189 abid:1\n\t"
    "ds_read_b128 v[68:71], v236 offset:4608\n\t"
    "v_smfmac_f32_16x16x64_bf16 v[8:11], a[124:127], v[40:47], v191 abid:1\n\t"
    "v_cvt_pk_bf16_f32 v102, v12, v13\n\t"
    "v_smfmac_f32_16x16x64_bf16 v[8:11], a[140:143], v[48:55], v193 abid:1\n\t"
    "s_waitcnt lgkmcnt(4)\n\t"
    "v_cvt_pk_bf16_f32 v96, v20, v21\n\t"
    "v_mfma_f32_16x16x32_bf16 v[8:11], v[80:83], v[88:91], v[8:11]\n\t"
    "s_add_u32 s65, s57, s67\n\t"
    "ds_read_b128 v[80:83], v245 offset:6144\n\t"
    "v_smfmac_f32_16x16x64_bf16 v[24:27], v[116:119], v[56:63], v209\n\t"
    "ds_read_b32 v89, v76 offset:4612\n\t"
    "v_cvt_pk_bf16_f32 v97, v22, v23\n\t"
    "v_smfmac_f32_16x16x64_bf16 v[24:27], v[136:139], v[40:47], v211 abid:1\n\t"
    "ds_read_b128 v[4:7], v244 offset:0\n\t"
    "v_cvt_pk_bf16_f32 v103, v14, v15\n\t"
    "v_smfmac_f32_16x16x64_bf16 v[24:27], v[156:159], v[48:55], v214\n\t"
    "ds_read_b32 v91, v77 offset:4612\n\t"
    "v_cvt_pk_bf16_f32 v98, v8, v9\n\t"
    "v_smfmac_f32_16x16x64_bf16 v[16:19], v[120:123], v[56:63], v209 abid:1\n\t"
    "ds_read_b128 v[20:23], v244 offset:128\n\t"
    "v_cvt_pk_bf16_f32 v99, v10, v11\n\t"
    "v_smfmac_f32_16x16x64_bf16 v[16:19], v[140:143], v[40:47], v212\n\t"
    "ds_read_b128 v[12:15], v244 offset:64\n\t"
    "v_cndmask_b32_dpp v10, v98, v102, vcc quad_perm:[0,0,2,2] row_mask:0xf bank_mask:0xf\n\t"
    "v_smfmac_f32_16x16x64_bf16 v[16:19], v[160:163], v[48:55], v214 abid:1\n\t"
    "ds_read_b32 v90, v223 offset:4612\n\t"
    "v_cndmask_b32_dpp v11, v99, v103, vcc quad_perm:[0,0,2,2] row_mask:0xf bank_mask:0xf\n\t"
    "v_smfmac_f32_16x16x64_bf16 v[32:35], v[144:147], v[40:47], v212 abid:1\n\t"
    "ds_read_b128 v[60:63], v236 offset:4736\n\t"
    "v_cndmask_b32_dpp v9, v97, v101, vcc quad_perm:[0,0,2,2] row_mask:0xf bank_mask:0xf\n\t"
    "v_smfmac_f32_16x16x64_bf16 v[32:35], v[164:167], v[48:55], v215\n\t"
    "s_waitcnt lgkmcnt(7)\n\t"
    "ds_read_b128 v[56:59], v217 offset:4736\n\t"
    "v_mfma_f32_16x16x32_bf16 v[32:35], v[80:83], v[92:95], v[32:35]\n\t"
    "ds_read_b32 v88, v222 offset:4612\n\t"
    "v_cndmask_b32_dpp v8, v96, v100, vcc quad_perm:[0,0,2,2] row_mask:0xf bank_mask:0xf\n\t"
    "v_smfmac_f32_16x16x64_bf16 v[36:39], v[148:151], v[40:47], v213\n\t"
    "s_not_b64 vcc, s[18:19]\n\t"
    "ds_read_b32 v94, v229 offset:4612\n\t"
    "v_cndmask_b32_dpp v99, v103, v99, vcc quad_perm:[1,1,3,3] row_mask:0xf bank_mask:0xf\n\t"
    "v_smfmac_f32_16x16x64_bf16 v[36:39], v[168:171], v[48:55], v215 abid:1\n\t"
    "ds_read_b128 v[80:83], v245 offset:2048\n\t"
    "v_cvt_pk_bf16_f32 v103, v18, v19\n\t"
    "v_smfmac_f32_16x16x64_bf16 v[28:31], v[152:155], v[40:47], v213 abid:1\n\t"
    "buffer_store_dwordx4 v[8:11], v241, s[44:47], s67 offen offset:0 sc1\n\t"
    "v_cndmask_b32_dpp v96, v100, v96, vcc quad_perm:[1,1,3,3] row_mask:0xf bank_mask:0xf\n\t"
    "v_cndmask_b32_dpp v97, v101, v97, vcc quad_perm:[1,1,3,3] row_mask:0xf bank_mask:0xf\n\t"
    "v_cvt_pk_bf16_f32 v101, v26, v27\n\t"
    "v_smfmac_f32_16x16x64_bf16 v[28:31], v[172:175], v[48:55], v216\n\t"
    "s_waitcnt lgkmcnt(9)\n\t"
    "ds_read_b128 v[8:11], v244 offset:192\n\t"
    "v_cvt_pk_bf16_f32 v100, v24, v25\n\t"
    "v_smfmac_f32_16x16x64_bf16 v[4:7], a[0:3], v[64:71], v176\n\t"
    "s_waitcnt lgkmcnt(4)\n\t"
    "ds_read_b128 v[24:27], v244 offset:256\n\t"
    "v_smfmac_f32_16x16x64_bf16 v[4:7], a[16:19], v[56:63], v178\n\t"
    "ds_read_b128 v[40:43], v217 offset:4864\n\t"
    "v_cndmask_b32_dpp v98, v102, v98, vcc quad_perm:[1,1,3,3] row_mask:0xf bank_mask:0xf\n\t"
    "v_smfmac_f32_16x16x64_bf16 v[12:15], a[4:7], v[64:71], v176 abid:1\n\t"
    "ds_read_b128 v[44:47], v236 offset:4864\n\t"
    "v_cvt_pk_bf16_f32 v102, v16, v17\n\t"
    "v_smfmac_f32_16x16x64_bf16 v[12:15], a[20:23], v[56:63], v178 abid:1\n\t"
    "ds_read_b32 v95, v87 offset:4612\n\t"
    "v_cvt_pk_bf16_f32 v28, v28, v29\n\t"
    "v_smfmac_f32_16x16x64_bf16 v[20:23], a[8:11], v[64:71], v177\n\t"
    "buffer_store_dwordx4 v[96:99], v241, s[44:47], s67 offen offset:1152 sc1\n\t"
    "ds_read_b128 v[48:51], v217 offset:4992\n\t"
    "v_cvt_pk_bf16_f32 v29, v30, v31\n\t"
    "v_smfmac_f32_16x16x64_bf16 v[20:23], a[24:27], v[56:63], v179\n\t"
    "s_waitcnt lgkmcnt(6)\n\t"
    "ds_read_b128 v[16:19], v244 offset:320\n\t"
    "v_cvt_pk_bf16_f32 v98, v36, v37\n\t"
    "v_mfma_f32_16x16x32_bf16 v[20:23], v[80:83], v[88:91], v[20:23]\n\t"
    "s_waitcnt lgkmcnt(6)\n\t"
    "ds_read_b128 v[80:83], v245 offset:5120\n\t"
    "v_smfmac_f32_16x16x64_bf16 v[8:11], a[12:15], v[64:71], v177 abid:1\n\t"
    "s_mov_b64 vcc, s[18:19]\n\t"
    "ds_read_b32 v92, v228 offset:4612\n\t"
    "v_cvt_pk_bf16_f32 v96, v32, v33\n\t"
    "v_smfmac_f32_16x16x64_bf16 v[8:11], a[28:31], v[56:63], v179 abid:1\n\t"
    "s_waitcnt lgkmcnt(5)\n\t"
    "buffer_load_dwordx4 v239, s[40:43], s68 offen offset:3584 nt lds\n\t"
    "ds_read_b32 v93, v86 offset:4612\n\t"
    "v_cvt_pk_bf16_f32 v97, v34, v35\n\t"
    "v_smfmac_f32_16x16x64_bf16 v[8:11], a[44:47], v[40:47], v181 abid:1\n\t"
    "ds_read_b128 v[32:35], v244 offset:384\n\t"
    "v_cvt_pk_bf16_f32 v99, v38, v39\n\t"
    "v_cndmask_b32_dpp v36, v96, v100, vcc quad_perm:[0,0,2,2] row_mask:0xf bank_mask:0xf\n\t"
    "v_cndmask_b32_dpp v38, v98, v102, vcc quad_perm:[0,0,2,2] row_mask:0xf bank_mask:0xf\n\t"
    "v_smfmac_f32_16x16x64_bf16 v[24:27], a[144:147], v[64:71], v194\n\t"
    "v_cndmask_b32_dpp v37, v97, v101, vcc quad_perm:[0,0,2,2] row_mask:0xf bank_mask:0xf\n\t"
    "v_smfmac_f32_16x16x64_bf16 v[24:27], a[164:167], v[56:63], v196 abid:1\n\t"
    "ds_read_b128 v[52:55], v236 offset:4992\n\t"
    "v_cndmask_b32_dpp v39, v99, v103, vcc quad_perm:[0,0,2,2] row_mask:0xf bank_mask:0xf\n\t"
    "v_smfmac_f32_16x16x64_bf16 v[24:27], a[184:187], v[40:47], v199\n\t"
    "s_waitcnt lgkmcnt(5)\n\t"
    "buffer_store_dwordx4 v[36:39], v241, s[44:47], s67 offen offset:128 sc1\n\t"
    "ds_write_b64 v242, v[28:29] offset:0\n\t"
    "v_add_u32_sdwa v90, v248, v235 dst_sel:DWORD dst_unused:UNUSED_PAD src0_sel:DWORD src1_sel:WORD_0\n\t"
    "v_smfmac_f32_16x16x64_bf16 v[16:19], a[148:151], v[64:71], v194 abid:1\n\t"
    "s_not_b64 vcc, s[18:19]\n\t"
    "v_add_u32_sdwa v89, v248, v234 dst_sel:DWORD dst_unused:UNUSED_PAD src0_sel:DWORD src1_sel:WORD_1\n\t"
    "v_smfmac_f32_16x16x64_bf16 v[16:19], a[168:171], v[56:63], v197\n\t"
    "ds_read_b128 v[36:39], v244 offset:448\n\t"
    "v_cndmask_b32_dpp v97, v101, v97, vcc quad_perm:[1,1,3,3] row_mask:0xf bank_mask:0xf\n\t"
    "v_smfmac_f32_16x16x64_bf16 v[16:19], a[188:191], v[40:47], v199 abid:1\n\t"
    "s_waitcnt lgkmcnt(4)\n\t"
    "ds_read_b128 v[28:31], v244 offset:512\n\t"
    "v_cndmask_b32_dpp v99, v103, v99, vcc quad_perm:[1,1,3,3] row_mask:0xf bank_mask:0xf\n\t"
    "v_mfma_f32_16x16x32_bf16 v[16:19], v[80:83], v[92:95], v[16:19]\n\t"
    "s_waitcnt lgkmcnt(4)\n\t"
    "ds_read_b128 v[80:83], v245 offset:8192\n\t"
    "v_cndmask_b32_dpp v96, v100, v96, vcc quad_perm:[1,1,3,3] row_mask:0xf bank_mask:0xf\n\t"
    "v_smfmac_f32_16x16x64_bf16 v[32:35], a[152:155], v[64:71], v195\n\t"
    "ds_read_b32 v90, v90 offset:4612\n\t"
    "v_cndmask_b32_dpp v98, v102, v98, vcc quad_perm:[1,1,3,3] row_mask:0xf bank_mask:0xf\n\t"
    "v_smfmac_f32_16x16x64_bf16 v[32:35], a[172:175], v[56:63], v197 abid:1\n\t"
    "buffer_store_dwordx4 v[96:99], v241, s[44:47], s67 offen offset:1280 sc1\n\t"
    "ds_read_b32 v93, v74 offset:4612\n\t"
    "v_add_u32_sdwa v91, v248, v235 dst_sel:DWORD dst_unused:UNUSED_PAD src0_sel:DWORD src1_sel:WORD_1\n\t"
    "v_smfmac_f32_16x16x64_bf16 v[32:35], a[192:195], v[40:47], v200\n\t"
    "s_waitcnt lgkmcnt(6)\n\t"
    "ds_read_b32 v89, v89 offset:4612\n\t"
    "v_add_u32_sdwa v88, v248, v234 dst_sel:DWORD dst_unused:UNUSED_PAD src0_sel:DWORD src1_sel:WORD_0\n\t"
    "v_smfmac_f32_16x16x64_bf16 v[32:35], a[212:215], v[48:55], v202 abid:1\n\t"
    "s_waitcnt lgkmcnt(5)\n\t"
    "s_bitcmp1_b32 s54, 11\n\t"
    "s_mov_b32 s68, 51904512\n\t"
    "s_cmov_b32 s68, 51978240\n\t"
    "v_smfmac_f32_16x16x64_bf16 v[36:39], a[156:159], v[64:71], v195 abid:1\n\t"
    "ds_read_b32 v88, v88 offset:4612\n\t"
    "ds_read_b32 v91, v91 offset:4612\n\t"
    "v_smfmac_f32_16x16x64_bf16 v[36:39], a[176:179], v[56:63], v198\n\t"
    "s_add_u32 m0, s52, 23040\n\t"
    "s_mov_b32 exec_hi, 0\n\t"
    "buffer_load_dwordx4 v237, s[40:43], s68 offen offset:0 nt lds\n\t"
    "s_mov_b32 exec_hi, -1\n\t"
    "v_smfmac_f32_16x16x64_bf16 v[36:39], a[196:199], v[40:47], v200 abid:1\n\t"
    "ds_read_b32 v94, v221 offset:4612\n\t"
    "v_smfmac_f32_16x16x64_bf16 v[36:39], a[216:219], v[48:55], v203\n\t"
    "s_waitcnt lgkmcnt(7)\n\t"
    "ds_read_b32 v95, v75 offset:4612\n\t"
    "v_smfmac_f32_16x16x64_bf16 v[28:31], a[160:163], v[64:71], v196\n\t"
    "ds_read_b128 v[64:67], v217 offset:5120\n\t"
    "ds_read_b128 v[68:71], v236 offset:5120\n\t"
    "v_smfmac_f32_16x16x64_bf16 v[28:31], a[180:183], v[56:63], v198 abid:1\n\t"
    "v_smfmac_f32_16x16x64_bf16 v[28:31], a[200:203], v[40:47], v201\n\t"
    "ds_read_b32 v92, v220 offset:4612\n\t"
    "v_smfmac_f32_16x16x64_bf16 v[28:31], a[220:223], v[48:55], v203 abid:1\n\t"
    "s_waitcnt lgkmcnt(5)\n\t"
    "ds_read_b128 v[60:63], v236 offset:5248\n\t"
    "v_mfma_f32_16x16x32_bf16 v[28:31], v[80:83], v[88:91], v[28:31]\n\t"
    "ds_read_b128 v[80:83], v245 offset:1024\n\t"
    "v_add_u32_sdwa v91, v248, v247 dst_sel:DWORD dst_unused:UNUSED_PAD src0_sel:DWORD src1_sel:WORD_1\n\t"
    "v_smfmac_f32_16x16x64_bf16 v[4:7], a[32:35], v[40:47], v180\n\t"
    "v_add_u32_sdwa v90, v248, v247 dst_sel:DWORD dst_unused:UNUSED_PAD src0_sel:DWORD src1_sel:WORD_0\n\t"
    "v_smfmac_f32_16x16x64_bf16 v[4:7], a[48:51], v[48:55], v182\n\t"
    "s_waitcnt lgkmcnt(3)\n\t"
    "ds_read_u16 v91, v91 offset:4608\n\t"
    "v_add_u32_sdwa v89, v248, v246 dst_sel:DWORD dst_unused:UNUSED_PAD src0_sel:DWORD src1_sel:WORD_1\n\t"
    "v_smfmac_f32_16x16x64_bf16 v[4:7], a[64:67], v[64:71], v184\n\t"
    "ds_read_u16 v90, v90 offset:4608\n\t"
    "v_add_u32_sdwa v88, v248, v246 dst_sel:DWORD dst_unused:UNUSED_PAD src0_sel:DWORD src1_sel:WORD_0\n\t"
    "v_smfmac_f32_16x16x64_bf16 v[12:15], a[36:39], v[40:47], v180 abid:1\n\t"
    "s_waitcnt vmcnt(21)\n\t"
    "s_barrier\n\t"
    "ds_read_u16 v88, v88 offset:4608\n\t"
    "ds_read_u16 v89, v89 offset:4608\n\t"
    "v_smfmac_f32_16x16x64_bf16 v[12:15], a[52:55], v[48:55], v182 abid:1\n\t"
    "buffer_load_dwordx4 v237, s[40:43], s68 offen offset:512 nt lds\n\t"
    "v_smfmac_f32_16x16x64_bf16 v[12:15], a[68:71], v[64:71], v184 abid:1\n\t"
    "s_waitcnt lgkmcnt(4)\n\t"
    "ds_read_b128 v[100:103], v243 offset:2048\n\t"
    "v_mfma_f32_16x16x32_bf16 v[12:15], v[80:83], v[92:95], v[12:15]\n\t"
    "ds_read_b128 v[56:59], v217 offset:5248\n\t"
    "v_smfmac_f32_16x16x64_bf16 v[20:23], a[40:43], v[40:47], v181\n\t"
    "ds_read_b32 v94, v227 offset:4612\n\t"
    "v_smfmac_f32_16x16x64_bf16 v[20:23], a[56:59], v[48:55], v183\n\t"
    "ds_read_b32 v93, v84 offset:4612\n\t"
    "v_smfmac_f32_16x16x64_bf16 v[20:23], a[72:75], v[64:71], v185\n\t"
    "s_waitcnt lgkmcnt(4)\n\t"
    "ds_read_b32 v95, v85 offset:4612\n\t"
    "s_set_gpr_idx_on s4, gpr_idx(SRC2,DST)\n\t"
    "v_mfma_f32_16x16x32_bf16 v[0:3], v[250:253], v[88:91], v[0:3]\n\t"
    "s_set_gpr_idx_off\n\t"
    "s_waitcnt lgkmcnt(4)\n\t"
    "s_mov_b32 exec_hi, 0\n\t"
    "buffer_store_dwordx4 v[100:103], v240, s[44:47], s65 offen offset:0 sc1\n\t"
    "s_mov_b32 exec_hi, -1\n\t"
    "ds_read_b32 v92, v226 offset:4612\n\t"
    "v_smfmac_f32_16x16x64_bf16 v[8:11], a[60:63], v[48:55], v183 abid:1\n\t"
    "ds_read_b128 v[80:83], v245 offset:4096\n\t"
    "v_smfmac_f32_16x16x64_bf16 v[8:11], a[76:79], v[64:71], v185 abid:1\n\t"
    "s_waitcnt lgkmcnt(5)\n\t"
    "ds_read_b128 v[44:47], v236 offset:5376\n\t"
    "v_smfmac_f32_16x16x64_bf16 v[8:11], a[92:95], v[56:63], v187 abid:1\n\t"
    "ds_read_b128 v[40:43], v217 offset:5376\n\t"
    "s_set_gpr_idx_on s5, gpr_idx(SRC2,DST)\n\t"
    "v_mfma_f32_16x16x32_bf16 v[0:3], v[252:255], v[88:91], v[0:3]\n\t"
    "s_set_gpr_idx_off\n\t"
    "s_cmp_lg_u32 s22, 0\n\t"
    "s_cbranch_scc1 .Lsl12_%=\n\t"
    ".Lslr12_%=:\n\t"
    "v_smfmac_f32_16x16x64_bf16 v[24:27], a[204:207], v[48:55], v201 abid:1\n\t"
    "s_mov_b64 vcc, s[18:19]\n\t"
    "ds_read_b32 v90, v233 offset:4612\n\t"
    "v_smfmac_f32_16x16x64_bf16 v[24:27], a[224:227], v[64:71], v204\n\t"
    "s_memrealtime s[84:85]\n\t"
    "ds_read_b32 v89, v106 offset:4612\n\t"
    "v_smfmac_f32_16x16x64_bf16 v[24:27], a[244:247], v[56:63], v206 abid:1\n\t"
    "s_waitcnt lgkmcnt(4)\n\t"
    "ds_read_b32 v88, v232 offset:4612\n\t"
    "v_mfma_f32_16x16x32_bf16 v[24:27], v[80:83], v[92:95], v[24:27]\n\t"
    "ds_read_b32 v91, v107 offset:4612\n\t"
    "v_smfmac_f32_16x16x64_bf16 v[16:19], a[208:211], v[48:55], v202\n\t"
    "s_add_u32 m0, s52, 23040\n\t"
    "s_nop 0\n\t"
    "buffer_load_dwordx4 v238, s[40:43], s68 offen offset:1536 nt lds\n\t"
    "v_smfmac_f32_16x16x64_bf16 v[16:19], a[228:231], v[64:71], v204 abid:1\n\t"
    "ds_read_b128 v[80:83], v245 offset:7168\n\t"
    "v_smfmac_f32_16x16x64_bf16 v[16:19], a[248:251], v[56:63], v207\n\t"
    "s_sub_i32 s87, s87, s99\n\t"
    "ds_read_b128 v[48:51], v217 offset:5504\n\t"
    "v_smfmac_f32_16x16x64_bf16 v[32:35], a[232:235], v[64:71], v205\n\t"
    "ds_read_b32 v93, v72 offset:4612\n\t"
    "v_smfmac_f32_16x16x64_bf16 v[32:35], a[252:255], v[56:63], v207 abid:1\n\t"
    "s_waitcnt lgkmcnt(7)\n\t"
    "s_sub_i32 s98, s98, s99\n\t"
    "v_smfmac_f32_16x16x64_bf16 v[32:35], v[124:127], v[40:47], v210\n\t"
    "s_add_i32 s86, s87, 1653\n\t"
    "ds_read_b32 v92, v218 offset:4612\n\t"
    "v_smfmac_f32_16x16x64_bf16 v[36:39], a[236:239], v[64:71], v205 abid:1\n\t"
    "s_add_i32 s98, s98, 72\n\t"
    "ds_read_b32 v94, v219 offset:4612\n\t"
    "ds_read_b32 v95, v73 offset:4612\n\t"
    "v_smfmac_f32_16x16x64_bf16 v[36:39], v[108:111], v[56:63], v208\n\t"
    "s_max_i32 s86, s86, s98\n\t"
    "v_smfmac_f32_16x16x64_bf16 v[36:39], v[128:131], v[40:47], v210 abid:1\n\t"
    "s_waitcnt lgkmcnt(5)\n\t"
    "ds_read_b128 v[52:55], v236 offset:5504\n\t"
    "v_mfma_f32_16x16x32_bf16 v[36:39], v[80:83], v[88:91], v[36:39]\n\t"
    "s_sub_u32 s89, s84, s24\n\t"
    "ds_read_b128 v[80:83], v245 offset:0\n\t"
    "v_smfmac_f32_16x16x64_bf16 v[28:31], a[240:243], v[64:71], v206\n\t"
    "s_cmp_lt_i32 s89, s86\n\t"
    "s_cbranch_scc1 .Lpc7_%=\n\t"
    ".Lpc7r_%=:\n\t"
    "ds_read_b128 v[64:67], v217 offset:5632\n\t"
    "ds_read_b128 v[68:71], v236 offset:5632\n\t"
    "v_smfmac_f32_16x16x64_bf16 v[28:31], v[112:115], v[56:63], v208 abid:1\n\t"
    "v_smfmac_f32_16x16x64_bf16 v[28:31], v[132:135], v[40:47], v211\n\t"
    "s_max_i32 s98, s89, s86\n\t"
    "ds_read_b32 v88, v224 offset:4612\n\t"
    "v_smfmac_f32_16x16x64_bf16 v[4:7], a[80:83], v[56:63], v186\n\t"
    "ds_read_b32 v90, v225 offset:4612\n\t"
    "v_smfmac_f32_16x16x64_bf16 v[4:7], a[96:99], v[40:47], v188\n\t"
    "s_waitcnt lgkmcnt(5)\n\t"
    "ds_read_b32 v89, v78 offset:4612\n\t"
    "v_smfmac_f32_16x16x64_bf16 v[4:7], a[112:115], v[48:55], v190\n\t"
    "s_waitcnt lgkmcnt(3)\n\t"
    "buffer_load_dwordx4 v239, s[40:43], s68 offen offset:2560 nt lds\n\t"
    "v_smfmac_f32_16x16x64_bf16 v[4:7], a[128:131], v[64:71], v192\n\t"
    "ds_read_b32 v91, v79 offset:4612\n\t"
    "v_mfma_f32_16x16x32_bf16 v[4:7], v[80:83], v[92:95], v[4:7]\n\t"
    "ds_read_b32 v93, v104 offset:4612\n\t"
    "v_smfmac_f32_16x16x64_bf16 v[12:15], a[84:87], v[56:63], v186 abid:1\n\t"
    "ds_read_b32 v92, v230 offset:4612\n\t"
    "v_smfmac_f32_16x16x64_bf16 v[12:15], a[100:103], v[40:47], v188 abid:1\n\t"
    "v_smfmac_f32_16x16x64_bf16 v[12:15], a[116:119], v[48:55], v190 abid:1\n\t"
    "ds_read_b128 v[80:83], v245 offset:3072\n\t"
    "v_smfmac_f32_16x16x64_bf16 v[12:15], a[132:135], v[64:71], v192 abid:1\n\t"
    "ds_read_b32 v95, v105 offset:4612\n\t"
    "v_smfmac_f32_16x16x64_bf16 v[20:23], a[88:91], v[56:63], v187\n\t"
    "v_cvt_pk_bf16_f32 v96, v4, v5\n\t"
    "v_smfmac_f32_16x16x64_bf16 v[20:23], a[104:107], v[40:47], v189\n\t"
    "ds_read_b32 v94, v231 offset:4612\n\t"
    "v_cvt_pk_bf16_f32 v97, v6, v7\n\t"
    "v_smfmac_f32_16x16x64_bf16 v[20:23], a[120:123], v[48:55], v191\n\t"
    "ds_read_b128 v[4:7], v244 offset:0\n\t"
    "v_cvt_pk_bf16_f32 v98, v12, v13\n\t"
    "v_smfmac_f32_16x16x64_bf16 v[20:23], a[136:139], v[64:71], v193\n\t"
    "s_bitcmp1_b32 s54, 7\n\t"
    "s_mov_b32 s66, 33030144\n\t"
    "s_cmov_b32 s66, 33103872\n\t"
    "v_smfmac_f32_16x16x64_bf16 v[8:11], a[108:111], v[40:47], v189 abid:1\n\t"
    "ds_read_b128 v[56:59], v217 offset:9216\n\t"
    "v_smfmac_f32_16x16x64_bf16 v[8:11], a[124:127], v[48:55], v191 abid:1\n\t"
    "ds_read_b128 v[60:63], v236 offset:9216\n\t"
    "v_cvt_pk_bf16_f32 v99, v14, v15\n\t"
    "v_smfmac_f32_16x16x64_bf16 v[8:11], a[140:143], v[64:71], v193 abid:1\n\t"
    "s_waitcnt lgkmcnt(5)\n\t"
    "v_cvt_pk_bf16_f32 v100, v20, v21\n\t"
    "v_mfma_f32_16x16x32_bf16 v[8:11], v[80:83], v[88:91], v[8:11]\n\t"
    "ds_read_b128 v[80:83], v245 offset:6144\n\t"
    "v_smfmac_f32_16x16x64_bf16 v[24:27], v[116:119], v[40:47], v209\n\t"
    "ds_read_b32 v91, v77 offset:9220\n\t"
    "v_smfmac_f32_16x16x64_bf16 v[24:27], v[136:139], v[48:55], v211 abid:1\n\t"
    "ds_read_b32 v89, v76 offset:9220\n\t"
    "v_smfmac_f32_16x16x64_bf16 v[24:27], v[156:159], v[64:71], v214\n\t"
    "v_smfmac_f32_16x16x64_bf16 v[16:19], v[120:123], v[40:47], v209 abid:1\n\t"
    "ds_read_b128 v[12:15], v244 offset:64\n\t"
    "v_cvt_pk_bf16_f32 v101, v22, v23\n\t"
    "v_smfmac_f32_16x16x64_bf16 v[16:19], v[140:143], v[48:55], v212\n\t"
    "ds_read_b128 v[20:23], v244 offset:128\n\t"
    "v_cvt_pk_bf16_f32 v102, v8, v9\n\t"
    "v_smfmac_f32_16x16x64_bf16 v[16:19], v[160:163], v[64:71], v214 abid:1\n\t"
    "ds_read_b32 v88, v222 offset:9220\n\t"
    "v_cvt_pk_bf16_f32 v103, v10, v11\n\t"
    "v_smfmac_f32_16x16x64_bf16 v[32:35], v[144:147], v[48:55], v212 abid:1\n\t"
    "ds_read_b128 v[40:43], v217 offset:9344\n\t"
    "v_cndmask_b32_dpp v9, v101, v97, vcc quad_perm:[0,0,2,2] row_mask:0xf bank_mask:0xf\n\t"
    "v_smfmac_f32_16x16x64_bf16 v[32:35], v[164:167], v[64:71], v215\n\t"
    "s_waitcnt lgkmcnt(6)\n\t"
    "ds_read_b128 v[44:47], v236 offset:9344\n\t"
    "v_cndmask_b32_dpp v11, v103, v99, vcc quad_perm:[0,0,2,2] row_mask:0xf bank_mask:0xf\n\t"
    "v_mfma_f32_16x16x32_bf16 v[32:35], v[80:83], v[92:95], v[32:35]\n\t"
    "ds_read_b128 v[80:83], v245 offset:2048\n\t"
    "v_cndmask_b32_dpp v8, v100, v96, vcc quad_perm:[0,0,2,2] row_mask:0xf bank_mask:0xf\n\t"
    "v_smfmac_f32_16x16x64_bf16 v[36:39], v[148:151], v[48:55], v213\n\t"
    "ds_read_b32 v90, v223 offset:9220\n\t"
    "v_cndmask_b32_dpp v10, v102, v98, vcc quad_perm:[0,0,2,2] row_mask:0xf bank_mask:0xf\n\t"
    "v_smfmac_f32_16x16x64_bf16 v[36:39], v[168:171], v[64:71], v215 abid:1\n\t"
    "s_not_b64 vcc, s[18:19]\n\t"
    "ds_read_b32 v94, v229 offset:9220\n\t"
    "v_cndmask_b32_dpp v102, v98, v102, vcc quad_perm:[1,1,3,3] row_mask:0xf bank_mask:0xf\n\t"
    "v_smfmac_f32_16x16x64_bf16 v[28:31], v[152:155], v[48:55], v213 abid:1\n\t"
    "buffer_store_dwordx4 v[8:11], v241, s[44:47], s66 offen offset:0 sc1\n\t"
    "ds_read_b32 v95, v87 offset:9220\n\t"
    "v_cndmask_b32_dpp v100, v96, v100, vcc quad_perm:[1,1,3,3] row_mask:0xf bank_mask:0xf\n\t"
    "v_cndmask_b32_dpp v103, v99, v103, vcc quad_perm:[1,1,3,3] row_mask:0xf bank_mask:0xf\n\t"
    "v_cvt_pk_bf16_f32 v98, v16, v17\n\t"
    "v_smfmac_f32_16x16x64_bf16 v[28:31], v[172:175], v[64:71], v216\n\t"
    "ds_read_b128 v[52:55], v236 offset:9472\n\t"
    "v_cndmask_b32_dpp v101, v97, v101, vcc quad_perm:[1,1,3,3] row_mask:0xf bank_mask:0xf\n\t"
    "v_smfmac_f32_16x16x64_bf16 v[4:7], a[0:3], v[56:63], v176\n\t"
    "s_waitcnt lgkmcnt(5)\n\t"
    "s_mov_b64 vcc, s[18:19]\n\t"
    "ds_read_b128 v[8:11], v244 offset:192\n\t"
    "v_cvt_pk_bf16_f32 v99, v18, v19\n\t"
    "v_smfmac_f32_16x16x64_bf16 v[4:7], a[16:19], v[40:47], v178\n\t"
    "ds_read_b32 v92, v228 offset:9220\n\t"
    "v_cvt_pk_bf16_f32 v97, v26, v27\n\t"
    "v_smfmac_f32_16x16x64_bf16 v[12:15], a[4:7], v[56:63], v176 abid:1\n\t"
    "ds_read_b128 v[48:51], v217 offset:9472\n\t"
    "v_cvt_pk_bf16_f32 v96, v24, v25\n\t"
    "v_smfmac_f32_16x16x64_bf16 v[12:15], a[20:23], v[40:47], v178 abid:1\n\t"
    "ds_read_b128 v[24:27], v244 offset:256\n\t"
    "v_cvt_pk_bf16_f32 v28, v28, v29\n\t"
    "v_cvt_pk_bf16_f32 v29, v30, v31\n\t"
    "v_smfmac_f32_16x16x64_bf16 v[20:23], a[8:11], v[56:63], v177\n\t"
    "buffer_store_dwordx4 v[100:103], v241, s[44:47], s66 offen offset:1152 sc1\n\t"
    "ds_write_b64 v242, v[28:29] offset:512\n\t"
    "v_smfmac_f32_16x16x64_bf16 v[20:23], a[24:27], v[40:47], v179\n\t"
    "s_waitcnt lgkmcnt(8)\n\t"
    "ds_read_b128 v[16:19], v244 offset:320\n\t"
    "v_cvt_pk_bf16_f32 v102, v36, v37\n\t"
    "v_mfma_f32_16x16x32_bf16 v[20:23], v[80:83], v[88:91], v[20:23]\n\t"
    "s_waitcnt lgkmcnt(5)\n\t"
    "ds_read_b128 v[80:83], v245 offset:5120\n\t"
    "v_cvt_pk_bf16_f32 v100, v32, v33\n\t"
    "v_cvt_pk_bf16_f32 v101, v34, v35\n\t"
    "v_smfmac_f32_16x16x64_bf16 v[8:11], a[12:15], v[56:63], v177 abid:1\n\t"
    "ds_read_b32 v93, v86 offset:9220\n\t"
    "v_cvt_pk_bf16_f32 v103, v38, v39\n\t"
    "v_smfmac_f32_16x16x64_bf16 v[8:11], a[28:31], v[40:47], v179 abid:1\n\t"
    "s_waitcnt lgkmcnt(5)\n\t"
    "buffer_load_dwordx4 v239, s[40:43], s68 offen offset:3584 nt lds\n\t"
    "v_smfmac_f32_16x16x64_bf16 v[8:11], a[44:47], v[48:55], v181 abid:1\n\t"
    "s_waitcnt lgkmcnt(4)\n\t"
    "ds_read_b128 v[32:35], v244 offset:384\n\t"
    "v_cndmask_b32_dpp v37, v101, v97, vcc quad_perm:[0,0,2,2] row_mask:0xf bank_mask:0xf\n\t"
    "v_cndmask_b32_dpp v38, v102, v98, vcc quad_perm:[0,0,2,2] row_mask:0xf bank_mask:0xf\n\t"
    "v_cndmask_b32_dpp v39, v103, v99, vcc quad_perm:[0,0,2,2] row_mask:0xf bank_mask:0xf\n\t"
    "v_smfmac_f32_16x16x64_bf16 v[24:27], a[144:147], v[56:63], v194\n\t"
    "ds_read_b128 v[68:71], v236 offset:9600\n\t"
    "v_cndmask_b32_dpp v36, v100, v96, vcc quad_perm:[0,0,2,2] row_mask:0xf bank_mask:0xf\n\t"
    "v_smfmac_f32_16x16x64_bf16 v[24:27], a[164:167], v[40:47], v196 abid:1\n\t"
    "ds_read_b128 v[64:67], v217 offset:9600\n\t"
    "v_smfmac_f32_16x16x64_bf16 v[24:27], a[184:187], v[48:55], v199\n\t"
    "s_waitcnt lgkmcnt(5)\n\t"
    "s_not_b64 vcc, s[18:19]\n\t"
    "buffer_store_dwordx4 v[36:39], v241, s[44:47], s66 offen offset:128 sc1\n\t"
    "ds_read_b128 v[28:31], v244 offset:512\n\t"
    "v_cndmask_b32_dpp v102, v98, v102, vcc quad_perm:[1,1,3,3] row_mask:0xf bank_mask:0xf\n\t"
    "v_add_u32_sdwa v88, v248, v234 dst_sel:DWORD dst_unused:UNUSED_PAD src0_sel:DWORD src1_sel:WORD_0\n\t"
    "v_add_u32_sdwa v91, v248, v235 dst_sel:DWORD dst_unused:UNUSED_PAD src0_sel:DWORD src1_sel:WORD_1\n\t"
    "v_smfmac_f32_16x16x64_bf16 v[16:19], a[148:151], v[56:63], v194 abid:1\n\t"
    "ds_read_b128 v[36:39], v244 offset:448\n\t"
    "v_add_u32_sdwa v90, v248, v235 dst_sel:DWORD dst_unused:UNUSED_PAD src0_sel:DWORD src1_sel:WORD_0\n\t"
    "v_smfmac_f32_16x16x64_bf16 v[16:19], a[168:171], v[40:47], v197\n\t"
    "v_cndmask_b32_dpp v103, v99, v103, vcc quad_perm:[1,1,3,3] row_mask:0xf bank_mask:0xf\n\t"
    "v_smfmac_f32_16x16x64_bf16 v[16:19], a[188:191], v[48:55], v199 abid:1\n\t"
    "s_waitcnt lgkmcnt(5)\n\t"
    "ds_read_b32 v88, v88 offset:9220\n\t"
    "v_cndmask_b32_dpp v100, v96, v100, vcc quad_perm:[1,1,3,3] row_mask:0xf bank_mask:0xf\n\t"
    "v_mfma_f32_16x16x32_bf16 v[16:19], v[80:83], v[92:95], v[16:19]\n\t"
    "s_waitcnt lgkmcnt(5)\n\t"
    "ds_read_b128 v[80:83], v245 offset:8192\n\t"
    "v_cndmask_b32_dpp v101, v97, v101, vcc quad_perm:[1,1,3,3] row_mask:0xf bank_mask:0xf\n\t"
    "v_smfmac_f32_16x16x64_bf16 v[32:35], a[152:155], v[56:63], v195\n\t"
    "ds_read_b32 v90, v90 offset:9220\n\t"
    "v_add_u32_sdwa v89, v248, v234 dst_sel:DWORD dst_unused:UNUSED_PAD src0_sel:DWORD src1_sel:WORD_1\n\t"
    "v_smfmac_f32_16x16x64_bf16 v[32:35], a[172:175], v[40:47], v197 abid:1\n\t"
    "buffer_store_dwordx4 v[100:103], v241, s[44:47], s66 offen offset:1280 sc1\n\t"
    "ds_read_b32 v93, v74 offset:9220\n\t"
    "v_smfmac_f32_16x16x64_bf16 v[32:35], a[192:195], v[48:55], v200\n\t"
    "s_waitcnt lgkmcnt(6)\n\t"
    "v_smfmac_f32_16x16x64_bf16 v[32:35], a[212:215], v[64:71], v202 abid:1\n\t"
    "s_waitcnt lgkmcnt(4)\n\t"
    "ds_read_b32 v89, v89 offset:9220\n\t"
    "ds_read_b32 v92, v220 offset:9220\n\t"
    "v_smfmac_f32_16x16x64_bf16 v[36:39], a[156:159], v[56:63], v195 abid:1\n\t"
    "s_bitcmp1_b32 s54, 12\n\t"
    "s_mov_b32 s68, 56623104\n\t"
    "s_cmov_b32 s68, 56696832\n\t"
    "v_smfmac_f32_16x16x64_bf16 v[36:39], a[176:179], v[40:47], v198\n\t"
    "ds_read_b32 v91, v91 offset:9220\n\t"
    "ds_read_b32 v94, v221 offset:9220\n\t"
    "v_smfmac_f32_16x16x64_bf16 v[36:39], a[196:199], v[48:55], v200 abid:1\n\t"
    "s_add_u32 s67, s57, s66\n\t"
    "s_mov_b64 vcc, s[18:19]\n\t"
    "v_smfmac_f32_16x16x64_bf16 v[36:39], a[216:219], v[64:71], v203\n\t"
    "s_add_u32 m0, s52, 0\n\t"
    "s_mov_b32 exec_hi, 0\n\t"
    "buffer_load_dwordx4 v237, s[40:43], s68 offen offset:0 nt lds\n\t"
    "s_mov_b32 exec_hi, -1\n\t"
    "v_smfmac_f32_16x16x64_bf16 v[28:31], a[160:163], v[56:63], v196\n\t"
    "ds_read_b128 v[56:59], v217 offset:9728\n\t"
    "ds_read_b128 v[60:63], v236 offset:9728\n\t"
    "v_smfmac_f32_16x16x64_bf16 v[28:31], a[180:183], v[40:47], v198 abid:1\n\t"
    "ds_read_b128 v[44:47], v236 offset:9856\n\t"
    "v_smfmac_f32_16x16x64_bf16 v[28:31], a[200:203], v[48:55], v201\n\t"
    "ds_read_b32 v95, v75 offset:9220\n\t"
    "v_smfmac_f32_16x16x64_bf16 v[28:31], a[220:223], v[64:71], v203 abid:1\n\t"
    "s_waitcnt lgkmcnt(5)\n\t"
    "v_mfma_f32_16x16x32_bf16 v[28:31], v[80:83], v[88:91], v[28:31]\n\t"
    "ds_read_b128 v[80:83], v245 offset:1024\n\t"
    "v_add_u32_sdwa v89, v248, v246 dst_sel:DWORD dst_unused:UNUSED_PAD src0_sel:DWORD src1_sel:WORD_1\n\t"
    "v_smfmac_f32_16x16x64_bf16 v[4:7], a[32:35], v[48:55], v180\n\t"
    "ds_read_b128 v[40:43], v217 offset:9856\n\t"
    "v_add_u32_sdwa v88, v248, v246 dst_sel:DWORD dst_unused:UNUSED_PAD src0_sel:DWORD src1_sel:WORD_0\n\t"
    "v_smfmac_f32_16x16x64_bf16 v[4:7], a[48:51], v[64:71], v182\n\t"
    "s_waitcnt lgkmcnt(4)\n\t"
    "ds_read_u16 v89, v89 offset:9216\n\t"
    "v_add_u32_sdwa v91, v248, v247 dst_sel:DWORD dst_unused:UNUSED_PAD src0_sel:DWORD src1_sel:WORD_1\n\t"
    "v_smfmac_f32_16x16x64_bf16 v[4:7], a[64:67], v[56:63], v184\n\t"
    "ds_read_u16 v88, v88 offset:9216\n\t"
    "ds_read_u16 v91, v91 offset:9216\n\t"
    "v_add_u32_sdwa v90, v248, v247 dst_sel:DWORD dst_unused:UNUSED_PAD src0_sel:DWORD src1_sel:WORD_0\n\t"
    "v_smfmac_f32_16x16x64_bf16 v[12:15], a[36:39], v[48:55], v180 abid:1\n\t"
    "s_waitcnt vmcnt(23)\n\t"
    "s_barrier\n\t"
    "ds_read_u16 v90, v90 offset:9216\n\t"
    "v_smfmac_f32_16x16x64_bf16 v[12:15], a[52:55], v[64:71], v182 abid:1\n\t"
    "ds_read_b128 v[96:99], v243 offset:2560\n\t"
    "v_smfmac_f32_16x16x64_bf16 v[12:15], a[68:71], v[56:63], v184 abid:1\n\t"
    "s_waitcnt lgkmcnt(6)\n\t"
    "v_mfma_f32_16x16x32_bf16 v[12:15], v[80:83], v[92:95], v[12:15]\n\t"
    "ds_read_b32 v93, v84 offset:9220\n\t"
    "v_smfmac_f32_16x16x64_bf16 v[20:23], a[40:43], v[48:55], v181\n\t"
    "ds_read_b32 v95, v85 offset:9220\n\t"
    "v_smfmac_f32_16x16x64_bf16 v[20:23], a[56:59], v[64:71], v183\n\t"
    "ds_read_b32 v94, v227 offset:9220\n\t"
    "v_smfmac_f32_16x16x64_bf16 v[20:23], a[72:75], v[56:63], v185\n\t"
    "s_waitcnt lgkmcnt(4)\n\t"
    "ds_read_b128 v[80:83], v245 offset:4096\n\t"
    "s_set_gpr_idx_on s4, gpr_idx(SRC2,DST)\n\t"
    "v_mfma_f32_16x16x32_bf16 v[0:3], v[250:253], v[88:91], v[0:3]\n\t"
    "s_set_gpr_idx_off\n\t"
    "s_waitcnt lgkmcnt(4)\n\t"
    "s_mov_b32 exec_hi, 0\n\t"
    "buffer_store_dwordx4 v[96:99], v240, s[44:47], s67 offen offset:0 sc1\n\t"
    "s_mov_b32 exec_hi, -1\n\t"
    "v_smfmac_f32_16x16x64_bf16 v[8:11], a[60:63], v[64:71], v183 abid:1\n\t"
    "ds_read_b32 v92, v226 offset:9220\n\t"
    "v_smfmac_f32_16x16x64_bf16 v[8:11], a[76:79], v[56:63], v185 abid:1\n\t"
    "ds_read_b128 v[48:51], v217 offset:9984\n\t"
    "v_smfmac_f32_16x16x64_bf16 v[8:11], a[92:95], v[40:47], v187 abid:1\n\t"
    "ds_read_b128 v[52:55], v236 offset:9984\n\t"
    "s_set_gpr_idx_on s5, gpr_idx(SRC2,DST)\n\t"
    "v_mfma_f32_16x16x32_bf16 v[0:3], v[252:255], v[88:91], v[0:3]\n\t"
    "s_set_gpr_idx_off\n\t"
    "s_add_u32 m0, s52, 0\n\t"
    "s_nop 0\n\t"
    "buffer_load_dwordx4 v237, s[40:43], s68 offen offset:512 nt lds\n\t"
    "s_cmp_lg_u32 s22, 0\n\t"
    "s_cbranch_scc1 .Lsl13_%=\n\t"
    ".Lslr13_%=:\n\t"
    "v_smfmac_f32_16x16x64_bf16 v[24:27], a[204:207], v[64:71], v201 abid:1\n\t"
    "ds_read_b32 v88, v232 offset:9220\n\t"
    "ds_read_b32 v89, v106 offset:9220\n\t"
    "v_smfmac_f32_16x16x64_bf16 v[24:27], a[224:227], v[56:63], v204\n\t"
    "s_memrealtime s[84:85]\n\t"
    "v_smfmac_f32_16x16x64_bf16 v[24:27], a[244:247], v[40:47], v206 abid:1\n\t"
    "s_waitcnt lgkmcnt(4)\n\t"
    "ds_read_b32 v91, v107 offset:9220\n\t"
    "v_mfma_f32_16x16x32_bf16 v[24:27], v[80:83], v[92:95], v[24:27]\n\t"
    "ds_read_b32 v90, v233 offset:9220\n\t"
    "v_smfmac_f32_16x16x64_bf16 v[16:19], a[208:211], v[64:71], v202\n\t"
    "ds_read_b32 v94, v219 offset:9220\n\t"
    "v_smfmac_f32_16x16x64_bf16 v[16:19], a[228:231], v[56:63], v204 abid:1\n\t"
    "ds_read_b128 v[80:83], v245 offset:7168\n\t"
    "v_smfmac_f32_16x16x64_bf16 v[16:19], a[248:251], v[40:47], v207\n\t"
    "s_sub_i32 s87, s87, s99\n\t"
    "ds_read_b128 v[68:71], v236 offset:10112\n\t"
    "v_smfmac_f32_16x16x64_bf16 v[32:35], a[232:235], v[56:63], v205\n\t"
    "v_smfmac_f32_16x16x64_bf16 v[32:35], a[252:255], v[40:47], v207 abid:1\n\t"
    "s_waitcnt lgkmcnt(7)\n\t"
    "s_sub_i32 s98, s98, s99\n\t"
    "ds_read_b32 v93, v72 offset:9220\n\t"
    "v_smfmac_f32_16x16x64_bf16 v[32:35], v[124:127], v[48:55], v210\n\t"
    "s_add_i32 s86, s87, 1775\n\t"
    "ds_read_b32 v92, v218 offset:9220\n\t"
    "v_smfmac_f32_16x16x64_bf16 v[36:39], a[236:239], v[56:63], v205 abid:1\n\t"
    "s_add_i32 s98, s98, 72\n\t"
    "ds_read_b32 v95, v73 offset:9220\n\t"
    "v_smfmac_f32_16x16x64_bf16 v[36:39], v[108:111], v[40:47], v208\n\t"
    "s_max_i32 s86, s86, s98\n\t"
    "v_smfmac_f32_16x16x64_bf16 v[36:39], v[128:131], v[48:55], v210 abid:1\n\t"
    "s_waitcnt lgkmcnt(4)\n\t"
    "ds_read_b128 v[64:67], v217 offset:10112\n\t"
    "v_mfma_f32_16x16x32_bf16 v[36:39], v[80:83], v[88:91], v[36:39]\n\t"
    "s_sub_u32 s89, s84, s24\n\t"
    "ds_read_b128 v[80:83], v245 offset:0\n\t"
    "v_smfmac_f32_16x16x64_bf16 v[28:31], a[240:243], v[56:63], v206\n\t"
    "s_cmp_lt_i32 s89, s86\n\t"
    "s_cbranch_scc1 .Lpc8_%=\n\t"
    ".Lpc8r_%=:\n\t"
    "ds_read_b128 v[56:59], v217 offset:10240\n\t"
    "ds_read_b128 v[60:63], v236 offset:10240\n\t"
    "v_smfmac_f32_16x16x64_bf16 v[28:31], v[112:115], v[40:47], v208 abid:1\n\t"
    "v_smfmac_f32_16x16x64_bf16 v[28:31], v[132:135], v[48:55], v211\n\t"
    "s_bitcmp1_b32 s54, 8\n\t"
    "s_mov_b32 s65, 37748736\n\t"
    "s_cmov_b32 s65, 37822464\n\t"
    "s_max_i32 s98, s89, s86\n\t"
    "v_smfmac_f32_16x16x64_bf16 v[4:7], a[80:83], v[40:47], v186\n\t"
    "ds_read_b32 v91, v79 offset:9220\n\t"
    "v_smfmac_f32_16x16x64_bf16 v[4:7], a[96:99], v[48:55], v188\n\t"
    "s_waitcnt lgkmcnt(4)\n\t"
    "ds_read_b32 v88, v224 offset:9220\n\t"
    "v_smfmac_f32_16x16x64_bf16 v[4:7], a[112:115], v[64:71], v190\n\t"
    "s_add_u32 m0, s52, 0\n\t"
    "s_waitcnt lgkmcnt(2)\n\t"
    "buffer_load_dwordx4 v238, s[40:43], s68 offen offset:1536 nt lds\n\t"
    "v_smfmac_f32_16x16x64_bf16 v[4:7], a[128:131], v[56:63], v192\n\t"
    "ds_read_b32 v89, v78 offset:9220\n\t"
    "v_mfma_f32_16x16x32_bf16 v[4:7], v[80:83], v[92:95], v[4:7]\n\t"
    "ds_read_b32 v93, v104 offset:9220\n\t"
    "v_smfmac_f32_16x16x64_bf16 v[12:15], a[84:87], v[40:47], v186 abid:1\n\t"
    "ds_read_b32 v90, v225 offset:9220\n\t"
    "v_smfmac_f32_16x16x64_bf16 v[12:15], a[100:103], v[48:55], v188 abid:1\n\t"
    "v_smfmac_f32_16x16x64_bf16 v[12:15], a[116:119], v[64:71], v190 abid:1\n\t"
    "ds_read_b32 v94, v231 offset:9220\n\t"
    "v_smfmac_f32_16x16x64_bf16 v[12:15], a[132:135], v[56:63], v192 abid:1\n\t"
    "ds_read_b128 v[80:83], v245 offset:3072\n\t"
    "v_smfmac_f32_16x16x64_bf16 v[20:23], a[88:91], v[40:47], v187\n\t"
    "ds_read_b32 v95, v105 offset:9220\n\t"
    "v_cvt_pk_bf16_f32 v100, v4, v5\n\t"
    "v_smfmac_f32_16x16x64_bf16 v[20:23], a[104:107], v[48:55], v189\n\t"
    "v_cvt_pk_bf16_f32 v101, v6, v7\n\t"
    "v_smfmac_f32_16x16x64_bf16 v[20:23], a[120:123], v[64:71], v191\n\t"
    "ds_read_b128 v[44:47], v236 offset:13824\n\t"
    "v_smfmac_f32_16x16x64_bf16 v[20:23], a[136:139], v[56:63], v193\n\t"
    "ds_read_b128 v[4:7], v244 offset:0\n\t"
    "v_smfmac_f32_16x16x64_bf16 v[8:11], a[108:111], v[48:55], v189 abid:1\n\t"
    "ds_read_b128 v[40:43], v217 offset:13824\n\t"
    "v_smfmac_f32_16x16x64_bf16 v[8:11], a[124:127], v[64:71], v191 abid:1\n\t"
    "s_add_u32 s66, s57, s65\n\t"
    "v_smfmac_f32_16x16x64_bf16 v[8:11], a[140:143], v[56:63], v193 abid:1\n\t"
    "s_waitcnt lgkmcnt(4)\n\t"
    "ds_read_b32 v92, v230 offset:9220\n\t"
    "v_cvt_pk_bf16_f32 v103, v14, v15\n\t"
    "v_mfma_f32_16x16x32_bf16 v[8:11], v[80:83], v[88:91], v[8:11]\n\t"
    "ds_read_b32 v88, v222 offset:13828\n\t"
    "v_cvt_pk_bf16_f32 v97, v22, v23\n\t"
    "v_smfmac_f32_16x16x64_bf16 v[24:27], v[116:119], v[48:55], v209\n\t"
    "ds_read_b32 v91, v77 offset:13828\n\t"
    "v_cvt_pk_bf16_f32 v102, v12, v13\n\t"
    "v_smfmac_f32_16x16x64_bf16 v[24:27], v[136:139], v[64:71], v211 abid:1\n\t"
    "ds_read_b128 v[80:83], v245 offset:6144\n\t"
    "v_cvt_pk_bf16_f32 v96, v20, v21\n\t"
    "v_smfmac_f32_16x16x64_bf16 v[24:27], v[156:159], v[56:63], v214\n\t"
    "s_bitcmp1_b32 s54, 9\n\t"
    "s_mov_b32 s67, 42467328\n\t"
    "s_cmov_b32 s67, 42541056\n\t"
    "v_cvt_pk_bf16_f32 v99, v10, v11\n\t"
    "v_smfmac_f32_16x16x64_bf16 v[16:19], v[120:123], v[48:55], v209 abid:1\n\t"
    "ds_read_b128 v[52:55], v236 offset:13952\n\t"
    "v_cvt_pk_bf16_f32 v98, v8, v9\n\t"
    "v_smfmac_f32_16x16x64_bf16 v[16:19], v[140:143], v[64:71], v212\n\t"
    "ds_read_b128 v[48:51], v217 offset:13952\n\t"
    "v_cndmask_b32_dpp v11, v99, v103, vcc quad_perm:[0,0,2,2] row_mask:0xf bank_mask:0xf\n\t"
    "v_smfmac_f32_16x16x64_bf16 v[16:19], v[160:163], v[56:63], v214 abid:1\n\t"
    "ds_read_b32 v89, v76 offset:13828\n\t"
    "v_cndmask_b32_dpp v9, v97, v101, vcc quad_perm:[0,0,2,2] row_mask:0xf bank_mask:0xf\n\t"
    "v_smfmac_f32_16x16x64_bf16 v[32:35], v[144:147], v[64:71], v212 abid:1\n\t"
    "v_cndmask_b32_dpp v8, v96, v100, vcc quad_perm:[0,0,2,2] row_mask:0xf bank_mask:0xf\n\t"
    "v_smfmac_f32_16x16x64_bf16 v[32:35], v[164:167], v[56:63], v215\n\t"
    "s_waitcnt lgkmcnt(3)\n\t"
    "ds_read_b128 v[12:15], v244 offset:64\n\t"
    "v_cndmask_b32_dpp v10, v98, v102, vcc quad_perm:[0,0,2,2] row_mask:0xf bank_mask:0xf\n\t"
    "v_mfma_f32_16x16x32_bf16 v[32:35], v[80:83], v[92:95], v[32:35]\n\t"
    "s_not_b64 vcc, s[18:19]\n\t"
    "ds_read_b32 v90, v223 offset:13828\n\t"
    "v_cndmask_b32_dpp v97, v101, v97, vcc quad_perm:[1,1,3,3] row_mask:0xf bank_mask:0xf\n\t"
    "v_smfmac_f32_16x16x64_bf16 v[36:39], v[148:151], v[64:71], v213\n\t"
    "ds_read_b128 v[80:83], v245 offset:2048\n\t"
    "v_cndmask_b32_dpp v99, v103, v99, vcc quad_perm:[1,1,3,3] row_mask:0xf bank_mask:0xf\n\t"
    "v_smfmac_f32_16x16x64_bf16 v[36:39], v[168:171], v[56:63], v215 abid:1\n\t"
    "ds_read_b128 v[20:23], v244 offset:128\n\t"
    "v_cndmask_b32_dpp v96, v100, v96, vcc quad_perm:[1,1,3,3] row_mask:0xf bank_mask:0xf\n\t"
    "v_smfmac_f32_16x16x64_bf16 v[28:31], v[152:155], v[64:71], v213 abid:1\n\t"
    "buffer_store_dwordx4 v[8:11], v241, s[44:47], s65 offen offset:0 sc1\n\t"
    "ds_read_b128 v[64:67], v217 offset:14080\n\t"
    "v_cndmask_b32_dpp v98, v102, v98, vcc quad_perm:[1,1,3,3] row_mask:0xf bank_mask:0xf\n\t"
    "v_cvt_pk_bf16_f32 v100, v24, v25\n\t"
    "v_cvt_pk_bf16_f32 v101, v26, v27\n\t"
    "v_smfmac_f32_16x16x64_bf16 v[28:31], v[172:175], v[56:63], v216\n\t"
    "v_smfmac_f32_16x16x64_bf16 v[4:7], a[0:3], v[40:47], v176\n\t"
    "s_waitcnt lgkmcnt(6)\n\t"
    "ds_read_b128 v[8:11], v244 offset:192\n\t"
    "v_smfmac_f32_16x16x64_bf16 v[4:7], a[16:19], v[48:55], v178\n\t"
    "s_waitcnt lgkmcnt(5)\n\t"
    "ds_read_b128 v[24:27], v244 offset:256\n\t"
    "v_cvt_pk_bf16_f32 v103, v18, v19\n\t"
    "v_smfmac_f32_16x16x64_bf16 v[12:15], a[4:7], v[40:47], v176 abid:1\n\t"
    "ds_read_b128 v[68:71], v236 offset:14080\n\t"
    "v_cvt_pk_bf16_f32 v102, v16, v17\n\t"
    "v_smfmac_f32_16x16x64_bf16 v[12:15], a[20:23], v[48:55], v178 abid:1\n\t"
    "s_waitcnt lgkmcnt(4)\n\t"
    "ds_read_b32 v95, v87 offset:13828\n\t"
    "v_smfmac_f32_16x16x64_bf16 v[20:23], a[8:11], v[40:47], v177\n\t"
    "buffer_store_dwordx4 v[96:99], v241, s[44:47], s65 offen offset:1152 sc1\n\t"
    "ds_read_b32 v94, v229 offset:13828\n\t"
    "v_cvt_pk_bf16_f32 v28, v28, v29\n\t"
    "v_cvt_pk_bf16_f32 v29, v30, v31\n\t"
    "v_smfmac_f32_16x16x64_bf16 v[20:23], a[24:27], v[48:55], v179\n\t"
    "ds_read_b128 v[16:19], v244 offset:320\n\t"
    "v_cvt_pk_bf16_f32 v96, v32, v33\n\t"
    "v_mfma_f32_16x16x32_bf16 v[20:23], v[80:83], v[88:91], v[20:23]\n\t"
    "s_waitcnt lgkmcnt(5)\n\t"
    "ds_read_b128 v[80:83], v245 offset:5120\n\t"
    "v_cvt_pk_bf16_f32 v97, v34, v35\n\t"
    "v_cvt_pk_bf16_f32 v98, v36, v37\n\t"
    "v_smfmac_f32_16x16x64_bf16 v[8:11], a[12:15], v[40:47], v177 abid:1\n\t"
    "ds_read_b32 v93, v86 offset:13828\n\t"
    "v_cvt_pk_bf16_f32 v99, v38, v39\n\t"
    "v_smfmac_f32_16x16x64_bf16 v[8:11], a[28:31], v[48:55], v179 abid:1\n\t"
    "s_waitcnt lgkmcnt(5)\n\t"
    "buffer_load_dwordx4 v239, s[40:43], s68 offen offset:2560 nt lds\n\t"
    "v_smfmac_f32_16x16x64_bf16 v[8:11], a[44:47], v[64:71], v181 abid:1\n\t"
    "s_mov_b64 vcc, s[18:19]\n\t"
    "ds_read_b32 v92, v228 offset:13828\n\t"
    "v_cndmask_b32_dpp v36, v96, v100, vcc quad_perm:[0,0,2,2] row_mask:0xf bank_mask:0xf\n\t"
    "v_cndmask_b32_dpp v38, v98, v102, vcc quad_perm:[0,0,2,2] row_mask:0xf bank_mask:0xf\n\t"
    "v_cndmask_b32_dpp v39, v99, v103, vcc quad_perm:[0,0,2,2] row_mask:0xf bank_mask:0xf\n\t"
    "v_smfmac_f32_16x16x64_bf16 v[24:27], a[144:147], v[40:47], v194\n\t"
    "ds_read_b128 v[32:35], v244 offset:384\n\t"
    "v_cndmask_b32_dpp v37, v97, v101, vcc quad_perm:[0,0,2,2] row_mask:0xf bank_mask:0xf\n\t"
    "v_smfmac_f32_16x16x64_bf16 v[24:27], a[164:167], v[48:55], v196 abid:1\n\t"
    "s_not_b64 vcc, s[18:19]\n\t"
    "ds_read_b128 v[60:63], v236 offset:14208\n\t"
    "v_cndmask_b32_dpp v99, v103, v99, vcc quad_perm:[1,1,3,3] row_mask:0xf bank_mask:0xf\n\t"
    "v_smfmac_f32_16x16x64_bf16 v[24:27], a[184:187], v[64:71], v199\n\t"
    "s_waitcnt lgkmcnt(5)\n\t"
    "buffer_store_dwordx4 v[36:39], v241, s[44:47], s65 offen offset:128 sc1\n\t"
    "ds_read_b128 v[56:59], v217 offset:14208\n\t"
    "v_cndmask_b32_dpp v96, v100, v96, vcc quad_perm:[1,1,3,3] row_mask:0xf bank_mask:0xf\n\t"
    "v_smfmac_f32_16x16x64_bf16 v[16:19], a[148:151], v[40:47], v194 abid:1\n\t"
    "ds_read_b128 v[36:39], v244 offset:448\n\t"
    "v_add_u32_sdwa v91, v248, v235 dst_sel:DWORD dst_unused:UNUSED_PAD src0_sel:DWORD src1_sel:WORD_1\n\t"
    "v_smfmac_f32_16x16x64_bf16 v[16:19], a[168:171], v[48:55], v197\n\t"
    "ds_write_b64 v242, v[28:29] offset:0\n\t"
    "v_smfmac_f32_16x16x64_bf16 v[16:19], a[188:191], v[64:71], v199 abid:1\n\t"
    "s_waitcnt lgkmcnt(5)\n\t"
    "v_add_u32_sdwa v89, v248, v234 dst_sel:DWORD dst_unused:UNUSED_PAD src0_sel:DWORD src1_sel:WORD_1\n\t"
    "v_mfma_f32_16x16x32_bf16 v[16:19], v[80:83], v[92:95], v[16:19]\n\t"
    "s_waitcnt lgkmcnt(4)\n\t"
    "ds_read_b32 v89, v89 offset:13828\n\t"
    "v_cndmask_b32_dpp v97, v101, v97, vcc quad_perm:[1,1,3,3] row_mask:0xf bank_mask:0xf\n\t"
    "v_smfmac_f32_16x16x64_bf16 v[32:35], a[152:155], v[40:47], v195\n\t"
    "ds_read_b128 v[80:83], v245 offset:8192\n\t"
    "v_cndmask_b32_dpp v98, v102, v98, vcc quad_perm:[1,1,3,3] row_mask:0xf bank_mask:0xf\n\t"
    "v_smfmac_f32_16x16x64_bf16 v[32:35], a[172:175], v[48:55], v197 abid:1\n\t"
    "buffer_store_dwordx4 v[96:99], v241, s[44:47], s65 offen offset:1280 sc1\n\t"
    "ds_read_b128 v[28:31], v244 offset:512\n\t"
    "ds_read_b32 v93, v74 offset:13828\n\t"
    "v_add_u32_sdwa v88, v248, v234 dst_sel:DWORD dst_unused:UNUSED_PAD src0_sel:DWORD src1_sel:WORD_0\n\t"
    "v_smfmac_f32_16x16x64_bf16 v[32:35], a[192:195], v[64:71], v200\n\t"
    "s_waitcnt lgkmcnt(6)\n\t"
    "v_smfmac_f32_16x16x64_bf16 v[32:35], a[212:215], v[56:63], v202 abid:1\n\t"
    "s_waitcnt lgkmcnt(5)\n\t"
    "ds_read_b32 v88, v88 offset:13828\n\t"
    "v_add_u32_sdwa v90, v248, v235 dst_sel:DWORD dst_unused:UNUSED_PAD src0_sel:DWORD src1_sel:WORD_0\n\t"
    "v_smfmac_f32_16x16x64_bf16 v[36:39], a[156:159], v[40:47], v195 abid:1\n\t"
    "ds_read_b32 v91, v91 offset:13828\n\t"
    "ds_read_b32 v94, v221 offset:13828\n\t"
    "v_smfmac_f32_16x16x64_bf16 v[36:39], a[176:179], v[48:55], v198\n\t"
    "buffer_load_dwordx4 v239, s[40:43], s68 offen offset:3584 nt lds\n\t"
    "ds_read_b32 v90, v90 offset:13828\n\t"
    "v_smfmac_f32_16x16x64_bf16 v[36:39], a[196:199], v[64:71], v200 abid:1\n\t"
    "v_smfmac_f32_16x16x64_bf16 v[36:39], a[216:219], v[56:63], v203\n\t"
    "s_waitcnt lgkmcnt(5)\n\t"
    "s_bitcmp1_b32 s54, 13\n\t"
    "s_mov_b32 s68, 61341696\n\t"
    "s_cmov_b32 s68, 61415424\n\t"
    "v_smfmac_f32_16x16x64_bf16 v[28:31], a[160:163], v[40:47], v196\n\t"
    "ds_read_b128 v[40:43], v217 offset:14336\n\t"
    "ds_read_b128 v[44:47], v236 offset:14336\n\t"
    "v_smfmac_f32_16x16x64_bf16 v[28:31], a[180:183], v[48:55], v198 abid:1\n\t"
    "ds_read_b32 v95, v75 offset:13828\n\t"
    "v_smfmac_f32_16x16x64_bf16 v[28:31], a[200:203], v[64:71], v201\n\t"
    "v_smfmac_f32_16x16x64_bf16 v[28:31], a[220:223], v[56:63], v203 abid:1\n\t"
    "s_waitcnt lgkmcnt(3)\n\t"
    "ds_read_b32 v92, v220 offset:13828\n\t"
    "v_mfma_f32_16x16x32_bf16 v[28:31], v[80:83], v[88:91], v[28:31]\n\t"
    "ds_read_b128 v[80:83], v245 offset:1024\n\t"
    "v_add_u32_sdwa v91, v248, v247 dst_sel:DWORD dst_unused:UNUSED_PAD src0_sel:DWORD src1_sel:WORD_1\n\t"
    "v_smfmac_f32_16x16x64_bf16 v[4:7], a[32:35], v[64:71], v180\n\t"
    "ds_read_u16 v91, v91 offset:13824\n\t"
    "v_add_u32_sdwa v88, v248, v246 dst_sel:DWORD dst_unused:UNUSED_PAD src0_sel:DWORD src1_sel:WORD_0\n\t"
    "v_smfmac_f32_16x16x64_bf16 v[4:7], a[48:51], v[56:63], v182\n\t"
    "s_waitcnt lgkmcnt(4)\n\t"
    "ds_read_u16 v88, v88 offset:13824\n\t"
    "v_add_u32_sdwa v89, v248, v246 dst_sel:DWORD dst_unused:UNUSED_PAD src0_sel:DWORD src1_sel:WORD_1\n\t"
    "v_smfmac_f32_16x16x64_bf16 v[4:7], a[64:67], v[40:47], v184\n\t"
    "ds_read_u16 v89, v89 offset:13824\n\t"
    "v_add_u32_sdwa v90, v248, v247 dst_sel:DWORD dst_unused:UNUSED_PAD src0_sel:DWORD src1_sel:WORD_0\n\t"
    "v_smfmac_f32_16x16x64_bf16 v[12:15], a[36:39], v[64:71], v180 abid:1\n\t"
    "s_waitcnt vmcnt(22)\n\t"
    "s_barrier\n\t"
    "ds_read_u16 v90, v90 offset:13824\n\t"
    "v_smfmac_f32_16x16x64_bf16 v[12:15], a[52:55], v[56:63], v182 abid:1\n\t"
    "s_add_u32 m0, s52, 4608\n\t"
    "s_mov_b32 exec_hi, 0\n\t"
    "buffer_load_dwordx4 v237, s[40:43], s68 offen offset:0 nt lds\n\t"
    "s_mov_b32 exec_hi, -1\n\t"
    "ds_read_b128 v[100:103], v243 offset:2048\n\t"
    "v_smfmac_f32_16x16x64_bf16 v[12:15], a[68:71], v[40:47], v184 abid:1\n\t"
    "s_waitcnt lgkmcnt(5)\n\t"
    "ds_read_b128 v[48:51], v217 offset:14464\n\t"
    "v_mfma_f32_16x16x32_bf16 v[12:15], v[80:83], v[92:95], v[12:15]\n\t"
    "ds_read_b128 v[52:55], v236 offset:14464\n\t"
    "v_smfmac_f32_16x16x64_bf16 v[20:23], a[40:43], v[64:71], v181\n\t"
    "ds_read_b128 v[80:83], v245 offset:4096\n\t"
    "v_smfmac_f32_16x16x64_bf16 v[20:23], a[56:59], v[56:63], v183\n\t"
    "ds_read_b32 v92, v226 offset:13828\n\t"
    "v_smfmac_f32_16x16x64_bf16 v[20:23], a[72:75], v[40:47], v185\n\t"
    "s_waitcnt lgkmcnt(5)\n\t"
    "ds_read_b128 v[64:67], v217 offset:14592\n\t"
    "s_set_gpr_idx_on s4, gpr_idx(SRC2,DST)\n\t"
    "v_mfma_f32_16x16x32_bf16 v[0:3], v[250:253], v[88:91], v[0:3]\n\t"
    "s_set_gpr_idx_off\n\t"
    "s_waitcnt lgkmcnt(5)\n\t"
    "s_mov_b32 exec_hi, 0\n\t"
    "buffer_store_dwordx4 v[100:103], v240, s[44:47], s66 offen offset:0 sc1\n\t"
    "s_mov_b32 exec_hi, -1\n\t"
    "ds_read_b32 v94, v227 offset:13828\n\t"
    "v_smfmac_f32_16x16x64_bf16 v[8:11], a[60:63], v[56:63], v183 abid:1\n\t"
    "ds_read_b32 v93, v84 offset:13828\n\t"
    "ds_read_b32 v95, v85 offset:13828\n\t"
    "v_smfmac_f32_16x16x64_bf16 v[8:11], a[76:79], v[40:47], v185 abid:1\n\t"
    "s_waitcnt lgkmcnt(6)\n\t"
    "ds_read_b128 v[68:71], v236 offset:14592\n\t"
    "v_smfmac_f32_16x16x64_bf16 v[8:11], a[92:95], v[48:55], v187 abid:1\n\t"
    "s_set_gpr_idx_on s5, gpr_idx(SRC2,DST)\n\t"
    "v_mfma_f32_16x16x32_bf16 v[0:3], v[252:255], v[88:91], v[0:3]\n\t"
    "s_set_gpr_idx_off\n\t"
    "s_cmp_lg_u32 s22, 0\n\t"
    "s_cbranch_scc1 .Lsl14_%=\n\t"
    ".Lslr14_%=:\n\t"
    "v_smfmac_f32_16x16x64_bf16 v[24:27], a[204:207], v[56:63], v201 abid:1\n\t"
    "ds_read_b32 v89, v106 offset:13828\n\t"
    "ds_read_b32 v91, v107 offset:13828\n\t"
    "v_smfmac_f32_16x16x64_bf16 v[24:27], a[224:227], v[40:47], v204\n\t"
    "s_memrealtime s[84:85]\n\t"
    "ds_read_b32 v90, v233 offset:13828\n\t"
    "v_smfmac_f32_16x16x64_bf16 v[24:27], a[244:247], v[48:55], v206 abid:1\n\t"
    "s_waitcnt lgkmcnt(4)\n\t"
    "ds_read_b32 v88, v232 offset:13828\n\t"
    "v_mfma_f32_16x16x32_bf16 v[24:27], v[80:83], v[92:95], v[24:27]\n\t"
    "ds_read_b128 v[80:83], v245 offset:7168\n\t"
    "v_smfmac_f32_16x16x64_bf16 v[16:19], a[208:211], v[56:63], v202\n\t"
    "s_add_u32 m0, s52, 4608\n\t"
    "s_nop 0\n\t"
    "buffer_load_dwordx4 v237, s[40:43], s68 offen offset:512 nt lds\n\t"
    "v_smfmac_f32_16x16x64_bf16 v[16:19], a[228:231], v[40:47], v204 abid:1\n\t"
    "ds_read_b128 v[60:63], v236 offset:14720\n\t"
    "v_smfmac_f32_16x16x64_bf16 v[16:19], a[248:251], v[48:55], v207\n\t"
    "s_sub_i32 s87, s87, s99\n\t"
    "ds_read_b32 v93, v72 offset:13828\n\t"
    "v_smfmac_f32_16x16x64_bf16 v[32:35], a[232:235], v[40:47], v205\n\t"
    "ds_read_b32 v95, v73 offset:13828\n\t"
    "v_smfmac_f32_16x16x64_bf16 v[32:35], a[252:255], v[48:55], v207 abid:1\n\t"
    "s_waitcnt lgkmcnt(8)\n\t"
    "s_sub_i32 s98, s98, s99\n\t"
    "v_smfmac_f32_16x16x64_bf16 v[32:35], v[124:127], v[64:71], v210\n\t"
    "s_add_i32 s86, s87, 1897\n\t"
    "ds_read_b128 v[56:59], v217 offset:14720\n\t"
    "v_smfmac_f32_16x16x64_bf16 v[36:39], a[236:239], v[40:47], v205 abid:1\n\t"
    "s_add_i32 s98, s98, 72\n\t"
    "ds_read_b32 v92, v218 offset:13828\n\t"
    "ds_read_b32 v94, v219 offset:13828\n\t"
    "v_smfmac_f32_16x16x64_bf16 v[36:39], v[108:111], v[48:55], v208\n\t"
    "s_max_i32 s86, s86, s98\n\t"
    "v_smfmac_f32_16x16x64_bf16 v[36:39], v[128:131], v[64:71], v210 abid:1\n\t"
    "s_waitcnt lgkmcnt(6)\n\t"
    "v_mfma_f32_16x16x32_bf16 v[36:39], v[80:83], v[88:91], v[36:39]\n\t"
    "s_sub_u32 s89, s84, s24\n\t"
    "ds_read_b128 v[80:83], v245 offset:0\n\t"
    "v_smfmac_f32_16x16x64_bf16 v[28:31], a[240:243], v[40:47], v206\n\t"
    "s_cmp_lt_i32 s89, s86\n\t"
    "s_cbranch_scc1 .Lpc9_%=\n\t"
    ".Lpc9r_%=:\n\t"
    "ds_read_b128 v[40:43], v217 offset:14848\n\t"
    "ds_read_b128 v[44:47], v236 offset:14848\n\t"
    "v_smfmac_f32_16x16x64_bf16 v[28:31], v[112:115], v[48:55], v208 abid:1\n\t"
    "v_smfmac_f32_16x16x64_bf16 v[28:31], v[132:135], v[64:71], v211\n\t"
    "s_max_i32 s98, s89, s86\n\t"
    "v_smfmac_f32_16x16x64_bf16 v[4:7], a[80:83], v[48:55], v186\n\t"
    "ds_read_b32 v89, v78 offset:13828\n\t"
    "v_smfmac_f32_16x16x64_bf16 v[4:7], a[96:99], v[64:71], v188\n\t"
    "s_waitcnt lgkmcnt(6)\n\t"
    "ds_read_b32 v91, v79 offset:13828\n\t"
    "v_smfmac_f32_16x16x64_bf16 v[4:7], a[112:115], v[56:63], v190\n\t"
    "s_waitcnt lgkmcnt(2)\n\t"
    "buffer_load_dwordx4 v238, s[40:43], s68 offen offset:1536 nt lds\n\t"
    "v_smfmac_f32_16x16x64_bf16 v[4:7], a[128:131], v[40:47], v192\n\t"
    "ds_read_b32 v88, v224 offset:13828\n\t"
    "v_mfma_f32_16x16x32_bf16 v[4:7], v[80:83], v[92:95], v[4:7]\n\t"
    "ds_read_b32 v94, v231 offset:13828\n\t"
    "v_smfmac_f32_16x16x64_bf16 v[12:15], a[84:87], v[48:55], v186 abid:1\n\t"
    "ds_read_b32 v90, v225 offset:13828\n\t"
    "v_smfmac_f32_16x16x64_bf16 v[12:15], a[100:103], v[64:71], v188 abid:1\n\t"
    "ds_read_b128 v[80:83], v245 offset:3072\n\t"
    "v_smfmac_f32_16x16x64_bf16 v[12:15], a[116:119], v[56:63], v190 abid:1\n\t"
    "v_cvt_pk_bf16_f32 v97, v6, v7\n\t"
    "v_smfmac_f32_16x16x64_bf16 v[12:15], a[132:135], v[40:47], v192 abid:1\n\t"
    "s_mov_b64 vcc, s[18:19]\n\t"
    "ds_read_b32 v95, v105 offset:13828\n\t"
    "v_smfmac_f32_16x16x64_bf16 v[20:23], a[88:91], v[48:55], v187\n\t"
    "ds_read_b32 v93, v104 offset:13828\n\t"
    "v_smfmac_f32_16x16x64_bf16 v[20:23], a[104:107], v[64:71], v189\n\t"
    "ds_read_b32 v92, v230 offset:13828\n\t"
    "v_cvt_pk_bf16_f32 v96, v4, v5\n\t"
    "v_smfmac_f32_16x16x64_bf16 v[20:23], a[120:123], v[56:63], v191\n\t"
    "v_cvt_pk_bf16_f32 v99, v14, v15\n\t"
    "v_smfmac_f32_16x16x64_bf16 v[20:23], a[136:139], v[40:47], v193\n\t"
    "ds_read_b128 v[48:51], v217 offset:18432\n\t"
    "v_smfmac_f32_16x16x64_bf16 v[8:11], a[108:111], v[64:71], v189 abid:1\n\t"
    "ds_read_b128 v[52:55], v236 offset:18432\n\t"
    "v_smfmac_f32_16x16x64_bf16 v[8:11], a[124:127], v[56:63], v191 abid:1\n\t"
    "ds_read_b128 v[4:7], v244 offset:0\n\t"
    "v_smfmac_f32_16x16x64_bf16 v[8:11], a[140:143], v[40:47], v193 abid:1\n\t"
    "s_waitcnt lgkmcnt(6)\n\t"
    "v_cvt_pk_bf16_f32 v101, v22, v23\n\t"
    "v_mfma_f32_16x16x32_bf16 v[8:11], v[80:83], v[88:91], v[8:11]\n\t"
    "ds_read_b128 v[80:83], v245 offset:6144\n\t"
    "v_cvt_pk_bf16_f32 v98, v12, v13\n\t"
    "v_smfmac_f32_16x16x64_bf16 v[24:27], v[116:119], v[64:71], v209\n\t"
    "ds_read_b32 v89, v76 offset:18436\n\t"
    "v_cvt_pk_bf16_f32 v100, v20, v21\n\t"
    "v_smfmac_f32_16x16x64_bf16 v[24:27], v[136:139], v[56:63], v211 abid:1\n\t"
    "ds_read_b32 v91, v77 offset:18436\n\t"
    "v_smfmac_f32_16x16x64_bf16 v[24:27], v[156:159], v[40:47], v214\n\t"
    "ds_read_b32 v90, v223 offset:18436\n\t"
    "v_smfmac_f32_16x16x64_bf16 v[16:19], v[120:123], v[64:71], v209 abid:1\n\t"
    "ds_read_b128 v[12:15], v244 offset:64\n\t"
    "v_cvt_pk_bf16_f32 v103, v10, v11\n\t"
    "v_smfmac_f32_16x16x64_bf16 v[16:19], v[140:143], v[56:63], v212\n\t"
    "ds_read_b128 v[64:67], v217 offset:18560\n\t"
    "v_cvt_pk_bf16_f32 v102, v8, v9\n\t"
    "v_smfmac_f32_16x16x64_bf16 v[16:19], v[160:163], v[40:47], v214 abid:1\n\t"
    "ds_read_b128 v[20:23], v244 offset:128\n\t"
    "v_cndmask_b32_dpp v8, v100, v96, vcc quad_perm:[0,0,2,2] row_mask:0xf bank_mask:0xf\n\t"
    "v_cndmask_b32_dpp v9, v101, v97, vcc quad_perm:[0,0,2,2] row_mask:0xf bank_mask:0xf\n\t"
    "v_smfmac_f32_16x16x64_bf16 v[32:35], v[144:147], v[56:63], v212 abid:1\n\t"
    "ds_read_b32 v88, v222 offset:18436\n\t"
    "v_cndmask_b32_dpp v10, v102, v98, vcc quad_perm:[0,0,2,2] row_mask:0xf bank_mask:0xf\n\t"
    "v_smfmac_f32_16x16x64_bf16 v[32:35], v[164:167], v[40:47], v215\n\t"
    "s_waitcnt lgkmcnt(7)\n\t"
    "s_add_u32 s65, s57, s67\n\t"
    "ds_read_b128 v[68:71], v236 offset:18560\n\t"
    "v_cndmask_b32_dpp v11, v103, v99, vcc quad_perm:[0,0,2,2] row_mask:0xf bank_mask:0xf\n\t"
    "v_mfma_f32_16x16x32_bf16 v[32:35], v[80:83], v[92:95], v[32:35]\n\t"
    "s_not_b64 vcc, s[18:19]\n\t"
    "v_cndmask_b32_dpp v101, v97, v101, vcc quad_perm:[1,1,3,3] row_mask:0xf bank_mask:0xf\n\t"
    "v_smfmac_f32_16x16x64_bf16 v[36:39], v[148:151], v[56:63], v213\n\t"
    "ds_read_b32 v95, v87 offset:18436\n\t"
    "v_cndmask_b32_dpp v100, v96, v100, vcc quad_perm:[1,1,3,3] row_mask:0xf bank_mask:0xf\n\t"
    "v_smfmac_f32_16x16x64_bf16 v[36:39], v[168:171], v[40:47], v215 abid:1\n\t"
    "ds_read_b32 v94, v229 offset:18436\n\t"
    "v_cvt_pk_bf16_f32 v97, v26, v27\n\t"
    "v_smfmac_f32_16x16x64_bf16 v[28:31], v[152:155], v[56:63], v213 abid:1\n\t"
    "buffer_store_dwordx4 v[8:11], v241, s[44:47], s67 offen offset:0 sc1\n\t"
    "ds_read_b128 v[80:83], v245 offset:2048\n\t"
    "v_cndmask_b32_dpp v103, v99, v103, vcc quad_perm:[1,1,3,3] row_mask:0xf bank_mask:0xf\n\t"
    "v_cvt_pk_bf16_f32 v96, v24, v25\n\t"
    "v_smfmac_f32_16x16x64_bf16 v[28:31], v[172:175], v[40:47], v216\n\t"
    "ds_read_b128 v[60:63], v236 offset:18688\n\t"
    "v_cndmask_b32_dpp v102, v98, v102, vcc quad_perm:[1,1,3,3] row_mask:0xf bank_mask:0xf\n\t"
    ".Lpad0_%=:\n\t"
    "s_cbranch_execz .Lpad1_%=\n\t"
    "v_smfmac_f32_16x16x64_bf16 v[4:7], a[0:3], v[48:55], v176\n\t"
    "s_waitcnt lgkmcnt(4)\n\t"
    "ds_read_b128 v[8:11], v244 offset:192\n\t"
    "v_smfmac_f32_16x16x64_bf16 v[4:7], a[16:19], v[64:71], v178\n\t"
    "ds_read_b128 v[56:59], v217 offset:18688\n\t"
    "v_cvt_pk_bf16_f32 v98, v16, v17\n\t"
    "v_smfmac_f32_16x16x64_bf16 v[12:15], a[4:7], v[48:55], v176 abid:1\n\t"
    "s_mov_b64 vcc, s[18:19]\n\t"
    "ds_read_b32 v92, v228 offset:18436\n\t"
    "v_smfmac_f32_16x16x64_bf16 v[12:15], a[20:23], v[64:71], v178 abid:1\n\t"
    "ds_read_b128 v[24:27], v244 offset:256\n\t"
    "v_cvt_pk_bf16_f32 v99, v18, v19\n\t"
    "v_smfmac_f32_16x16x64_bf16 v[20:23], a[8:11], v[48:55], v177\n\t"
    "buffer_store_dwordx4 v[100:103], v241, s[44:47], s67 offen offset:1152 sc1\n\t"
    "v_cvt_pk_bf16_f32 v28, v28, v29\n\t"
    "v_cvt_pk_bf16_f32 v29, v30, v31\n\t"
    "v_smfmac_f32_16x16x64_bf16 v[20:23], a[24:27], v[64:71], v179\n\t"
    "s_waitcnt lgkmcnt(5)\n\t"
    "ds_read_b128 v[16:19], v244 offset:320\n\t"
    "v_cvt_pk_bf16_f32 v101, v34, v35\n\t"
    "v_mfma_f32_16x16x32_bf16 v[20:23], v[80:83], v[88:91], v[20:23]\n\t"
    "s_waitcnt lgkmcnt(4)\n\t"
    "ds_read_b128 v[80:83], v245 offset:5120\n\t"
    "v_cvt_pk_bf16_f32 v100, v32, v33\n\t"
    "v_cvt_pk_bf16_f32 v102, v36, v37\n\t"
    "v_cvt_pk_bf16_f32 v103, v38, v39\n\t"
    "v_smfmac_f32_16x16x64_bf16 v[8:11], a[12:15], v[48:55], v177 abid:1\n\t"
    "ds_read_b32 v93, v86 offset:18436\n\t"
    "v_add_u32_sdwa v89, v248, v234 dst_sel:DWORD dst_unused:UNUSED_PAD src0_sel:DWORD src1_sel:WORD_1\n\t"
    "v_smfmac_f32_16x16x64_bf16 v[8:11], a[28:31], v[64:71], v179 abid:1\n\t"
    "s_waitcnt lgkmcnt(5)\n\t"
    "buffer_load_dwordx4 v239, s[40:43], s68 offen offset:2560 nt lds\n\t"
    "v_smfmac_f32_16x16x64_bf16 v[8:11], a[44:47], v[56:63], v181 abid:1\n\t"
    "s_waitcnt lgkmcnt(3)\n\t"
    "ds_read_b128 v[32:35], v244 offset:384\n\t"
    "v_cndmask_b32_dpp v38, v102, v98, vcc quad_perm:[0,0,2,2] row_mask:0xf bank_mask:0xf\n\t"
    "v_cndmask_b32_dpp v39, v103, v99, vcc quad_perm:[0,0,2,2] row_mask:0xf bank_mask:0xf\n\t"
    "v_smfmac_f32_16x16x64_bf16 v[24:27], a[144:147], v[48:55], v194\n\t"
    "ds_read_b32 v89, v89 offset:18436\n\t"
    "v_cndmask_b32_dpp v37, v101, v97, vcc quad_perm:[0,0,2,2] row_mask:0xf bank_mask:0xf\n\t"
    "v_smfmac_f32_16x16x64_bf16 v[24:27], a[164:167], v[64:71], v196 abid:1\n\t"
    "ds_read_b128 v[40:43], v217 offset:18816\n\t"
    "v_cndmask_b32_dpp v36, v100, v96, vcc quad_perm:[0,0,2,2] row_mask:0xf bank_mask:0xf\n\t"
    "v_smfmac_f32_16x16x64_bf16 v[24:27], a[184:187], v[56:63], v199\n\t"
    "s_waitcnt lgkmcnt(5)\n\t"
    "s_not_b64 vcc, s[18:19]\n\t"
    "buffer_store_dwordx4 v[36:39], v241, s[44:47], s67 offen offset:128 sc1\n\t"
    "v_cndmask_b32_dpp v102, v98, v102, vcc quad_perm:[1,1,3,3] row_mask:0xf bank_mask:0xf\n\t"
    "v_add_u32_sdwa v90, v248, v235 dst_sel:DWORD dst_unused:UNUSED_PAD src0_sel:DWORD src1_sel:WORD_0\n\t"
    "v_smfmac_f32_16x16x64_bf16 v[16:19], a[148:151], v[48:55], v194 abid:1\n\t"
    "ds_read_b128 v[44:47], v236 offset:18816\n\t"
    "v_cndmask_b32_dpp v100, v96, v100, vcc quad_perm:[1,1,3,3] row_mask:0xf bank_mask:0xf\n\t"
    "v_smfmac_f32_16x16x64_bf16 v[16:19], a[168:171], v[64:71], v197\n\t"
    "ds_read_b128 v[36:39], v244 offset:448\n\t"
    "v_cndmask_b32_dpp v101, v97, v101, vcc quad_perm:[1,1,3,3] row_mask:0xf bank_mask:0xf\n\t"
    "v_smfmac_f32_16x16x64_bf16 v[16:19], a[188:191], v[56:63], v199 abid:1\n\t"
    "s_waitcnt lgkmcnt(5)\n\t"
    "v_cndmask_b32_dpp v103, v99, v103, vcc quad_perm:[1,1,3,3] row_mask:0xf bank_mask:0xf\n\t"
    "v_mfma_f32_16x16x32_bf16 v[16:19], v[80:83], v[92:95], v[16:19]\n\t"
    "s_waitcnt lgkmcnt(4)\n\t"
    "ds_write_b64 v242, v[28:29] offset:512\n\t"
    "v_smfmac_f32_16x16x64_bf16 v[32:35], a[152:155], v[48:55], v195\n\t"
    "ds_read_b128 v[80:83], v245 offset:8192\n\t"
    "v_add_u32_sdwa v88, v248, v234 dst_sel:DWORD dst_unused:UNUSED_PAD src0_sel:DWORD src1_sel:WORD_0\n\t"
    "v_smfmac_f32_16x16x64_bf16 v[32:35], a[172:175], v[64:71], v197 abid:1\n\t"
    "buffer_store_dwordx4 v[100:103], v241, s[44:47], s67 offen offset:1280 sc1\n\t"
    "ds_read_b128 v[28:31], v244 offset:512\n\t"
    "v_add_u32_sdwa v91, v248, v235 dst_sel:DWORD dst_unused:UNUSED_PAD src0_sel:DWORD src1_sel:WORD_1\n\t"
    "v_smfmac_f32_16x16x64_bf16 v[32:35], a[192:195], v[56:63], v200\n\t"
    "s_waitcnt lgkmcnt(4)\n\t"
    "ds_read_b32 v88, v88 offset:18436\n\t"
    "v_smfmac_f32_16x16x64_bf16 v[32:35], a[212:215], v[40:47], v202 abid:1\n\t"
    "s_waitcnt lgkmcnt(4)\n\t"
    "ds_read_b32 v90, v90 offset:18436\n\t"
    "v_smfmac_f32_16x16x64_bf16 v[36:39], a[156:159], v[48:55], v195 abid:1\n\t"
    "ds_read_b32 v94, v221 offset:18436\n\t"
    "v_smfmac_f32_16x16x64_bf16 v[36:39], a[176:179], v[64:71], v198\n\t"
    "ds_read_b32 v91, v91 offset:18436\n\t"
    "v_smfmac_f32_16x16x64_bf16 v[36:39], a[196:199], v[56:63], v200 abid:1\n\t"
    "ds_read_b32 v93, v74 offset:18436\n\t"
    "v_smfmac_f32_16x16x64_bf16 v[36:39], a[216:219], v[40:47], v203\n\t"
    "s_waitcnt lgkmcnt(5)\n\t"
    "buffer_load_dwordx4 v239, s[40:43], s68 offen offset:3584 nt lds\n\t"
    "v_smfmac_f32_16x16x64_bf16 v[28:31], a[160:163], v[48:55], v196\n\t"
    "ds_read_b128 v[48:51], v217 offset:18944\n\t"
    "ds_read_b128 v[52:55], v236 offset:18944\n\t"
    "v_smfmac_f32_16x16x64_bf16 v[28:31], a[180:183], v[64:71], v198 abid:1\n\t"
    "ds_read_b32 v92, v220 offset:18436\n\t"
    "v_smfmac_f32_16x16x64_bf16 v[28:31], a[200:203], v[56:63], v201\n\t"
    "ds_read_b32 v95, v75 offset:18436\n\t"
    "v_smfmac_f32_16x16x64_bf16 v[28:31], a[220:223], v[40:47], v203 abid:1\n\t"
    "s_waitcnt lgkmcnt(5)\n\t"
    "ds_read_b128 v[68:71], v236 offset:19072\n\t"
    "v_mfma_f32_16x16x32_bf16 v[28:31], v[80:83], v[88:91], v[28:31]\n\t"
    "ds_read_b128 v[80:83], v245 offset:1024\n\t"
    "v_add_u32_sdwa v88, v248, v246 dst_sel:DWORD dst_unused:UNUSED_PAD src0_sel:DWORD src1_sel:WORD_0\n\t"
    "v_add_u32_sdwa v89, v248, v246 dst_sel:DWORD dst_unused:UNUSED_PAD src0_sel:DWORD src1_sel:WORD_1\n\t"
    "v_smfmac_f32_16x16x64_bf16 v[4:7], a[32:35], v[56:63], v180\n\t"
    "ds_read_u16 v89, v89 offset:18432\n\t"
    "v_add_u32_sdwa v91, v248, v247 dst_sel:DWORD dst_unused:UNUSED_PAD src0_sel:DWORD src1_sel:WORD_1\n\t"
    "v_smfmac_f32_16x16x64_bf16 v[4:7], a[48:51], v[40:47], v182\n\t"
    "s_waitcnt lgkmcnt(5)\n\t"
    "ds_read_u16 v88, v88 offset:18432\n\t"
    "v_add_u32_sdwa v90, v248, v247 dst_sel:DWORD dst_unused:UNUSED_PAD src0_sel:DWORD src1_sel:WORD_0\n\t"
    "v_smfmac_f32_16x16x64_bf16 v[4:7], a[64:67], v[48:55], v184\n\t"
    "ds_read_u16 v90, v90 offset:18432\n\t"
    "v_smfmac_f32_16x16x64_bf16 v[12:15], a[36:39], v[56:63], v180 abid:1\n\t"
    "s_waitcnt vmcnt(22)\n\t"
    "s_barrier\n\t"
    "ds_read_u16 v91, v91 offset:18432\n\t"
    "v_smfmac_f32_16x16x64_bf16 v[12:15], a[52:55], v[40:47], v182 abid:1\n\t"
    "ds_read_b128 v[96:99], v243 offset:2560\n\t"
    "ds_read_b128 v[64:67], v217 offset:19072\n\t"
    "v_smfmac_f32_16x16x64_bf16 v[12:15], a[68:71], v[48:55], v184 abid:1\n\t"
    "s_waitcnt lgkmcnt(6)\n\t"
    "s_bitcmp1_b32 s54, 14\n\t"
    "s_mov_b32 s68, 66060288\n\t"
    "s_cmov_b32 s68, 66134016\n\t"
    "v_mfma_f32_16x16x32_bf16 v[12:15], v[80:83], v[92:95], v[12:15]\n\t"
    "v_smfmac_f32_16x16x64_bf16 v[20:23], a[40:43], v[56:63], v181\n\t"
    "ds_read_b32 v92, v226 offset:18436\n\t"
    "ds_read_b32 v93, v84 offset:18436\n\t"
    "v_smfmac_f32_16x16x64_bf16 v[20:23], a[56:59], v[40:47], v183\n\t"
    "ds_read_b128 v[80:83], v245 offset:4096\n\t"
    "v_smfmac_f32_16x16x64_bf16 v[20:23], a[72:75], v[48:55], v185\n\t"
    "s_waitcnt lgkmcnt(5)\n\t"
    "s_set_gpr_idx_on s4, gpr_idx(SRC2,DST)\n\t"
    "v_mfma_f32_16x16x32_bf16 v[0:3], v[250:253], v[88:91], v[0:3]\n\t"
    "s_set_gpr_idx_off\n\t"
    "s_waitcnt lgkmcnt(4)\n\t"
    "s_mov_b32 exec_hi, 0\n\t"
    "buffer_store_dwordx4 v[96:99], v240, s[44:47], s65 offen offset:0 sc1\n\t"
    "s_mov_b32 exec_hi, -1\n\t"
    "v_smfmac_f32_16x16x64_bf16 v[8:11], a[60:63], v[40:47], v183 abid:1\n\t"
    "ds_read_b32 v94, v227 offset:18436\n\t"
    "ds_read_b32 v95, v85 offset:18436\n\t"
    "v_smfmac_f32_16x16x64_bf16 v[8:11], a[76:79], v[48:55], v185 abid:1\n\t"
    "s_waitcnt lgkmcnt(5)\n\t"
    "ds_read_b128 v[60:63], v236 offset:19200\n\t"
    "v_smfmac_f32_16x16x64_bf16 v[8:11], a[92:95], v[64:71], v187 abid:1\n\t"
    "s_set_gpr_idx_on s5, gpr_idx(SRC2,DST)\n\t"
    "v_mfma_f32_16x16x32_bf16 v[0:3], v[252:255], v[88:91], v[0:3]\n\t"
    "s_set_gpr_idx_off\n\t"
    "s_add_u32 m0, s52, 9216\n\t"
    "s_mov_b32 exec_hi, 0\n\t"
    "buffer_load_dwordx4 v237, s[40:43], s68 offen offset:0 nt lds\n\t"
    "s_mov_b32 exec_hi, -1\n\t"
    "s_cmp_lg_u32 s22, 0\n\t"
    "s_cbranch_scc1 .Lsl15_%=\n\t"
    ".Lslr15_%=:\n\t"
    "v_smfmac_f32_16x16x64_bf16 v[24:27], a[204:207], v[40:47], v201 abid:1\n\t"
    "ds_read_b32 v88, v232 offset:18436\n\t"
    "ds_read_b32 v89, v106 offset:18436\n\t"
    "v_smfmac_f32_16x16x64_bf16 v[24:27], a[224:227], v[48:55], v204\n\t"
    "s_memrealtime s[84:85]\n\t"
    "ds_read_b128 v[56:59], v217 offset:19200\n\t"
    "v_smfmac_f32_16x16x64_bf16 v[24:27], a[244:247], v[64:71], v206 abid:1\n\t"
    "s_waitcnt lgkmcnt(4)\n\t"
    "ds_read_b32 v91, v107 offset:18436\n\t"
    "v_mfma_f32_16x16x32_bf16 v[24:27], v[80:83], v[92:95], v[24:27]\n\t"
    "s_bitcmp1_b32 s54, 10\n\t"
    "s_mov_b32 s66, 47185920\n\t"
    "s_cmov_b32 s66, 47259648\n\t"
    "v_smfmac_f32_16x16x64_bf16 v[16:19], a[208:211], v[40:47], v202\n\t"
    "ds_read_b32 v93, v72 offset:18436\n\t"
    "v_smfmac_f32_16x16x64_bf16 v[16:19], a[228:231], v[48:55], v204 abid:1\n\t"
    "ds_read_b32 v90, v233 offset:18436\n\t"
    "v_smfmac_f32_16x16x64_bf16 v[16:19], a[248:251], v[64:71], v207\n\t"
    "s_sub_i32 s87, s87, s99\n\t"
    "ds_read_b128 v[80:83], v245 offset:7168\n\t"
    "v_smfmac_f32_16x16x64_bf16 v[32:35], a[232:235], v[48:55], v205\n\t"
    "ds_read_b128 v[40:43], v217 offset:19328\n\t"
    "v_smfmac_f32_16x16x64_bf16 v[32:35], a[252:255], v[64:71], v207 abid:1\n\t"
    "s_waitcnt lgkmcnt(5)\n\t"
    "s_sub_i32 s98, s98, s99\n\t"
    "v_smfmac_f32_16x16x64_bf16 v[32:35], v[124:127], v[56:63], v210\n\t"
    "s_add_i32 s86, s87, 2019\n\t"
    "ds_read_b32 v92, v218 offset:18436\n\t"
    "ds_read_b32 v94, v219 offset:18436\n\t"
    "v_smfmac_f32_16x16x64_bf16 v[36:39], a[236:239], v[48:55], v205 abid:1\n\t"
    "s_add_i32 s98, s98, 72\n\t"
    "ds_read_b32 v95, v73 offset:18436\n\t"
    "v_smfmac_f32_16x16x64_bf16 v[36:39], v[108:111], v[64:71], v208\n\t"
    "s_max_i32 s86, s86, s98\n\t"
    "ds_read_b128 v[44:47], v236 offset:19328\n\t"
    "v_smfmac_f32_16x16x64_bf16 v[36:39], v[128:131], v[56:63], v210 abid:1\n\t"
    "s_waitcnt lgkmcnt(5)\n\t"
    "v_mfma_f32_16x16x32_bf16 v[36:39], v[80:83], v[88:91], v[36:39]\n\t"
    "s_sub_u32 s89, s84, s24\n\t"
    "ds_read_b128 v[80:83], v245 offset:0\n\t"
    "v_smfmac_f32_16x16x64_bf16 v[28:31], a[240:243], v[48:55], v206\n\t"
    "s_cmp_lt_i32 s89, s86\n\t"
    "s_cbranch_scc1 .Lpc10_%=\n\t"
    ".Lpc10r_%=:\n\t"
    "ds_read_b128 v[52:55], v236 offset:19456\n\t"
    "v_smfmac_f32_16x16x64_bf16 v[28:31], v[112:115], v[64:71], v208 abid:1\n\t"
    "ds_read_b128 v[48:51], v217 offset:19456\n\t"
    "v_smfmac_f32_16x16x64_bf16 v[28:31], v[132:135], v[56:63], v211\n\t"
    "s_max_i32 s98, s89, s86\n\t"
    "ds_read_b32 v88, v224 offset:18436\n\t"
    "v_smfmac_f32_16x16x64_bf16 v[4:7], a[80:83], v[64:71], v186\n\t"
    "v_smfmac_f32_16x16x64_bf16 v[4:7], a[96:99], v[56:63], v188\n\t"
    "s_waitcnt lgkmcnt(4)\n\t"
    "s_mov_b64 vcc, s[18:19]\n\t"
    "ds_read_b32 v90, v225 offset:18436\n\t"
    "v_smfmac_f32_16x16x64_bf16 v[4:7], a[112:115], v[40:47], v190\n\t"
    "s_add_u32 m0, s52, 9216\n\t"
    "s_waitcnt lgkmcnt(2)\n\t"
    "buffer_load_dwordx4 v237, s[40:43], s68 offen offset:512 nt lds\n\t"
    "v_smfmac_f32_16x16x64_bf16 v[4:7], a[128:131], v[48:55], v192\n\t"
    "ds_read_b32 v91, v79 offset:18436\n\t"
    "v_mfma_f32_16x16x32_bf16 v[4:7], v[80:83], v[92:95], v[4:7]\n\t"
    "ds_read_b32 v89, v78 offset:18436\n\t"
    "v_smfmac_f32_16x16x64_bf16 v[12:15], a[84:87], v[64:71], v186 abid:1\n\t"
    "ds_read_b128 v[80:83], v245 offset:3072\n\t"
    "v_smfmac_f32_16x16x64_bf16 v[12:15], a[100:103], v[56:63], v188 abid:1\n\t"
    "v_smfmac_f32_16x16x64_bf16 v[12:15], a[116:119], v[40:47], v190 abid:1\n\t"
    "ds_read_b32 v92, v230 offset:18436\n\t"
    "v_smfmac_f32_16x16x64_bf16 v[12:15], a[132:135], v[48:55], v192 abid:1\n\t"
    "ds_read_b32 v95, v105 offset:18436\n\t"
    "v_cvt_pk_bf16_f32 v101, v6, v7\n\t"
    "v_smfmac_f32_16x16x64_bf16 v[20:23], a[88:91], v[64:71], v187\n\t"
    "ds_read_b128 v[68:71], v236 offset:23040\n\t"
    "v_cvt_pk_bf16_f32 v100, v4, v5\n\t"
    "v_smfmac_f32_16x16x64_bf16 v[20:23], a[104:107], v[56:63], v189\n\t"
    "v_smfmac_f32_16x16x64_bf16 v[20:23], a[120:123], v[40:47], v191\n\t"
    "ds_read_b32 v93, v104 offset:18436\n\t"
    "v_cvt_pk_bf16_f32 v102, v12, v13\n\t"
    "v_smfmac_f32_16x16x64_bf16 v[20:23], a[136:139], v[48:55], v193\n\t"
    "ds_read_b128 v[4:7], v244 offset:0\n\t"
    "v_smfmac_f32_16x16x64_bf16 v[8:11], a[108:111], v[56:63], v189 abid:1\n\t"
    "ds_read_b32 v94, v231 offset:18436\n\t"
    "v_cvt_pk_bf16_f32 v103, v14, v15\n\t"
    "v_smfmac_f32_16x16x64_bf16 v[8:11], a[124:127], v[40:47], v191 abid:1\n\t"
    "v_smfmac_f32_16x16x64_bf16 v[8:11], a[140:143], v[48:55], v193 abid:1\n\t"
    "s_waitcnt lgkmcnt(6)\n\t"
    "ds_read_b128 v[12:15], v244 offset:64\n\t"
    "v_mfma_f32_16x16x32_bf16 v[8:11], v[80:83], v[88:91], v[8:11]\n\t"
    "ds_read_b32 v90, v223 offset:23044\n\t"
    "v_cvt_pk_bf16_f32 v97, v22, v23\n\t"
    "v_smfmac_f32_16x16x64_bf16 v[24:27], v[116:119], v[56:63], v209\n\t"
    "ds_read_b128 v[80:83], v245 offset:6144\n\t"
    "v_cvt_pk_bf16_f32 v96, v20, v21\n\t"
    "v_smfmac_f32_16x16x64_bf16 v[24:27], v[136:139], v[40:47], v211 abid:1\n\t"
    "v_smfmac_f32_16x16x64_bf16 v[24:27], v[156:159], v[48:55], v214\n\t"
    "ds_read_b32 v89, v76 offset:23044\n\t"
    "v_cvt_pk_bf16_f32 v98, v8, v9\n\t"
    "v_smfmac_f32_16x16x64_bf16 v[16:19], v[120:123], v[56:63], v209 abid:1\n\t"
    "ds_read_b128 v[64:67], v217 offset:23040\n\t"
    "v_cvt_pk_bf16_f32 v99, v10, v11\n\t"
    "v_smfmac_f32_16x16x64_bf16 v[16:19], v[140:143], v[40:47], v212\n\t"
    "ds_read_b128 v[60:63], v236 offset:23168\n\t"
    "v_cndmask_b32_dpp v8, v96, v100, vcc quad_perm:[0,0,2,2] row_mask:0xf bank_mask:0xf\n\t"
    "v_smfmac_f32_16x16x64_bf16 v[16:19], v[160:163], v[48:55], v214 abid:1\n\t"
    "v_cndmask_b32_dpp v11, v99, v103, vcc quad_perm:[0,0,2,2] row_mask:0xf bank_mask:0xf\n\t"
    "v_smfmac_f32_16x16x64_bf16 v[32:35], v[144:147], v[40:47], v212 abid:1\n\t"
    "s_add_u32 s67, s57, s66\n\t"
    "ds_read_b128 v[56:59], v217 offset:23168\n\t"
    "v_cndmask_b32_dpp v10, v98, v102, vcc quad_perm:[0,0,2,2] row_mask:0xf bank_mask:0xf\n\t"
    "v_smfmac_f32_16x16x64_bf16 v[32:35], v[164:167], v[48:55], v215\n\t"
    "s_waitcnt lgkmcnt(4)\n\t"
    "ds_read_b128 v[20:23], v244 offset:128\n\t"
    "v_mfma_f32_16x16x32_bf16 v[32:35], v[80:83], v[92:95], v[32:35]\n\t"
    "ds_read_b32 v88, v222 offset:23044\n\t"
    "v_cndmask_b32_dpp v9, v97, v101, vcc quad_perm:[0,0,2,2] row_mask:0xf bank_mask:0xf\n\t"
    "v_smfmac_f32_16x16x64_bf16 v[36:39], v[148:151], v[40:47], v213\n\t"
    "ds_read_b32 v95, v87 offset:23044\n\t"
    "v_smfmac_f32_16x16x64_bf16 v[36:39], v[168:171], v[48:55], v215 abid:1\n\t"
    "s_not_b64 vcc, s[18:19]\n\t"
    "ds_read_b128 v[80:83], v245 offset:2048\n\t"
    "v_cndmask_b32_dpp v98, v102, v98, vcc quad_perm:[1,1,3,3] row_mask:0xf bank_mask:0xf\n\t"
    "v_smfmac_f32_16x16x64_bf16 v[28:31], v[152:155], v[40:47], v213 abid:1\n\t"
    "buffer_store_dwordx4 v[8:11], v241, s[44:47], s66 offen offset:0 sc1\n\t"
    "ds_read_b32 v91, v77 offset:23044\n\t"
    "v_cndmask_b32_dpp v96, v100, v96, vcc quad_perm:[1,1,3,3] row_mask:0xf bank_mask:0xf\n\t"
    "v_cndmask_b32_dpp v97, v101, v97, vcc quad_perm:[1,1,3,3] row_mask:0xf bank_mask:0xf\n\t"
    "v_cvt_pk_bf16_f32 v100, v24, v25\n\t"
    "v_smfmac_f32_16x16x64_bf16 v[28:31], v[172:175], v[48:55], v216\n\t"
    "s_waitcnt lgkmcnt(7)\n\t"
    "ds_read_b128 v[8:11], v244 offset:192\n\t"
    "v_cndmask_b32_dpp v99, v103, v99, vcc quad_perm:[1,1,3,3] row_mask:0xf bank_mask:0xf\n\t"
    "v_smfmac_f32_16x16x64_bf16 v[4:7], a[0:3], v[64:71], v176\n\t"
    "s_waitcnt lgkmcnt(6)\n\t"
    "ds_read_b128 v[40:43], v217 offset:23296\n\t"
    "v_cvt_pk_bf16_f32 v101, v26, v27\n\t"
    "v_smfmac_f32_16x16x64_bf16 v[4:7], a[16:19], v[56:63], v178\n\t"
    "ds_read_b128 v[24:27], v244 offset:256\n\t"
    "v_cvt_pk_bf16_f32 v102, v16, v17\n\t"
    "v_smfmac_f32_16x16x64_bf16 v[12:15], a[4:7], v[64:71], v176 abid:1\n\t"
    "ds_read_b128 v[44:47], v236 offset:23296\n\t"
    "v_smfmac_f32_16x16x64_bf16 v[12:15], a[20:23], v[56:63], v178 abid:1\n\t"
    "s_waitcnt lgkmcnt(8)\n\t"
    "v_cvt_pk_bf16_f32 v103, v18, v19\n\t"
    "v_smfmac_f32_16x16x64_bf16 v[20:23], a[8:11], v[64:71], v177\n\t"
    "buffer_store_dwordx4 v[96:99], v241, s[44:47], s66 offen offset:1152 sc1\n\t"
    "ds_read_b128 v[16:19], v244 offset:320\n\t"
    "ds_read_b32 v92, v228 offset:23044\n\t"
    "v_smfmac_f32_16x16x64_bf16 v[20:23], a[24:27], v[56:63], v179\n\t"
    "s_waitcnt lgkmcnt(6)\n\t"
    "ds_read_b32 v94, v229 offset:23044\n\t"
    "v_cvt_pk_bf16_f32 v97, v34, v35\n\t"
    "v_mfma_f32_16x16x32_bf16 v[20:23], v[80:83], v[88:91], v[20:23]\n\t"
    "s_waitcnt lgkmcnt(6)\n\t"
    "s_mov_b64 vcc, s[18:19]\n\t"
    "ds_read_b128 v[80:83], v245 offset:5120\n\t"
    "v_cvt_pk_bf16_f32 v96, v32, v33\n\t"
    "v_cvt_pk_bf16_f32 v99, v38, v39\n\t"
    "v_add_u32_sdwa v90, v248, v235 dst_sel:DWORD dst_unused:UNUSED_PAD src0_sel:DWORD src1_sel:WORD_0\n\t"
    "v_smfmac_f32_16x16x64_bf16 v[8:11], a[12:15], v[64:71], v177 abid:1\n\t"
    "ds_read_b128 v[48:51], v217 offset:23424\n\t"
    "v_cvt_pk_bf16_f32 v98, v36, v37\n\t"
    "v_smfmac_f32_16x16x64_bf16 v[8:11], a[28:31], v[56:63], v179 abid:1\n\t"
    "s_waitcnt lgkmcnt(5)\n\t"
    "buffer_load_dwordx4 v238, s[40:43], s68 offen offset:1536 nt lds\n\t"
    "v_smfmac_f32_16x16x64_bf16 v[8:11], a[44:47], v[40:47], v181 abid:1\n\t"
    "ds_read_b32 v93, v86 offset:23044\n\t"
    "v_cndmask_b32_dpp v37, v97, v101, vcc quad_perm:[0,0,2,2] row_mask:0xf bank_mask:0xf\n\t"
    "v_cndmask_b32_dpp v39, v99, v103, vcc quad_perm:[0,0,2,2] row_mask:0xf bank_mask:0xf\n\t"
    "v_smfmac_f32_16x16x64_bf16 v[24:27], a[144:147], v[64:71], v194\n\t"
    "ds_read_b128 v[32:35], v244 offset:384\n\t"
    "v_cndmask_b32_dpp v36, v96, v100, vcc quad_perm:[0,0,2,2] row_mask:0xf bank_mask:0xf\n\t"
    "v_smfmac_f32_16x16x64_bf16 v[24:27], a[164:167], v[56:63], v196 abid:1\n\t"
    "ds_read_b32 v90, v90 offset:23044\n\t"
    "v_cndmask_b32_dpp v38, v98, v102, vcc quad_perm:[0,0,2,2] row_mask:0xf bank_mask:0xf\n\t"
    "v_smfmac_f32_16x16x64_bf16 v[24:27], a[184:187], v[40:47], v199\n\t"
    "s_waitcnt lgkmcnt(7)\n\t"
    "s_not_b64 vcc, s[18:19]\n\t"
    "buffer_store_dwordx4 v[36:39], v241, s[44:47], s66 offen offset:128 sc1\n\t"
    "v_cndmask_b32_dpp v96, v100, v96, vcc quad_perm:[1,1,3,3] row_mask:0xf bank_mask:0xf\n\t"
    "v_cvt_pk_bf16_f32 v28, v28, v29\n\t"
    "v_add_u32_sdwa v88, v248, v234 dst_sel:DWORD dst_unused:UNUSED_PAD src0_sel:DWORD src1_sel:WORD_0\n\t"
    "v_add_u32_sdwa v89, v248, v234 dst_sel:DWORD dst_unused:UNUSED_PAD src0_sel:DWORD src1_sel:WORD_1\n\t"
    "v_smfmac_f32_16x16x64_bf16 v[16:19], a[148:151], v[64:71], v194 abid:1\n\t"
    "ds_read_b128 v[52:55], v236 offset:23424\n\t"
    "v_cvt_pk_bf16_f32 v29, v30, v31\n\t"
    "v_smfmac_f32_16x16x64_bf16 v[16:19], a[168:171], v[56:63], v197\n\t"
    "ds_read_b128 v[36:39], v244 offset:448\n\t"
    "v_cndmask_b32_dpp v98, v102, v98, vcc quad_perm:[1,1,3,3] row_mask:0xf bank_mask:0xf\n\t"
    "v_smfmac_f32_16x16x64_bf16 v[16:19], a[188:191], v[40:47], v199 abid:1\n\t"
    "s_waitcnt lgkmcnt(4)\n\t"
    "ds_write_b64 v242, v[28:29] offset:0\n\t"
    "v_mfma_f32_16x16x32_bf16 v[16:19], v[80:83], v[92:95], v[16:19]\n\t"
    "s_waitcnt lgkmcnt(4)\n\t"
    "ds_read_b32 v88, v88 offset:23044\n\t"
    "v_cndmask_b32_dpp v97, v101, v97, vcc quad_perm:[1,1,3,3] row_mask:0xf bank_mask:0xf\n\t"
    "v_smfmac_f32_16x16x64_bf16 v[32:35], a[152:155], v[64:71], v195\n\t"
    "ds_read_b128 v[28:31], v244 offset:512\n\t"
    "v_cndmask_b32_dpp v99, v103, v99, vcc quad_perm:[1,1,3,3] row_mask:0xf bank_mask:0xf\n\t"
    "v_smfmac_f32_16x16x64_bf16 v[32:35], a[172:175], v[56:63], v197 abid:1\n\t"
    "buffer_store_dwordx4 v[96:99], v241, s[44:47], s66 offen offset:1280 sc1\n\t"
    "ds_read_b128 v[80:83], v245 offset:8192\n\t"
    "v_smfmac_f32_16x16x64_bf16 v[32:35], a[192:195], v[40:47], v200\n\t"
    "s_waitcnt lgkmcnt(5)\n\t"
    "ds_read_b32 v89, v89 offset:23044\n\t"
    "v_smfmac_f32_16x16x64_bf16 v[32:35], a[212:215], v[48:55], v202 abid:1\n\t"
    "s_waitcnt lgkmcnt(5)\n\t"
    "ds_read_b32 v93, v74 offset:23044\n\t"
    "v_add_u32_sdwa v91, v248, v235 dst_sel:DWORD dst_unused:UNUSED_PAD src0_sel:DWORD src1_sel:WORD_1\n\t"
    "v_smfmac_f32_16x16x64_bf16 v[36:39], a[156:159], v[64:71], v195 abid:1\n\t"
    "ds_read_b32 v91, v91 offset:23044\n\t"
    "ds_read_b32 v92, v220 offset:23044\n\t"
    "v_smfmac_f32_16x16x64_bf16 v[36:39], a[176:179], v[56:63], v198\n\t"
    "buffer_load_dwordx4 v239, s[40:43], s68 offen offset:2560 nt lds\n\t"
    "v_smfmac_f32_16x16x64_bf16 v[36:39], a[196:199], v[40:47], v200 abid:1\n\t"
    "ds_read_b32 v94, v221 offset:23044\n\t"
    "v_smfmac_f32_16x16x64_bf16 v[36:39], a[216:219], v[48:55], v203\n\t"
    "s_waitcnt lgkmcnt(6)\n\t"
    "s_bitcmp1_b32 s54, 11\n\t"
    "s_mov_b32 s65, 51904512\n\t"
    "s_cmov_b32 s65, 51978240\n\t"
    "v_smfmac_f32_16x16x64_bf16 v[28:31], a[160:163], v[64:71], v196\n\t"
    "ds_read_b128 v[64:67], v217 offset:23552\n\t"
    "ds_read_b128 v[68:71], v236 offset:23552\n\t"
    "v_smfmac_f32_16x16x64_bf16 v[28:31], a[180:183], v[56:63], v198 abid:1\n\t"
    "ds_read_b128 v[56:59], v217 offset:23680\n\t"
    "v_smfmac_f32_16x16x64_bf16 v[28:31], a[200:203], v[40:47], v201\n\t"
    "v_smfmac_f32_16x16x64_bf16 v[28:31], a[220:223], v[48:55], v203 abid:1\n\t"
    "s_waitcnt lgkmcnt(5)\n\t"
    "ds_read_b32 v95, v75 offset:23044\n\t"
    "v_mfma_f32_16x16x32_bf16 v[28:31], v[80:83], v[88:91], v[28:31]\n\t"
    "ds_read_b128 v[80:83], v245 offset:1024\n\t"
    "v_add_u32_sdwa v88, v248, v246 dst_sel:DWORD dst_unused:UNUSED_PAD src0_sel:DWORD src1_sel:WORD_0\n\t"
    "v_smfmac_f32_16x16x64_bf16 v[4:7], a[32:35], v[40:47], v180\n\t"
    "ds_read_u16 v88, v88 offset:23040\n\t"
    "v_add_u32_sdwa v91, v248, v247 dst_sel:DWORD dst_unused:UNUSED_PAD src0_sel:DWORD src1_sel:WORD_1\n\t"
    "v_smfmac_f32_16x16x64_bf16 v[4:7], a[48:51], v[48:55], v182\n\t"
    "s_waitcnt lgkmcnt(4)\n\t"
    "ds_read_u16 v91, v91 offset:23040\n\t"
    "v_add_u32_sdwa v90, v248, v247 dst_sel:DWORD dst_unused:UNUSED_PAD src0_sel:DWORD src1_sel:WORD_0\n\t"
    "v_smfmac_f32_16x16x64_bf16 v[4:7], a[64:67], v[64:71], v184\n\t"
    "ds_read_b128 v[60:63], v236 offset:23680\n\t"
    "v_add_u32_sdwa v89, v248, v246 dst_sel:DWORD dst_unused:UNUSED_PAD src0_sel:DWORD src1_sel:WORD_1\n\t"
    "v_smfmac_f32_16x16x64_bf16 v[12:15], a[36:39], v[40:47], v180 abid:1\n\t"
    "s_waitcnt vmcnt(19)\n\t"
    "s_barrier\n\t"
    "ds_read_u16 v89, v89 offset:23040\n\t"
    "ds_read_u16 v90, v90 offset:23040\n\t"
    "v_smfmac_f32_16x16x64_bf16 v[12:15], a[52:55], v[48:55], v182 abid:1\n\t"
    "buffer_load_dwordx4 v239, s[40:43], s68 offen offset:3584 nt lds\n\t"
    "v_smfmac_f32_16x16x64_bf16 v[12:15], a[68:71], v[64:71], v184 abid:1\n\t"
    "s_waitcnt lgkmcnt(5)\n\t"
    "ds_read_b128 v[100:103], v243 offset:2048\n\t"
    "v_mfma_f32_16x16x32_bf16 v[12:15], v[80:83], v[92:95], v[12:15]\n\t"
    "ds_read_b32 v95, v85 offset:23044\n\t"
    "v_smfmac_f32_16x16x64_bf16 v[20:23], a[40:43], v[40:47], v181\n\t"
    "ds_read_b32 v94, v227 offset:23044\n\t"
    "v_smfmac_f32_16x16x64_bf16 v[20:23], a[56:59], v[48:55], v183\n\t"
    "ds_read_b32 v93, v84 offset:23044\n\t"
    "v_smfmac_f32_16x16x64_bf16 v[20:23], a[72:75], v[64:71], v185\n\t"
    "s_waitcnt lgkmcnt(4)\n\t"
    "ds_read_b32 v92, v226 offset:23044\n\t"
    "s_set_gpr_idx_on s4, gpr_idx(SRC2,DST)\n\t"
    "v_mfma_f32_16x16x32_bf16 v[0:3], v[250:253], v[88:91], v[0:3]\n\t"
    "s_set_gpr_idx_off\n\t"
    "s_waitcnt lgkmcnt(4)\n\t"
    "s_mov_b32 exec_hi, 0\n\t"
    "buffer_store_dwordx4 v[100:103], v240, s[44:47], s67 offen offset:0 sc1\n\t"
    "s_mov_b32 exec_hi, -1\n\t"
    "ds_read_b128 v[40:43], v217 offset:23808\n\t"
    "v_smfmac_f32_16x16x64_bf16 v[8:11], a[60:63], v[48:55], v183 abid:1\n\t"
    "ds_read_b128 v[80:83], v245 offset:4096\n\t"
    "v_smfmac_f32_16x16x64_bf16 v[8:11], a[76:79], v[64:71], v185 abid:1\n\t"
    "v_smfmac_f32_16x16x64_bf16 v[8:11], a[92:95], v[56:63], v187 abid:1\n\t"
    "ds_read_b128 v[44:47], v236 offset:23808\n\t"
    "s_set_gpr_idx_on s5, gpr_idx(SRC2,DST)\n\t"
    "v_mfma_f32_16x16x32_bf16 v[0:3], v[252:255], v[88:91], v[0:3]\n\t"
    "s_set_gpr_idx_off\n\t"
    "s_cmp_lg_u32 s22, 0\n\t"
    "s_cbranch_scc1 .Lsl16_%=\n\t"
    ".Lslr16_%=:\n\t"
    "v_smfmac_f32_16x16x64_bf16 v[24:27], a[204:207], v[48:55], v201 abid:1\n\t"
    "ds_read_b32 v89, v106 offset:23044\n\t"
    "ds_read_b32 v90, v233 offset:23044\n\t"
    "v_smfmac_f32_16x16x64_bf16 v[24:27], a[224:227], v[64:71], v204\n\t"
    "s_memrealtime s[84:85]\n\t"
    "ds_read_b32 v91, v107 offset:23044\n\t"
    "v_smfmac_f32_16x16x64_bf16 v[24:27], a[244:247], v[56:63], v206 abid:1\n\t"
    "s_waitcnt lgkmcnt(4)\n\t"
    "ds_read_b32 v88, v232 offset:23044\n\t"
    "v_mfma_f32_16x16x32_bf16 v[24:27], v[80:83], v[92:95], v[24:27]\n\t"
    "s_bitcmp1_b32 s54, 15\n\t"
    "s_mov_b32 s68, 70778880\n\t"
    "s_cmov_b32 s68, 70852608\n\t"
    "v_smfmac_f32_16x16x64_bf16 v[16:19], a[208:211], v[48:55], v202\n\t"
    "s_add_u32 m0, s52, 13824\n\t"
    "s_mov_b32 exec_hi, 0\n\t"
    "buffer_load_dwordx4 v237, s[40:43], s68 offen offset:0 nt lds\n\t"
    "s_mov_b32 exec_hi, -1\n\t"
    "v_smfmac_f32_16x16x64_bf16 v[16:19], a[228:231], v[64:71], v204 abid:1\n\t"
    "ds_read_b32 v92, v218 offset:23044\n\t"
    "ds_read_b32 v94, v219 offset:23044\n\t"
    "v_smfmac_f32_16x16x64_bf16 v[16:19], a[248:251], v[56:63], v207\n\t"
    "s_sub_i32 s87, s87, s99\n\t"
    "ds_read_b128 v[80:83], v245 offset:7168\n\t"
    "v_smfmac_f32_16x16x64_bf16 v[32:35], a[232:235], v[64:71], v205\n\t"
    "v_smfmac_f32_16x16x64_bf16 v[32:35], a[252:255], v[56:63], v207 abid:1\n\t"
    "s_waitcnt lgkmcnt(7)\n\t"
    "s_sub_i32 s98, s98, s99\n\t"
    "ds_read_b128 v[52:55], v236 offset:23936\n\t"
    "v_smfmac_f32_16x16x64_bf16 v[32:35], v[124:127], v[40:47], v210\n\t"
    "s_add_i32 s86, s87, 2140\n\t"
    "ds_read_b32 v95, v73 offset:23044\n\t"
    "v_smfmac_f32_16x16x64_bf16 v[36:39], a[236:239], v[64:71], v205 abid:1\n\t"
    "s_add_i32 s98, s98, 71\n\t"
    "ds_read_b128 v[48:51], v217 offset:23936\n\t"
    "v_smfmac_f32_16x16x64_bf16 v[36:39], v[108:111], v[56:63], v208\n\t"
    "s_max_i32 s86, s86, s98\n\t"
    "v_smfmac_f32_16x16x64_bf16 v[36:39], v[128:131], v[40:47], v210 abid:1\n\t"
    "s_waitcnt lgkmcnt(3)\n\t"
    "ds_read_b32 v93, v72 offset:23044\n\t"
    "v_mfma_f32_16x16x32_bf16 v[36:39], v[80:83], v[88:91], v[36:39]\n\t"
    "s_sub_u32 s89, s84, s24\n\t"
    "ds_read_b128 v[80:83], v245 offset:0\n\t"
    "v_smfmac_f32_16x16x64_bf16 v[28:31], a[240:243], v[64:71], v206\n\t"
    "s_cmp_lt_i32 s89, s86\n\t"
    "s_cbranch_scc1 .Lpc11_%=\n\t"
    ".Lpc11r_%=:\n\t"
    "ds_read_b128 v[64:67], v217 offset:24064\n\t"
    "ds_read_b128 v[68:71], v236 offset:24064\n\t"
    "v_smfmac_f32_16x16x64_bf16 v[28:31], v[112:115], v[56:63], v208 abid:1\n\t"
    "v_smfmac_f32_16x16x64_bf16 v[28:31], v[132:135], v[40:47], v211\n\t"
    "s_max_i32 s98, s89, s86\n\t"
    "v_smfmac_f32_16x16x64_bf16 v[4:7], a[80:83], v[56:63], v186\n\t"
    "ds_read_b32 v88, v224 offset:23044\n\t"
    "v_smfmac_f32_16x16x64_bf16 v[4:7], a[96:99], v[40:47], v188\n\t"
    "s_waitcnt lgkmcnt(5)\n\t"
    "ds_read_b32 v89, v78 offset:23044\n\t"
    "ds_read_b32 v90, v225 offset:23044\n\t"
    "v_smfmac_f32_16x16x64_bf16 v[4:7], a[112:115], v[48:55], v190\n\t"
    "s_waitcnt lgkmcnt(3)\n\t"
    "buffer_load_dwordx4 v237, s[40:43], s68 offen offset:512 nt lds\n\t"
    "v_smfmac_f32_16x16x64_bf16 v[4:7], a[128:131], v[64:71], v192\n\t"
    "s_mov_b64 vcc, s[18:19]\n\t"
    "v_mfma_f32_16x16x32_bf16 v[4:7], v[80:83], v[92:95], v[4:7]\n\t"
    "ds_read_b32 v92, v230 offset:23044\n\t"
    "v_smfmac_f32_16x16x64_bf16 v[12:15], a[84:87], v[56:63], v186 abid:1\n\t"
    "ds_read_b128 v[80:83], v245 offset:3072\n\t"
    "v_smfmac_f32_16x16x64_bf16 v[12:15], a[100:103], v[40:47], v188 abid:1\n\t"
    "ds_read_b32 v95, v105 offset:23044\n\t"
    "v_smfmac_f32_16x16x64_bf16 v[12:15], a[116:119], v[48:55], v190 abid:1\n\t"
    "ds_read_b32 v93, v104 offset:23044\n\t"
    "v_smfmac_f32_16x16x64_bf16 v[12:15], a[132:135], v[64:71], v192 abid:1\n\t"
    "v_cvt_pk_bf16_f32 v97, v6, v7\n\t"
    "v_smfmac_f32_16x16x64_bf16 v[20:23], a[88:91], v[56:63], v187\n\t"
    "ds_read_b32 v91, v79 offset:23044\n\t"
    "v_smfmac_f32_16x16x64_bf16 v[20:23], a[104:107], v[40:47], v189\n\t"
    "ds_read_b128 v[60:63], v236 offset:0\n\t"
    "v_cvt_pk_bf16_f32 v96, v4, v5\n\t"
    "v_smfmac_f32_16x16x64_bf16 v[20:23], a[120:123], v[48:55], v191\n\t"
    "ds_read_b128 v[4:7], v244 offset:0\n\t"
    "s_cmp_eq_u32 s82, 1008\n\t"
    "s_cbranch_scc1 .Lepq_%=\n\t"
    ".Lepqr_%=:\n\t"
    "v_smfmac_f32_16x16x64_bf16 v[20:23], a[136:139], v[64:71], v193\n\t"
    "v_cvt_pk_bf16_f32 v98, v12, v13\n\t"
    "v_smfmac_f32_16x16x64_bf16 v[8:11], a[108:111], v[40:47], v189 abid:1\n\t"
    "ds_read_b128 v[56:59], v217 offset:0\n\t"
    "v_smfmac_f32_16x16x64_bf16 v[8:11], a[124:127], v[48:55], v191 abid:1\n\t"
    "ds_read_b32 v94, v231 offset:23044\n\t"
    "v_cvt_pk_bf16_f32 v99, v14, v15\n\t"
    "v_smfmac_f32_16x16x64_bf16 v[8:11], a[140:143], v[64:71], v193 abid:1\n\t"
    "s_waitcnt lgkmcnt(4)\n\t"
    "ds_read_b128 v[12:15], v244 offset:64\n\t"
    "v_cvt_pk_bf16_f32 v100, v20, v21\n\t"
    "v_mfma_f32_16x16x32_bf16 v[8:11], v[80:83], v[88:91], v[8:11]\n\t"
    "ds_read_b32 v91, v77 offset:4\n\t"
    "v_smfmac_f32_16x16x64_bf16 v[24:27], v[116:119], v[40:47], v209\n\t"
    "ds_read_b32 v89, v76 offset:4\n\t"
    "v_cvt_pk_bf16_f32 v101, v22, v23\n\t"
    "v_smfmac_f32_16x16x64_bf16 v[24:27], v[136:139], v[48:55], v211 abid:1\n\t"
    "s_add_u32 s66, s57, s65\n\t"
    "ds_read_b128 v[80:83], v245 offset:6144\n\t"
    "v_smfmac_f32_16x16x64_bf16 v[24:27], v[156:159], v[64:71], v214\n\t"
    "v_cvt_pk_bf16_f32 v102, v8, v9\n\t"
    "v_smfmac_f32_16x16x64_bf16 v[16:19], v[120:123], v[40:47], v209 abid:1\n\t"
    "ds_read_b128 v[40:43], v217 offset:128\n\t"
    "v_cndmask_b32_dpp v9, v101, v97, vcc quad_perm:[0,0,2,2] row_mask:0xf bank_mask:0xf\n\t"
    "v_smfmac_f32_16x16x64_bf16 v[16:19], v[140:143], v[48:55], v212\n\t"
    "ds_read_b32 v90, v223 offset:4\n\t"
    "v_cvt_pk_bf16_f32 v103, v10, v11\n\t"
    "v_smfmac_f32_16x16x64_bf16 v[16:19], v[160:163], v[64:71], v214 abid:1\n\t"
    "ds_read_b32 v88, v222 offset:4\n\t"
    "v_cndmask_b32_dpp v10, v102, v98, vcc quad_perm:[0,0,2,2] row_mask:0xf bank_mask:0xf\n\t"
    "v_smfmac_f32_16x16x64_bf16 v[32:35], v[144:147], v[48:55], v212 abid:1\n\t"
    "ds_read_b128 v[44:47], v236 offset:128\n\t"
    "v_cndmask_b32_dpp v8, v100, v96, vcc quad_perm:[0,0,2,2] row_mask:0xf bank_mask:0xf\n\t"
    "v_smfmac_f32_16x16x64_bf16 v[32:35], v[164:167], v[64:71], v215\n\t"
    "s_waitcnt lgkmcnt(4)\n\t"
    "ds_read_b128 v[20:23], v244 offset:128\n\t"
    "v_mfma_f32_16x16x32_bf16 v[32:35], v[80:83], v[92:95], v[32:35]\n\t"
    "v_cndmask_b32_dpp v11, v103, v99, vcc quad_perm:[0,0,2,2] row_mask:0xf bank_mask:0xf\n\t"
    "v_smfmac_f32_16x16x64_bf16 v[36:39], v[148:151], v[48:55], v213\n\t"
    "s_not_b64 vcc, s[18:19]\n\t"
    "ds_read_b32 v94, v229 offset:4\n\t"
    "v_cndmask_b32_dpp v103, v99, v103, vcc quad_perm:[1,1,3,3] row_mask:0xf bank_mask:0xf\n\t"
    "v_smfmac_f32_16x16x64_bf16 v[36:39], v[168:171], v[64:71], v215 abid:1\n\t"
    "ds_read_b32 v92, v228 offset:4\n\t"
    "v_cndmask_b32_dpp v102, v98, v102, vcc quad_perm:[1,1,3,3] row_mask:0xf bank_mask:0xf\n\t"
    "v_smfmac_f32_16x16x64_bf16 v[28:31], v[152:155], v[48:55], v213 abid:1\n\t"
    "buffer_store_dwordx4 v[8:11], v241, s[44:47], s65 offen offset:0 sc1\n\t"
    "ds_read_b128 v[52:55], v236 offset:256\n\t"
    "v_cndmask_b32_dpp v100, v96, v100, vcc quad_perm:[1,1,3,3] row_mask:0xf bank_mask:0xf\n\t"
    "v_cvt_pk_bf16_f32 v96, v24, v25\n\t"
    "v_smfmac_f32_16x16x64_bf16 v[28:31], v[172:175], v[64:71], v216\n\t"
    "ds_read_b128 v[80:83], v245 offset:2048\n\t"
    "v_smfmac_f32_16x16x64_bf16 v[4:7], a[0:3], v[56:63], v176\n\t"
    "s_waitcnt lgkmcnt(5)\n\t"
    ".Lpad1_%=:\n\t"
    "s_cbranch_execz .Lpad2_%=\n\t"
    "ds_read_b128 v[8:11], v244 offset:192\n\t"
    "v_cndmask_b32_dpp v101, v97, v101, vcc quad_perm:[1,1,3,3] row_mask:0xf bank_mask:0xf\n\t"
    "v_smfmac_f32_16x16x64_bf16 v[4:7], a[16:19], v[40:47], v178\n\t"
    "ds_read_b128 v[48:51], v217 offset:256\n\t"
    "v_cvt_pk_bf16_f32 v97, v26, v27\n\t"
    "v_smfmac_f32_16x16x64_bf16 v[12:15], a[4:7], v[56:63], v176 abid:1\n\t"
    "s_mov_b64 vcc, s[18:19]\n\t"
    "ds_read_b32 v95, v87 offset:4\n\t"
    "v_cvt_pk_bf16_f32 v98, v16, v17\n\t"
    "v_smfmac_f32_16x16x64_bf16 v[12:15], a[20:23], v[40:47], v178 abid:1\n\t"
    "s_waitcnt lgkmcnt(7)\n\t"
    "ds_read_b128 v[24:27], v244 offset:256\n\t"
    "v_smfmac_f32_16x16x64_bf16 v[20:23], a[8:11], v[56:63], v177\n\t"
    "buffer_store_dwordx4 v[100:103], v241, s[44:47], s65 offen offset:1152 sc1\n\t"
    "v_cvt_pk_bf16_f32 v99, v18, v19\n\t"
    "v_cvt_pk_bf16_f32 v28, v28, v29\n\t"
    "v_cvt_pk_bf16_f32 v29, v30, v31\n\t"
    "v_smfmac_f32_16x16x64_bf16 v[20:23], a[24:27], v[40:47], v179\n\t"
    "s_waitcnt lgkmcnt(4)\n\t"
    "ds_read_b128 v[16:19], v244 offset:320\n\t"
    "v_cvt_pk_bf16_f32 v103, v38, v39\n\t"
    "v_mfma_f32_16x16x32_bf16 v[20:23], v[80:83], v[88:91], v[20:23]\n\t"
    "s_waitcnt lgkmcnt(4)\n\t"
    "ds_read_b128 v[80:83], v245 offset:5120\n\t"
    "v_cvt_pk_bf16_f32 v100, v32, v33\n\t"
    "v_cvt_pk_bf16_f32 v101, v34, v35\n\t"
    "v_smfmac_f32_16x16x64_bf16 v[8:11], a[12:15], v[56:63], v177 abid:1\n\t"
    "ds_read_b128 v[32:35], v244 offset:384\n\t"
    "v_cvt_pk_bf16_f32 v102, v36, v37\n\t"
    "v_smfmac_f32_16x16x64_bf16 v[8:11], a[28:31], v[40:47], v179 abid:1\n\t"
    "s_add_u32 m0, s52, 13824\n\t"
    "s_waitcnt lgkmcnt(5)\n\t"
    "buffer_load_dwordx4 v238, s[40:43], s68 offen offset:1536 nt lds\n\t"
    "v_smfmac_f32_16x16x64_bf16 v[8:11], a[44:47], v[48:55], v181 abid:1\n\t"
    "s_waitcnt lgkmcnt(3)\n\t"
    "ds_read_b32 v93, v86 offset:4\n\t"
    "v_cndmask_b32_dpp v37, v101, v97, vcc quad_perm:[0,0,2,2] row_mask:0xf bank_mask:0xf\n\t"
    "v_cndmask_b32_dpp v38, v102, v98, vcc quad_perm:[0,0,2,2] row_mask:0xf bank_mask:0xf\n\t"
    "v_smfmac_f32_16x16x64_bf16 v[24:27], a[144:147], v[56:63], v194\n\t"
    "ds_read_b128 v[68:71], v236 offset:384\n\t"
    "v_cndmask_b32_dpp v36, v100, v96, vcc quad_perm:[0,0,2,2] row_mask:0xf bank_mask:0xf\n\t"
    "v_smfmac_f32_16x16x64_bf16 v[24:27], a[164:167], v[40:47], v196 abid:1\n\t"
    "ds_read_b128 v[64:67], v217 offset:384\n\t"
    "v_cndmask_b32_dpp v39, v103, v99, vcc quad_perm:[0,0,2,2] row_mask:0xf bank_mask:0xf\n\t"
    "v_smfmac_f32_16x16x64_bf16 v[24:27], a[184:187], v[48:55], v199\n\t"
    "s_waitcnt lgkmcnt(5)\n\t"
    "s_not_b64 vcc, s[18:19]\n\t"
    "buffer_store_dwordx4 v[36:39], v241, s[44:47], s65 offen offset:128 sc1\n\t"
    "ds_write_b64 v242, v[28:29] offset:512\n\t"
    "v_cndmask_b32_dpp v102, v98, v102, vcc quad_perm:[1,1,3,3] row_mask:0xf bank_mask:0xf\n\t"
    "v_smfmac_f32_16x16x64_bf16 v[16:19], a[148:151], v[56:63], v194 abid:1\n\t"
    "v_add_u32_sdwa v88, v248, v234 dst_sel:DWORD dst_unused:UNUSED_PAD src0_sel:DWORD src1_sel:WORD_0\n\t"
    "v_smfmac_f32_16x16x64_bf16 v[16:19], a[168:171], v[40:47], v197\n\t"
    "ds_read_b128 v[36:39], v244 offset:448\n\t"
    "v_add_u32_sdwa v90, v248, v235 dst_sel:DWORD dst_unused:UNUSED_PAD src0_sel:DWORD src1_sel:WORD_0\n\t"
    "v_smfmac_f32_16x16x64_bf16 v[16:19], a[188:191], v[48:55], v199 abid:1\n\t"
    "s_waitcnt lgkmcnt(4)\n\t"
    "ds_read_b128 v[28:31], v244 offset:512\n\t"
    "v_cndmask_b32_dpp v101, v97, v101, vcc quad_perm:[1,1,3,3] row_mask:0xf bank_mask:0xf\n\t"
    "v_mfma_f32_16x16x32_bf16 v[16:19], v[80:83], v[92:95], v[16:19]\n\t"
    "ds_read_b32 v88, v88 offset:4\n\t"
    "v_cndmask_b32_dpp v100, v96, v100, vcc quad_perm:[1,1,3,3] row_mask:0xf bank_mask:0xf\n\t"
    "v_smfmac_f32_16x16x64_bf16 v[32:35], a[152:155], v[56:63], v195\n\t"
    "ds_read_b32 v93, v74 offset:4\n\t"
    "v_cndmask_b32_dpp v103, v99, v103, vcc quad_perm:[1,1,3,3] row_mask:0xf bank_mask:0xf\n\t"
    "v_smfmac_f32_16x16x64_bf16 v[32:35], a[172:175], v[40:47], v197 abid:1\n\t"
    "buffer_store_dwordx4 v[100:103], v241, s[44:47], s65 offen offset:1280 sc1\n\t"
    "ds_read_b128 v[80:83], v245 offset:8192\n\t"
    "v_add_u32_sdwa v89, v248, v234 dst_sel:DWORD dst_unused:UNUSED_PAD src0_sel:DWORD src1_sel:WORD_1\n\t"
    "v_smfmac_f32_16x16x64_bf16 v[32:35], a[192:195], v[48:55], v200\n\t"
    "s_waitcnt lgkmcnt(6)\n\t"
    "ds_read_b32 v90, v90 offset:4\n\t"
    "v_add_u32_sdwa v91, v248, v235 dst_sel:DWORD dst_unused:UNUSED_PAD src0_sel:DWORD src1_sel:WORD_1\n\t"
    "v_smfmac_f32_16x16x64_bf16 v[32:35], a[212:215], v[64:71], v202 abid:1\n\t"
    "s_waitcnt lgkmcnt(5)\n\t"
    "ds_read_b32 v95, v75 offset:4\n\t"
    "v_smfmac_f32_16x16x64_bf16 v[36:39], a[156:159], v[56:63], v195 abid:1\n\t"
    "ds_read_b32 v91, v91 offset:4\n\t"
    "v_smfmac_f32_16x16x64_bf16 v[36:39], a[176:179], v[40:47], v198\n\t"
    "ds_read_b32 v89, v89 offset:4\n\t"
    "v_smfmac_f32_16x16x64_bf16 v[36:39], a[196:199], v[48:55], v200 abid:1\n\t"
    "ds_read_b32 v94, v221 offset:4\n\t"
    "v_smfmac_f32_16x16x64_bf16 v[36:39], a[216:219], v[64:71], v203\n\t"
    "s_waitcnt lgkmcnt(8)\n\t"
    "buffer_load_dwordx4 v239, s[40:43], s68 offen offset:2560 nt lds\n\t"
    "v_smfmac_f32_16x16x64_bf16 v[28:31], a[160:163], v[56:63], v196\n\t"
    "ds_read_b128 v[56:59], v217 offset:512\n\t"
    "ds_read_b128 v[60:63], v236 offset:512\n\t"
    "v_smfmac_f32_16x16x64_bf16 v[28:31], a[180:183], v[40:47], v198 abid:1\n\t"
    "ds_read_b32 v92, v220 offset:4\n\t"
    "v_smfmac_f32_16x16x64_bf16 v[28:31], a[200:203], v[48:55], v201\n\t"
    "ds_read_b128 v[40:43], v217 offset:640\n\t"
    "v_smfmac_f32_16x16x64_bf16 v[28:31], a[220:223], v[64:71], v203 abid:1\n\t"
    "s_waitcnt lgkmcnt(5)\n\t"
    "v_mfma_f32_16x16x32_bf16 v[28:31], v[80:83], v[88:91], v[28:31]\n\t"
    "ds_read_b128 v[80:83], v245 offset:1024\n\t"
    "v_add_u32_sdwa v88, v248, v246 dst_sel:DWORD dst_unused:UNUSED_PAD src0_sel:DWORD src1_sel:WORD_0\n\t"
    "v_smfmac_f32_16x16x64_bf16 v[4:7], a[32:35], v[48:55], v180\n\t"
    "ds_read_u16 v88, v88 offset:0\n\t"
    "v_add_u32_sdwa v91, v248, v247 dst_sel:DWORD dst_unused:UNUSED_PAD src0_sel:DWORD src1_sel:WORD_1\n\t"
    "v_smfmac_f32_16x16x64_bf16 v[4:7], a[48:51], v[64:71], v182\n\t"
    "s_waitcnt lgkmcnt(4)\n\t"
    "ds_read_b128 v[44:47], v236 offset:640\n\t"
    "v_add_u32_sdwa v89, v248, v246 dst_sel:DWORD dst_unused:UNUSED_PAD src0_sel:DWORD src1_sel:WORD_1\n\t"
    "v_smfmac_f32_16x16x64_bf16 v[4:7], a[64:67], v[56:63], v184\n\t"
    "ds_read_u16 v91, v91 offset:0\n\t"
    "v_add_u32_sdwa v90, v248, v247 dst_sel:DWORD dst_unused:UNUSED_PAD src0_sel:DWORD src1_sel:WORD_0\n\t"
    "v_smfmac_f32_16x16x64_bf16 v[12:15], a[36:39], v[48:55], v180 abid:1\n\t"
    "s_waitcnt vmcnt(19)\n\t"
    "s_barrier\n\t"
    "ds_read_u16 v89, v89 offset:0\n\t"
    "ds_read_u16 v90, v90 offset:0\n\t"
    "v_smfmac_f32_16x16x64_bf16 v[12:15], a[52:55], v[64:71], v182 abid:1\n\t"
    "s_bitcmp1_b32 s54, 12\n\t"
    "s_mov_b32 s67, 56623104\n\t"
    "s_cmov_b32 s67, 56696832\n\t"
    "ds_read_b128 v[96:99], v243 offset:2560\n\t"
    "v_smfmac_f32_16x16x64_bf16 v[12:15], a[68:71], v[56:63], v184 abid:1\n\t"
    "s_waitcnt lgkmcnt(6)\n\t"
    "v_mfma_f32_16x16x32_bf16 v[12:15], v[80:83], v[92:95], v[12:15]\n\t"
    "ds_read_b32 v95, v85 offset:4\n\t"
    "v_smfmac_f32_16x16x64_bf16 v[20:23], a[40:43], v[48:55], v181\n\t"
    "ds_read_b32 v92, v226 offset:4\n\t"
    "v_smfmac_f32_16x16x64_bf16 v[20:23], a[56:59], v[64:71], v183\n\t"
    "ds_read_b32 v94, v227 offset:4\n\t"
    "v_smfmac_f32_16x16x64_bf16 v[20:23], a[72:75], v[56:63], v185\n\t"
    "s_waitcnt lgkmcnt(4)\n\t"
    "s_set_gpr_idx_on s4, gpr_idx(SRC2,DST)\n\t"
    "v_mfma_f32_16x16x32_bf16 v[0:3], v[250:253], v[88:91], v[0:3]\n\t"
    "s_set_gpr_idx_off\n\t"
    "s_waitcnt lgkmcnt(3)\n\t"
    "s_mov_b32 exec_hi, 0\n\t"
    "buffer_store_dwordx4 v[96:99], v240, s[44:47], s66 offen offset:0 sc1\n\t"
    "s_mov_b32 exec_hi, -1\n\t"
    "ds_read_b32 v93, v84 offset:4\n\t"
    "v_smfmac_f32_16x16x64_bf16 v[8:11], a[60:63], v[64:71], v183 abid:1\n\t"
    "ds_read_b128 v[80:83], v245 offset:4096\n\t"
    "v_smfmac_f32_16x16x64_bf16 v[8:11], a[76:79], v[56:63], v185 abid:1\n\t"
    "ds_read_b128 v[48:51], v217 offset:768\n\t"
    "v_smfmac_f32_16x16x64_bf16 v[8:11], a[92:95], v[40:47], v187 abid:1\n\t"
    "ds_read_b128 v[52:55], v236 offset:768\n\t"
    "s_set_gpr_idx_on s5, gpr_idx(SRC2,DST)\n\t"
    "v_mfma_f32_16x16x32_bf16 v[0:3], v[252:255], v[88:91], v[0:3]\n\t"
    "s_set_gpr_idx_off\n\t"
    "s_add_u32 m0, s52, 13824\n\t"
    "s_nop 0\n\t"
    "buffer_load_dwordx4 v239, s[40:43], s68 offen offset:3584 nt lds\n\t"
    "s_cmp_lg_u32 s22, 0\n\t"
    "s_cbranch_scc1 .Lsl17_%=\n\t"
    ".Lslr17_%=:\n\t"
    "v_smfmac_f32_16x16x64_bf16 v[24:27], a[204:207], v[64:71], v201 abid:1\n\t"
    "ds_read_b32 v91, v107 offset:4\n\t"
    "v_smfmac_f32_16x16x64_bf16 v[24:27], a[224:227], v[56:63], v204\n\t"
    "s_memrealtime s[84:85]\n\t"
    "ds_read_b32 v88, v232 offset:4\n\t"
    "v_smfmac_f32_16x16x64_bf16 v[24:27], a[244:247], v[40:47], v206 abid:1\n\t"
    "s_waitcnt lgkmcnt(4)\n\t"
    "ds_read_b32 v89, v106 offset:4\n\t"
    "v_mfma_f32_16x16x32_bf16 v[24:27], v[80:83], v[92:95], v[24:27]\n\t"
    "ds_read_b32 v94, v219 offset:4\n\t"
    "v_smfmac_f32_16x16x64_bf16 v[16:19], a[208:211], v[64:71], v202\n\t"
    "ds_read_b128 v[80:83], v245 offset:7168\n\t"
    "v_smfmac_f32_16x16x64_bf16 v[16:19], a[228:231], v[56:63], v204 abid:1\n\t"
    "ds_read_b32 v90, v233 offset:4\n\t"
    "v_smfmac_f32_16x16x64_bf16 v[16:19], a[248:251], v[40:47], v207\n\t"
    "s_sub_i32 s87, s87, s99\n\t"
    "ds_read_b32 v92, v218 offset:4\n\t"
    "v_smfmac_f32_16x16x64_bf16 v[32:35], a[232:235], v[56:63], v205\n\t"
    "ds_read_b128 v[68:71], v236 offset:896\n\t"
    "v_smfmac_f32_16x16x64_bf16 v[32:35], a[252:255], v[40:47], v207 abid:1\n\t"
    "s_waitcnt lgkmcnt(8)\n\t"
    "s_sub_i32 s98, s98, s99\n\t"
    "v_smfmac_f32_16x16x64_bf16 v[32:35], v[124:127], v[48:55], v210\n\t"
    "s_add_i32 s86, s87, 2258\n\t"
    "ds_read_b32 v93, v72 offset:4\n\t"
    "v_smfmac_f32_16x16x64_bf16 v[36:39], a[236:239], v[56:63], v205 abid:1\n\t"
    "s_add_i32 s98, s98, 68\n\t"
    "ds_read_b32 v95, v73 offset:4\n\t"
    "v_smfmac_f32_16x16x64_bf16 v[36:39], v[108:111], v[40:47], v208\n\t"
    "s_max_i32 s86, s86, s98\n\t"
    "ds_read_b128 v[64:67], v217 offset:896\n\t"
    "v_smfmac_f32_16x16x64_bf16 v[36:39], v[128:131], v[48:55], v210 abid:1\n\t"
    "s_waitcnt lgkmcnt(5)\n\t"
    "v_mfma_f32_16x16x32_bf16 v[36:39], v[80:83], v[88:91], v[36:39]\n\t"
    "s_sub_u32 s89, s84, s24\n\t"
    "ds_read_b128 v[80:83], v245 offset:0\n\t"
    "v_smfmac_f32_16x16x64_bf16 v[28:31], a[240:243], v[56:63], v206\n\t"
    "s_cmp_lt_i32 s89, s86\n\t"
    "s_cbranch_scc1 .Lpc12_%=\n\t"
    ".Lpc12r_%=:\n\t"
    "ds_read_b128 v[56:59], v217 offset:1024\n\t"
    "ds_read_b128 v[60:63], v236 offset:1024\n\t"
    "v_smfmac_f32_16x16x64_bf16 v[28:31], v[112:115], v[40:47], v208 abid:1\n\t"
    "s_mov_b64 vcc, s[18:19]\n\t"
    "v_smfmac_f32_16x16x64_bf16 v[28:31], v[132:135], v[48:55], v211\n\t"
    "s_max_i32 s98, s89, s86\n\t"
    "ds_read_b32 v91, v79 offset:4\n\t"
    "v_smfmac_f32_16x16x64_bf16 v[4:7], a[80:83], v[40:47], v186\n\t"
    "v_smfmac_f32_16x16x64_bf16 v[4:7], a[96:99], v[48:55], v188\n\t"
    "s_waitcnt lgkmcnt(4)\n\t"
    "ds_read_b32 v90, v225 offset:4\n\t"
    "v_smfmac_f32_16x16x64_bf16 v[4:7], a[112:115], v[64:71], v190\n\t"
    "s_waitcnt lgkmcnt(2)\n\t"
    "ds_read_b32 v89, v78 offset:4\n\t"
    "v_smfmac_f32_16x16x64_bf16 v[4:7], a[128:131], v[56:63], v192\n\t"
    "v_mfma_f32_16x16x32_bf16 v[4:7], v[80:83], v[92:95], v[4:7]\n\t"
    "ds_read_b32 v94, v231 offset:4\n\t"
    "v_smfmac_f32_16x16x64_bf16 v[12:15], a[84:87], v[40:47], v186 abid:1\n\t"
    "ds_read_b32 v95, v105 offset:4\n\t"
    "v_smfmac_f32_16x16x64_bf16 v[12:15], a[100:103], v[48:55], v188 abid:1\n\t"
    "ds_read_b128 v[80:83], v245 offset:3072\n\t"
    "v_smfmac_f32_16x16x64_bf16 v[12:15], a[116:119], v[64:71], v190 abid:1\n\t"
    "ds_read_b32 v88, v224 offset:4\n\t"
    "v_cvt_pk_bf16_f32 v100, v4, v5\n\t"
    "v_smfmac_f32_16x16x64_bf16 v[12:15], a[132:135], v[56:63], v192 abid:1\n\t"
    "v_smfmac_f32_16x16x64_bf16 v[20:23], a[88:91], v[40:47], v187\n\t"
    "ds_read_b128 v[40:43], v217 offset:4608\n\t"
    "v_cvt_pk_bf16_f32 v101, v6, v7\n\t"
    "v_smfmac_f32_16x16x64_bf16 v[20:23], a[104:107], v[48:55], v189\n\t"
    "ds_read_b32 v93, v104 offset:4\n\t"
    "v_smfmac_f32_16x16x64_bf16 v[20:23], a[120:123], v[64:71], v191\n\t"
    "ds_read_b128 v[44:47], v236 offset:4608\n\t"
    "v_cvt_pk_bf16_f32 v103, v14, v15\n\t"
    "v_smfmac_f32_16x16x64_bf16 v[20:23], a[136:139], v[56:63], v193\n\t"
    "v_smfmac_f32_16x16x64_bf16 v[8:11], a[108:111], v[48:55], v189 abid:1\n\t"
    "ds_read_b128 v[4:7], v244 offset:0\n\t"
    "v_smfmac_f32_16x16x64_bf16 v[8:11], a[124:127], v[64:71], v191 abid:1\n\t"
    "ds_read_b32 v92, v230 offset:4\n\t"
    "v_cvt_pk_bf16_f32 v102, v12, v13\n\t"
    "v_smfmac_f32_16x16x64_bf16 v[8:11], a[140:143], v[56:63], v193 abid:1\n\t"
    "s_waitcnt lgkmcnt(5)\n\t"
    "v_mfma_f32_16x16x32_bf16 v[8:11], v[80:83], v[88:91], v[8:11]\n\t"
    "ds_read_b128 v[80:83], v245 offset:6144\n\t"
    "v_smfmac_f32_16x16x64_bf16 v[24:27], v[116:119], v[48:55], v209\n\t"
    "ds_read_b32 v91, v77 offset:4612\n\t"
    "v_smfmac_f32_16x16x64_bf16 v[24:27], v[136:139], v[64:71], v211 abid:1\n\t"
    "ds_read_b32 v89, v76 offset:4612\n\t"
    "v_cvt_pk_bf16_f32 v96, v20, v21\n\t"
    "v_smfmac_f32_16x16x64_bf16 v[24:27], v[156:159], v[56:63], v214\n\t"
    "ds_read_b128 v[12:15], v244 offset:64\n\t"
    "v_cvt_pk_bf16_f32 v97, v22, v23\n\t"
    "v_smfmac_f32_16x16x64_bf16 v[16:19], v[120:123], v[48:55], v209 abid:1\n\t"
    "v_cvt_pk_bf16_f32 v98, v8, v9\n\t"
    "v_smfmac_f32_16x16x64_bf16 v[16:19], v[140:143], v[64:71], v212\n\t"
    "ds_read_b32 v90, v223 offset:4612\n\t"
    "v_smfmac_f32_16x16x64_bf16 v[16:19], v[160:163], v[56:63], v214 abid:1\n\t"
    "ds_read_b128 v[48:51], v217 offset:4736\n\t"
    "v_cvt_pk_bf16_f32 v99, v10, v11\n\t"
    "v_smfmac_f32_16x16x64_bf16 v[32:35], v[144:147], v[64:71], v212 abid:1\n\t"
    "ds_read_b32 v88, v222 offset:4612\n\t"
    "v_cndmask_b32_dpp v10, v98, v102, vcc quad_perm:[0,0,2,2] row_mask:0xf bank_mask:0xf\n\t"
    "v_smfmac_f32_16x16x64_bf16 v[32:35], v[164:167], v[56:63], v215\n\t"
    "s_waitcnt lgkmcnt(6)\n\t"
    "ds_read_b128 v[52:55], v236 offset:4736\n\t"
    "v_cndmask_b32_dpp v11, v99, v103, vcc quad_perm:[0,0,2,2] row_mask:0xf bank_mask:0xf\n\t"
    "v_mfma_f32_16x16x32_bf16 v[32:35], v[80:83], v[92:95], v[32:35]\n\t"
    "ds_read_b128 v[80:83], v245 offset:2048\n\t"
    "v_cndmask_b32_dpp v9, v97, v101, vcc quad_perm:[0,0,2,2] row_mask:0xf bank_mask:0xf\n\t"
    "v_smfmac_f32_16x16x64_bf16 v[36:39], v[148:151], v[64:71], v213\n\t"
    "v_cndmask_b32_dpp v8, v96, v100, vcc quad_perm:[0,0,2,2] row_mask:0xf bank_mask:0xf\n\t"
    "v_smfmac_f32_16x16x64_bf16 v[36:39], v[168:171], v[56:63], v215 abid:1\n\t"
    "s_not_b64 vcc, s[18:19]\n\t"
    "ds_read_b128 v[20:23], v244 offset:128\n\t"
    "v_smfmac_f32_16x16x64_bf16 v[28:31], v[152:155], v[64:71], v213 abid:1\n\t"
    "buffer_store_dwordx4 v[8:11], v241, s[44:47], s67 offen offset:0 sc1\n\t"
    "ds_read_b128 v[68:71], v236 offset:4864\n\t"
    "v_cndmask_b32_dpp v97, v101, v97, vcc quad_perm:[1,1,3,3] row_mask:0xf bank_mask:0xf\n\t"
    "v_cndmask_b32_dpp v99, v103, v99, vcc quad_perm:[1,1,3,3] row_mask:0xf bank_mask:0xf\n\t"
    "v_cvt_pk_bf16_f32 v101, v26, v27\n\t"
    "v_smfmac_f32_16x16x64_bf16 v[28:31], v[172:175], v[56:63], v216\n\t"
    "ds_read_b128 v[64:67], v217 offset:4864\n\t"
    "v_cndmask_b32_dpp v96, v100, v96, vcc quad_perm:[1,1,3,3] row_mask:0xf bank_mask:0xf\n\t"
    "v_smfmac_f32_16x16x64_bf16 v[4:7], a[0:3], v[40:47], v176\n\t"
    "s_waitcnt lgkmcnt(4)\n\t"
    "ds_read_b128 v[8:11], v244 offset:192\n\t"
    "v_cvt_pk_bf16_f32 v100, v24, v25\n\t"
    "v_smfmac_f32_16x16x64_bf16 v[4:7], a[16:19], v[48:55], v178\n\t"
    "ds_read_b128 v[24:27], v244 offset:256\n\t"
    "v_cvt_pk_bf16_f32 v103, v18, v19\n\t"
    "v_smfmac_f32_16x16x64_bf16 v[12:15], a[4:7], v[40:47], v176 abid:1\n\t"
    "ds_read_b32 v92, v228 offset:4612\n\t"
    "v_cndmask_b32_dpp v98, v102, v98, vcc quad_perm:[1,1,3,3] row_mask:0xf bank_mask:0xf\n\t"
    "v_smfmac_f32_16x16x64_bf16 v[12:15], a[20:23], v[48:55], v178 abid:1\n\t"
    "s_waitcnt lgkmcnt(5)\n\t"
    "ds_read_b128 v[60:63], v236 offset:4992\n\t"
    "v_smfmac_f32_16x16x64_bf16 v[20:23], a[8:11], v[40:47], v177\n\t"
    "buffer_store_dwordx4 v[96:99], v241, s[44:47], s67 offen offset:1152 sc1\n\t"
    "v_cvt_pk_bf16_f32 v102, v16, v17\n\t"
    "v_cvt_pk_bf16_f32 v28, v28, v29\n\t"
    "v_cvt_pk_bf16_f32 v29, v30, v31\n\t"
    "v_smfmac_f32_16x16x64_bf16 v[20:23], a[24:27], v[48:55], v179\n\t"
    "ds_read_b128 v[16:19], v244 offset:320\n\t"
    "v_cvt_pk_bf16_f32 v98, v36, v37\n\t"
    "v_mfma_f32_16x16x32_bf16 v[20:23], v[80:83], v[88:91], v[20:23]\n\t"
    "s_waitcnt lgkmcnt(4)\n\t"
    "ds_read_b32 v93, v86 offset:4612\n\t"
    "v_cvt_pk_bf16_f32 v96, v32, v33\n\t"
    "v_cvt_pk_bf16_f32 v97, v34, v35\n\t"
    "v_cvt_pk_bf16_f32 v99, v38, v39\n\t"
    "v_smfmac_f32_16x16x64_bf16 v[8:11], a[12:15], v[40:47], v177 abid:1\n\t"
    "s_mov_b64 vcc, s[18:19]\n\t"
    "ds_read_b32 v95, v87 offset:4612\n\t"
    "v_cndmask_b32_dpp v38, v98, v102, vcc quad_perm:[0,0,2,2] row_mask:0xf bank_mask:0xf\n\t"
    "v_smfmac_f32_16x16x64_bf16 v[8:11], a[28:31], v[48:55], v179 abid:1\n\t"
    "ds_read_b128 v[80:83], v245 offset:5120\n\t"
    "v_cndmask_b32_dpp v37, v97, v101, vcc quad_perm:[0,0,2,2] row_mask:0xf bank_mask:0xf\n\t"
    "v_smfmac_f32_16x16x64_bf16 v[8:11], a[44:47], v[64:71], v181 abid:1\n\t"
    "s_waitcnt lgkmcnt(6)\n\t"
    "ds_read_b32 v94, v229 offset:4612\n\t"
    "v_cndmask_b32_dpp v36, v96, v100, vcc quad_perm:[0,0,2,2] row_mask:0xf bank_mask:0xf\n\t"
    "v_cndmask_b32_dpp v39, v99, v103, vcc quad_perm:[0,0,2,2] row_mask:0xf bank_mask:0xf\n\t"
    "v_smfmac_f32_16x16x64_bf16 v[24:27], a[144:147], v[40:47], v194\n\t"
    "s_not_b64 vcc, s[18:19]\n\t"
    "buffer_store_dwordx4 v[36:39], v241, s[44:47], s67 offen offset:128 sc1\n\t"
    "ds_read_b128 v[32:35], v244 offset:384\n\t"
    "v_cndmask_b32_dpp v96, v100, v96, vcc quad_perm:[1,1,3,3] row_mask:0xf bank_mask:0xf\n\t"
    "v_cndmask_b32_dpp v98, v102, v98, vcc quad_perm:[1,1,3,3] row_mask:0xf bank_mask:0xf\n\t"
    "v_cndmask_b32_dpp v99, v103, v99, vcc quad_perm:[1,1,3,3] row_mask:0xf bank_mask:0xf\n\t"
    "v_smfmac_f32_16x16x64_bf16 v[24:27], a[164:167], v[48:55], v196 abid:1\n\t"
    "ds_read_b128 v[56:59], v217 offset:4992\n\t"
    "v_add_u32_sdwa v90, v248, v235 dst_sel:DWORD dst_unused:UNUSED_PAD src0_sel:DWORD src1_sel:WORD_0\n\t"
    "v_smfmac_f32_16x16x64_bf16 v[24:27], a[184:187], v[64:71], v199\n\t"
    "s_waitcnt lgkmcnt(6)\n\t"
    "ds_read_b128 v[36:39], v244 offset:448\n\t"
    "v_smfmac_f32_16x16x64_bf16 v[16:19], a[148:151], v[40:47], v194 abid:1\n\t"
    "ds_read_b32 v90, v90 offset:4612\n\t"
    "v_cndmask_b32_dpp v97, v101, v97, vcc quad_perm:[1,1,3,3] row_mask:0xf bank_mask:0xf\n\t"
    "v_smfmac_f32_16x16x64_bf16 v[16:19], a[168:171], v[48:55], v197\n\t"
    "v_add_u32_sdwa v89, v248, v234 dst_sel:DWORD dst_unused:UNUSED_PAD src0_sel:DWORD src1_sel:WORD_1\n\t"
    "v_smfmac_f32_16x16x64_bf16 v[16:19], a[188:191], v[64:71], v199 abid:1\n\t"
    "s_waitcnt lgkmcnt(4)\n\t"
    "s_mov_b64 vcc, s[18:19]\n\t"
    "v_add_u32_sdwa v91, v248, v235 dst_sel:DWORD dst_unused:UNUSED_PAD src0_sel:DWORD src1_sel:WORD_1\n\t"
    "v_mfma_f32_16x16x32_bf16 v[16:19], v[80:83], v[92:95], v[16:19]\n\t"
    "s_waitcnt lgkmcnt(3)\n\t"
    "buffer_store_dwordx4 v[96:99], v241, s[44:47], s67 offen offset:1280 sc1\n\t"
    "ds_read_b32 v91, v91 offset:4612\n\t"
    "ds_write_b64 v242, v[28:29] offset:0\n\t"
    "v_smfmac_f32_16x16x64_bf16 v[32:35], a[152:155], v[40:47], v195\n\t"
    "s_add_u32 s65, s57, s67\n\t"
    "ds_read_b128 v[28:31], v244 offset:512\n\t"
    "v_add_u32_sdwa v88, v248, v234 dst_sel:DWORD dst_unused:UNUSED_PAD src0_sel:DWORD src1_sel:WORD_0\n\t"
    "v_smfmac_f32_16x16x64_bf16 v[32:35], a[172:175], v[48:55], v197 abid:1\n\t"
    "v_smfmac_f32_16x16x64_bf16 v[32:35], a[192:195], v[64:71], v200\n\t"
    "s_waitcnt lgkmcnt(5)\n\t"
    "ds_read_b128 v[80:83], v245 offset:8192\n\t"
    "v_smfmac_f32_16x16x64_bf16 v[32:35], a[212:215], v[56:63], v202 abid:1\n\t"
    "s_waitcnt lgkmcnt(5)\n\t"
    "ds_read_b32 v94, v221 offset:4612\n\t"
    "v_smfmac_f32_16x16x64_bf16 v[36:39], a[156:159], v[40:47], v195 abid:1\n\t"
    "ds_read_b32 v88, v88 offset:4612\n\t"
    "ds_read_b32 v89, v89 offset:4612\n\t"
    "v_smfmac_f32_16x16x64_bf16 v[36:39], a[176:179], v[48:55], v198\n\t"
    "v_smfmac_f32_16x16x64_bf16 v[36:39], a[196:199], v[64:71], v200 abid:1\n\t"
    "ds_read_b32 v92, v220 offset:4612\n\t"
    "v_smfmac_f32_16x16x64_bf16 v[36:39], a[216:219], v[56:63], v203\n\t"
    "s_waitcnt lgkmcnt(5)\n\t"
    "ds_read_b32 v93, v74 offset:4612\n\t"
    "v_smfmac_f32_16x16x64_bf16 v[28:31], a[160:163], v[40:47], v196\n\t"
    "ds_read_b128 v[40:43], v217 offset:5120\n\t"
    "ds_read_b128 v[44:47], v236 offset:5120\n\t"
    "v_smfmac_f32_16x16x64_bf16 v[28:31], a[180:183], v[48:55], v198 abid:1\n\t"
    "v_smfmac_f32_16x16x64_bf16 v[28:31], a[200:203], v[64:71], v201\n\t"
    "ds_read_b32 v95, v75 offset:4612\n\t"
    "v_smfmac_f32_16x16x64_bf16 v[28:31], a[220:223], v[56:63], v203 abid:1\n\t"
    "s_waitcnt lgkmcnt(5)\n\t"
    "ds_read_b128 v[48:51], v217 offset:5248\n\t"
    "v_mfma_f32_16x16x32_bf16 v[28:31], v[80:83], v[88:91], v[28:31]\n\t"
    "ds_read_b128 v[80:83], v245 offset:1024\n\t"
    "v_add_u32_sdwa v89, v248, v246 dst_sel:DWORD dst_unused:UNUSED_PAD src0_sel:DWORD src1_sel:WORD_1\n\t"
    "v_smfmac_f32_16x16x64_bf16 v[4:7], a[32:35], v[64:71], v180\n\t"
    "ds_read_b128 v[52:55], v236 offset:5248\n\t"
    "v_add_u32_sdwa v90, v248, v247 dst_sel:DWORD dst_unused:UNUSED_PAD src0_sel:DWORD src1_sel:WORD_0\n\t"
    "v_smfmac_f32_16x16x64_bf16 v[4:7], a[48:51], v[56:63], v182\n\t"
    "s_waitcnt lgkmcnt(4)\n\t"
    "ds_read_u16 v89, v89 offset:4608\n\t"
    "v_add_u32_sdwa v88, v248, v246 dst_sel:DWORD dst_unused:UNUSED_PAD src0_sel:DWORD src1_sel:WORD_0\n\t"
    "v_smfmac_f32_16x16x64_bf16 v[4:7], a[64:67], v[40:47], v184\n\t"
    "ds_read_u16 v90, v90 offset:4608\n\t"
    "v_add_u32_sdwa v91, v248, v247 dst_sel:DWORD dst_unused:UNUSED_PAD src0_sel:DWORD src1_sel:WORD_1\n\t"
    "v_smfmac_f32_16x16x64_bf16 v[12:15], a[36:39], v[64:71], v180 abid:1\n\t"
    "s_waitcnt vmcnt(15)\n\t"
    "s_barrier\n\t"
    "ds_read_u16 v88, v88 offset:4608\n\t"
    "ds_read_u16 v91, v91 offset:4608\n\t"
    "v_smfmac_f32_16x16x64_bf16 v[12:15], a[52:55], v[56:63], v182 abid:1\n\t"
    "ds_read_b128 v[100:103], v243 offset:2048\n\t"
    "v_smfmac_f32_16x16x64_bf16 v[12:15], a[68:71], v[40:47], v184 abid:1\n\t"
    "s_waitcnt lgkmcnt(6)\n\t"
    "v_mfma_f32_16x16x32_bf16 v[12:15], v[80:83], v[92:95], v[12:15]\n\t"
    "ds_read_b32 v95, v85 offset:4612\n\t"
    "v_smfmac_f32_16x16x64_bf16 v[20:23], a[40:43], v[64:71], v181\n\t"
    "ds_read_b32 v94, v227 offset:4612\n\t"
    "v_smfmac_f32_16x16x64_bf16 v[20:23], a[56:59], v[56:63], v183\n\t"
    "v_smfmac_f32_16x16x64_bf16 v[20:23], a[72:75], v[40:47], v185\n\t"
    "s_waitcnt lgkmcnt(3)\n\t"
    "ds_read_b32 v93, v84 offset:4612\n\t"
    "s_set_gpr_idx_on s4, gpr_idx(SRC2,DST)\n\t"
    "v_mfma_f32_16x16x32_bf16 v[0:3], v[250:253], v[88:91], v[0:3]\n\t"
    "s_set_gpr_idx_off\n\t"
    "s_waitcnt lgkmcnt(3)\n\t"
    "s_mov_b32 exec_hi, 0\n\t"
    "buffer_store_dwordx4 v[100:103], v240, s[44:47], s65 offen offset:0 sc1\n\t"
    "s_mov_b32 exec_hi, -1\n\t"
    "ds_read_b128 v[80:83], v245 offset:4096\n\t"
    "v_smfmac_f32_16x16x64_bf16 v[8:11], a[60:63], v[56:63], v183 abid:1\n\t"
    "ds_read_b32 v92, v226 offset:4612\n\t"
    "v_smfmac_f32_16x16x64_bf16 v[8:11], a[76:79], v[40:47], v185 abid:1\n\t"
    "ds_read_b128 v[68:71], v236 offset:5376\n\t"
    "v_smfmac_f32_16x16x64_bf16 v[8:11], a[92:95], v[48:55], v187 abid:1\n\t"
    "ds_read_b128 v[64:67], v217 offset:5376\n\t"
    "s_set_gpr_idx_on s5, gpr_idx(SRC2,DST)\n\t"
    "v_mfma_f32_16x16x32_bf16 v[0:3], v[252:255], v[88:91], v[0:3]\n\t"
    "s_set_gpr_idx_off\n\t"
    "s_cmp_lg_u32 s22, 0\n\t"
    "s_cbranch_scc1 .Lsl18_%=\n\t"
    ".Lslr18_%=:\n\t"
    "v_smfmac_f32_16x16x64_bf16 v[24:27], a[204:207], v[56:63], v201 abid:1\n\t"
    "ds_read_b32 v90, v233 offset:4612\n\t"
    "v_smfmac_f32_16x16x64_bf16 v[24:27], a[224:227], v[40:47], v204\n\t"
    "s_memrealtime s[84:85]\n\t"
    "ds_read_b32 v88, v232 offset:4612\n\t"
    "v_smfmac_f32_16x16x64_bf16 v[24:27], a[244:247], v[48:55], v206 abid:1\n\t"
    "s_waitcnt lgkmcnt(4)\n\t"
    "ds_read_b32 v91, v107 offset:4612\n\t"
    "v_mfma_f32_16x16x32_bf16 v[24:27], v[80:83], v[92:95], v[24:27]\n\t"
    "v_smfmac_f32_16x16x64_bf16 v[16:19], a[208:211], v[56:63], v202\n\t"
    "ds_read_b32 v89, v106 offset:4612\n\t"
    "ds_read_b32 v92, v218 offset:4612\n\t"
    "v_smfmac_f32_16x16x64_bf16 v[16:19], a[228:231], v[40:47], v204 abid:1\n\t"
    "ds_read_b128 v[60:63], v236 offset:5504\n\t"
    "v_smfmac_f32_16x16x64_bf16 v[16:19], a[248:251], v[48:55], v207\n\t"
    "s_sub_i32 s87, s87, s99\n\t"
    "ds_read_b128 v[80:83], v245 offset:7168\n\t"
    "v_smfmac_f32_16x16x64_bf16 v[32:35], a[232:235], v[40:47], v205\n\t"
    "v_smfmac_f32_16x16x64_bf16 v[32:35], a[252:255], v[48:55], v207 abid:1\n\t"
    "s_waitcnt lgkmcnt(7)\n\t"
    "s_sub_i32 s98, s98, s99\n\t"
    "ds_read_b32 v95, v73 offset:4612\n\t"
    "v_smfmac_f32_16x16x64_bf16 v[32:35], v[124:127], v[64:71], v210\n\t"
    "s_add_i32 s86, s87, 2368\n\t"
    "ds_read_b32 v93, v72 offset:4612\n\t"
    "v_smfmac_f32_16x16x64_bf16 v[36:39], a[236:239], v[40:47], v205 abid:1\n\t"
    "s_add_i32 s98, s98, 60\n\t"
    "ds_read_b32 v94, v219 offset:4612\n\t"
    "v_smfmac_f32_16x16x64_bf16 v[36:39], v[108:111], v[48:55], v208\n\t"
    "s_max_i32 s86, s86, s98\n\t"
    "v_smfmac_f32_16x16x64_bf16 v[36:39], v[128:131], v[64:71], v210 abid:1\n\t"
    "s_waitcnt lgkmcnt(3)\n\t"
    "ds_read_b128 v[56:59], v217 offset:5504\n\t"
    "v_mfma_f32_16x16x32_bf16 v[36:39], v[80:83], v[88:91], v[36:39]\n\t"
    "s_sub_u32 s89, s84, s24\n\t"
    "ds_read_b128 v[80:83], v245 offset:0\n\t"
    "v_smfmac_f32_16x16x64_bf16 v[28:31], a[240:243], v[40:47], v206\n\t"
    "s_cmp_lt_i32 s89, s86\n\t"
    "s_cbranch_scc1 .Lpc13_%=\n\t"
    ".Lpc13r_%=:\n\t"
    "ds_read_b128 v[40:43], v217 offset:5632\n\t"
    "ds_read_b128 v[44:47], v236 offset:5632\n\t"
    "v_smfmac_f32_16x16x64_bf16 v[28:31], v[112:115], v[48:55], v208 abid:1\n\t"
    "v_smfmac_f32_16x16x64_bf16 v[28:31], v[132:135], v[64:71], v211\n\t"
    "s_max_i32 s98, s89, s86\n\t"
    "v_smfmac_f32_16x16x64_bf16 v[4:7], a[80:83], v[48:55], v186\n\t"
    "ds_read_b32 v88, v224 offset:4612\n\t"
    "ds_read_b32 v91, v79 offset:4612\n\t"
    "v_smfmac_f32_16x16x64_bf16 v[4:7], a[96:99], v[64:71], v188\n\t"
    "s_waitcnt lgkmcnt(5)\n\t"
    "ds_read_b32 v90, v225 offset:4612\n\t"
    "v_smfmac_f32_16x16x64_bf16 v[4:7], a[112:115], v[56:63], v190\n\t"
    "s_waitcnt lgkmcnt(3)\n\t"
    "v_smfmac_f32_16x16x64_bf16 v[4:7], a[128:131], v[40:47], v192\n\t"
    "v_mfma_f32_16x16x32_bf16 v[4:7], v[80:83], v[92:95], v[4:7]\n\t"
    "ds_read_b32 v95, v105 offset:4612\n\t"
    "v_smfmac_f32_16x16x64_bf16 v[12:15], a[84:87], v[48:55], v186 abid:1\n\t"
    "ds_read_b32 v93, v104 offset:4612\n\t"
    "v_smfmac_f32_16x16x64_bf16 v[12:15], a[100:103], v[64:71], v188 abid:1\n\t"
    "ds_read_b128 v[80:83], v245 offset:3072\n\t"
    "v_smfmac_f32_16x16x64_bf16 v[12:15], a[116:119], v[56:63], v190 abid:1\n\t"
    "v_cvt_pk_bf16_f32 v97, v6, v7\n\t"
    "v_smfmac_f32_16x16x64_bf16 v[12:15], a[132:135], v[40:47], v192 abid:1\n\t"
    "v_smfmac_f32_16x16x64_bf16 v[20:23], a[88:91], v[48:55], v187\n\t"
    "ds_read_b32 v89, v78 offset:4612\n\t"
    "v_smfmac_f32_16x16x64_bf16 v[20:23], a[104:107], v[64:71], v189\n\t"
    "ds_read_b128 v[52:55], v236 offset:9216\n\t"
    "v_smfmac_f32_16x16x64_bf16 v[20:23], a[120:123], v[56:63], v191\n\t"
    "ds_read_b32 v92, v230 offset:4612\n\t"
    "v_smfmac_f32_16x16x64_bf16 v[20:23], a[136:139], v[40:47], v193\n\t"
    "s_bitcmp1_b32 s54, 13\n\t"
    "s_mov_b32 s66, 61341696\n\t"
    "s_cmov_b32 s66, 61415424\n\t"
    "s_add_u32 s67, s57, s66\n\t"
    "v_smfmac_f32_16x16x64_bf16 v[8:11], a[108:111], v[64:71], v189 abid:1\n\t"
    "ds_read_b128 v[48:51], v217 offset:9216\n\t"
    "v_cvt_pk_bf16_f32 v96, v4, v5\n\t"
    "s_cmp_eq_u32 s82, 1008\n\t"
    "s_cbranch_scc1 .Lepoch_%=\n\t"
    ".Lepochr_%=:\n\t"
    "v_smfmac_f32_16x16x64_bf16 v[8:11], a[124:127], v[56:63], v191 abid:1\n\t"
    "ds_read_b128 v[4:7], v244 offset:0\n\t"
    "v_cvt_pk_bf16_f32 v98, v12, v13\n\t"
    "v_smfmac_f32_16x16x64_bf16 v[8:11], a[140:143], v[40:47], v193 abid:1\n\t"
    "s_waitcnt lgkmcnt(4)\n\t"
    "ds_read_b32 v94, v231 offset:4612\n\t"
    "v_cvt_pk_bf16_f32 v99, v14, v15\n\t"
    "v_mfma_f32_16x16x32_bf16 v[8:11], v[80:83], v[88:91], v[8:11]\n\t"
    "ds_read_b128 v[80:83], v245 offset:6144\n\t"
    "v_cvt_pk_bf16_f32 v101, v22, v23\n\t"
    "v_smfmac_f32_16x16x64_bf16 v[24:27], v[116:119], v[64:71], v209\n\t"
    "v_smfmac_f32_16x16x64_bf16 v[24:27], v[136:139], v[56:63], v211 abid:1\n\t"
    "ds_read_b32 v90, v223 offset:9220\n\t"
    "v_cvt_pk_bf16_f32 v100, v20, v21\n\t"
    "v_smfmac_f32_16x16x64_bf16 v[24:27], v[156:159], v[40:47], v214\n\t"
    "ds_read_b32 v89, v76 offset:9220\n\t"
    "v_cvt_pk_bf16_f32 v102, v8, v9\n\t"
    "v_smfmac_f32_16x16x64_bf16 v[16:19], v[120:123], v[64:71], v209 abid:1\n\t"
    "ds_read_b128 v[64:67], v217 offset:9344\n\t"
    "v_cvt_pk_bf16_f32 v103, v10, v11\n\t"
    "v_smfmac_f32_16x16x64_bf16 v[16:19], v[140:143], v[56:63], v212\n\t"
    "ds_read_b128 v[12:15], v244 offset:64\n\t"
    "v_cndmask_b32_dpp v8, v100, v96, vcc quad_perm:[0,0,2,2] row_mask:0xf bank_mask:0xf\n\t"
    "v_smfmac_f32_16x16x64_bf16 v[16:19], v[160:163], v[40:47], v214 abid:1\n\t"
    "ds_read_b128 v[68:71], v236 offset:9344\n\t"
    "v_cndmask_b32_dpp v11, v103, v99, vcc quad_perm:[0,0,2,2] row_mask:0xf bank_mask:0xf\n\t"
    "v_smfmac_f32_16x16x64_bf16 v[32:35], v[144:147], v[56:63], v212 abid:1\n\t"
    "ds_read_b32 v88, v222 offset:9220\n\t"
    "v_smfmac_f32_16x16x64_bf16 v[32:35], v[164:167], v[40:47], v215\n\t"
    "s_waitcnt lgkmcnt(6)\n\t"
    "ds_read_b128 v[20:23], v244 offset:128\n\t"
    "v_cndmask_b32_dpp v9, v101, v97, vcc quad_perm:[0,0,2,2] row_mask:0xf bank_mask:0xf\n\t"
    "v_mfma_f32_16x16x32_bf16 v[32:35], v[80:83], v[92:95], v[32:35]\n\t"
    "v_cndmask_b32_dpp v10, v102, v98, vcc quad_perm:[0,0,2,2] row_mask:0xf bank_mask:0xf\n\t"
    "v_smfmac_f32_16x16x64_bf16 v[36:39], v[148:151], v[56:63], v213\n\t"
    "s_not_b64 vcc, s[18:19]\n\t"
    "ds_read_b128 v[80:83], v245 offset:2048\n\t"
    "v_cndmask_b32_dpp v101, v97, v101, vcc quad_perm:[1,1,3,3] row_mask:0xf bank_mask:0xf\n\t"
    "v_smfmac_f32_16x16x64_bf16 v[36:39], v[168:171], v[40:47], v215 abid:1\n\t"
    "ds_read_b32 v95, v87 offset:9220\n\t"
    "v_cndmask_b32_dpp v103, v99, v103, vcc quad_perm:[1,1,3,3] row_mask:0xf bank_mask:0xf\n\t"
    "v_smfmac_f32_16x16x64_bf16 v[28:31], v[152:155], v[56:63], v213 abid:1\n\t"
    "buffer_store_dwordx4 v[8:11], v241, s[44:47], s66 offen offset:0 sc1\n\t"
    "ds_read_b32 v91, v77 offset:9220\n\t"
    "v_cndmask_b32_dpp v100, v96, v100, vcc quad_perm:[1,1,3,3] row_mask:0xf bank_mask:0xf\n\t"
    "v_cvt_pk_bf16_f32 v97, v26, v27\n\t"
    "v_cvt_pk_bf16_f32 v99, v18, v19\n\t"
    "v_smfmac_f32_16x16x64_bf16 v[28:31], v[172:175], v[40:47], v216\n\t"
    "ds_read_b128 v[8:11], v244 offset:192\n\t"
    "v_smfmac_f32_16x16x64_bf16 v[4:7], a[0:3], v[48:55], v176\n\t"
    "s_waitcnt lgkmcnt(6)\n\t"
    "ds_read_b128 v[44:47], v236 offset:9600\n\t"
    "v_cndmask_b32_dpp v102, v98, v102, vcc quad_perm:[1,1,3,3] row_mask:0xf bank_mask:0xf\n\t"
    "v_smfmac_f32_16x16x64_bf16 v[4:7], a[16:19], v[64:71], v178\n\t"
    "ds_read_b128 v[56:59], v217 offset:9472\n\t"
    "v_cvt_pk_bf16_f32 v98, v16, v17\n\t"
    "v_smfmac_f32_16x16x64_bf16 v[12:15], a[4:7], v[48:55], v176 abid:1\n\t"
    "ds_read_b128 v[60:63], v236 offset:9472\n\t"
    "v_cvt_pk_bf16_f32 v96, v24, v25\n\t"
    "v_smfmac_f32_16x16x64_bf16 v[12:15], a[20:23], v[64:71], v178 abid:1\n\t"
    "s_waitcnt lgkmcnt(7)\n\t"
    "ds_read_b128 v[24:27], v244 offset:256\n\t"
    "v_smfmac_f32_16x16x64_bf16 v[20:23], a[8:11], v[48:55], v177\n\t"
    "s_mov_b64 vcc, s[18:19]\n\t"
    "buffer_store_dwordx4 v[100:103], v241, s[44:47], s66 offen offset:1152 sc1\n\t"
    "ds_read_b32 v94, v229 offset:9220\n\t"
    "v_smfmac_f32_16x16x64_bf16 v[20:23], a[24:27], v[64:71], v179\n\t"
    "s_waitcnt lgkmcnt(6)\n\t"
    ".Lpad2_%=:\n\t"
    "s_cbranch_execz .Lpad3_%=\n\t"
    "ds_read_b128 v[16:19], v244 offset:320\n\t"
    "v_cvt_pk_bf16_f32 v102, v36, v37\n\t"
    "v_mfma_f32_16x16x32_bf16 v[20:23], v[80:83], v[88:91], v[20:23]\n\t"
    "s_waitcnt lgkmcnt(6)\n\t"
    "ds_read_b128 v[80:83], v245 offset:5120\n\t"
    "v_cvt_pk_bf16_f32 v100, v32, v33\n\t"
    "v_cvt_pk_bf16_f32 v103, v38, v39\n\t"
    "v_smfmac_f32_16x16x64_bf16 v[8:11], a[12:15], v[48:55], v177 abid:1\n\t"
    "ds_read_b32 v93, v86 offset:9220\n\t"
    "v_cvt_pk_bf16_f32 v101, v34, v35\n\t"
    "v_smfmac_f32_16x16x64_bf16 v[8:11], a[28:31], v[64:71], v179 abid:1\n\t"
    "s_waitcnt lgkmcnt(5)\n\t"
    "v_cndmask_b32_dpp v36, v100, v96, vcc quad_perm:[0,0,2,2] row_mask:0xf bank_mask:0xf\n\t"
    "v_smfmac_f32_16x16x64_bf16 v[8:11], a[44:47], v[56:63], v181 abid:1\n\t"
    "s_waitcnt lgkmcnt(4)\n\t"
    "ds_read_b32 v92, v228 offset:9220\n\t"
    "v_cndmask_b32_dpp v37, v101, v97, vcc quad_perm:[0,0,2,2] row_mask:0xf bank_mask:0xf\n\t"
    "v_cndmask_b32_dpp v38, v102, v98, vcc quad_perm:[0,0,2,2] row_mask:0xf bank_mask:0xf\n\t"
    "v_cndmask_b32_dpp v39, v103, v99, vcc quad_perm:[0,0,2,2] row_mask:0xf bank_mask:0xf\n\t"
    "v_smfmac_f32_16x16x64_bf16 v[24:27], a[144:147], v[48:55], v194\n\t"
    "s_not_b64 vcc, s[18:19]\n\t"
    "buffer_store_dwordx4 v[36:39], v241, s[44:47], s66 offen offset:128 sc1\n\t"
    "ds_read_b128 v[32:35], v244 offset:384\n\t"
    "v_cvt_pk_bf16_f32 v28, v28, v29\n\t"
    "v_cvt_pk_bf16_f32 v29, v30, v31\n\t"
    "v_add_u32_sdwa v88, v248, v234 dst_sel:DWORD dst_unused:UNUSED_PAD src0_sel:DWORD src1_sel:WORD_0\n\t"
    "v_smfmac_f32_16x16x64_bf16 v[24:27], a[164:167], v[64:71], v196 abid:1\n\t"
    "ds_read_b128 v[36:39], v244 offset:448\n\t"
    "v_add_u32_sdwa v91, v248, v235 dst_sel:DWORD dst_unused:UNUSED_PAD src0_sel:DWORD src1_sel:WORD_1\n\t"
    "v_smfmac_f32_16x16x64_bf16 v[24:27], a[184:187], v[56:63], v199\n\t"
    "s_waitcnt lgkmcnt(5)\n\t"
    "v_cndmask_b32_dpp v101, v97, v101, vcc quad_perm:[1,1,3,3] row_mask:0xf bank_mask:0xf\n\t"
    "v_smfmac_f32_16x16x64_bf16 v[16:19], a[148:151], v[48:55], v194 abid:1\n\t"
    "ds_read_b128 v[40:43], v217 offset:9600\n\t"
    "v_cndmask_b32_dpp v103, v99, v103, vcc quad_perm:[1,1,3,3] row_mask:0xf bank_mask:0xf\n\t"
    "v_smfmac_f32_16x16x64_bf16 v[16:19], a[168:171], v[64:71], v197\n\t"
    "ds_read_b32 v91, v91 offset:9220\n\t"
    "v_cndmask_b32_dpp v100, v96, v100, vcc quad_perm:[1,1,3,3] row_mask:0xf bank_mask:0xf\n\t"
    "v_smfmac_f32_16x16x64_bf16 v[16:19], a[188:191], v[56:63], v199 abid:1\n\t"
    "s_waitcnt lgkmcnt(4)\n\t"
    "v_cndmask_b32_dpp v102, v98, v102, vcc quad_perm:[1,1,3,3] row_mask:0xf bank_mask:0xf\n\t"
    "v_mfma_f32_16x16x32_bf16 v[16:19], v[80:83], v[92:95], v[16:19]\n\t"
    "s_waitcnt lgkmcnt(3)\n\t"
    "buffer_store_dwordx4 v[100:103], v241, s[44:47], s66 offen offset:1280 sc1\n\t"
    "ds_write_b64 v242, v[28:29] offset:512\n\t"
    "v_smfmac_f32_16x16x64_bf16 v[32:35], a[152:155], v[48:55], v195\n\t"
    "ds_read_b32 v95, v75 offset:9220\n\t"
    "v_smfmac_f32_16x16x64_bf16 v[32:35], a[172:175], v[64:71], v197 abid:1\n\t"
    "ds_read_b128 v[28:31], v244 offset:512\n\t"
    "v_add_u32_sdwa v89, v248, v234 dst_sel:DWORD dst_unused:UNUSED_PAD src0_sel:DWORD src1_sel:WORD_1\n\t"
    "v_smfmac_f32_16x16x64_bf16 v[32:35], a[192:195], v[56:63], v200\n\t"
    "s_waitcnt lgkmcnt(4)\n\t"
    "v_add_u32_sdwa v90, v248, v235 dst_sel:DWORD dst_unused:UNUSED_PAD src0_sel:DWORD src1_sel:WORD_0\n\t"
    "v_smfmac_f32_16x16x64_bf16 v[32:35], a[212:215], v[40:47], v202 abid:1\n\t"
    "ds_read_b32 v90, v90 offset:9220\n\t"
    "v_smfmac_f32_16x16x64_bf16 v[36:39], a[156:159], v[48:55], v195 abid:1\n\t"
    "ds_read_b32 v88, v88 offset:9220\n\t"
    "ds_read_b32 v89, v89 offset:9220\n\t"
    "v_smfmac_f32_16x16x64_bf16 v[36:39], a[176:179], v[64:71], v198\n\t"
    "ds_read_b128 v[80:83], v245 offset:8192\n\t"
    "v_smfmac_f32_16x16x64_bf16 v[36:39], a[196:199], v[56:63], v200 abid:1\n\t"
    "v_smfmac_f32_16x16x64_bf16 v[36:39], a[216:219], v[40:47], v203\n\t"
    "s_waitcnt lgkmcnt(4)\n\t"
    "ds_read_b32 v93, v74 offset:9220\n\t"
    "v_smfmac_f32_16x16x64_bf16 v[28:31], a[160:163], v[48:55], v196\n\t"
    "ds_read_b128 v[48:51], v217 offset:9728\n\t"
    "ds_read_b128 v[52:55], v236 offset:9728\n\t"
    "v_smfmac_f32_16x16x64_bf16 v[28:31], a[180:183], v[64:71], v198 abid:1\n\t"
    "v_smfmac_f32_16x16x64_bf16 v[28:31], a[200:203], v[56:63], v201\n\t"
    "ds_read_b32 v92, v220 offset:9220\n\t"
    "v_smfmac_f32_16x16x64_bf16 v[28:31], a[220:223], v[40:47], v203 abid:1\n\t"
    "s_waitcnt lgkmcnt(4)\n\t"
    "ds_read_b32 v94, v221 offset:9220\n\t"
    "v_mfma_f32_16x16x32_bf16 v[28:31], v[80:83], v[88:91], v[28:31]\n\t"
    "ds_read_b128 v[80:83], v245 offset:1024\n\t"
    "v_add_u32_sdwa v90, v248, v247 dst_sel:DWORD dst_unused:UNUSED_PAD src0_sel:DWORD src1_sel:WORD_0\n\t"
    "v_add_u32_sdwa v91, v248, v247 dst_sel:DWORD dst_unused:UNUSED_PAD src0_sel:DWORD src1_sel:WORD_1\n\t"
    "v_smfmac_f32_16x16x64_bf16 v[4:7], a[32:35], v[56:63], v180\n\t"
    "ds_read_u16 v90, v90 offset:9216\n\t"
    "v_add_u32_sdwa v89, v248, v246 dst_sel:DWORD dst_unused:UNUSED_PAD src0_sel:DWORD src1_sel:WORD_1\n\t"
    "v_smfmac_f32_16x16x64_bf16 v[4:7], a[48:51], v[40:47], v182\n\t"
    "s_waitcnt lgkmcnt(4)\n\t"
    "ds_read_u16 v89, v89 offset:9216\n\t"
    "v_add_u32_sdwa v88, v248, v246 dst_sel:DWORD dst_unused:UNUSED_PAD src0_sel:DWORD src1_sel:WORD_0\n\t"
    "v_smfmac_f32_16x16x64_bf16 v[4:7], a[64:67], v[48:55], v184\n\t"
    "ds_read_u16 v88, v88 offset:9216\n\t"
    "v_smfmac_f32_16x16x64_bf16 v[12:15], a[36:39], v[56:63], v180 abid:1\n\t"
    "s_waitcnt vmcnt(9)\n\t"
    "s_barrier\n\t"
    "ds_read_u16 v91, v91 offset:9216\n\t"
    "v_smfmac_f32_16x16x64_bf16 v[12:15], a[52:55], v[40:47], v182 abid:1\n\t"
    "ds_read_b128 v[96:99], v243 offset:2560\n\t"
    "v_smfmac_f32_16x16x64_bf16 v[12:15], a[68:71], v[48:55], v184 abid:1\n\t"
    "s_waitcnt lgkmcnt(5)\n\t"
    "ds_read_b128 v[64:67], v217 offset:9856\n\t"
    "v_mfma_f32_16x16x32_bf16 v[12:15], v[80:83], v[92:95], v[12:15]\n\t"
    "ds_read_b128 v[68:71], v236 offset:9856\n\t"
    "v_smfmac_f32_16x16x64_bf16 v[20:23], a[40:43], v[56:63], v181\n\t"
    "ds_read_b32 v92, v226 offset:9220\n\t"
    "v_smfmac_f32_16x16x64_bf16 v[20:23], a[56:59], v[40:47], v183\n\t"
    "ds_read_b128 v[80:83], v245 offset:4096\n\t"
    "v_smfmac_f32_16x16x64_bf16 v[20:23], a[72:75], v[48:55], v185\n\t"
    "s_waitcnt lgkmcnt(5)\n\t"
    "ds_read_b32 v94, v227 offset:9220\n\t"
    "s_set_gpr_idx_on s4, gpr_idx(SRC2,DST)\n\t"
    "v_mfma_f32_16x16x32_bf16 v[0:3], v[250:253], v[88:91], v[0:3]\n\t"
    "s_set_gpr_idx_off\n\t"
    "s_waitcnt lgkmcnt(5)\n\t"
    "s_mov_b32 exec_hi, 0\n\t"
    "buffer_store_dwordx4 v[96:99], v240, s[44:47], s67 offen offset:0 sc1\n\t"
    "s_mov_b32 exec_hi, -1\n\t"
    "v_smfmac_f32_16x16x64_bf16 v[8:11], a[60:63], v[40:47], v183 abid:1\n\t"
    "ds_read_b32 v93, v84 offset:9220\n\t"
    "ds_read_b32 v95, v85 offset:9220\n\t"
    "v_smfmac_f32_16x16x64_bf16 v[8:11], a[76:79], v[48:55], v185 abid:1\n\t"
    "s_waitcnt lgkmcnt(5)\n\t"
    "v_smfmac_f32_16x16x64_bf16 v[8:11], a[92:95], v[64:71], v187 abid:1\n\t"
    "ds_read_b128 v[60:63], v236 offset:9984\n\t"
    "s_set_gpr_idx_on s5, gpr_idx(SRC2,DST)\n\t"
    "v_mfma_f32_16x16x32_bf16 v[0:3], v[252:255], v[88:91], v[0:3]\n\t"
    "s_set_gpr_idx_off\n\t"
    "s_cmp_lg_u32 s22, 0\n\t"
    "s_cbranch_scc1 .Lsl19_%=\n\t"
    ".Lslr19_%=:\n\t"
    "v_smfmac_f32_16x16x64_bf16 v[24:27], a[204:207], v[40:47], v201 abid:1\n\t"
    "ds_read_b128 v[56:59], v217 offset:9984\n\t"
    "v_smfmac_f32_16x16x64_bf16 v[24:27], a[224:227], v[48:55], v204\n\t"
    "s_memrealtime s[84:85]\n\t"
    "ds_read_b32 v88, v232 offset:9220\n\t"
    "ds_read_b32 v90, v233 offset:9220\n\t"
    "v_smfmac_f32_16x16x64_bf16 v[24:27], a[244:247], v[64:71], v206 abid:1\n\t"
    "s_waitcnt lgkmcnt(4)\n\t"
    "v_mfma_f32_16x16x32_bf16 v[24:27], v[80:83], v[92:95], v[24:27]\n\t"
    "s_mov_b32 s77, 268368\n\t"
    "ds_read_b32 v94, v219 offset:9220\n\t"
    "v_smfmac_f32_16x16x64_bf16 v[16:19], a[208:211], v[40:47], v202\n\t"
    "ds_read_b32 v89, v106 offset:9220\n\t"
    "v_smfmac_f32_16x16x64_bf16 v[16:19], a[228:231], v[48:55], v204 abid:1\n\t"
    "s_mov_b32 s76, 3238002688\n\t"
    "ds_read_b128 v[80:83], v245 offset:7168\n\t"
    "v_smfmac_f32_16x16x64_bf16 v[16:19], a[248:251], v[64:71], v207\n\t"
    "s_sub_i32 s87, s87, s99\n\t"
    "ds_read_b32 v91, v107 offset:9220\n\t"
    "v_smfmac_f32_16x16x64_bf16 v[32:35], a[232:235], v[48:55], v205\n\t"
    "s_bitcmp1_b32 s54, 15\n\t"
    "s_mov_b32 s67, 70778880\n\t"
    "s_cmov_b32 s67, 70852608\n\t"
    "v_smfmac_f32_16x16x64_bf16 v[32:35], a[252:255], v[64:71], v207 abid:1\n\t"
    "s_waitcnt lgkmcnt(6)\n\t"
    "s_lshr_b64 s[80:81], s[76:77], s69\n\t"
    "s_sub_i32 s98, s98, s99\n\t"
    "ds_read_b32 v92, v218 offset:9220\n\t"
    "v_smfmac_f32_16x16x64_bf16 v[32:35], v[124:127], v[56:63], v210\n\t"
    "s_add_i32 s86, s87, 2475\n\t"
    "ds_read_b32 v93, v72 offset:9220\n\t"
    "ds_read_b32 v95, v73 offset:9220\n\t"
    "v_smfmac_f32_16x16x64_bf16 v[36:39], a[236:239], v[48:55], v205 abid:1\n\t"
    "s_add_i32 s98, s98, 57\n\t"
    "ds_read_b128 v[40:43], v217 offset:10112\n\t"
    "v_smfmac_f32_16x16x64_bf16 v[36:39], v[108:111], v[64:71], v208\n\t"
    "s_max_i32 s86, s86, s98\n\t"
    "v_smfmac_f32_16x16x64_bf16 v[36:39], v[128:131], v[56:63], v210 abid:1\n\t"
    "s_waitcnt lgkmcnt(4)\n\t"
    "ds_read_b128 v[44:47], v236 offset:10112\n\t"
    "v_mfma_f32_16x16x32_bf16 v[36:39], v[80:83], v[88:91], v[36:39]\n\t"
    "s_sub_u32 s89, s84, s24\n\t"
    "ds_read_b128 v[80:83], v245 offset:0\n\t"
    "v_smfmac_f32_16x16x64_bf16 v[28:31], a[240:243], v[48:55], v206\n\t"
    "s_mov_b64 vcc, s[18:19]\n\t"
    "s_cmp_lt_i32 s89, s86\n\t"
    "s_cbranch_scc1 .Lpc14_%=\n\t"
    ".Lpc14r_%=:\n\t"
    "ds_read_b128 v[52:55], v236 offset:10240\n\t"
    "v_smfmac_f32_16x16x64_bf16 v[28:31], v[112:115], v[64:71], v208 abid:1\n\t"
    "ds_read_b128 v[48:51], v217 offset:10240\n\t"
    "v_smfmac_f32_16x16x64_bf16 v[28:31], v[132:135], v[56:63], v211\n\t"
    "s_bitcmp1_b32 s54, 14\n\t"
    "s_mov_b32 s65, 66060288\n\t"
    "s_cmov_b32 s65, 66134016\n\t"
    "s_max_i32 s98, s89, s86\n\t"
    "v_smfmac_f32_16x16x64_bf16 v[4:7], a[80:83], v[64:71], v186\n\t"
    "ds_read_b32 v91, v79 offset:9220\n\t"
    "v_smfmac_f32_16x16x64_bf16 v[4:7], a[96:99], v[56:63], v188\n\t"
    "s_waitcnt lgkmcnt(4)\n\t"
    "ds_read_b32 v88, v224 offset:9220\n\t"
    "v_smfmac_f32_16x16x64_bf16 v[4:7], a[112:115], v[40:47], v190\n\t"
    "s_waitcnt lgkmcnt(2)\n\t"
    "v_smfmac_f32_16x16x64_bf16 v[4:7], a[128:131], v[48:55], v192\n\t"
    "ds_read_b32 v90, v225 offset:9220\n\t"
    "v_mfma_f32_16x16x32_bf16 v[4:7], v[80:83], v[92:95], v[4:7]\n\t"
    "ds_read_b32 v93, v104 offset:9220\n\t"
    "v_smfmac_f32_16x16x64_bf16 v[12:15], a[84:87], v[64:71], v186 abid:1\n\t"
    "ds_read_b32 v92, v230 offset:9220\n\t"
    "v_smfmac_f32_16x16x64_bf16 v[12:15], a[100:103], v[56:63], v188 abid:1\n\t"
    "v_smfmac_f32_16x16x64_bf16 v[12:15], a[116:119], v[40:47], v190 abid:1\n\t"
    "ds_read_b32 v89, v78 offset:9220\n\t"
    "v_cvt_pk_bf16_f32 v101, v6, v7\n\t"
    "v_smfmac_f32_16x16x64_bf16 v[12:15], a[132:135], v[48:55], v192 abid:1\n\t"
    "v_cvt_pk_bf16_f32 v100, v4, v5\n\t"
    "v_smfmac_f32_16x16x64_bf16 v[20:23], a[88:91], v[64:71], v187\n\t"
    "ds_read_b128 v[80:83], v245 offset:3072\n\t"
    "v_smfmac_f32_16x16x64_bf16 v[20:23], a[104:107], v[56:63], v189\n\t"
    "ds_read_b128 v[64:67], v217 offset:13824\n\t"
    "v_smfmac_f32_16x16x64_bf16 v[20:23], a[120:123], v[40:47], v191\n\t"
    "s_mov_b32 s77, 268368\n\t"
    "ds_read_b128 v[4:7], v244 offset:256\n\t"
    "v_smfmac_f32_16x16x64_bf16 v[20:23], a[136:139], v[48:55], v193\n\t"
    "s_and_b32 s80, s80, 63\n\t"
    "ds_read_b32 v95, v105 offset:9220\n\t"
    "v_cvt_pk_bf16_f32 v103, v14, v15\n\t"
    "v_smfmac_f32_16x16x64_bf16 v[8:11], a[108:111], v[56:63], v189 abid:1\n\t"
    "v_cvt_pk_bf16_f32 v102, v12, v13\n\t"
    "v_smfmac_f32_16x16x64_bf16 v[8:11], a[124:127], v[40:47], v191 abid:1\n\t"
    "ds_read_b128 v[68:71], v236 offset:13824\n\t"
    "v_smfmac_f32_16x16x64_bf16 v[8:11], a[140:143], v[48:55], v193 abid:1\n\t"
    "s_waitcnt lgkmcnt(4)\n\t"
    "ds_read_b32 v94, v231 offset:9220\n\t"
    "v_cvt_pk_bf16_f32 v96, v20, v21\n\t"
    "v_mfma_f32_16x16x32_bf16 v[8:11], v[80:83], v[88:91], v[8:11]\n\t"
    "ds_read_b128 v[80:83], v245 offset:6144\n\t"
    "v_smfmac_f32_16x16x64_bf16 v[24:27], v[116:119], v[56:63], v209\n\t"
    "v_cvt_pk_bf16_f32 v97, v22, v23\n\t"
    "v_smfmac_f32_16x16x64_bf16 v[24:27], v[136:139], v[40:47], v211 abid:1\n\t"
    "s_lshl_b32 s80, s80, 2\n\t"
    "ds_read_b32 v88, v230 offset:13828\n\t"
    "v_smfmac_f32_16x16x64_bf16 v[24:27], v[156:159], v[48:55], v214\n\t"
    "ds_read_b128 v[20:23], v244 offset:384\n\t"
    "v_cvt_pk_bf16_f32 v98, v8, v9\n\t"
    "v_smfmac_f32_16x16x64_bf16 v[16:19], v[120:123], v[56:63], v209 abid:1\n\t"
    "ds_read_b128 v[12:15], v244 offset:320\n\t"
    "v_cvt_pk_bf16_f32 v99, v10, v11\n\t"
    "v_smfmac_f32_16x16x64_bf16 v[16:19], v[140:143], v[40:47], v212\n\t"
    "ds_read_b32 v91, v105 offset:13828\n\t"
    "v_cndmask_b32_dpp v9, v97, v101, vcc quad_perm:[0,0,2,2] row_mask:0xf bank_mask:0xf\n\t"
    "v_smfmac_f32_16x16x64_bf16 v[16:19], v[160:163], v[48:55], v214 abid:1\n\t"
    "ds_read_b32 v89, v104 offset:13828\n\t"
    "v_cndmask_b32_dpp v11, v99, v103, vcc quad_perm:[0,0,2,2] row_mask:0xf bank_mask:0xf\n\t"
    "v_smfmac_f32_16x16x64_bf16 v[32:35], v[144:147], v[40:47], v212 abid:1\n\t"
    "ds_read_b128 v[56:59], v217 offset:13952\n\t"
    "v_cndmask_b32_dpp v10, v98, v102, vcc quad_perm:[0,0,2,2] row_mask:0xf bank_mask:0xf\n\t"
    "v_smfmac_f32_16x16x64_bf16 v[32:35], v[164:167], v[48:55], v215\n\t"
    "s_waitcnt lgkmcnt(6)\n\t"
    "ds_read_b128 v[60:63], v236 offset:13952\n\t"
    "v_cndmask_b32_dpp v8, v96, v100, vcc quad_perm:[0,0,2,2] row_mask:0xf bank_mask:0xf\n\t"
    "v_mfma_f32_16x16x32_bf16 v[32:35], v[80:83], v[92:95], v[32:35]\n\t"
    "s_not_b64 vcc, s[18:19]\n\t"
    "ds_read_b32 v90, v231 offset:13828\n\t"
    "v_cndmask_b32_dpp v96, v100, v96, vcc quad_perm:[1,1,3,3] row_mask:0xf bank_mask:0xf\n\t"
    "v_smfmac_f32_16x16x64_bf16 v[36:39], v[148:151], v[40:47], v213\n\t"
    "s_add_u32 s66, s57, s65\n\t"
    "ds_read_b32 v94, v229 offset:13828\n\t"
    "v_cndmask_b32_dpp v98, v102, v98, vcc quad_perm:[1,1,3,3] row_mask:0xf bank_mask:0xf\n\t"
    "v_smfmac_f32_16x16x64_bf16 v[36:39], v[168:171], v[48:55], v215 abid:1\n\t"
    "ds_read_b32 v93, v86 offset:13828\n\t"
    "v_cvt_pk_bf16_f32 v100, v24, v25\n\t"
    "v_smfmac_f32_16x16x64_bf16 v[28:31], v[152:155], v[40:47], v213 abid:1\n\t"
    "buffer_store_dwordx4 v[8:11], v241, s[44:47], s65 offen offset:0 sc1\n\t"
    "ds_read_b128 v[80:83], v245 offset:6144\n\t"
    "v_cndmask_b32_dpp v97, v101, v97, vcc quad_perm:[1,1,3,3] row_mask:0xf bank_mask:0xf\n\t"
    "v_cndmask_b32_dpp v99, v103, v99, vcc quad_perm:[1,1,3,3] row_mask:0xf bank_mask:0xf\n\t"
    "v_cvt_pk_bf16_f32 v101, v26, v27\n\t"
    "v_smfmac_f32_16x16x64_bf16 v[28:31], v[172:175], v[48:55], v216\n\t"
    "ds_read_b128 v[48:51], v217 offset:14208\n\t"
    "v_cvt_pk_bf16_f32 v102, v16, v17\n\t"
    "v_smfmac_f32_16x16x64_bf16 v[4:7], a[144:147], v[64:71], v194\n\t"
    "s_waitcnt lgkmcnt(5)\n\t"
    "ds_read_b128 v[24:27], v244 offset:448\n\t"
    "v_cvt_pk_bf16_f32 v103, v18, v19\n\t"
    "v_smfmac_f32_16x16x64_bf16 v[4:7], a[164:167], v[56:63], v196 abid:1\n\t"
    "ds_read_b128 v[16:19], v244 offset:512\n\t"
    "v_smfmac_f32_16x16x64_bf16 v[12:15], a[148:151], v[64:71], v194 abid:1\n\t"
    "ds_read_b128 v[52:55], v236 offset:14208\n\t"
    "v_cvt_pk_bf16_f32 v28, v28, v29\n\t"
    "v_smfmac_f32_16x16x64_bf16 v[12:15], a[168:171], v[56:63], v197\n\t"
    "s_mov_b64 vcc, s[18:19]\n\t"
    "ds_read_b128 v[40:43], v217 offset:14080\n\t"
    "v_smfmac_f32_16x16x64_bf16 v[20:23], a[152:155], v[64:71], v195\n\t"
    "buffer_store_dwordx4 v[96:99], v241, s[44:47], s65 offen offset:1152 sc1\n\t"
    "ds_read_b128 v[44:47], v236 offset:14080\n\t"
    "v_cvt_pk_bf16_f32 v29, v30, v31\n\t"
    "v_smfmac_f32_16x16x64_bf16 v[20:23], a[172:175], v[56:63], v197 abid:1\n\t"
    "s_waitcnt lgkmcnt(6)\n\t"
    "ds_read_b32 v92, v228 offset:13828\n\t"
    "v_cvt_pk_bf16_f32 v98, v36, v37\n\t"
    "v_mfma_f32_16x16x32_bf16 v[20:23], v[80:83], v[88:91], v[20:23]\n\t"
    "s_waitcnt lgkmcnt(5)\n\t"
    "ds_read_b32 v95, v87 offset:13828\n\t"
    "v_cvt_pk_bf16_f32 v97, v34, v35\n\t"
    "v_cvt_pk_bf16_f32 v99, v38, v39\n\t"
    "v_smfmac_f32_16x16x64_bf16 v[24:27], a[156:159], v[64:71], v195 abid:1\n\t"
    "ds_read_b128 v[80:83], v245 offset:5120\n\t"
    "v_cvt_pk_bf16_f32 v96, v32, v33\n\t"
    "v_smfmac_f32_16x16x64_bf16 v[24:27], a[176:179], v[56:63], v198\n\t"
    "s_waitcnt lgkmcnt(6)\n\t"
    "ds_write_b64 v242, v[28:29] offset:0\n\t"
    "v_cndmask_b32_dpp v38, v98, v102, vcc quad_perm:[0,0,2,2] row_mask:0xf bank_mask:0xf\n\t"
    "v_add_u32_sdwa v89, v248, v246 dst_sel:DWORD dst_unused:UNUSED_PAD src0_sel:DWORD src1_sel:WORD_1\n\t"
    "v_add_u32_sdwa v91, v248, v247 dst_sel:DWORD dst_unused:UNUSED_PAD src0_sel:DWORD src1_sel:WORD_1\n\t"
    "v_smfmac_f32_16x16x64_bf16 v[16:19], a[160:163], v[64:71], v196\n\t"
    "ds_read_u16 v89, v89 offset:13824\n\t"
    "v_cndmask_b32_dpp v36, v96, v100, vcc quad_perm:[0,0,2,2] row_mask:0xf bank_mask:0xf\n\t"
    "v_cndmask_b32_dpp v37, v97, v101, vcc quad_perm:[0,0,2,2] row_mask:0xf bank_mask:0xf\n\t"
    "v_cndmask_b32_dpp v39, v99, v103, vcc quad_perm:[0,0,2,2] row_mask:0xf bank_mask:0xf\n\t"
    "v_smfmac_f32_16x16x64_bf16 v[16:19], a[180:183], v[56:63], v198 abid:1\n\t"
    "s_waitcnt lgkmcnt(5)\n\t"
    "s_not_b64 vcc, s[18:19]\n\t"
    "buffer_store_dwordx4 v[36:39], v241, s[44:47], s65 offen offset:128 sc1\n\t"
    "ds_read_u16 v91, v91 offset:13824\n\t"
    "v_cndmask_b32_dpp v97, v101, v97, vcc quad_perm:[1,1,3,3] row_mask:0xf bank_mask:0xf\n\t"
    "v_add_u32_sdwa v88, v248, v246 dst_sel:DWORD dst_unused:UNUSED_PAD src0_sel:DWORD src1_sel:WORD_0\n\t"
    "v_add_u32_sdwa v90, v248, v247 dst_sel:DWORD dst_unused:UNUSED_PAD src0_sel:DWORD src1_sel:WORD_0\n\t"
    "v_smfmac_f32_16x16x64_bf16 v[4:7], a[184:187], v[40:47], v199\n\t"
    "ds_read_u16 v88, v88 offset:13824\n\t"
    "ds_read_u16 v90, v90 offset:13824\n\t"
    "v_smfmac_f32_16x16x64_bf16 v[4:7], a[204:207], v[48:55], v201 abid:1\n\t"
    "ds_read_b128 v[68:71], v236 offset:14336\n\t"
    "v_cndmask_b32_dpp v96, v100, v96, vcc quad_perm:[1,1,3,3] row_mask:0xf bank_mask:0xf\n\t"
    "v_smfmac_f32_16x16x64_bf16 v[12:15], a[188:191], v[40:47], v199 abid:1\n\t"
    "s_mov_b32 s76, 3238002688\n\t"
    "ds_read_b128 v[64:67], v217 offset:14336\n\t"
    "v_cndmask_b32_dpp v99, v103, v99, vcc quad_perm:[1,1,3,3] row_mask:0xf bank_mask:0xf\n\t"
    "v_smfmac_f32_16x16x64_bf16 v[12:15], a[208:211], v[48:55], v202\n\t"
    "s_waitcnt lgkmcnt(7)\n\t"
    "ds_read_b128 v[56:59], v217 offset:14464\n\t"
    "v_cndmask_b32_dpp v98, v102, v98, vcc quad_perm:[1,1,3,3] row_mask:0xf bank_mask:0xf\n\t"
    "v_mfma_f32_16x16x32_bf16 v[12:15], v[80:83], v[92:95], v[12:15]\n\t"
    "ds_read_b32 v94, v227 offset:13828\n\t"
    "v_smfmac_f32_16x16x64_bf16 v[20:23], a[192:195], v[40:47], v200\n\t"
    "buffer_store_dwordx4 v[96:99], v241, s[44:47], s65 offen offset:1280 sc1\n\t"
    "ds_read_b128 v[80:83], v245 offset:4096\n\t"
    "ds_read_b32 v92, v226 offset:13828\n\t"
    "v_smfmac_f32_16x16x64_bf16 v[20:23], a[212:215], v[48:55], v202 abid:1\n\t"
    "s_waitcnt lgkmcnt(6)\n\t"
    "ds_read_b128 v[60:63], v236 offset:14464\n\t"
    "s_set_gpr_idx_on s80, gpr_idx(SRC2,DST)\n\t"
    "v_mfma_f32_16x16x32_bf16 v[0:3], v[250:253], v[88:91], v[0:3]\n\t"
    "s_set_gpr_idx_off\n\t"
    "s_lshr_b64 s[80:81], s[76:77], s70\n\t"
    "s_barrier\n\t"
    "ds_read_b128 v[100:103], v243 offset:2048\n\t"
    "v_smfmac_f32_16x16x64_bf16 v[24:27], a[196:199], v[40:47], v200 abid:1\n\t"
    "s_and_b32 s80, s80, 63\n\t"
    "ds_read_b32 v93, v84 offset:13828\n\t"
    "ds_read_b32 v95, v85 offset:13828\n\t"
    "v_smfmac_f32_16x16x64_bf16 v[24:27], a[216:219], v[48:55], v203\n\t"
    "s_lshl_b32 s80, s80, 2\n\t"
    "ds_read_b128 v[8:11], v244 offset:0\n\t"
    "s_set_gpr_idx_on s80, gpr_idx(SRC2,DST)\n\t"
    "v_mfma_f32_16x16x32_bf16 v[0:3], v[252:255], v[88:91], v[0:3]\n\t"
    "s_set_gpr_idx_off\n\t"
    "s_cmp_lg_u32 s22, 0\n\t"
    "s_cbranch_scc1 .Lsl20_%=\n\t"
    ".Lslr20_%=:\n\t"
    "v_smfmac_f32_16x16x64_bf16 v[16:19], a[200:203], v[40:47], v201\n\t"
    "ds_read_b128 v[44:47], v236 offset:14592\n\t"
    "v_add_u32_sdwa v89, v248, v234 dst_sel:DWORD dst_unused:UNUSED_PAD src0_sel:DWORD src1_sel:WORD_1\n\t"
    "v_smfmac_f32_16x16x64_bf16 v[16:19], a[220:223], v[48:55], v203 abid:1\n\t"
    "s_waitcnt lgkmcnt(10)\n\t"
    "ds_read_b32 v89, v89 offset:13828\n\t"
    "v_smfmac_f32_16x16x64_bf16 v[4:7], a[224:227], v[64:71], v204\n\t"
    "s_waitcnt lgkmcnt(6)\n\t"
    "s_mov_b32 s76, 2388482\n\t"
    "ds_read_b128 v[52:55], v236 offset:14720\n\t"
    "v_smfmac_f32_16x16x64_bf16 v[4:7], a[244:247], v[56:63], v206 abid:1\n\t"
    "s_waitcnt lgkmcnt(4)\n\t"
    "s_mov_b32 exec_hi, 0\n\t"
    "buffer_store_dwordx4 v[100:103], v240, s[44:47], s66 offen offset:0 sc1\n\t"
    "s_mov_b32 exec_hi, -1\n\t"
    "ds_read_b128 v[40:43], v217 offset:14592\n\t"
    "v_add_u32_sdwa v88, v248, v234 dst_sel:DWORD dst_unused:UNUSED_PAD src0_sel:DWORD src1_sel:WORD_0\n\t"
    "v_add_u32_sdwa v91, v248, v235 dst_sel:DWORD dst_unused:UNUSED_PAD src0_sel:DWORD src1_sel:WORD_1\n\t"
    "v_mfma_f32_16x16x32_bf16 v[4:7], v[80:83], v[92:95], v[4:7]\n\t"
    "ds_read_b128 v[80:83], v245 offset:8192\n\t"
    "v_add_u32_sdwa v90, v248, v235 dst_sel:DWORD dst_unused:UNUSED_PAD src0_sel:DWORD src1_sel:WORD_0\n\t"
    "v_smfmac_f32_16x16x64_bf16 v[12:15], a[228:231], v[64:71], v204 abid:1\n\t"
    "ds_read_b32 v90, v90 offset:13828\n\t"
    "v_smfmac_f32_16x16x64_bf16 v[12:15], a[248:251], v[56:63], v207\n\t"
    "ds_read_b32 v88, v88 offset:13828\n\t"
    "ds_read_b32 v91, v91 offset:13828\n\t"
    "v_smfmac_f32_16x16x64_bf16 v[20:23], a[232:235], v[64:71], v205\n\t"
    "v_smfmac_f32_16x16x64_bf16 v[20:23], a[252:255], v[56:63], v207 abid:1\n\t"
    "s_mov_b64 vcc, s[18:19]\n\t"
    "ds_read_b128 v[48:51], v217 offset:14720\n\t"
    "v_smfmac_f32_16x16x64_bf16 v[24:27], a[236:239], v[64:71], v205 abid:1\n\t"
    "ds_read_b32 v94, v233 offset:13828\n\t"
    "v_smfmac_f32_16x16x64_bf16 v[24:27], v[108:111], v[56:63], v208\n\t"
    "s_mov_b32 s77, 0\n\t"
    "ds_read_b32 v93, v106 offset:13828\n\t"
    "v_smfmac_f32_16x16x64_bf16 v[16:19], a[240:243], v[64:71], v206\n\t"
    "ds_read_b128 v[64:67], v217 offset:14848\n\t"
    "ds_read_b128 v[68:71], v236 offset:14848\n\t"
    "v_smfmac_f32_16x16x64_bf16 v[16:19], v[112:115], v[56:63], v208 abid:1\n\t"
    "s_waitcnt lgkmcnt(5)\n\t"
    "v_mfma_f32_16x16x32_bf16 v[16:19], v[80:83], v[88:91], v[16:19]\n\t"
    "ds_read_b128 v[56:59], v217 offset:13824\n\t"
    "v_add_u32_sdwa v89, v248, v246 dst_sel:DWORD dst_unused:UNUSED_PAD src0_sel:DWORD src1_sel:WORD_1\n\t"
    "v_smfmac_f32_16x16x64_bf16 v[4:7], v[116:119], v[40:47], v209\n\t"
    "s_waitcnt lgkmcnt(5)\n\t"
    "ds_read_b32 v95, v107 offset:13828\n\t"
    "v_add_u32_sdwa v91, v248, v247 dst_sel:DWORD dst_unused:UNUSED_PAD src0_sel:DWORD src1_sel:WORD_1\n\t"
    "v_smfmac_f32_16x16x64_bf16 v[4:7], v[136:139], v[48:55], v211 abid:1\n\t"
    "s_waitcnt lgkmcnt(2)\n\t"
    "s_lshr_b64 s[80:81], s[76:77], s69\n\t"
    "ds_read_b128 v[60:63], v236 offset:13824\n\t"
    "v_add_u32_sdwa v90, v248, v247 dst_sel:DWORD dst_unused:UNUSED_PAD src0_sel:DWORD src1_sel:WORD_0\n\t"
    "v_smfmac_f32_16x16x64_bf16 v[4:7], v[156:159], v[64:71], v214\n\t"
    "ds_read_b32 v92, v232 offset:13828\n\t"
    "v_smfmac_f32_16x16x64_bf16 v[12:15], v[120:123], v[40:47], v209 abid:1\n\t"
    "s_mov_b32 s76, 2388482\n\t"
    "ds_read_b128 v[36:39], v244 offset:192\n\t"
    "v_add_u32_sdwa v88, v248, v246 dst_sel:DWORD dst_unused:UNUSED_PAD src0_sel:DWORD src1_sel:WORD_0\n\t"
    "v_smfmac_f32_16x16x64_bf16 v[12:15], v[140:143], v[48:55], v212\n\t"
    "ds_read_b128 v[28:31], v244 offset:128\n\t"
    "v_smfmac_f32_16x16x64_bf16 v[12:15], v[160:163], v[64:71], v214 abid:1\n\t"
    "ds_read_b128 v[80:83], v245 offset:7168\n\t"
    "v_smfmac_f32_16x16x64_bf16 v[20:23], v[124:127], v[40:47], v210\n\t"
    "ds_read_u16 v89, v89 offset:13824\n\t"
    "v_cvt_pk_bf16_f32 v97, v6, v7\n\t"
    "v_smfmac_f32_16x16x64_bf16 v[20:23], v[144:147], v[48:55], v212 abid:1\n\t"
    "ds_read_u16 v90, v90 offset:13824\n\t"
    "v_smfmac_f32_16x16x64_bf16 v[20:23], v[164:167], v[64:71], v215\n\t"
    "ds_read_u16 v88, v88 offset:13824\n\t"
    "v_cvt_pk_bf16_f32 v96, v4, v5\n\t"
    "v_smfmac_f32_16x16x64_bf16 v[24:27], v[128:131], v[40:47], v210 abid:1\n\t"
    "ds_read_u16 v91, v91 offset:13824\n\t"
    "v_smfmac_f32_16x16x64_bf16 v[24:27], v[148:151], v[48:55], v213\n\t"
    "ds_read_b128 v[32:35], v244 offset:64\n\t"
    "v_cvt_pk_bf16_f32 v98, v12, v13\n\t"
    "v_smfmac_f32_16x16x64_bf16 v[24:27], v[168:171], v[64:71], v215 abid:1\n\t"
    "s_waitcnt lgkmcnt(5)\n\t"
    "v_mfma_f32_16x16x32_bf16 v[24:27], v[80:83], v[92:95], v[24:27]\n\t"
    "ds_read_b128 v[80:83], v245 offset:0\n\t"
    "ds_read_b32 v94, v219 offset:13828\n\t"
    "v_smfmac_f32_16x16x64_bf16 v[16:19], v[132:135], v[40:47], v211\n\t"
    "ds_read_b128 v[40:43], v217 offset:13952\n\t"
    "ds_read_b128 v[44:47], v236 offset:13952\n\t"
    "v_smfmac_f32_16x16x64_bf16 v[16:19], v[152:155], v[48:55], v213 abid:1\n\t"
    "ds_read_b32 v93, v72 offset:13828\n\t"
    "v_cvt_pk_bf16_f32 v101, v22, v23\n\t"
    "v_smfmac_f32_16x16x64_bf16 v[16:19], v[172:175], v[64:71], v216\n\t"
    "ds_read_b32 v92, v218 offset:13828\n\t"
    "ds_read_b32 v95, v73 offset:13828\n\t"
    "v_smfmac_f32_16x16x64_bf16 v[8:11], a[0:3], v[56:63], v176\n\t"
    "s_waitcnt lgkmcnt(7)\n\t"
    "v_cvt_pk_bf16_f32 v99, v14, v15\n\t"
    "v_smfmac_f32_16x16x64_bf16 v[32:35], a[4:7], v[56:63], v176 abid:1\n\t"
    "ds_read_b128 v[52:55], v236 offset:14080\n\t"
    "v_cvt_pk_bf16_f32 v103, v26, v27\n\t"
    "v_smfmac_f32_16x16x64_bf16 v[28:31], a[8:11], v[56:63], v177\n\t"
    "s_and_b32 s80, s80, 63\n\t"
    "ds_read_b128 v[48:51], v217 offset:14080\n\t"
    "v_cvt_pk_bf16_f32 v102, v24, v25\n\t"
    "v_smfmac_f32_16x16x64_bf16 v[36:39], a[12:15], v[56:63], v177 abid:1\n\t"
    "s_waitcnt lgkmcnt(5)\n\t"
    "ds_read_b128 v[68:71], v236 offset:14208\n\t"
    "v_cvt_pk_bf16_f32 v100, v20, v21\n\t"
    "v_smfmac_f32_16x16x64_bf16 v[8:11], a[16:19], v[40:47], v178\n\t"
    "ds_read_b128 v[64:67], v217 offset:14208\n\t"
    "v_cndmask_b32_dpp v27, v103, v99, vcc quad_perm:[0,0,2,2] row_mask:0xf bank_mask:0xf\n\t"
    "v_smfmac_f32_16x16x64_bf16 v[32:35], a[20:23], v[40:47], v178 abid:1\n\t"
    "s_waitcnt lgkmcnt(4)\n\t"
    "s_lshl_b32 s80, s80, 2\n\t"
    "ds_read_b128 v[56:59], v217 offset:14336\n\t"
    "v_cndmask_b32_dpp v24, v100, v96, vcc quad_perm:[0,0,2,2] row_mask:0xf bank_mask:0xf\n\t"
    "v_mfma_f32_16x16x32_bf16 v[8:11], v[80:83], v[92:95], v[8:11]\n\t"
    "s_mov_b32 s77, 0\n\t"
    "ds_read_b32 v94, v221 offset:13828\n\t"
    "v_cndmask_b32_dpp v26, v102, v98, vcc quad_perm:[0,0,2,2] row_mask:0xf bank_mask:0xf\n\t"
    "v_smfmac_f32_16x16x64_bf16 v[28:31], a[24:27], v[40:47], v179\n\t"
    "s_add_u32 s65, s57, s67\n\t"
    "ds_read_b32 v92, v220 offset:13828\n\t"
    "v_cndmask_b32_dpp v25, v101, v97, vcc quad_perm:[0,0,2,2] row_mask:0xf bank_mask:0xf\n\t"
    "v_smfmac_f32_16x16x64_bf16 v[36:39], a[28:31], v[40:47], v179 abid:1\n\t"
    "s_waitcnt lgkmcnt(5)\n\t"
    "s_not_b64 vcc, s[18:19]\n\t"
    "buffer_store_dwordx4 v[24:27], v241, s[44:47], s67 offen offset:128 sc1\n\t"
    "ds_read_b32 v95, v75 offset:13828\n\t"
    "v_cndmask_b32_dpp v100, v96, v100, vcc quad_perm:[1,1,3,3] row_mask:0xf bank_mask:0xf\n\t"
    "v_smfmac_f32_16x16x64_bf16 v[8:11], a[32:35], v[48:55], v180\n\t"
    "ds_read_b128 v[80:83], v245 offset:1024\n\t"
    "v_cndmask_b32_dpp v101, v97, v101, vcc quad_perm:[1,1,3,3] row_mask:0xf bank_mask:0xf\n\t"
    "v_smfmac_f32_16x16x64_bf16 v[32:35], a[36:39], v[48:55], v180 abid:1\n\t"
    "ds_read_b32 v93, v74 offset:13828\n\t"
    "s_set_gpr_idx_on s80, gpr_idx(SRC2,DST)\n\t"
    "v_mfma_f32_16x16x32_bf16 v[0:3], v[250:253], v[88:91], v[0:3]\n\t"
    "s_set_gpr_idx_off\n\t"
    "ds_read_b128 v[60:63], v236 offset:14336\n\t"
    "v_smfmac_f32_16x16x64_bf16 v[28:31], a[40:43], v[48:55], v181\n\t"
    "ds_read_b128 v[44:47], v236 offset:14464\n\t"
    "v_cndmask_b32_dpp v102, v98, v102, vcc quad_perm:[1,1,3,3] row_mask:0xf bank_mask:0xf\n\t"
    "v_smfmac_f32_16x16x64_bf16 v[36:39], a[44:47], v[48:55], v181 abid:1\n\t"
    "s_waitcnt lgkmcnt(8)\n\t"
    "v_cndmask_b32_dpp v103, v99, v103, vcc quad_perm:[1,1,3,3] row_mask:0xf bank_mask:0xf\n\t"
    "v_smfmac_f32_16x16x64_bf16 v[8:11], a[48:51], v[64:71], v182\n\t"
    "s_lshr_b64 s[80:81], s[76:77], s70\n\t"
    "buffer_store_dwordx4 v[100:103], v241, s[44:47], s67 offen offset:1280 sc1\n\t"
    "ds_read_b128 v[40:43], v217 offset:14464\n\t"
    "v_cvt_pk_bf16_f32 v16, v16, v17\n\t"
    "v_smfmac_f32_16x16x64_bf16 v[32:35], a[52:55], v[64:71], v182 abid:1\n\t"
    "ds_read_b128 v[52:55], v236 offset:14592\n\t"
    "v_smfmac_f32_16x16x64_bf16 v[28:31], a[56:59], v[64:71], v183\n\t"
    "s_waitcnt lgkmcnt(4)\n\t"
    "ds_read_b128 v[48:51], v217 offset:14592\n\t"
    "v_mfma_f32_16x16x32_bf16 v[32:35], v[80:83], v[92:95], v[32:35]\n\t"
    "s_and_b32 s80, s80, 63\n\t"
    "ds_read_b32 v92, v222 offset:13828\n\t"
    "v_smfmac_f32_16x16x64_bf16 v[36:39], a[60:63], v[64:71], v183 abid:1\n\t"
    "s_waitcnt lgkmcnt(5)\n\t"
    "ds_read_b128 v[80:83], v245 offset:2048\n\t"
    "v_smfmac_f32_16x16x64_bf16 v[8:11], a[64:67], v[56:63], v184\n\t"
    "ds_read_b128 v[68:71], v236 offset:14720\n\t"
    "v_smfmac_f32_16x16x64_bf16 v[32:35], a[68:71], v[56:63], v184 abid:1\n\t"
    "ds_read_b32 v93, v76 offset:13828\n\t"
    "ds_read_b32 v94, v223 offset:13828\n\t"
    "v_smfmac_f32_16x16x64_bf16 v[28:31], a[72:75], v[56:63], v185\n\t"
    "s_lshl_b32 s80, s80, 2\n\t"
    "ds_read_b32 v95, v77 offset:13828\n\t"
    "s_set_gpr_idx_on s80, gpr_idx(SRC2,DST)\n\t"
    "v_mfma_f32_16x16x32_bf16 v[0:3], v[252:255], v[88:91], v[0:3]\n\t"
    "s_set_gpr_idx_off\n\t"
    "s_cmp_lg_u32 s22, 0\n\t"
    "s_cbranch_scc1 .Lsl21_%=\n\t"
    ".Lslr21_%=:\n\t"
    "v_smfmac_f32_16x16x64_bf16 v[36:39], a[76:79], v[56:63], v185 abid:1\n\t"
    "s_waitcnt lgkmcnt(8)\n\t"
    "ds_read_b128 v[56:59], v217 offset:14848\n\t"
    "v_smfmac_f32_16x16x64_bf16 v[8:11], a[80:83], v[40:47], v186\n\t"
    "ds_read_b128 v[64:67], v217 offset:14720\n\t"
    "v_cvt_pk_bf16_f32 v17, v18, v19\n\t"
    "v_smfmac_f32_16x16x64_bf16 v[32:35], a[84:87], v[40:47], v186 abid:1\n\t"
    "ds_write_b64 v242, v[16:17] offset:512\n\t"
    "v_smfmac_f32_16x16x64_bf16 v[28:31], a[88:91], v[40:47], v187\n\t"
    "ds_read_b32 v90, v225 offset:13828\n\t"
    "v_smfmac_f32_16x16x64_bf16 v[36:39], a[92:95], v[40:47], v187 abid:1\n\t"
    "s_waitcnt lgkmcnt(4)\n\t"
    "ds_read_b128 v[60:63], v236 offset:14848\n\t"
    "v_mfma_f32_16x16x32_bf16 v[28:31], v[80:83], v[92:95], v[28:31]\n\t"
    "s_waitcnt lgkmcnt(2)\n\t"
    "s_barrier\n\t"
    "ds_read_b128 v[96:99], v243 offset:2560\n\t"
    "v_smfmac_f32_16x16x64_bf16 v[8:11], a[96:99], v[48:55], v188\n\t"
    "ds_read_b128 v[80:83], v245 offset:3072\n\t"
    "v_smfmac_f32_16x16x64_bf16 v[32:35], a[100:103], v[48:55], v188 abid:1\n\t"
    "ds_read_b32 v91, v79 offset:13828\n\t"
    "v_smfmac_f32_16x16x64_bf16 v[28:31], a[104:107], v[48:55], v189\n\t"
    "ds_read_b32 v88, v224 offset:13828\n\t"
    "ds_read_b32 v89, v78 offset:13828\n\t"
    "v_smfmac_f32_16x16x64_bf16 v[36:39], a[108:111], v[48:55], v189 abid:1\n\t"
    "v_smfmac_f32_16x16x64_bf16 v[8:11], a[112:115], v[64:71], v190\n\t"
    "v_smfmac_f32_16x16x64_bf16 v[32:35], a[116:119], v[64:71], v190 abid:1\n\t"
    "v_smfmac_f32_16x16x64_bf16 v[28:31], a[120:123], v[64:71], v191\n\t"
    "s_waitcnt lgkmcnt(4)\n\t"
    "s_mov_b32 exec_hi, 0\n\t"
    "buffer_store_dwordx4 v[96:99], v240, s[44:47], s65 offen offset:0 sc1\n\t"
    "s_mov_b32 exec_hi, -1\n\t"
    "v_smfmac_f32_16x16x64_bf16 v[36:39], a[124:127], v[64:71], v191 abid:1\n\t"
    "v_smfmac_f32_16x16x64_bf16 v[8:11], a[128:131], v[56:63], v192\n\t"
    "s_waitcnt lgkmcnt(0)\n\t"
    "v_mfma_f32_16x16x32_bf16 v[36:39], v[80:83], v[88:91], v[36:39]\n\t"
    "v_smfmac_f32_16x16x64_bf16 v[32:35], a[132:135], v[56:63], v192 abid:1\n\t"
    "v_smfmac_f32_16x16x64_bf16 v[28:31], a[136:139], v[56:63], v193\n\t"
    "s_mov_b64 vcc, s[18:19]\n\t"
    "v_cvt_pk_bf16_f32 v101, v10, v11\n\t"
    "v_smfmac_f32_16x16x64_bf16 v[36:39], a[140:143], v[56:63], v193 abid:1\n\t"
    "s_nop 3\n\t"
    "s_bitcmp1_b32 s54, 15\n\t"
    "s_mov_b32 s66, 70778880\n\t"
    "s_cmov_b32 s66, 70852608\n\t"
    "v_cvt_pk_bf16_f32 v100, v8, v9\n\t"
    "v_cvt_pk_bf16_f32 v102, v32, v33\n\t"
    "v_cvt_pk_bf16_f32 v103, v34, v35\n\t"
    ".Lpad3_%=:\n\t"
    "s_cbranch_execz .Lpad4_%=\n\t"
    "s_nop 3\n\t"
    "v_cvt_pk_bf16_f32 v96, v28, v29\n\t"
    "v_cvt_pk_bf16_f32 v97, v30, v31\n\t"
    "s_nop 3\n\t"
    "v_cvt_pk_bf16_f32 v98, v36, v37\n\t"
    "v_cvt_pk_bf16_f32 v99, v38, v39\n\t"
    "v_cndmask_b32_dpp v36, v96, v100, vcc quad_perm:[0,0,2,2] row_mask:0xf bank_mask:0xf\n\t"
    "v_cndmask_b32_dpp v37, v97, v101, vcc quad_perm:[0,0,2,2] row_mask:0xf bank_mask:0xf\n\t"
    "v_cndmask_b32_dpp v38, v98, v102, vcc quad_perm:[0,0,2,2] row_mask:0xf bank_mask:0xf\n\t"
    "v_cndmask_b32_dpp v39, v99, v103, vcc quad_perm:[0,0,2,2] row_mask:0xf bank_mask:0xf\n\t"
    "s_not_b64 vcc, s[18:19]\n\t"
    "buffer_store_dwordx4 v[36:39], v241, s[44:47], s66 offen offset:0 sc1\n\t"
    "s_nop 1\n\t"
    "v_cndmask_b32_dpp v96, v100, v96, vcc quad_perm:[1,1,3,3] row_mask:0xf bank_mask:0xf\n\t"
    "v_cndmask_b32_dpp v97, v101, v97, vcc quad_perm:[1,1,3,3] row_mask:0xf bank_mask:0xf\n\t"
    "v_cndmask_b32_dpp v98, v102, v98, vcc quad_perm:[1,1,3,3] row_mask:0xf bank_mask:0xf\n\t"
    "v_cndmask_b32_dpp v99, v103, v99, vcc quad_perm:[1,1,3,3] row_mask:0xf bank_mask:0xf\n\t"
    "buffer_store_dwordx4 v[96:99], v241, s[44:47], s66 offen offset:1152 sc1\n\t"
    "s_nop 1\n\t"
    "s_barrier\n\t"
    ".Lpad4_%=:\n\t"
    "s_cbranch_execz .Lwarmr_%=\n\t"
    "spmm_code_end:\n\t"
    : "+{v0}"(tid0)
    : "{s[34:35]}"(ka), "{s62}"(blockIdx.x), "s"(lds0)
    : "v1", "v2", "v3", "v4", "v5", "v6", "v7", "v8", "v9", "v10", "v11", "v12", "v13", "v14", "v15", "v16",
      "v17", "v18", "v19", "v20", "v21", "v22", "v23", "v24", "v25", "v26", "v27", "v28", "v29", "v30", "v31", "v32",
      "v33", "v34", "v35", "v36", "v37", "v38", "v39", "v40", "v41", "v42", "v43", "v44", "v45", "v46", "v47", "v48",
      "v49", "v50", "v51", "v52", "v53", "v54", "v55", "v56", "v57", "v58", "v59", "v60", "v61", "v62", "v63", "v64",
      "v65", "v66", "v67", "v68", "v69", "v70", "v71", "v72", "v73", "v74", "v75", "v76", "v77", "v78", "v79", "v80",
      "v81", "v82", "v83", "v84", "v85", "v86", "v87", "v88", "v89", "v90", "v91", "v92", "v93", "v94", "v95", "v96",
      "v97", "v98", "v99", "v100", "v101", "v102", "v103", "v104", "v105", "v106", "v107", "v108", "v109", "v110", "v111", "v112",
      "v113", "v114", "v115", "v116", "v117", "v118", "v119", "v120", "v121", "v122", "v123", "v124", "v125", "v126", "v127", "v128",
      "v129", "v130", "v131", "v132", "v133", "v134", "v135", "v136", "v137", "v138", "v139", "v140", "v141", "v142", "v143", "v144",
      "v145", "v146", "v147", "v148", "v149", "v150", "v151", "v152", "v153", "v154", "v155", "v156", "v157", "v158", "v159", "v160",
      "v161", "v162", "v163", "v164", "v165", "v166", "v167", "v168", "v169", "v170", "v171", "v172", "v173", "v174", "v175", "v176",
      "v177", "v178", "v179", "v180", "v181", "v182", "v183", "v184", "v185", "v186", "v187", "v188", "v189", "v190", "v191", "v192",
      "v193", "v194", "v195", "v196", "v197", "v198", "v199", "v200", "v201", "v202", "v203", "v204", "v205", "v206", "v207", "v208",
      "v209", "v210", "v211", "v212", "v213", "v214", "v215", "v216", "v217", "v218", "v219", "v220", "v221", "v222", "v223", "v224",
      "v225", "v226", "v227", "v228", "v229", "v230", "v231", "v232", "v233", "v234", "v235", "v236", "v237", "v238", "v239", "v240",
      "v241", "v242", "v243", "v244", "v245", "v246", "v247", "v248", "v249", "v250", "v251", "v252", "v253", "v254", "v255", "a0",
      "a1", "a2", "a3", "a4", "a5", "a6", "a7", "a8", "a9", "a10", "a11", "a12", "a13", "a14", "a15", "a16",
      "a17", "a18", "a19", "a20", "a21", "a22", "a23", "a24", "a25", "a26", "a27", "a28", "a29", "a30", "a31", "a32",
      "a33", "a34", "a35", "a36", "a37", "a38", "a39", "a40", "a41", "a42", "a43", "a44", "a45", "a46", "a47", "a48",
      "a49", "a50", "a51", "a52", "a53", "a54", "a55", "a56", "a57", "a58", "a59", "a60", "a61", "a62", "a63", "a64",
      "a65", "a66", "a67", "a68", "a69", "a70", "a71", "a72", "a73", "a74", "a75", "a76", "a77", "a78", "a79", "a80",
      "a81", "a82", "a83", "a84", "a85", "a86", "a87", "a88", "a89", "a90", "a91", "a92", "a93", "a94", "a95", "a96",
      "a97", "a98", "a99", "a100", "a101", "a102", "a103", "a104", "a105", "a106", "a107", "a108", "a109", "a110", "a111", "a112",
      "a113", "a114", "a115", "a116", "a117", "a118", "a119", "a120", "a121", "a122", "a123", "a124", "a125", "a126", "a127", "a128",
      "a129", "a130", "a131", "a132", "a133", "a134", "a135", "a136", "a137", "a138", "a139", "a140", "a141", "a142", "a143", "a144",
      "a145", "a146", "a147", "a148", "a149", "a150", "a151", "a152", "a153", "a154", "a155", "a156", "a157", "a158", "a159", "a160",
      "a161", "a162", "a163", "a164", "a165", "a166", "a167", "a168", "a169", "a170", "a171", "a172", "a173", "a174", "a175", "a176",
      "a177", "a178", "a179", "a180", "a181", "a182", "a183", "a184", "a185", "a186", "a187", "a188", "a189", "a190", "a191", "a192",
      "a193", "a194", "a195", "a196", "a197", "a198", "a199", "a200", "a201", "a202", "a203", "a204", "a205", "a206", "a207", "a208",
      "a209", "a210", "a211", "a212", "a213", "a214", "a215", "a216", "a217", "a218", "a219", "a220", "a221", "a222", "a223", "a224",
      "a225", "a226", "a227", "a228", "a229", "a230", "a231", "a232", "a233", "a234", "a235", "a236", "a237", "a238", "a239", "a240",
      "a241", "a242", "a243", "a244", "a245", "a246", "a247", "a248", "a249", "a250", "a251", "a252", "a253", "a254", "a255", "s4",
      "s5", "s6", "s7", "s8", "s9", "s10", "s11", "s12", "s13", "s14", "s15", "s16", "s17", "s18", "s19", "s20",
      "s21", "s22", "s23", "s24", "s25", "s26", "s27", "s28", "s29", "s30", "s31", "s36", "s37", "s38", "s39", "s40",
      "s41", "s42", "s43", "s44", "s45", "s46", "s47", "s48", "s49", "s50", "s51", "s52", "s53", "s54", "s55", "s56",
      "s57", "s58", "s59", "s60", "s61", "s63", "s64", "s65", "s66", "s67", "s68", "s69", "s70", "s71", "s72", "s73",
      "s74", "s75", "s76", "s77", "s78", "s79", "s80", "s81", "s82", "s83", "s84", "s85", "s86", "s87", "s88", "s89",
      "s90", "s91", "s92", "s93", "s94", "s95", "s96", "s97", "s98", "s99", "vcc", "scc", "memory");
}

}

extern "C" void kernel_launch(void* const* d_in, const int* in_sizes, int n_in, void* d_out, int out_size,
                              void* d_ws, size_t ws_size, hipStream_t stream) {
  const uint16_t* x = (const uint16_t*)d_in[0];
  const uint16_t* w = (const uint16_t*)d_in[1];
  const uint16_t* b = (const uint16_t*)d_in[2];
  if (in_sizes[0] != NBLK * NTILE * TROWS * DM) return;
  spmm_main<<<NBLK, 256, 0, stream>>>(x, w, b, (uint16_t*)d_out, (char*)d_ws);
}
```
